```python
import torch

def _softmax(x, axis=-1):
    return torch.softmax(x, dim=axis)
def _gelu(x):
    return torch.nn.functional.gelu(x, approximate="tanh")
def _silu(x):
    return torch.nn.functional.silu(x)
def _sigmoid(x):
    return torch.sigmoid(x)
def _layernorm(x, axis=-1, eps=1e-5):
    m = x.mean(dim=axis, keepdim=True)
    v = x.var(dim=axis, keepdim=True, unbiased=False)
    return (x - m) / torch.sqrt(v + eps)
def _scatter_add(tgt, idx, src, axis=0):
    return tgt.clone().index_add_(axis, idx, src)
def _segment_sum(src, seg, n):
    out = torch.zeros((n,) + src.shape[1:], dtype=src.dtype)
    return out.index_add_(0, seg, src)
def _pad(x, pw):
    flat = tuple(v for pair in reversed(pw) for v in pair)
    return torch.nn.functional.pad(x, flat)

kernel_name = 'sequential_preLN__ssm_conv_s4__glu_gated_reglu__prompt'

def setup_inputs():

    _w = (lambda a: (lambda t: t.to(torch.bfloat16) if t.is_floating_point() else t)(torch.as_tensor(a)))
    def _A_setup():
        B, L, D, N = 1, 2048, 640, 192
        A_re = -(lambda _x: torch.exp(torch.as_tensor(_x)))((lambda *s: torch.randn(*s, dtype=torch.bfloat16) * 3.2)(D, N))
        A_im = torch.pi * (lambda *s: torch.randn(*s, dtype=torch.bfloat16) * 0.05)(D, N)
        B_re = (lambda *s: torch.randn(*s, dtype=torch.bfloat16) * 0.05)(D, N)
        B_im = (lambda *s: torch.randn(*s, dtype=torch.bfloat16) * 0.1)(D, N)
        C_re = (lambda *s: torch.randn(*s, dtype=torch.bfloat16) * 0.1)(D, N)
        C_im = (lambda *s: torch.randn(*s, dtype=torch.bfloat16) * 0.05)(D, N)
        log_dt = (lambda *s: torch.randn(*s, dtype=torch.bfloat16) * 0.05)(D) * 0.5 - 4.0
        Dskip = (lambda *s: torch.randn(*s, dtype=torch.bfloat16) * 0.05)(D)
        return {
            "u": _w((lambda *s: torch.randn(*s, dtype=torch.bfloat16) * 0.05)(B, L, D)),
            "A_re": _w(A_re),
            "A_im": _w(A_im),
            "B_re": _w(B_re),
            "B_im": _w(B_im),
            "C_re": _w(C_re),
            "C_im": _w(C_im),
            "log_dt": _w(log_dt),
            "Dskip": _w(Dskip),
        }
    _A_in = _A_setup()

    def _B_setup():
        B, S, D, F = 1, 2048, 640, 1536
        return {
            "x": _w((lambda *s: torch.randn(*s, dtype=torch.bfloat16) * 0.05)(B, S, D)),
            "W_gate": _w((lambda *s: torch.randn(*s, dtype=torch.bfloat16) * 0.4)(D, F) * (2.0 / D) ** 0.5),
            "W_up": _w((lambda *s: torch.randn(*s, dtype=torch.bfloat16) * 0.4)(D, F) * (2.0 / D) ** 0.5),
            "W_down": _w((lambda *s: torch.randn(*s, dtype=torch.bfloat16) * 0.4)(F, D) * (1.0 / F) ** 0.5),
        }
    _B_in = _B_setup()

    return {
        "A_u": _A_in["u"],
        "A_A_re": _A_in["A_re"],
        "A_A_im": _A_in["A_im"],
        "A_B_re": _A_in["B_re"],
        "A_B_im": _A_in["B_im"],
        "A_C_re": _A_in["C_re"],
        "A_C_im": _A_in["C_im"],
        "A_log_dt": _A_in["log_dt"],
        "A_Dskip": _A_in["Dskip"],
        "B_x": _B_in["x"],
        "B_W_gate": _B_in["W_gate"],
        "B_W_up": _B_in["W_up"],
        "B_W_down": _B_in["W_down"],
    }

def reference(A_u, A_A_re, A_A_im, A_B_re, A_B_im, A_C_re, A_C_im, A_log_dt, A_Dskip, B_x, B_W_gate, B_W_up, B_W_down):
    def _A_ref(u, A_re, A_im, B_re, B_im, C_re, C_im, log_dt, Dskip):
        Bsz, L, D = u.shape
        dt = torch.exp(log_dt)[:, None]
        A = A_re + 1j * A_im
        Bc = B_re + 1j * B_im
        Cc = C_re + 1j * C_im
        logdA = dt * A
        dB = dt * Bc
        l = (lambda x, dt: x.to(dt))(torch.arange(L), A_re.dtype)
        P = torch.exp(logdA[:, :, None] * l[None, None, :])
        K = torch.einsum("dn,dnl,dn->dl", Cc, P, dB).real
        L2 = 2 * L
        Kp = _pad(K, ((0, 0), (0, L)))
        up = (lambda x, *d: x.permute(*d))(u, 0, 2, 1)
        up = _pad(up, ((0, 0), (0, 0), (0, L)))
        Kf = (lambda x, axes=(-2,-1): torch.fft.rfft2(x.float(), dim=axes))(Kp[:, None, :], axes=(-2, -1))
        Uf = (lambda x, axes=(-2,-1): torch.fft.rfft2(x.float(), dim=axes))(up[:, :, None, :], axes=(-2, -1))
        Yf = Uf * Kf[None, :, :, :]
        y = (lambda x, s, axes=(-2,-1): torch.fft.irfft2(x, s=s, dim=axes).to(torch.bfloat16))(Yf, (1, L2), axes=(-2, -1))[:, :, 0, :L]
        y = (lambda x, *d: x.permute(*d))(y, 0, 2, 1) + u * Dskip[None, None, :]
        return y

    def _B_ref(x, W_gate, W_up, W_down):
        gate = torch.einsum("bsd,df->bsf", x, W_gate)
        up = torch.einsum("bsd,df->bsf", x, W_up)
        gate = gate * (lambda x, dt: x.to(dt))(gate > 0, gate.dtype)
        h = gate * up
        return torch.einsum("bsf,fd->bsd", h, W_down)

    x = A_u
    h = x + _A_ref(_layernorm(x, axis=-1), A_A_re, A_A_im, A_B_re, A_B_im, A_C_re, A_C_im, A_log_dt, A_Dskip)
    h2 = h + _B_ref(_layernorm(h, axis=-1), B_W_gate, B_W_up, B_W_down)
    return h2
```

```cpp
#include <hip/hip_runtime.h>
#include <hip/hip_bf16.h>
#include <stdint.h>

typedef unsigned short u16;
#define L_ 2048
#define D_ 640
#define N_ 192
#define F_ 1536

__device__ __forceinline__ float bf2f(u16 v) { return __uint_as_float(((unsigned)v) << 16); }
__device__ __forceinline__ u16 f2bf(float f) { unsigned u = __float_as_uint(f); u += 0x7fffu + ((u >> 16) & 1u); return (u16)(u >> 16); }
__device__ __forceinline__ float rbf(float f) { return bf2f(f2bf(f)); }

__global__ void ln_kernel(const u16* __restrict__ x, u16* __restrict__ out) {
    __shared__ float red[256];
    int l = blockIdx.x, t = threadIdx.x;
    float s = 0.f;
    for (int d = t; d < D_; d += 256) s += bf2f(x[l * D_ + d]);
    red[t] = s; __syncthreads();
    for (int o = 128; o > 0; o >>= 1) { if (t < o) red[t] += red[t + o]; __syncthreads(); }
    float mean = red[0] / D_; __syncthreads();
    float v = 0.f;
    for (int d = t; d < D_; d += 256) { float q = bf2f(x[l * D_ + d]) - mean; v += q * q; }
    red[t] = v; __syncthreads();
    for (int o = 128; o > 0; o >>= 1) { if (t < o) red[t] += red[t + o]; __syncthreads(); }
    float var = red[0] / D_;
    float m = rbf(mean), vv = rbf(var);
    float sd = rbf(sqrtf(rbf(vv + 1e-5f)));
    for (int d = t; d < D_; d += 256) { float q = rbf(bf2f(x[l * D_ + d]) - m); out[l * D_ + d] = f2bf(q / sd); }
}

__global__ void kgen_kernel(const u16* Are, const u16* Aim, const u16* Bre, const u16* Bim, const u16* Cre, const u16* Cim, const u16* logdt, float* K) {
    int d = blockIdx.y; int l = blockIdx.x * 256 + threadIdx.x;
    float dt = rbf(expf(bf2f(logdt[d])));
    float lf = rbf((float)l);
    float acc = 0.f;
    for (int n = 0; n < N_; ++n) {
        float are = dt * bf2f(Are[d * N_ + n]), aim = dt * bf2f(Aim[d * N_ + n]);
        float bre = dt * bf2f(Bre[d * N_ + n]), bim = dt * bf2f(Bim[d * N_ + n]);
        float cre = bf2f(Cre[d * N_ + n]), cim = bf2f(Cim[d * N_ + n]);
        float wre = cre * bre - cim * bim, wim = cre * bim + cim * bre;
        float e = expf(are * lf); float sn, cs; sincosf(aim * lf, &sn, &cs);
        acc += e * (wre * cs - wim * sn);
    }
    K[d * L_ + l] = acc;
}

__global__ void conv_kernel(const u16* x, const u16* u, const float* K, const u16* Dskip, u16* h) {
    int l = blockIdx.x;
    for (int d = threadIdx.x; d < D_; d += blockDim.x) {
        float acc = 0.f;
        for (int j = 0; j <= l; ++j) acc += K[d * L_ + j] * bf2f(u[(l - j) * D_ + d]);
        float y = rbf(acc);
        float sk = rbf(bf2f(u[l * D_ + d]) * bf2f(Dskip[d]));
        float yy = rbf(y + sk);
        h[l * D_ + d] = f2bf(bf2f(x[l * D_ + d]) + yy);
    }
}

__global__ void gemm1_kernel(const u16* z, const u16* Wg, const u16* Wu, u16* hm) {
    int l = blockIdx.y; int f = blockIdx.x * 256 + threadIdx.x;
    float g = 0.f, up = 0.f;
    for (int d = 0; d < D_; ++d) { float a = bf2f(z[l * D_ + d]); g += a * bf2f(Wg[d * F_ + f]); up += a * bf2f(Wu[d * F_ + f]); }
    g = rbf(g); up = rbf(up);
    if (!(g > 0.f)) g = g * 0.f;
    hm[l * F_ + f] = f2bf(g * up);
}
__global__ void gemm2_kernel(const u16* hm, const u16* Wd, const u16* h, u16* out) {
    int l = blockIdx.x;
    for (int d = threadIdx.x; d < D_; d += blockDim.x) {
        float a = 0.f;
        for (int f = 0; f < F_; ++f) a += bf2f(hm[l * F_ + f]) * bf2f(Wd[f * D_ + d]);
        out[l * D_ + d] = f2bf(bf2f(h[l * D_ + d]) + rbf(a));
    }
}

extern "C" void kernel_launch(void* const* d_in, const int* in_sizes, int n_in,
                              void* d_out, int out_size, void* d_ws, size_t ws_size,
                              hipStream_t stream) {
    const u16* x = (const u16*)d_in[0];
    const u16* Are = (const u16*)d_in[1]; const u16* Aim = (const u16*)d_in[2];
    const u16* Bre = (const u16*)d_in[3]; const u16* Bim = (const u16*)d_in[4];
    const u16* Cre = (const u16*)d_in[5]; const u16* Cim = (const u16*)d_in[6];
    const u16* logdt = (const u16*)d_in[7]; const u16* Dskip = (const u16*)d_in[8];
    const u16* Wg = (const u16*)d_in[10]; const u16* Wu = (const u16*)d_in[11]; const u16* Wd = (const u16*)d_in[12];
    char* ws = (char*)d_ws;
    u16* u = (u16*)ws; ws += L_ * D_ * 2;
    u16* h = (u16*)ws; ws += L_ * D_ * 2;
    u16* z = (u16*)ws; ws += L_ * D_ * 2;
    u16* hm = (u16*)ws; ws += L_ * F_ * 2;
    float* K = (float*)ws; ws += D_ * L_ * 4;
    ln_kernel<<<L_, 256, 0, stream>>>(x, u);
    kgen_kernel<<<dim3(L_ / 256, D_), 256, 0, stream>>>(Are, Aim, Bre, Bim, Cre, Cim, logdt, K);
    conv_kernel<<<L_, 320, 0, stream>>>(x, u, K, Dskip, h);
    ln_kernel<<<L_, 256, 0, stream>>>(h, z);
    gemm1_kernel<<<dim3(F_ / 256, L_), 256, 0, stream>>>(z, Wg, Wu, hm);
    gemm2_kernel<<<L_, 320, 0, stream>>>(hm, Wd, h, (u16*)d_out);
}
```

```cpp
#include <hip/hip_runtime.h>
#include <stdint.h>

typedef unsigned short u16;
typedef unsigned int u32;
typedef short bf16x8 __attribute__((ext_vector_type(8)));
typedef short s16x4 __attribute__((ext_vector_type(4)));
typedef float f32x2 __attribute__((ext_vector_type(2)));
typedef float f32x4 __attribute__((ext_vector_type(4)));
typedef float f32x16 __attribute__((ext_vector_type(16)));
typedef unsigned u32x4 __attribute__((ext_vector_type(4)));
typedef unsigned u32x2 __attribute__((ext_vector_type(2)));
typedef __bf16 bf16x2_t __attribute__((ext_vector_type(2)));
#define LAS __attribute__((address_space(3)))
#define DI __device__ __forceinline__

constexpr int L_ = 2048, D_ = 640, NM_ = 192, F_ = 1536;

constexpr size_t WS_UX  = 0;
constexpr size_t WS_KL  = WS_UX + (size_t)D_ * L_ * 4;
constexpr size_t WS_HT  = WS_KL + (size_t)D_ * L_ * 2;
constexpr size_t WS_HM  = WS_HT + (size_t)D_ * L_ * 2;
constexpr size_t WS_CS  = WS_HM + (size_t)L_ * F_ * 2;
constexpr size_t WS_W1T = WS_CS + 3072 * 4;
constexpr size_t WS_W2T = WS_W1T + (size_t)D_ * F_ * 2 * 2;
constexpr size_t WS_END = WS_W2T + (size_t)F_ * D_ * 2;

DI float bflo(u32 v) { return __uint_as_float(v << 16); }
DI float bfhi(u32 v) { return __uint_as_float(v & 0xffff0000u); }
DI float bf2f(u16 v) { return __uint_as_float(((u32)v) << 16); }
DI u32 cvtpk(float lo, float hi) { f32x2 v = {lo, hi}; bf16x2_t r = __builtin_convertvector(v, bf16x2_t); return __builtin_bit_cast(u32, r); }
DI float rbf(float f) { return bflo(cvtpk(f, f)); }

DI float wave_sum(float v) {
    v += __builtin_bit_cast(float, __builtin_amdgcn_update_dpp(0, __builtin_bit_cast(int, v), 0xB1, 0xF, 0xF, true));
    v += __builtin_bit_cast(float, __builtin_amdgcn_update_dpp(0, __builtin_bit_cast(int, v), 0x4E, 0xF, 0xF, true));
    v += __builtin_bit_cast(float, __builtin_amdgcn_update_dpp(0, __builtin_bit_cast(int, v), 0x141, 0xF, 0xF, true));
    v += __builtin_bit_cast(float, __builtin_amdgcn_update_dpp(0, __builtin_bit_cast(int, v), 0x140, 0xF, 0xF, true));
    { auto r = __builtin_amdgcn_permlane16_swap(__float_as_uint(v), __float_as_uint(v), false, false); v = __uint_as_float(r[0]) + __uint_as_float(r[1]); }
    { auto r = __builtin_amdgcn_permlane32_swap(__float_as_uint(v), __float_as_uint(v), false, false); v = __uint_as_float(r[0]) + __uint_as_float(r[1]); }
    return v;
}

struct cf { float re, im; };
DI cf cmul(cf a, cf b) { return { a.re * b.re - a.im * b.im, a.re * b.im + a.im * b.re }; }
DI cf csq(cf a) { return { a.re * a.re - a.im * a.im, 2.f * a.re * a.im }; }
DI cf cexp_p(float ar, float br, float p) {
    float e = __builtin_amdgcn_exp2f(ar * p);
    float th = __builtin_amdgcn_fractf(br * p);
    return { e * __builtin_amdgcn_cosf(th), e * __builtin_amdgcn_sinf(th) };
}

constexpr int KG_PITCH = 784;
constexpr int KG_AROWS = 20, KG_BROWS = 32;
constexpr int K1_LDS = (KG_AROWS + KG_BROWS) * KG_PITCH;

DI void k1_ln(int b, const u16* __restrict__ x, u32* __restrict__ UX, char* smem) {
    const int t = threadIdx.x, w = t >> 6, lane = t & 63;
    const int row0 = 256 * (b & 7) + 8 * (b >> 3);
    const u32* x32 = (const u32*)x;
    u32 ra[2][5], ca[8], cb[8];
#pragma unroll
    for (int rr = 0; rr < 2; ++rr)
#pragma unroll
        for (int i = 0; i < 5; ++i) ra[rr][i] = x32[(row0 + 2 * w + rr) * 320 + lane + 64 * i];
#pragma unroll
    for (int r = 0; r < 8; ++r) ca[r] = x32[(row0 + r) * 320 + t];
    if (w == 0) {
#pragma unroll
        for (int r = 0; r < 8; ++r) cb[r] = x32[(row0 + r) * 320 + 256 + t];
    }
    f32x2* st = (f32x2*)smem;
#pragma unroll
    for (int rr = 0; rr < 2; ++rr) {
        float s = 0.f;
#pragma unroll
        for (int i = 0; i < 5; ++i) s += bflo(ra[rr][i]) + bfhi(ra[rr][i]);
        const float mean = wave_sum(s) * (1.f / 640.f);
        float v = 0.f;
#pragma unroll
        for (int i = 0; i < 5; ++i) { float a = bflo(ra[rr][i]) - mean, c = bfhi(ra[rr][i]) - mean; v += a * a + c * c; }
        const float var = wave_sum(v) * (1.f / 640.f);
        const float m = rbf(mean);
        const float sd = rbf(__builtin_sqrtf(rbf(rbf(var) + 1e-5f)));
        if (lane == 0) st[2 * w + rr] = f32x2{m, 1.f / sd};
    }
    __syncthreads();
    f32x2 s8[8];
#pragma unroll
    for (int r = 0; r < 8; ++r) s8[r] = st[r];
    auto emit = [&](const u32(&c)[8], int col) {
        u32 o0[8], o1[8];
#pragma unroll
        for (int r = 0; r < 8; ++r) {
            const float q0 = rbf(bflo(c[r]) - s8[r].x), q1 = rbf(bfhi(c[r]) - s8[r].x);
            const u32 uu = cvtpk(q0 * s8[r].y, q1 * s8[r].y);
            o0[r] = (uu & 0xffffu) | (c[r] << 16);
            o1[r] = (uu >> 16) | (c[r] & 0xffff0000u);
        }
        u32x4* p0 = (u32x4*)(UX + (size_t)(2 * col) * L_ + row0);
        u32x4* p1 = (u32x4*)(UX + (size_t)(2 * col + 1) * L_ + row0);
        p0[0] = u32x4{o0[0], o0[1], o0[2], o0[3]}; p0[1] = u32x4{o0[4], o0[5], o0[6], o0[7]};
        p1[0] = u32x4{o1[0], o1[1], o1[2], o1[3]}; p1[1] = u32x4{o1[4], o1[5], o1[6], o1[7]};
    };
    emit(ca, t);
    if (w == 0) emit(cb, 256 + t);
}

DI void k1_kgen(int d, const u16* __restrict__ Are, const u16* __restrict__ Aim, const u16* __restrict__ Bre, const u16* __restrict__ Bim,
                const u16* __restrict__ Cre, const u16* __restrict__ Cim, const u16* __restrict__ logdt, u16* __restrict__ KL, char* smem) {
    const int t = threadIdx.x, w = t >> 6, lane = t & 63;
    if (w < 3) {
        const int n = d * NM_ + 64 * w + lane;
        const float are = bf2f(Are[n]), aim = bf2f(Aim[n]), bre = bf2f(Bre[n]), bim = bf2f(Bim[n]), cre = bf2f(Cre[n]), cim = bf2f(Cim[n]);
        const float dt = rbf(__expf(bf2f(logdt[d])));
        const float ar = (dt * are) * 1.4426950408889634f, br = (dt * aim) * 0.15915494309189535f;
        const float dbr = dt * bre, dbi = dt * bim;
        const cf W = { cre * dbr - cim * dbi, cre * dbi + cim * dbr };
        char* wa = smem + w * 256 + lane * 4;
        char* wb = wa + KG_AROWS * KG_PITCH;
        auto putA = [&](int row, cf v) { *(u32*)(wa + row * KG_PITCH) = cvtpk(v.re, -v.im); };
        auto putB = [&](int row, cf v) { *(u32*)(wb + row * KG_PITCH) = cvtpk(v.re, v.im); };
        const cf l1 = cexp_p(ar, br, 1.f), l8 = cexp_p(ar, br, 8.f), l256 = cexp_p(ar, br, 256.f);
        const cf l2 = csq(l1), l4 = csq(l2);
        { cf v = W; putA(0, v);
#pragma unroll
          for (int r = 1; r < 8; ++r) { v = cmul(v, l1); putA(r, v); } }
        cf x256 = cmul(W, l256);
        { cf v = x256; putA(8, v);
#pragma unroll
          for (int r = 1; r < 4; ++r) { v = cmul(v, l2); putA(8 + r, v); } }
        cf v512 = cmul(x256, l256); putA(12, v512); putA(13, cmul(v512, l4));
        cf v768 = cmul(v512, l256); putA(14, v768); putA(15, cmul(v768, l4));
        cf v = cmul(v768, l256); putA(16, v);
        v = cmul(v, l256); putA(17, v);
        v = cmul(v, l256); putA(18, v);
        v = cmul(v, l256); putA(19, v);
        v = cmul(v, l256);
        const float k2048 = wave_sum(v.re);
        { const cf l16 = csq(l8);
          cf e0 = {1.f, 0.f}, e1 = l8; putB(0, e0); putB(1, e1);
#pragma unroll
          for (int c = 2; c < 32; c += 2) { e0 = cmul(e0, l16); e1 = cmul(e1, l16); putB(c, e0); putB(c + 1, e1); } }
        asm volatile("" ::: "memory");
        const int r32 = lane & 31, h = lane >> 5;
        const char* pa = smem + (r32 < KG_AROWS ? r32 : KG_AROWS - 1) * KG_PITCH + w * 256 + h * 16;
        const char* pb = smem + (KG_AROWS + r32) * KG_PITCH + w * 256 + h * 16;
        f32x16 acc = {};
#pragma unroll
        for (int s = 0; s < 8; ++s) {
            const bf16x8 a = *(const bf16x8*)(pa + 32 * s), bb = *(const bf16x8*)(pb + 32 * s);
            acc = __builtin_amdgcn_mfma_f32_32x32x16_bf16(a, bb, acc, 0, 0, 0);
        }
        auto putP = [&](int i, float val) { *(float*)(smem + (i >> 6) * KG_PITCH + w * 256 + (i & 63) * 4) = val; };
        const int c = r32;
        if (h == 0) {
#pragma unroll
            for (int q = 0; q < 4; ++q) { putP(q + 8 * c, acc[q]); putP(256 + q + 4 * c, acc[4 + q]); putP(512 + 32 * q + c, acc[8 + q]); }
        } else {
#pragma unroll
            for (int q = 0; q < 4; ++q) { putP(4 + q + 8 * c, acc[q]); putP(384 + (q & 1) + 64 * (q >> 1) + 2 * c, acc[4 + q]); }
            if (c == 0) putP(640, k2048);
        }
    }
    __syncthreads();
    int i0; u32 pat;
    if (t < 32)       { i0 = 8 * t;              pat = 0x76543210u; }
    else if (t < 64)  { i0 = 256 + 4 * (t - 32); pat = 0x43222100u; }
    else if (t < 128) { i0 = 384 + 2 * (t - 64); pat = 0x22111000u; }
    else              { i0 = 512 + (t - 128);    pat = (t & 1) ? 0x11110000u : 0x11100000u; }
    float kv[8];
#pragma unroll
    for (int j = 0; j < 8; ++j) {
        const int i = i0 + ((pat >> (4 * j)) & 15);
        const char* p = smem + (i >> 6) * KG_PITCH + (i & 63) * 4;
        kv[j] = *(const float*)p + *(const float*)(p + 256) + *(const float*)(p + 512);
    }
    *(u32x4*)(KL + (size_t)d * L_ + 8 * t) = u32x4{cvtpk(kv[0], kv[1]), cvtpk(kv[2], kv[3]), cvtpk(kv[4], kv[5]), cvtpk(kv[6], kv[7])};
}

DI void k1_colsum(int j, const u16* __restrict__ Wg, const u16* __restrict__ Wu, float* __restrict__ CS, char* smem) {
    const int t = threadIdx.x, ch = t & 7, rg = t >> 3;
    const u16* W = (j < 24 ? Wg + 64 * j : Wu + 64 * (j - 24)) + 8 * ch;
    u32x4 v[20];
#pragma unroll
    for (int i = 0; i < 20; ++i) v[i] = *(const u32x4*)(W + (size_t)(rg + 32 * i) * F_);
    float a[8] = {};
#pragma unroll
    for (int i = 0; i < 20; ++i)
#pragma unroll
        for (int q = 0; q < 4; ++q) { a[2 * q] += bflo(v[i][q]); a[2 * q + 1] += bfhi(v[i][q]); }
    float* red = (float*)smem;
#pragma unroll
    for (int q = 0; q < 8; ++q) red[rg * 65 + ch * 8 + q] = a[q];
    __syncthreads();
    if (t < 64) { float s = 0.f;
#pragma unroll
        for (int r = 0; r < 32; ++r) s += red[r * 65 + t];
        CS[64 * j + t] = s; }
}

__global__ __launch_bounds__(256, 4) void k1_prep(const u16* __restrict__ x, const u16* __restrict__ Are, const u16* __restrict__ Aim, const u16* __restrict__ Bre,
                                                  const u16* __restrict__ Bim, const u16* __restrict__ Cre, const u16* __restrict__ Cim, const u16* __restrict__ logdt,
                                                  const u16* __restrict__ Wg, const u16* __restrict__ Wu, char* __restrict__ ws) {
    __shared__ __attribute__((aligned(16))) char smem[K1_LDS];
    const int b = blockIdx.x;
    if (b < 256) k1_ln(b, x, (u32*)(ws + WS_UX), smem);
    else if (b < 896) k1_kgen(b - 256, Are, Aim, Bre, Bim, Cre, Cim, logdt, (u16*)(ws + WS_KL), smem);
    else k1_colsum(b - 896, Wg, Wu, (float*)(ws + WS_CS), smem);
}

constexpr int CV_KC = 0, CV_CS = 4160;
constexpr int CV_U = 4 * CV_CS;
constexpr int CV_RED = CV_U + 4608;
constexpr int CV_LDS = CV_RED + 24576;

DI s16x4 trread(const LAS char* p) { typedef short v4i16_t __attribute__((ext_vector_type(4))); return __builtin_bit_cast(s16x4, __builtin_amdgcn_ds_read_tr16_b64_v4i16((LAS v4i16_t*)p)); }
DI bf16x8 cat8(s16x4 a, s16x4 b) { return __builtin_shufflevector(a, b, 0, 1, 2, 3, 4, 5, 6, 7); }

__global__ __launch_bounds__(256, 3) void k2_conv(const u16* __restrict__ Dskip, const u16* __restrict__ Wg, const u16* __restrict__ Wu, const u16* __restrict__ Wd, char* __restrict__ ws) {
    __shared__ __attribute__((aligned(16))) char smem[CV_LDS];
    const int d = blockIdx.x, t = threadIdx.x, lane = t & 63;
    const int w = __builtin_amdgcn_readfirstlane(t >> 6);
    const u32* UX = (const u32*)(ws + WS_UX) + (size_t)d * L_;
    const u16* KL = (const u16*)(ws + WS_KL) + (size_t)d * L_;
    const int al = lane & 15, g = lane >> 4;
    u32x4 ux[2];
#pragma unroll
    for (int i = 0; i < 2; ++i) ux[i] = *(const u32x4*)(UX + 256 * (2 * w + i) + 16 * al + 4 * g);
    const u32x4 kc = *(const u32x4*)(KL + 8 * t);
    u32x4 kp = {0, 0, 0, 0};
    if (t > 0) kp = *(const u32x4*)(KL + 8 * t - 8);
    u32x4 wv[3]; u32 wdst[3];
#pragma unroll
    for (int i = 0; i < 3; ++i) {
        const int c = d * 576 + i * 256 + t;
        const bool ok = (i < 2) || (t < 64);
        const u16* src;
        if (c < 245760) {
            const int s = c / 15360, r = c % 15360, st = r / 1536, n = r % 1536;
            const int jq = n / 96, m = n % 96, mt = m >> 4, q = (m >> 2) & 3, blk = (m >> 1) & 1, cc = m & 1;
            const int k = 64 * st + 4 * jq + q, f = 96 * s + 16 * mt + 8 * cc;
            src = (blk ? Wu : Wg) + (size_t)k * F_ + f;
        } else {
            const int c2 = c - 245760, ds = c2 / 15360, r = c2 % 15360, st = r / 640, n = r % 640;
            const int j = n / 80, m = n % 80, mt = m >> 4, rr = (m >> 1) & 7, cc = m & 1;
            const int k = 64 * st + 32 * (j >> 2) + 16 * ((j >> 1) & 1) + 8 * (rr >> 2) + 4 * (j & 1) + (rr & 3);
            src = Wd + (size_t)k * D_ + 80 * ds + 16 * mt + 8 * cc;
        }
        wdst[i] = (u32)c;
        if (ok) wv[i] = *(const u32x4*)src;
    }
    const float dsk = bf2f(Dskip[d]);
    if (t < 30) *(u32x4*)(smem + CV_U + 16 * t) = u32x4{0, 0, 0, 0};
    if (t >= 64 && t < 72) { const int q = (t - 64) >> 1, hh = (t - 64) & 1; *(u32x4*)(smem + CV_KC + q * CV_CS + 16 * hh) = u32x4{0, 0, 0, 0}; }
#pragma unroll
    for (int i = 0; i < 2; ++i) {
        const u32 p0 = (ux[i][0] & 0xffffu) | (ux[i][1] << 16), p1 = (ux[i][2] & 0xffffu) | (ux[i][3] << 16);
        *(u32x2*)(smem + CV_U + 480 + 2 * (256 * (2 * w + i) + 16 * al + 4 * g)) = u32x2{p0, p1};
    }
    {
        const u32 k2 = kp[2], k3 = kp[3], k4 = kc[0], k5 = kc[1], k6 = kc[2], k7 = kc[3];
        const u32 a2 = __builtin_amdgcn_alignbit(k3, k2, 16), a3 = __builtin_amdgcn_alignbit(k4, k3, 16), a4 = __builtin_amdgcn_alignbit(k5, k4, 16),
                  a5 = __builtin_amdgcn_alignbit(k6, k5, 16), a6 = __builtin_amdgcn_alignbit(k7, k6, 16);
        char* kb = smem + CV_KC + 32 + 16 * t;
        *(u32x4*)(kb + 0 * CV_CS) = u32x4{k4, k5, k6, k7};
        *(u32x4*)(kb + 1 * CV_CS) = u32x4{a3, a4, a5, a6};
        *(u32x4*)(kb + 2 * CV_CS) = u32x4{k3, k4, k5, k6};
        *(u32x4*)(kb + 3 * CV_CS) = u32x4{a2, a3, a4, a5};
    }
    __syncthreads();
    f32x4 acc[8];
#pragma unroll
    for (int i = 0; i < 8; ++i) acc[i] = f32x4{0.f, 0.f, 0.f, 0.f};
    const LAS char* lds = (const LAS char*)smem;
    const int q = (lane & 15) >> 2, p = lane & 3;
#pragma unroll
    for (int i = 0; i < 2; ++i) {
        const int mu = 2 * w + i;
        const LAS char* ub = lds + CV_U + 448 + 32 * al + 16 * g - 64 * mu;
        const LAS char* kbp = lds + CV_KC + q * CV_CS + 56 - 16 * g + 8 * p + 64 * mu;
        bf16x8 U[8];
#pragma unroll
        for (int s = 0; s < 8; ++s) U[s] = *(const LAS bf16x8*)(ub + 512 * s);
#pragma unroll
        for (int m = 0; m < 8; ++m) {
            const bf16x8 A = cat8(trread(kbp + 512 * m + 8), trread(kbp + 512 * m));
#pragma unroll
            for (int tau = m; tau < 8; ++tau) acc[tau] = __builtin_amdgcn_mfma_f32_16x16x32_bf16(A, U[tau - m], acc[tau], 0, 0, 0);
        }
    }
    {
        char* rb = smem + CV_RED + w * 6144 + lane * 16;
        int slot = 0;
#pragma unroll
        for (int tau = 0; tau < 8; ++tau) { if ((tau >> 1) != w) { *(f32x4*)(rb + slot * 1024) = acc[tau]; ++slot; } }
    }
    __syncthreads();
    {
        u32x4* W1T = (u32x4*)(ws + WS_W1T);
#pragma unroll
        for (int i = 0; i < 3; ++i) if ((i < 2) || (t < 64)) W1T[wdst[i]] = wv[i];
    }
    u16* HT = (u16*)(ws + WS_HT) + (size_t)d * L_;
#pragma unroll
    for (int i = 0; i < 2; ++i) {
        f32x4 y;
        if (w == 0) y = acc[i]; else if (w == 1) y = acc[2 + i]; else if (w == 2) y = acc[4 + i]; else y = acc[6 + i];
        const int tau = 2 * w + i;
#pragma unroll
        for (int ow = 0; ow < 4; ++ow) {
            if (ow != w) {
                const int slot = tau - ((ow < w) ? 2 : 0) * 0;
                (void)slot;
                const int sl = (tau < 2 * ow) ? tau : tau - 2;
                y += *(const f32x4*)(smem + CV_RED + ow * 6144 + sl * 1024 + lane * 16);
            }
        }
        u32 o[4];
        float hv[4];
#pragma unroll
        for (int r = 0; r < 4; ++r) {
            const float u = bflo(ux[i][r]), xx = bfhi(ux[i][r]);
            const float yy = rbf(rbf(y[r]) + rbf(u * dsk));
            hv[r] = xx + yy;
        }
        (void)o;
        *(u32x2*)(HT + 256 * tau + 16 * al + 4 * g) = u32x2{cvtpk(hv[0], hv[1]), cvtpk(hv[2], hv[3])};
    }
}

constexpr int G1_STAGE = 40960, G1_NS = 3, G1_NST = 10;
constexpr int G1_EX = G1_NS * G1_STAGE;
constexpr int G1_LDS = G1_EX + 768 + 2048 + 1024;

DI void glds16(const void* g, u32 lds_base) {
    u32 sv; asm volatile("s_mov_b32 %0, m0\n\ts_mov_b32 m0, %2\n\ts_nop 0\n\tglobal_load_lds_dwordx4 %1, off\n\ts_mov_b32 m0, %0" : "=&s"(sv) : "v"(g), "s"(lds_base) : "memory");
}
#define WAITV(n) asm volatile("s_waitcnt vmcnt(" #n ")" ::: "memory")
#define WAITL0() asm volatile("s_waitcnt lgkmcnt(0)" ::: "memory")
#define BAR() do { asm volatile("" ::: "memory"); __builtin_amdgcn_s_barrier(); asm volatile("" ::: "memory"); } while (0)

__global__ __launch_bounds__(768, 3) void k3_gemm1(char* __restrict__ ws) {
    __shared__ __attribute__((aligned(1024))) char smem[G1_LDS];
    const int b = blockIdx.x, t = threadIdx.x, lane = t & 63;
    const int w = __builtin_amdgcn_readfirstlane(t >> 6);
    const int xcd = b & 7, j = b >> 3;
    const int P = (xcd & 1) * 8 + (j & 7), S = (xcd >> 1) * 4 + (j >> 3);
    const u32 lds0 = (u32)(uintptr_t)smem;
    if (w >= 4) {
        const int lw = w - 4;
        const char* wsrc = ws + WS_W1T + (size_t)S * 10 * 24576 + lw * 1024 + lane * 16;
        const int nt = lane >> 4, q = (lane >> 2) & 3, c4 = lane & 3;
        const char* hsrc = ws + WS_HT + ((size_t)(4 * lw + q) * L_ + 128 * P + 32 * nt + 8 * c4) * 2;
        auto issue = [&](int st) {
            const u32 base = lds0 + (st % G1_NS) * G1_STAGE;
#pragma unroll
            for (int i = 0; i < 3; ++i) glds16(wsrc + (size_t)st * 24576 + i * 8192, base + lw * 1024 + i * 8192);
#pragma unroll
            for (int i = 0; i < 2; ++i) glds16(hsrc + (size_t)(st * 64 + 32 * i) * L_ * 2, base + 24576 + (lw + 8 * i) * 1024);
        };
        if (lw == 0 && lane < 48) {
            const int half = lane / 24, c = lane % 24;
            glds16((const float*)(ws + WS_CS) + half * 1536 + 96 * S + 4 * c, lds0 + G1_EX);
        }
        issue(0); issue(1);
        float s1 = 0.f, s2 = 0.f;
        const int snt = lw & 3, sk = lw >> 2;
        const int h = lane >> 5, blk = (lane >> 4) & 1, qq = (lane & 15) >> 2, p = lane & 3;
        const LAS char* sb = (const LAS char*)smem + 24576 + (8 * sk + 2 * h) * 1024 + snt * 256 + qq * 64 + blk * 32 + 8 * p;
        const bf16x2_t ones = __builtin_bit_cast(bf16x2_t, 0x3f803f80u);
#pragma unroll 1
        for (int st = 0; st < G1_NST; ++st) {
            if (st + 1 < G1_NST) WAITV(5); else WAITV(0);
            BAR();
            if (st + 2 < G1_NST) issue(st + 2);
            const LAS char* sp = sb + (st % G1_NS) * G1_STAGE;
#pragma unroll
            for (int ks = 0; ks < 2; ++ks)
#pragma unroll
                for (int tt = 0; tt < 2; ++tt) {
                    const s16x4 v = trread(sp + ks * 4096 + tt * 1024);
                    const u32x2 vv = __builtin_bit_cast(u32x2, v);
#pragma unroll
                    for (int e = 0; e < 2; ++e) {
                        const u32 ze = vv[e]; const bf16x2_t z = __builtin_bit_cast(bf16x2_t, ze);
                        s1 = __builtin_amdgcn_fdot2_f32_bf16(z, ones, s1, false);
                        s2 = __builtin_amdgcn_fdot2_f32_bf16(z, z, s2, false);
                    }
                }
            WAITL0();
        }
        { auto r = __builtin_amdgcn_permlane32_swap(__float_as_uint(s1), __float_as_uint(s1), false, false); s1 = __uint_as_float(r[0]) + __uint_as_float(r[1]); }
        { auto r = __builtin_amdgcn_permlane32_swap(__float_as_uint(s2), __float_as_uint(s2), false, false); s2 = __uint_as_float(r[0]) + __uint_as_float(r[1]); }
        if (lane < 32) *(f32x2*)(smem + G1_EX + 768 + (lw * 32 + lane) * 8) = f32x2{s1, s2};
        WAITL0();
        BAR();
        return;
    }
    const int wm = w & 1, wn = w >> 1;
    const int h = lane >> 5, blk = (lane >> 4) & 1, q = (lane & 15) >> 2, p = lane & 3;
    f32x16 acc[3][2];
#pragma unroll
    for (int a = 0; a < 3; ++a)
#pragma unroll
        for (int c = 0; c < 2; ++c)
#pragma unroll
            for (int r = 0; r < 16; ++r) acc[a][c][r] = 0.f;
    const LAS char* abase = (const LAS char*)smem + h * 3072 + q * 64 + blk * 32 + 8 * p + wm * 768;
    const LAS char* bbase = (const LAS char*)smem + 24576 + h * 2048 + q * 64 + blk * 32 + 8 * p + wn * 512;
#pragma unroll 1
    for (int st = 0; st < G1_NST; ++st) {
        BAR();
        const LAS char* ap = abase + (st % G1_NS) * G1_STAGE;
        const LAS char* bp = bbase + (st % G1_NS) * G1_STAGE;
#pragma unroll
        for (int ks = 0; ks < 4; ++ks) {
            bf16x8 A[3], B[2];
#pragma unroll
            for (int c = 0; c < 2; ++c) B[c] = cat8(trread(bp + ks * 4096 + c * 256), trread(bp + ks * 4096 + 1024 + c * 256));
#pragma unroll
            for (int a = 0; a < 3; ++a) A[a] = cat8(trread(ap + ks * 6144 + a * 256), trread(ap + ks * 6144 + 1536 + a * 256));
#pragma unroll
            for (int a = 0; a < 3; ++a)
#pragma unroll
                for (int c = 0; c < 2; ++c) acc[a][c] = __builtin_amdgcn_mfma_f32_32x32x16_bf16(A[a], B[c], acc[a][c], 0, 0, 0);
        }
        WAITL0();
    }
    BAR();
    const int c32 = lane & 31;
    u16* HM = (u16*)(ws + WS_HM);
#pragma unroll
    for (int c = 0; c < 2; ++c) {
        const int ntile = 2 * wn + c;
        const f32x2 pa = *(const f32x2*)(smem + G1_EX + 768 + (ntile * 32 + c32) * 8);
        const f32x2 pb = *(const f32x2*)(smem + G1_EX + 768 + ((ntile + 4) * 32 + c32) * 8);
        const float mean = (pa.x + pb.x) * (1.f / 640.f);
        float var = (pa.y + pb.y) * (1.f / 640.f) - mean * mean;
        var = var > 0.f ? var : 0.f;
        const float m = rbf(mean);
        const float rs = 1.f / rbf(__builtin_sqrtf(rbf(rbf(var) + 1e-5f)));
        const int l = 128 * P + 32 * ntile + c32;
#pragma unroll
        for (int a = 0; a < 3; ++a) {
            const int fl = 16 * (3 * wm + a);
            const f32x4 cg0 = *(const f32x4*)(smem + G1_EX + (fl + 4 * h) * 4), cg1 = *(const f32x4*)(smem + G1_EX + (fl + 8 + 4 * h) * 4);
            const f32x4 cu0 = *(const f32x4*)(smem + G1_EX + 384 + (fl + 4 * h) * 4), cu1 = *(const f32x4*)(smem + G1_EX + 384 + (fl + 8 + 4 * h) * 4);
            float hv[8];
#pragma unroll
            for (int r = 0; r < 8; ++r) {
                const float csg = r < 4 ? cg0[r & 3] : cg1[r & 3], csu = r < 4 ? cu0[r & 3] : cu1[r & 3];
                float gt = rbf((acc[a][c][r] - m * csg) * rs);
                const float up = rbf((acc[a][c][8 + r] - m * csu) * rs);
                gt = gt > 0.f ? gt : gt * 0.f;
                hv[r] = gt * up;
            }
            u32 lo0 = cvtpk(hv[0], hv[1]), lo1 = cvtpk(hv[2], hv[3]), hi0 = cvtpk(hv[4], hv[5]), hi1 = cvtpk(hv[6], hv[7]);
            { auto r = __builtin_amdgcn_permlane32_swap(lo0, hi0, false, false); lo0 = r[0]; hi0 = r[1]; }
            { auto r = __builtin_amdgcn_permlane32_swap(lo1, hi1, false, false); lo1 = r[0]; hi1 = r[1]; }
            const int f0 = 96 * S + fl;
            const int kb = f0 >> 5, oct = ((f0 & 31) >> 3) + h;
            *(u32x4*)(HM + ((size_t)((l >> 4) * 48 + kb) * 64 + (l & 15) + 16 * oct) * 8) = u32x4{lo0, lo1, hi0, hi1};
        }
    }
}

constexpr int G2_STAGE = 18432, G2_NS = 7, G2_NST = 24;
constexpr int G2_RES = G2_NS * G2_STAGE;
constexpr int G2_LDS = G2_RES + 10240;

__global__ __launch_bounds__(768, 3) void k4_gemm2(char* __restrict__ ws, u16* __restrict__ out) {
    __shared__ __attribute__((aligned(1024))) char smem[G2_LDS];
    const int b = blockIdx.x, t = threadIdx.x, lane = t & 63;
    const int w = __builtin_amdgcn_readfirstlane(t >> 6);
    const int xcd = b & 7, j = b >> 3;
    const int P = (xcd & 3) * 8 + (j & 7), S = (xcd >> 2) * 4 + (j >> 3);
    const u32 lds0 = (u32)(uintptr_t)smem;
    if (w >= 4) {
        const int lw = w - 4;
        const char* wsrc = ws + WS_W2T + (size_t)S * 24 * 10240 + lane * 16;
        const char* msrc = ws + WS_HM + (size_t)(4 * P) * 48 * 1024 + lane * 16;
        auto piece = [&](int st, int pc) {
            const u32 base = lds0 + (st % G2_NS) * G2_STAGE + pc * 1024;
            const char* src = pc < 10 ? wsrc + (size_t)st * 10240 + pc * 1024
                                      : msrc + (size_t)(((pc - 10) >> 1) * 48 + 2 * st + ((pc - 10) & 1)) * 1024;
            glds16(src, base);
        };
        auto issue = [&](int st) { piece(st, lw); piece(st, lw + 8); if (lw < 2) piece(st, 16 + lw); };
        {
            for (int pc = lw; pc < 10; pc += 8) {
                const int row = 8 * pc + (lane >> 3), ch = (lane & 7) ^ (((lane >> 3) & 3) << 1);
                glds16(ws + WS_HT + ((size_t)(80 * S + row) * L_ + 64 * P + 8 * ch) * 2, lds0 + G2_RES + pc * 1024);
            }
        }
#pragma unroll 1
        for (int st = 0; st < G2_NS - 1; ++st) issue(st);
#pragma unroll 1
        for (int st = 0; st < G2_NST; ++st) {
            const int rem = (G2_NST - 1 - st) < (G2_NS - 2) ? (G2_NST - 1 - st) : (G2_NS - 2);
            if (lw < 2) { switch (rem) { case 5: WAITV(15); break; case 4: WAITV(12); break; case 3: WAITV(9); break; case 2: WAITV(6); break; case 1: WAITV(3); break; default: WAITV(0); } }
            else        { switch (rem) { case 5: WAITV(10); break; case 4: WAITV(8); break; case 3: WAITV(6); break; case 2: WAITV(4); break; case 1: WAITV(2); break; default: WAITV(0); } }
            BAR();
            if (st + G2_NS - 1 < G2_NST) issue(st + G2_NS - 1);
        }
        return;
    }
    const int g = lane >> 4, q = (lane & 15) >> 2, p = lane & 3;
    f32x4 acc[5];
#pragma unroll
    for (int a = 0; a < 5; ++a) acc[a] = f32x4{0.f, 0.f, 0.f, 0.f};
    const LAS char* abase = (const LAS char*)smem + (g >> 1) * 2560 + (g & 1) * 128 + q * 32 + 8 * p;
    const LAS char* bbase = (const LAS char*)smem + 10240 + w * 2048 + lane * 16;
#pragma unroll 1
    for (int st = 0; st < G2_NST; ++st) {
        BAR();
        const int so = (st % G2_NS) * G2_STAGE;
#pragma unroll
        for (int ks = 0; ks < 2; ++ks) {
            const bf16x8 B = *(const LAS bf16x8*)(bbase + so + ks * 1024);
#pragma unroll
            for (int a = 0; a < 5; ++a) {
                const bf16x8 A = cat8(trread(abase + so + ks * 5120 + a * 256), trread(abase + so + ks * 5120 + 1280 + a * 256));
                acc[a] = __builtin_amdgcn_mfma_f32_16x16x32_bf16(A, B, acc[a], 0, 0, 0);
            }
        }
        WAITL0();
    }
    const int l = 64 * P + 16 * w + (lane & 15);
#pragma unroll
    for (int a = 0; a < 5; ++a) {
        const int row = 16 * a + 4 * g + q;
        const int ch = (2 * w + (p >> 1)) ^ ((row & 3) << 1);
        const s16x4 hr = trread((const LAS char*)smem + G2_RES + row * 128 + ch * 16 + 8 * (p & 1));
        const u32x2 hh = __builtin_bit_cast(u32x2, hr);
        const float o0 = bflo(hh[0]) + rbf(acc[a][0]), o1 = bfhi(hh[0]) + rbf(acc[a][1]), o2 = bflo(hh[1]) + rbf(acc[a][2]), o3 = bfhi(hh[1]) + rbf(acc[a][3]);
        *(u32x2*)(out + (size_t)l * D_ + 80 * S + 16 * a + 4 * g) = u32x2{cvtpk(o0, o1), cvtpk(o2, o3)};
    }
}

extern "C" void kernel_launch(void* const* d_in, const int* in_sizes, int n_in, void* d_out, int out_size, void* d_ws, size_t ws_size, hipStream_t stream) {
    const u16* x = (const u16*)d_in[0];
    const u16* Are = (const u16*)d_in[1]; const u16* Aim = (const u16*)d_in[2];
    const u16* Bre = (const u16*)d_in[3]; const u16* Bim = (const u16*)d_in[4];
    const u16* Cre = (const u16*)d_in[5]; const u16* Cim = (const u16*)d_in[6];
    const u16* logdt = (const u16*)d_in[7]; const u16* Dskip = (const u16*)d_in[8];
    const u16* Wg = (const u16*)d_in[10]; const u16* Wu = (const u16*)d_in[11]; const u16* Wd = (const u16*)d_in[12];
    char* ws = (char*)d_ws;
    k1_prep<<<944, 256, 0, stream>>>(x, Are, Aim, Bre, Bim, Cre, Cim, logdt, Wg, Wu, ws);
    k2_conv<<<640, 256, 0, stream>>>(Dskip, Wg, Wu, Wd, ws);
    k3_gemm1<<<256, 768, 0, stream>>>(ws);
    k4_gemm2<<<256, 768, 0, stream>>>(ws, (u16*)d_out);
}
```

```cpp
#include <hip/hip_runtime.h>
#include <stdint.h>

typedef unsigned short u16;
typedef unsigned int u32;
typedef short bf16x8 __attribute__((ext_vector_type(8)));
typedef short s16x4 __attribute__((ext_vector_type(4)));
typedef float f32x2 __attribute__((ext_vector_type(2)));
typedef float f32x4 __attribute__((ext_vector_type(4)));
typedef float f32x16 __attribute__((ext_vector_type(16)));
typedef unsigned u32x4 __attribute__((ext_vector_type(4)));
typedef unsigned u32x2 __attribute__((ext_vector_type(2)));
typedef __bf16 bf16x2_t __attribute__((ext_vector_type(2)));
#define LAS __attribute__((address_space(3)))
#define DI __device__ __forceinline__

#define STAMP(ws, k, j) do {} while (0)

constexpr int L_ = 2048, D_ = 640, NM_ = 192, F_ = 1536;

constexpr u32 WS_QT  = 0;
constexpr u32 WS_KL  = WS_QT + D_ * L_ * 2;
constexpr u32 WS_HC  = WS_KL + D_ * L_ * 2;
constexpr u32 WS_HM  = WS_HC + D_ * L_ * 2;
constexpr u32 WS_CS  = WS_HM + L_ * F_ * 2;
constexpr u32 WS_ST  = WS_CS + 3072 * 4;
constexpr u32 WS_RS  = WS_ST + 2048 * 8;
constexpr u32 WS_W1T = WS_RS + 2048 * 4;
constexpr u32 WS_W2T = WS_W1T + D_ * F_ * 2 * 2;
constexpr u32 WS_END = WS_W2T + F_ * D_ * 2;

DI float bflo(u32 v) { return __uint_as_float(v << 16); }
DI float bfhi(u32 v) { return __uint_as_float(v & 0xffff0000u); }
DI float bf2f(u16 v) { return __uint_as_float(((u32)v) << 16); }
DI u32 cvtpk(float lo, float hi) { f32x2 v = {lo, hi}; bf16x2_t r = __builtin_convertvector(v, bf16x2_t); return __builtin_bit_cast(u32, r); }
DI float rbf(float f) { return bflo(cvtpk(f, f)); }

typedef __amdgpu_buffer_rsrc_t rsrc_t;
DI rsrc_t mkrsrc(void* p, u32 bytes) { return __builtin_amdgcn_make_buffer_rsrc(p, 0, bytes, 0x00020000); }
#define AUX_KL 0
#define AUX_WT 0
#define AUX_H 0
#define AUX_HM 0
template <int AUX = 0> DI void st16(rsrc_t r, u32 off, u32x4 v) { __builtin_amdgcn_raw_buffer_store_b128(v, r, off, 0, AUX); }
template <int AUX = 0> DI void st8(rsrc_t r, u32 off, u32x2 v) { __builtin_amdgcn_raw_buffer_store_b64(v, r, off, 0, AUX); }
template <int AUX = 0> DI void st4(rsrc_t r, u32 off, u32 v) { __builtin_amdgcn_raw_buffer_store_b32(v, r, off, 0, AUX); }

template <int CTRL> DI float dppadd(float v) { return v + __builtin_bit_cast(float, __builtin_amdgcn_update_dpp(0, __builtin_bit_cast(int, v), CTRL, 0xF, 0xF, true)); }
DI float swap16add(float v) { auto r = __builtin_amdgcn_permlane16_swap(__float_as_uint(v), __float_as_uint(v), false, false); const u32 a = r[0], b = r[1]; return __uint_as_float(a) + __uint_as_float(b); }
DI float swap32add(float v) { auto r = __builtin_amdgcn_permlane32_swap(__float_as_uint(v), __float_as_uint(v), false, false); const u32 a = r[0], b = r[1]; return __uint_as_float(a) + __uint_as_float(b); }
DI float wave_sum(float v) {
    v = dppadd<0xB1>(v);
    v = dppadd<0x4E>(v);
    v = dppadd<0x141>(v);
    v = dppadd<0x140>(v);
    return swap32add(swap16add(v));
}

struct cm { f32x2 a, s; };
DI cm mkcm(f32x2 b) { return { b, f32x2{-b.y, b.x} }; }
DI f32x2 cmul(f32x2 v, const cm& m) { return v.xx * m.a + v.yy * m.s; }
DI f32x2 csq(f32x2 a) { return f32x2{ a.x * a.x - a.y * a.y, 2.f * a.x * a.y }; }
DI f32x2 cexp_p(float ar, float br, float p) {
    float e = __builtin_amdgcn_exp2f(ar * p);
    float th = __builtin_amdgcn_fractf(br * p);
    return f32x2{ e * __builtin_amdgcn_cosf(th), e * __builtin_amdgcn_sinf(th) };
}

constexpr int KG_PITCH = 784;
constexpr int KG_AROWS = 20, KG_BROWS = 32;
constexpr int K1_LDS = (KG_AROWS + KG_BROWS) * KG_PITCH;

DI void k1_ln(int b, const u16* __restrict__ x, char* __restrict__ ws, char* smem) {
    const int t = threadIdx.x, w = t >> 6, lane = t & 63;
    const int row0 = 256 * (b & 7) + 16 * (b >> 3);
    const u32* x32 = (const u32*)x;
    u32 ra[4][5], ca[16], cb[16];
#pragma unroll
    for (int rr = 0; rr < 4; ++rr)
#pragma unroll
        for (int i = 0; i < 5; ++i) ra[rr][i] = x32[(row0 + 4 * w + rr) * 320 + lane + 64 * i];
#pragma unroll
    for (int r = 0; r < 16; ++r) ca[r] = x32[(row0 + r) * 320 + t];
    if (w == 0) {
#pragma unroll
        for (int r = 0; r < 16; ++r) cb[r] = x32[(row0 + r) * 320 + 256 + t];
    }
    const rsrc_t rs = mkrsrc(ws, WS_END);
    float* st = (float*)smem;
#pragma unroll
    for (int rr = 0; rr < 4; ++rr) {
        float s = 0.f;
#pragma unroll
        for (int i = 0; i < 5; ++i) s += bflo(ra[rr][i]) + bfhi(ra[rr][i]);
        const float mean = wave_sum(s) * (1.f / 640.f);
        float v = 0.f;
#pragma unroll
        for (int i = 0; i < 5; ++i) { float a = bflo(ra[rr][i]) - mean, c = bfhi(ra[rr][i]) - mean; v += a * a + c * c; }
        const float var = wave_sum(v) * (1.f / 640.f);
        const float m = rbf(mean);
        const float sd = rbf(__builtin_sqrtf(rbf(rbf(var) + 1e-5f)));
        if (lane == 0) { st[4 * w + rr] = m; st8(rs, WS_ST + (row0 + 4 * w + rr) * 8, u32x2{__float_as_uint(m), __float_as_uint(1.f / sd)}); }
    }
    __syncthreads();
    float m16[16];
#pragma unroll
    for (int r = 0; r < 16; r += 4) { const f32x4 v = *(const f32x4*)(st + r); m16[r] = v[0]; m16[r + 1] = v[1]; m16[r + 2] = v[2]; m16[r + 3] = v[3]; }
    auto emit = [&](const u32(&c)[16], int col) {
        u32 o0[8], o1[8];
#pragma unroll
        for (int r = 0; r < 16; r += 2) {
            const u32 qa = cvtpk(bflo(c[r]) - m16[r], bflo(c[r + 1]) - m16[r + 1]);
            const u32 qb = cvtpk(bfhi(c[r]) - m16[r], bfhi(c[r + 1]) - m16[r + 1]);
            o0[r >> 1] = qa; o1[r >> 1] = qb;
        }
        const u32 p0 = WS_QT + ((2 * col) * L_ + row0) * 2, p1 = p0 + L_ * 2;
        st16(rs, p0, u32x4{o0[0], o0[1], o0[2], o0[3]}); st16(rs, p0 + 16, u32x4{o0[4], o0[5], o0[6], o0[7]});
        st16(rs, p1, u32x4{o1[0], o1[1], o1[2], o1[3]}); st16(rs, p1 + 16, u32x4{o1[4], o1[5], o1[6], o1[7]});
    };
    emit(ca, t);
    if (w == 0) emit(cb, 256 + t);
}

#define ADDTID2(base, v0, o0, v1, o1) asm volatile("s_mov_b32 m0, %2\n\ts_nop 0\n\tds_write_addtid_b32 %0 offset:%3\n\tds_write_addtid_b32 %1 offset:%4" :: "v"(v0), "v"(v1), "s"(base), "i"(o0), "i"(o1) : "memory")

DI int kskew(int i) { const int a = i + (i >> 5); return (a >> 6) * KG_PITCH + (a & 63) * 4; }

DI void k1_kgen(int d, const u16* __restrict__ Are, const u16* __restrict__ Aim, const u16* __restrict__ Bre, const u16* __restrict__ Bim,
                const u16* __restrict__ Cre, const u16* __restrict__ Cim, const u16* __restrict__ logdt, char* __restrict__ ws, char* smem) {
    const int t = threadIdx.x, lane = t & 63;
    const int w = __builtin_amdgcn_readfirstlane(t >> 6);
    if (w < 3) {
        const int n = d * NM_ + 64 * w + lane;
        const float are = bf2f(Are[n]), aim = bf2f(Aim[n]), bre = bf2f(Bre[n]), bim = bf2f(Bim[n]), cre = bf2f(Cre[n]), cim = bf2f(Cim[n]);
        const float dt = rbf(__expf(bf2f(logdt[d])));
        const float ar = (dt * are) * 1.4426950408889634f, br = (dt * aim) * 0.15915494309189535f;
        const float dbr = dt * bre, dbi = dt * bim;
        const f32x2 Wc = { cre * dbr - cim * dbi, -(cre * dbi + cim * dbr) };
        const f32x2 l1 = cexp_p(ar, br, 1.f), l8 = cexp_p(ar, br, 8.f), l256 = cexp_p(ar, br, 256.f);
        const f32x2 l2 = csq(l1), l4 = csq(l2), l16 = csq(l8);
        const cm c1 = mkcm(f32x2{l1.x, -l1.y}), c2 = mkcm(f32x2{l2.x, -l2.y}), c4 = mkcm(f32x2{l4.x, -l4.y}), c256 = mkcm(f32x2{l256.x, -l256.y}), m16 = mkcm(l16);
        const u32 base = (u32)(uintptr_t)smem + w * 256;
        auto pk = [](f32x2 v) { return cvtpk(v.x, v.y); };
        constexpr int PB = KG_AROWS * KG_PITCH;
        f32x2 a0 = Wc, x256 = cmul(Wc, c256), e0 = {1.f, 0.f}, e1 = l8;
        ADDTID2(base, pk(a0), 0 * KG_PITCH, pk(x256), 8 * KG_PITCH);
        ADDTID2(base, pk(e0), PB + 0 * KG_PITCH, pk(e1), PB + 1 * KG_PITCH);
        f32x2 y = x256, v = cmul(x256, c256);
#define KG_E(c) do { e0 = cmul(e0, m16); e1 = cmul(e1, m16); ADDTID2(base, pk(e0), PB + (c) * KG_PITCH, pk(e1), PB + ((c) + 1) * KG_PITCH); } while (0)
        a0 = cmul(a0, c1); y = cmul(y, c2); ADDTID2(base, pk(a0), 1 * KG_PITCH, pk(y), 9 * KG_PITCH); KG_E(2);
        a0 = cmul(a0, c1); y = cmul(y, c2); ADDTID2(base, pk(a0), 2 * KG_PITCH, pk(y), 10 * KG_PITCH); KG_E(4);
        a0 = cmul(a0, c1); y = cmul(y, c2); ADDTID2(base, pk(a0), 3 * KG_PITCH, pk(y), 11 * KG_PITCH); KG_E(6);
        a0 = cmul(a0, c1); y = cmul(v, c4); ADDTID2(base, pk(a0), 4 * KG_PITCH, pk(v), 12 * KG_PITCH); v = cmul(v, c256); KG_E(8);
        a0 = cmul(a0, c1); ADDTID2(base, pk(a0), 5 * KG_PITCH, pk(y), 13 * KG_PITCH); y = cmul(v, c4); KG_E(10);
        a0 = cmul(a0, c1); ADDTID2(base, pk(a0), 6 * KG_PITCH, pk(v), 14 * KG_PITCH); v = cmul(v, c256); KG_E(12);
        a0 = cmul(a0, c1); ADDTID2(base, pk(a0), 7 * KG_PITCH, pk(y), 15 * KG_PITCH); KG_E(14);
        y = cmul(v, c256); KG_E(16);
        ADDTID2(base, pk(v), 16 * KG_PITCH, pk(y), 17 * KG_PITCH); v = cmul(y, c256); KG_E(18);
        y = cmul(v, c256); KG_E(20);
        ADDTID2(base, pk(v), 18 * KG_PITCH, pk(y), 19 * KG_PITCH); v = cmul(y, c256); KG_E(22);
        const float k2048 = wave_sum(v.x);
        KG_E(24); KG_E(26); KG_E(28); KG_E(30);
        const int r32 = lane & 31, h = lane >> 5;
        const char* pa = smem + (r32 < KG_AROWS ? r32 : KG_AROWS - 1) * KG_PITCH + w * 256 + h * 16;
        const char* pb = smem + (KG_AROWS + r32) * KG_PITCH + w * 256 + h * 16;
        f32x16 acc = {};
#pragma unroll
        for (int s = 0; s < 8; ++s) {
            const bf16x8 a = *(const bf16x8*)(pa + 32 * s), bb = *(const bf16x8*)(pb + 32 * s);
            acc = __builtin_amdgcn_mfma_f32_32x32x16_bf16(a, bb, acc, 0, 0, 0);
        }
        char* pw = smem + w * 256;
        const int c = r32;
        if (h == 0) {
#pragma unroll
            for (int q = 0; q < 4; ++q) { *(float*)(pw + kskew(q + 8 * c)) = acc[q]; *(float*)(pw + kskew(256 + q + 4 * c)) = acc[4 + q]; *(float*)(pw + kskew(512 + 32 * q + c)) = acc[8 + q]; }
        } else {
#pragma unroll
            for (int q = 0; q < 4; ++q) { *(float*)(pw + kskew(4 + q + 8 * c)) = acc[q]; *(float*)(pw + kskew(384 + (q & 1) + 64 * (q >> 1) + 2 * c)) = acc[4 + q]; }
            if (c == 0) *(float*)(pw + kskew(640)) = k2048;
        }
    }
    __syncthreads();
    auto rd = [&](int i) { const char* p = smem + kskew(i); return *(const float*)p + *(const float*)(p + 256) + *(const float*)(p + 512); };
    u32x4 o;
    if (w == 0) {
        const int i0 = t < 32 ? 8 * t : 256 + 4 * (t - 32);
        const u32 pat = t < 32 ? 0x76543210u : 0x43222100u;
        float kv[8];
#pragma unroll
        for (int j = 0; j < 8; ++j) kv[j] = rd(i0 + ((pat >> (4 * j)) & 15));
        o = u32x4{cvtpk(kv[0], kv[1]), cvtpk(kv[2], kv[3]), cvtpk(kv[4], kv[5]), cvtpk(kv[6], kv[7])};
    } else if (w == 1) {
        const int i0 = 384 + 2 * (t - 64);
        const float k0 = rd(i0), k1 = rd(i0 + 1), k2 = rd(i0 + 2);
        o = u32x4{cvtpk(k0, k0), cvtpk(k0, k1), cvtpk(k1, k1), cvtpk(k2, k2)};
    } else {
        const int i0 = 512 + (t - 128);
        const float k0 = rd(i0), k1 = rd(i0 + 1);
        const u32 lo = cvtpk(k0, k0), hi = cvtpk(k1, k1);
        o = u32x4{lo, lo, cvtpk((t & 1) ? k1 : k0, k1), hi};
    }
    st16<AUX_KL>(mkrsrc(ws, WS_END), WS_KL + (d * L_ + 8 * t) * 2, o);
}

DI void k1_colsum(int j, const u16* __restrict__ Wg, const u16* __restrict__ Wu, char* __restrict__ ws, char* smem) {
    const int t = threadIdx.x, ch = t & 7, rg = t >> 3;
    const u16* W = (j < 24 ? Wg + 64 * j : Wu + 64 * (j - 24)) + 8 * ch;
    u32x4 v[20];
#pragma unroll
    for (int i = 0; i < 20; ++i) v[i] = *(const u32x4*)(W + (size_t)(rg + 32 * i) * F_);
    float a[8] = {};
#pragma unroll
    for (int i = 0; i < 20; ++i)
#pragma unroll
        for (int q = 0; q < 4; ++q) { a[2 * q] += bflo(v[i][q]); a[2 * q + 1] += bfhi(v[i][q]); }
    float* red = (float*)smem;
#pragma unroll
    for (int q = 0; q < 8; ++q) red[rg * 65 + ch * 8 + q] = a[q];
    __syncthreads();
    if (t < 64) { float s = 0.f;
#pragma unroll
        for (int r = 0; r < 32; ++r) s += red[r * 65 + t];
        *(u16*)(ws + WS_CS + (64 * j + t) * 2) = (u16)cvtpk(s * (1.f / 640.f), 0.f); }
}

__global__ __launch_bounds__(256, 4) void k1_prep(const u16* __restrict__ x, const u16* __restrict__ Are, const u16* __restrict__ Aim, const u16* __restrict__ Bre,
                                                  const u16* __restrict__ Bim, const u16* __restrict__ Cre, const u16* __restrict__ Cim, const u16* __restrict__ logdt,
                                                  const u16* __restrict__ Wg, const u16* __restrict__ Wu, char* __restrict__ ws) {
    __shared__ __attribute__((aligned(16))) char smem[K1_LDS];
    const int b = blockIdx.x;
    STAMP(ws, (b < 128 ? 0 : b < 176 ? 2 : 1), 0);
    if (b < 128) k1_ln(b, x, ws, smem);
    else if (b < 176) k1_colsum(b - 128, Wg, Wu, ws, smem);
    else k1_kgen(b - 176, Are, Aim, Bre, Bim, Cre, Cim, logdt, ws, smem);
    STAMP(ws, (b < 128 ? 0 : b < 176 ? 2 : 1), 1);
}

constexpr int CV_KC = 0, CV_CS = 4160;
constexpr int CV_U = 4 * CV_CS;
constexpr int CV_RED = CV_U + 4608;
constexpr int CV_LDS = CV_RED + 24576;

DI s16x4 trread(const LAS char* p) { typedef short v4i16_t __attribute__((ext_vector_type(4))); return __builtin_bit_cast(s16x4, __builtin_amdgcn_ds_read_tr16_b64_v4i16((LAS v4i16_t*)p)); }
#define trread_g1 trread
DI bf16x8 cat8(s16x4 a, s16x4 b) { return __builtin_shufflevector(a, b, 0, 1, 2, 3, 4, 5, 6, 7); }

#define WAITV(n) asm volatile("s_waitcnt vmcnt(" #n ")" ::: "memory")
#define WAITL0() asm volatile("s_waitcnt lgkmcnt(0)" ::: "memory")
template <int N> DI void waitv() { asm volatile("s_waitcnt vmcnt(%0)" :: "n"(N) : "memory"); }
#define BAR() do { asm volatile("" ::: "memory"); __builtin_amdgcn_s_barrier(); asm volatile("" ::: "memory"); } while (0)
#define SGB(m, n) __builtin_amdgcn_sched_group_barrier(m, n, 0)
#define SCHEDB() __builtin_amdgcn_sched_barrier(0)
#define MFMA16(a, b, c) __builtin_amdgcn_mfma_f32_16x16x32_bf16(a, b, c, 0, 0, 0)

DI void k2_retile(int rb, const u16* __restrict__ Wg, const u16* __restrict__ Wu, const u16* __restrict__ Wd, char* __restrict__ ws) {
    const int t = threadIdx.x;
    const rsrc_t rs = mkrsrc(ws, WS_END);
#pragma unroll 1
    for (int it = 0; it < 2; ++it) {
        const int n = 360 * rb + 256 * it + t;
        if (it == 1 && t >= 104) break;
        u32x4 v[8]; u32 dst;
        if (n < 30720) {
            const int fc = n % 192, kg = (n / 192) % 80, up = n / 15360;
            const u16* src = (up ? Wu : Wg) + (8 * kg) * F_ + 8 * fc;
#pragma unroll
            for (int j = 0; j < 8; ++j) v[j] = *(const u32x4*)(src + j * F_);
            const u32x4 c = *(const u32x4*)(ws + WS_CS + (up * F_ + 8 * fc) * 2);
            const int f = 8 * fc, sl = f / 96, fl = f % 96, mt = 2 * (fl >> 4) + up;
            dst = WS_W1T + ((sl * 10 + (kg >> 3)) * 24 + ((kg >> 2) & 1) * 12 + mt) * 1024 + ((fl & 15) + 16 * (kg & 3)) * 16;
#pragma unroll
            for (int i = 0; i < 4; ++i) {
                const float c0 = bflo(c[i]), c1 = bfhi(c[i]);
                st16(rs, dst + (2 * i) * 16, u32x4{cvtpk(bflo(v[0][i]) - c0, bflo(v[1][i]) - c0), cvtpk(bflo(v[2][i]) - c0, bflo(v[3][i]) - c0),
                                                   cvtpk(bflo(v[4][i]) - c0, bflo(v[5][i]) - c0), cvtpk(bflo(v[6][i]) - c0, bflo(v[7][i]) - c0)});
                st16(rs, dst + (2 * i + 1) * 16, u32x4{cvtpk(bfhi(v[0][i]) - c1, bfhi(v[1][i]) - c1), cvtpk(bfhi(v[2][i]) - c1, bfhi(v[3][i]) - c1),
                                                       cvtpk(bfhi(v[4][i]) - c1, bfhi(v[5][i]) - c1), cvtpk(bfhi(v[6][i]) - c1, bfhi(v[7][i]) - c1)});
            }
        } else {
            const int n2 = n - 30720, dc = n2 % 80, kg = n2 / 80;
            const u16* src = Wd + (8 * kg) * D_ + 8 * dc;
#pragma unroll
            for (int j = 0; j < 8; ++j) v[j] = *(const u32x4*)(src + j * D_);
            const int ds = dc / 10, dl = 8 * (dc % 10);
            dst = WS_W2T + ((ds * 24 + (kg >> 3)) * 10 + ((kg >> 2) & 1) * 5 + (dl >> 4)) * 1024 + ((dl & 15) + 16 * (kg & 3)) * 16;
#pragma unroll
            for (int i = 0; i < 4; ++i) {
                st16(rs, dst + (2 * i) * 16, u32x4{__builtin_amdgcn_perm(v[1][i], v[0][i], 0x05040100u), __builtin_amdgcn_perm(v[3][i], v[2][i], 0x05040100u),
                                                   __builtin_amdgcn_perm(v[5][i], v[4][i], 0x05040100u), __builtin_amdgcn_perm(v[7][i], v[6][i], 0x05040100u)});
                st16(rs, dst + (2 * i + 1) * 16, u32x4{__builtin_amdgcn_perm(v[1][i], v[0][i], 0x07060302u), __builtin_amdgcn_perm(v[3][i], v[2][i], 0x07060302u),
                                                       __builtin_amdgcn_perm(v[5][i], v[4][i], 0x07060302u), __builtin_amdgcn_perm(v[7][i], v[6][i], 0x07060302u)});
            }
        }
    }
}

__global__ __launch_bounds__(256, 3) void k2_conv(const u16* __restrict__ Dskip, const u16* __restrict__ Wg, const u16* __restrict__ Wu, const u16* __restrict__ Wd, char* __restrict__ ws) {
    __shared__ __attribute__((aligned(16))) char smem[CV_LDS];
    const int d = blockIdx.x, t = threadIdx.x, lane = t & 63;
    STAMP(ws, (d < 640 ? 3 : 6), 0);
    if (d >= 640) { k2_retile(d - 640, Wg, Wu, Wd, ws); STAMP(ws, 6, 1); return; }
    const int w = __builtin_amdgcn_readfirstlane(t >> 6);
    const u16* QT = (const u16*)(ws + WS_QT) + d * L_;
    const u16* KL = (const u16*)(ws + WS_KL) + d * L_;
    const int al = lane & 15, g = lane >> 4;
    const u32x4 kc = *(const u32x4*)(KL + 8 * t);
    const u32x4 kpl = *(const u32x4*)(KL + (t > 0 ? 8 * t - 8 : 0));
    u32x2 qv[2]; f32x4 sa[2], sb[2];
#pragma unroll
    for (int i = 0; i < 2; ++i) qv[i] = *(const u32x2*)(QT + 256 * (2 * w + i) + 16 * al + 4 * g);
#pragma unroll
    for (int i = 0; i < 2; ++i) { const f32x4* p = (const f32x4*)(ws + WS_ST + (256 * (2 * w + i) + 16 * al + 4 * g) * 8); sa[i] = p[0]; sb[i] = p[1]; }
    const float dsk = bf2f(Dskip[d]);
    if (t < 30) *(u32x4*)(smem + CV_U + 16 * t) = u32x4{0, 0, 0, 0};
    if (t >= 64 && t < 72) { const int q = (t - 64) >> 1, hh = (t - 64) & 1; *(u32x4*)(smem + CV_KC + q * CV_CS + 16 * hh) = u32x4{0, 0, 0, 0}; }
    {
        const u32 k2 = t > 0 ? kpl[2] : 0u, k3 = t > 0 ? kpl[3] : 0u, k4 = kc[0], k5 = kc[1], k6 = kc[2], k7 = kc[3];
        const u32 a2 = __builtin_amdgcn_alignbit(k3, k2, 16), a3 = __builtin_amdgcn_alignbit(k4, k3, 16), a4 = __builtin_amdgcn_alignbit(k5, k4, 16),
                  a5 = __builtin_amdgcn_alignbit(k6, k5, 16), a6 = __builtin_amdgcn_alignbit(k7, k6, 16);
        char* kb = smem + CV_KC + 32 + 16 * t;
        *(u32x4*)(kb + 0 * CV_CS) = u32x4{k4, k5, k6, k7};
        *(u32x4*)(kb + 1 * CV_CS) = u32x4{a3, a4, a5, a6};
        *(u32x4*)(kb + 2 * CV_CS) = u32x4{k3, k4, k5, k6};
        *(u32x4*)(kb + 3 * CV_CS) = u32x4{a2, a3, a4, a5};
    }
    u32x2 uv[2];
#pragma unroll
    for (int i = 0; i < 2; ++i) {
        uv[i] = u32x2{cvtpk(bflo(qv[i][0]) * sa[i][1], bfhi(qv[i][0]) * sa[i][3]), cvtpk(bflo(qv[i][1]) * sb[i][1], bfhi(qv[i][1]) * sb[i][3])};
        *(u32x2*)(smem + CV_U + 480 + 2 * (256 * (2 * w + i) + 16 * al + 4 * g)) = uv[i];
    }
    STAMP(ws, 3, 1);
    __syncthreads();
    STAMP(ws, 3, 2);
    f32x4 acc[8];
#pragma unroll
    for (int i = 0; i < 8; ++i) acc[i] = f32x4{0.f, 0.f, 0.f, 0.f};
    {
        const LAS char* lds = (const LAS char*)smem;
        const int q = (lane & 15) >> 2, p = lane & 3;
        const LAS char* ub0 = lds + CV_U + 448 + 32 * al + 16 * g - 128 * w;
        const LAS char* ub1 = ub0 - 64;
        const LAS char* kb0 = lds + CV_KC + q * CV_CS + 56 - 16 * g + 8 * p + 128 * w;
        const LAS char* kb1 = kb0 + 64;
        bf16x8 U0[8], U1[8]; s16x4 A0[8][2], A1[8][2];
        A0[0][0] = trread(kb0 + 8); A0[0][1] = trread(kb0 + 0); U0[0] = *(const LAS bf16x8*)(ub0 + 0); U0[1] = *(const LAS bf16x8*)(ub0 + 512); U0[2] = *(const LAS bf16x8*)(ub0 + 1024); U0[3] = *(const LAS bf16x8*)(ub0 + 1536); U0[4] = *(const LAS bf16x8*)(ub0 + 2048); U0[5] = *(const LAS bf16x8*)(ub0 + 2560); U0[6] = *(const LAS bf16x8*)(ub0 + 3072); U0[7] = *(const LAS bf16x8*)(ub0 + 3584); A0[1][0] = trread(kb0 + 520); A0[1][1] = trread(kb0 + 512); A0[2][0] = trread(kb0 + 1032); A0[2][1] = trread(kb0 + 1024); A0[3][0] = trread(kb0 + 1544); A0[3][1] = trread(kb0 + 1536); A0[4][0] = trread(kb0 + 2056); A0[4][1] = trread(kb0 + 2048); A0[5][0] = trread(kb0 + 2568); A0[5][1] = trread(kb0 + 2560); A0[6][0] = trread(kb0 + 3080); A0[6][1] = trread(kb0 + 3072); A0[7][0] = trread(kb0 + 3592); A0[7][1] = trread(kb0 + 3584);
        SCHEDB();
        acc[0] = MFMA16(cat8(A0[0][0], A0[0][1]), U0[0], acc[0]);
        SCHEDB();
        acc[1] = MFMA16(cat8(A0[0][0], A0[0][1]), U0[1], acc[1]);
        SCHEDB();
        acc[2] = MFMA16(cat8(A0[0][0], A0[0][1]), U0[2], acc[2]);
        SCHEDB();
        acc[3] = MFMA16(cat8(A0[0][0], A0[0][1]), U0[3], acc[3]);
        SCHEDB();
        acc[4] = MFMA16(cat8(A0[0][0], A0[0][1]), U0[4], acc[4]);
        SCHEDB();
        acc[5] = MFMA16(cat8(A0[0][0], A0[0][1]), U0[5], acc[5]);
        SCHEDB();
        acc[6] = MFMA16(cat8(A0[0][0], A0[0][1]), U0[6], acc[6]);
        SCHEDB();
        acc[7] = MFMA16(cat8(A0[0][0], A0[0][1]), U0[7], acc[7]);
        SCHEDB();
        acc[1] = MFMA16(cat8(A0[1][0], A0[1][1]), U0[0], acc[1]);
        A1[0][0] = trread(kb1 + 8);
        SCHEDB();
        acc[2] = MFMA16(cat8(A0[1][0], A0[1][1]), U0[1], acc[2]);
        A1[0][1] = trread(kb1 + 0);
        SCHEDB();
        acc[3] = MFMA16(cat8(A0[1][0], A0[1][1]), U0[2], acc[3]);
        U1[0] = *(const LAS bf16x8*)(ub1 + 0);
        SCHEDB();
        acc[4] = MFMA16(cat8(A0[1][0], A0[1][1]), U0[3], acc[4]);
        U1[1] = *(const LAS bf16x8*)(ub1 + 512);
        SCHEDB();
        acc[5] = MFMA16(cat8(A0[1][0], A0[1][1]), U0[4], acc[5]);
        U1[2] = *(const LAS bf16x8*)(ub1 + 1024);
        SCHEDB();
        acc[6] = MFMA16(cat8(A0[1][0], A0[1][1]), U0[5], acc[6]);
        U1[3] = *(const LAS bf16x8*)(ub1 + 1536);
        SCHEDB();
        acc[7] = MFMA16(cat8(A0[1][0], A0[1][1]), U0[6], acc[7]);
        U1[4] = *(const LAS bf16x8*)(ub1 + 2048);
        SCHEDB();
        acc[2] = MFMA16(cat8(A0[2][0], A0[2][1]), U0[0], acc[2]);
        U1[5] = *(const LAS bf16x8*)(ub1 + 2560);
        SCHEDB();
        acc[3] = MFMA16(cat8(A0[2][0], A0[2][1]), U0[1], acc[3]);
        U1[6] = *(const LAS bf16x8*)(ub1 + 3072);
        SCHEDB();
        acc[4] = MFMA16(cat8(A0[2][0], A0[2][1]), U0[2], acc[4]);
        U1[7] = *(const LAS bf16x8*)(ub1 + 3584);
        SCHEDB();
        acc[5] = MFMA16(cat8(A0[2][0], A0[2][1]), U0[3], acc[5]);
        A1[1][0] = trread(kb1 + 520);
        SCHEDB();
        acc[6] = MFMA16(cat8(A0[2][0], A0[2][1]), U0[4], acc[6]);
        A1[1][1] = trread(kb1 + 512);
        SCHEDB();
        acc[7] = MFMA16(cat8(A0[2][0], A0[2][1]), U0[5], acc[7]);
        A1[2][0] = trread(kb1 + 1032);
        SCHEDB();
        acc[3] = MFMA16(cat8(A0[3][0], A0[3][1]), U0[0], acc[3]);
        A1[2][1] = trread(kb1 + 1024);
        SCHEDB();
        acc[4] = MFMA16(cat8(A0[3][0], A0[3][1]), U0[1], acc[4]);
        A1[3][0] = trread(kb1 + 1544);
        SCHEDB();
        acc[5] = MFMA16(cat8(A0[3][0], A0[3][1]), U0[2], acc[5]);
        A1[3][1] = trread(kb1 + 1536);
        SCHEDB();
        acc[6] = MFMA16(cat8(A0[3][0], A0[3][1]), U0[3], acc[6]);
        A1[4][0] = trread(kb1 + 2056);
        SCHEDB();
        acc[7] = MFMA16(cat8(A0[3][0], A0[3][1]), U0[4], acc[7]);
        A1[4][1] = trread(kb1 + 2048);
        SCHEDB();
        acc[4] = MFMA16(cat8(A0[4][0], A0[4][1]), U0[0], acc[4]);
        A1[5][0] = trread(kb1 + 2568);
        SCHEDB();
        acc[5] = MFMA16(cat8(A0[4][0], A0[4][1]), U0[1], acc[5]);
        A1[5][1] = trread(kb1 + 2560);
        SCHEDB();
        acc[6] = MFMA16(cat8(A0[4][0], A0[4][1]), U0[2], acc[6]);
        A1[6][0] = trread(kb1 + 3080);
        SCHEDB();
        acc[7] = MFMA16(cat8(A0[4][0], A0[4][1]), U0[3], acc[7]);
        A1[6][1] = trread(kb1 + 3072);
        SCHEDB();
        acc[5] = MFMA16(cat8(A0[5][0], A0[5][1]), U0[0], acc[5]);
        A1[7][0] = trread(kb1 + 3592);
        SCHEDB();
        acc[6] = MFMA16(cat8(A0[5][0], A0[5][1]), U0[1], acc[6]);
        A1[7][1] = trread(kb1 + 3584);
        SCHEDB();
        acc[7] = MFMA16(cat8(A0[5][0], A0[5][1]), U0[2], acc[7]);
        SCHEDB();
        acc[6] = MFMA16(cat8(A0[6][0], A0[6][1]), U0[0], acc[6]);
        SCHEDB();
        acc[7] = MFMA16(cat8(A0[6][0], A0[6][1]), U0[1], acc[7]);
        SCHEDB();
        acc[7] = MFMA16(cat8(A0[7][0], A0[7][1]), U0[0], acc[7]);
        SCHEDB();
        acc[0] = MFMA16(cat8(A1[0][0], A1[0][1]), U1[0], acc[0]); SCHEDB();
        acc[1] = MFMA16(cat8(A1[0][0], A1[0][1]), U1[1], acc[1]); SCHEDB();
        acc[2] = MFMA16(cat8(A1[0][0], A1[0][1]), U1[2], acc[2]); SCHEDB();
        acc[3] = MFMA16(cat8(A1[0][0], A1[0][1]), U1[3], acc[3]); SCHEDB();
        acc[4] = MFMA16(cat8(A1[0][0], A1[0][1]), U1[4], acc[4]); SCHEDB();
        acc[5] = MFMA16(cat8(A1[0][0], A1[0][1]), U1[5], acc[5]); SCHEDB();
        acc[6] = MFMA16(cat8(A1[0][0], A1[0][1]), U1[6], acc[6]); SCHEDB();
        acc[7] = MFMA16(cat8(A1[0][0], A1[0][1]), U1[7], acc[7]); SCHEDB();
        acc[1] = MFMA16(cat8(A1[1][0], A1[1][1]), U1[0], acc[1]); SCHEDB();
        acc[2] = MFMA16(cat8(A1[1][0], A1[1][1]), U1[1], acc[2]); SCHEDB();
        acc[3] = MFMA16(cat8(A1[1][0], A1[1][1]), U1[2], acc[3]); SCHEDB();
        acc[4] = MFMA16(cat8(A1[1][0], A1[1][1]), U1[3], acc[4]); SCHEDB();
        acc[5] = MFMA16(cat8(A1[1][0], A1[1][1]), U1[4], acc[5]); SCHEDB();
        acc[6] = MFMA16(cat8(A1[1][0], A1[1][1]), U1[5], acc[6]); SCHEDB();
        acc[7] = MFMA16(cat8(A1[1][0], A1[1][1]), U1[6], acc[7]); SCHEDB();
        acc[2] = MFMA16(cat8(A1[2][0], A1[2][1]), U1[0], acc[2]); SCHEDB();
        acc[3] = MFMA16(cat8(A1[2][0], A1[2][1]), U1[1], acc[3]); SCHEDB();
        acc[4] = MFMA16(cat8(A1[2][0], A1[2][1]), U1[2], acc[4]); SCHEDB();
        acc[5] = MFMA16(cat8(A1[2][0], A1[2][1]), U1[3], acc[5]); SCHEDB();
        acc[6] = MFMA16(cat8(A1[2][0], A1[2][1]), U1[4], acc[6]); SCHEDB();
        acc[7] = MFMA16(cat8(A1[2][0], A1[2][1]), U1[5], acc[7]); SCHEDB();
        acc[3] = MFMA16(cat8(A1[3][0], A1[3][1]), U1[0], acc[3]); SCHEDB();
        acc[4] = MFMA16(cat8(A1[3][0], A1[3][1]), U1[1], acc[4]); SCHEDB();
        acc[5] = MFMA16(cat8(A1[3][0], A1[3][1]), U1[2], acc[5]); SCHEDB();
        acc[6] = MFMA16(cat8(A1[3][0], A1[3][1]), U1[3], acc[6]); SCHEDB();
        acc[7] = MFMA16(cat8(A1[3][0], A1[3][1]), U1[4], acc[7]); SCHEDB();
        acc[4] = MFMA16(cat8(A1[4][0], A1[4][1]), U1[0], acc[4]); SCHEDB();
        acc[5] = MFMA16(cat8(A1[4][0], A1[4][1]), U1[1], acc[5]); SCHEDB();
        acc[6] = MFMA16(cat8(A1[4][0], A1[4][1]), U1[2], acc[6]); SCHEDB();
        acc[7] = MFMA16(cat8(A1[4][0], A1[4][1]), U1[3], acc[7]); SCHEDB();
        acc[5] = MFMA16(cat8(A1[5][0], A1[5][1]), U1[0], acc[5]); SCHEDB();
        acc[6] = MFMA16(cat8(A1[5][0], A1[5][1]), U1[1], acc[6]); SCHEDB();
        acc[7] = MFMA16(cat8(A1[5][0], A1[5][1]), U1[2], acc[7]); SCHEDB();
        acc[6] = MFMA16(cat8(A1[6][0], A1[6][1]), U1[0], acc[6]); SCHEDB();
        acc[7] = MFMA16(cat8(A1[6][0], A1[6][1]), U1[1], acc[7]); SCHEDB();
        acc[7] = MFMA16(cat8(A1[7][0], A1[7][1]), U1[0], acc[7]); SCHEDB();
    }
    STAMP(ws, 3, 3);
    {
        char* rb = smem + CV_RED + w * 6144 + lane * 16;
        int slot = 0;
#pragma unroll
        for (int tau = 0; tau < 8; ++tau) { if ((tau >> 1) != w) { *(f32x4*)(rb + slot * 1024) = acc[tau]; ++slot; } }
    }
    const rsrc_t rs = mkrsrc(ws, WS_END);
    __syncthreads();
#pragma unroll
    for (int i = 0; i < 2; ++i) {
        f32x4 y;
        if (w == 0) y = acc[i]; else if (w == 1) y = acc[2 + i]; else if (w == 2) y = acc[4 + i]; else y = acc[6 + i];
        const int tau = 2 * w + i;
#pragma unroll
        for (int ow = 0; ow < 4; ++ow) {
            if (ow != w) {
                const int sl = (tau < 2 * ow) ? tau : tau - 2;
                y += *(const f32x4*)(smem + CV_RED + ow * 6144 + sl * 1024 + lane * 16);
            }
        }
        float hv[4];
#pragma unroll
        for (int r = 0; r < 4; ++r) {
            const u32 uu = uv[i][r >> 1], qq = qv[i][r >> 1];
            const float u = (r & 1) ? bfhi(uu) : bflo(uu), q0 = (r & 1) ? bfhi(qq) : bflo(qq);
            const u32 yk = cvtpk(y[r], u * dsk);
            hv[r] = q0 + rbf(bflo(yk) + bfhi(yk));
        }
        st8<AUX_H>(rs, WS_HC + (d * L_ + 256 * tau + 16 * al + 4 * g) * 2, u32x2{cvtpk(hv[0], hv[1]), cvtpk(hv[2], hv[3])});
    }
    STAMP(ws, 3, 4);
}

#define G1_UNROLL 1
constexpr int G1_STAGE = 40960, G1_NS = 4, G1_NST = 10;
constexpr int G1_LDS = G1_NS * G1_STAGE;

DI void glds16(const void* g, u32 lds_base) {
    u32 sv; asm volatile("s_mov_b32 %0, m0\n\ts_mov_b32 m0, %2\n\ts_nop 0\n\tglobal_load_lds_dwordx4 %1, off\n\ts_mov_b32 m0, %0" : "=&s"(sv) : "v"(g), "s"(lds_base) : "memory");
}

__global__ __launch_bounds__(768, 3) void k3_gemm1(char* __restrict__ ws) {
    __shared__ __attribute__((aligned(1024))) char smem[G1_LDS];
    const int b = blockIdx.x, t = threadIdx.x, lane = t & 63;
    const int w = __builtin_amdgcn_readfirstlane(t >> 6);
    const int xcd = b & 7, j = b >> 3;
    const int P = (xcd & 1) * 8 + (j & 7), S = (xcd >> 1) * 4 + (j >> 3);
    const u32 lds0 = (u32)(uintptr_t)smem;
    const rsrc_t wsr = mkrsrc(ws, WS_END);
    if (w >= 4) {
        const int lw = w - 4;
        const char* wsrc = ws + WS_W1T + S * 10 * 24576 + lw * 1024 + lane * 16;
        const int hk = 16 * ((lw >> 2) & 1) + 4 * ((lw >> 1) & 1) + 8 * ((lane >> 3) & 1) + ((lane >> 1) & 3);
        const char* hsrc = ws + WS_HC + (hk * L_ + 128 * P + 64 * (lw & 1) + 16 * (lane >> 4) + 8 * (lane & 1)) * 2;
        auto issue = [&](int st, int slot) {
            const u32 base = lds0 + slot * G1_STAGE;
#pragma unroll
            for (int i = 0; i < 2; ++i) glds16(hsrc + (st * 64 + 32 * i) * L_ * 2, base + 24576 + (lw + 8 * i) * 1024);
#pragma unroll
            for (int i = 0; i < 3; ++i) glds16(wsrc + st * 24576 + i * 8192, base + lw * 1024 + i * 8192);
        };
#define ISSUE(a, b) issue(a, b)
        issue(0, 0); issue(1, 1);
        waitv<5>(); BAR();
        issue(2, 2);
        waitv<5>(); BAR();
        ISSUE(3, 3);
        waitv<5>(); BAR();
        ISSUE(4, 0);
        waitv<5>(); BAR();
        ISSUE(5, 1);
        waitv<5>(); BAR();
        ISSUE(6, 2);
        waitv<5>(); BAR();
        ISSUE(7, 3);
        waitv<5>(); BAR();
        ISSUE(8, 0);
        waitv<5>(); BAR();
        ISSUE(9, 1);
        waitv<5>(); BAR();
        waitv<0>(); BAR();
        BAR();
        BAR();
        return;
    }
    STAMP(ws, 4, 0);
    const int wm = w & 1, wn = w >> 1;
    const int g = lane >> 4, q = (lane & 15) >> 2, p = lane & 3;
    f32x4 acc[6][4];
#pragma unroll
    for (int a = 0; a < 6; ++a)
#pragma unroll
        for (int c = 0; c < 4; ++c) acc[a][c] = f32x4{0.f, 0.f, 0.f, 0.f};
    const LAS char* abase = (const LAS char*)smem + wm * 6144 + lane * 16;
    const LAS char* bbase = (const LAS char*)smem + 24576 + (g >> 1) * 4096 + (g & 1) * 128 + q * 32 + 8 * p + wn * 1024;
    const LAS char* sbase = (const LAS char*)smem + 24576 + (2 * w + (lane >> 5)) * 2048 + (S >> 1) * 256 + ((lane >> 2) & 7) * 32 + (S & 1) * 16 + (lane & 3) * 4;
    float s1a = 0.f, s1b = 0.f, s2a = 0.f, s2b = 0.f;
    bf16x8 AL[3], AH[3]; s16x4 B0[4][2], B1[4][2];
    u32 sv;
    BAR();
    STAMP(ws, 4, 1);
    AL[0] = *(const LAS bf16x8*)(abase + 0); B0[0][0] = trread(bbase + 0); B0[0][1] = trread(bbase + 2048); B0[1][0] = trread(bbase + 256); B0[1][1] = trread(bbase + 2304); AL[1] = *(const LAS bf16x8*)(abase + 1024); B0[2][0] = trread(bbase + 512); B0[2][1] = trread(bbase + 2560); B0[3][0] = trread(bbase + 768); B0[3][1] = trread(bbase + 2816); AL[2] = *(const LAS bf16x8*)(abase + 2048);
    SCHEDB();
#pragma unroll 1
    for (int st = 0; st < G1_NST; ++st) {
        acc[0+0][0] = MFMA16(AL[0], cat8(B0[0][0], B0[0][1]), acc[0+0][0]);
        AH[0] = *(const LAS bf16x8*)(abase + 3072);
        SCHEDB();
        acc[0+0][1] = MFMA16(AL[0], cat8(B0[1][0], B0[1][1]), acc[0+0][1]);
        AH[1] = *(const LAS bf16x8*)(abase + 4096);
        SCHEDB();
        acc[0+1][0] = MFMA16(AL[1], cat8(B0[0][0], B0[0][1]), acc[0+1][0]);
        B1[0][0] = trread(bbase + 8192);
        SCHEDB();
        acc[0+1][1] = MFMA16(AL[1], cat8(B0[1][0], B0[1][1]), acc[0+1][1]);
        B1[0][1] = trread(bbase + 10240);
        SCHEDB();
        acc[0+0][2] = MFMA16(AL[0], cat8(B0[2][0], B0[2][1]), acc[0+0][2]);
        B1[1][0] = trread(bbase + 8448);
        SCHEDB();
        acc[0+1][2] = MFMA16(AL[1], cat8(B0[2][0], B0[2][1]), acc[0+1][2]);
        B1[1][1] = trread(bbase + 10496);
        SCHEDB();
        acc[0+0][3] = MFMA16(AL[0], cat8(B0[3][0], B0[3][1]), acc[0+0][3]);
        AH[2] = *(const LAS bf16x8*)(abase + 5120);
        SCHEDB();
        acc[0+1][3] = MFMA16(AL[1], cat8(B0[3][0], B0[3][1]), acc[0+1][3]);
        SCHEDB();
        acc[0+2][0] = MFMA16(AL[2], cat8(B0[0][0], B0[0][1]), acc[0+2][0]);
        SCHEDB();
        acc[0+2][1] = MFMA16(AL[2], cat8(B0[1][0], B0[1][1]), acc[0+2][1]);
        SCHEDB();
        acc[0+2][2] = MFMA16(AL[2], cat8(B0[2][0], B0[2][1]), acc[0+2][2]);
        SCHEDB();
        acc[0+2][3] = MFMA16(AL[2], cat8(B0[3][0], B0[3][1]), acc[0+2][3]);
        SCHEDB();
        acc[3+0][0] = MFMA16(AH[0], cat8(B0[0][0], B0[0][1]), acc[3+0][0]);
        AL[0] = *(const LAS bf16x8*)(abase + 12288);
        SCHEDB();
        acc[3+0][1] = MFMA16(AH[0], cat8(B0[1][0], B0[1][1]), acc[3+0][1]);
        AL[1] = *(const LAS bf16x8*)(abase + 13312);
        SCHEDB();
        acc[3+1][0] = MFMA16(AH[1], cat8(B0[0][0], B0[0][1]), acc[3+1][0]);
        sv = *(const LAS u32*)(sbase);
        SCHEDB();
        acc[3+1][1] = MFMA16(AH[1], cat8(B0[1][0], B0[1][1]), acc[3+1][1]);
        B1[2][0] = trread(bbase + 8704);
        SCHEDB();
        acc[3+0][2] = MFMA16(AH[0], cat8(B0[2][0], B0[2][1]), acc[3+0][2]);
        B1[2][1] = trread(bbase + 10752);
        SCHEDB();
        acc[3+1][2] = MFMA16(AH[1], cat8(B0[2][0], B0[2][1]), acc[3+1][2]);
        B1[3][0] = trread(bbase + 8960);
        SCHEDB();
        acc[3+0][3] = MFMA16(AH[0], cat8(B0[3][0], B0[3][1]), acc[3+0][3]);
        B1[3][1] = trread(bbase + 11008);
        SCHEDB();
        acc[3+1][3] = MFMA16(AH[1], cat8(B0[3][0], B0[3][1]), acc[3+1][3]);
        AL[2] = *(const LAS bf16x8*)(abase + 14336);
        SCHEDB();
        acc[3+2][0] = MFMA16(AH[2], cat8(B0[0][0], B0[0][1]), acc[3+2][0]);
        SCHEDB();
        acc[3+2][1] = MFMA16(AH[2], cat8(B0[1][0], B0[1][1]), acc[3+2][1]);
        SCHEDB();
        acc[3+2][2] = MFMA16(AH[2], cat8(B0[2][0], B0[2][1]), acc[3+2][2]);
        SCHEDB();
        acc[3+2][3] = MFMA16(AH[2], cat8(B0[3][0], B0[3][1]), acc[3+2][3]);
        SCHEDB();
        acc[0+0][0] = MFMA16(AL[0], cat8(B1[0][0], B1[0][1]), acc[0+0][0]);
        AH[0] = *(const LAS bf16x8*)(abase + 15360);
        SCHEDB();
        acc[0+0][1] = MFMA16(AL[0], cat8(B1[1][0], B1[1][1]), acc[0+0][1]);
        AH[1] = *(const LAS bf16x8*)(abase + 16384);
        SCHEDB();
        acc[0+1][0] = MFMA16(AL[1], cat8(B1[0][0], B1[0][1]), acc[0+1][0]);
        AH[2] = *(const LAS bf16x8*)(abase + 17408);
        SCHEDB();
        acc[0+1][1] = MFMA16(AL[1], cat8(B1[1][0], B1[1][1]), acc[0+1][1]);
        SCHEDB();
        acc[0+0][2] = MFMA16(AL[0], cat8(B1[2][0], B1[2][1]), acc[0+0][2]);
        SCHEDB();
        acc[0+1][2] = MFMA16(AL[1], cat8(B1[2][0], B1[2][1]), acc[0+1][2]);
        SCHEDB();
        acc[0+0][3] = MFMA16(AL[0], cat8(B1[3][0], B1[3][1]), acc[0+0][3]);
        SCHEDB();
        acc[0+1][3] = MFMA16(AL[1], cat8(B1[3][0], B1[3][1]), acc[0+1][3]);
        SCHEDB();
        acc[0+2][0] = MFMA16(AL[2], cat8(B1[0][0], B1[0][1]), acc[0+2][0]);
        SCHEDB();
        acc[0+2][1] = MFMA16(AL[2], cat8(B1[1][0], B1[1][1]), acc[0+2][1]);
        SCHEDB();
        acc[0+2][2] = MFMA16(AL[2], cat8(B1[2][0], B1[2][1]), acc[0+2][2]);
        SCHEDB();
        acc[0+2][3] = MFMA16(AL[2], cat8(B1[3][0], B1[3][1]), acc[0+2][3]);
        SCHEDB();
        { const float a = bflo(sv), c = bfhi(sv); s1a += a; s1b += c; s2a = __builtin_fmaf(a, a, s2a); s2b = __builtin_fmaf(c, c, s2b); }
        WAITL0();
        BAR();
        { const int dlt = (st & 3) == 3 ? -3 * G1_STAGE : G1_STAGE; abase += dlt; bbase += dlt; sbase += dlt; }
        acc[3+0][0] = MFMA16(AH[0], cat8(B1[0][0], B1[0][1]), acc[3+0][0]);
        AL[0] = *(const LAS bf16x8*)(abase + 0);
        SCHEDB();
        acc[3+0][1] = MFMA16(AH[0], cat8(B1[1][0], B1[1][1]), acc[3+0][1]);
        B0[0][0] = trread(bbase + 0);
        SCHEDB();
        acc[3+1][0] = MFMA16(AH[1], cat8(B1[0][0], B1[0][1]), acc[3+1][0]);
        B0[0][1] = trread(bbase + 2048);
        SCHEDB();
        acc[3+1][1] = MFMA16(AH[1], cat8(B1[1][0], B1[1][1]), acc[3+1][1]);
        B0[1][0] = trread(bbase + 256);
        SCHEDB();
        acc[3+0][2] = MFMA16(AH[0], cat8(B1[2][0], B1[2][1]), acc[3+0][2]);
        B0[1][1] = trread(bbase + 2304);
        SCHEDB();
        acc[3+1][2] = MFMA16(AH[1], cat8(B1[2][0], B1[2][1]), acc[3+1][2]);
        AL[1] = *(const LAS bf16x8*)(abase + 1024);
        SCHEDB();
        acc[3+0][3] = MFMA16(AH[0], cat8(B1[3][0], B1[3][1]), acc[3+0][3]);
        B0[2][0] = trread(bbase + 512);
        SCHEDB();
        acc[3+1][3] = MFMA16(AH[1], cat8(B1[3][0], B1[3][1]), acc[3+1][3]);
        B0[2][1] = trread(bbase + 2560);
        SCHEDB();
        acc[3+2][0] = MFMA16(AH[2], cat8(B1[0][0], B1[0][1]), acc[3+2][0]);
        B0[3][0] = trread(bbase + 768);
        SCHEDB();
        acc[3+2][1] = MFMA16(AH[2], cat8(B1[1][0], B1[1][1]), acc[3+2][1]);
        B0[3][1] = trread(bbase + 2816);
        SCHEDB();
        acc[3+2][2] = MFMA16(AH[2], cat8(B1[2][0], B1[2][1]), acc[3+2][2]);
        AL[2] = *(const LAS bf16x8*)(abase + 2048);
        SCHEDB();
        acc[3+2][3] = MFMA16(AH[2], cat8(B1[3][0], B1[3][1]), acc[3+2][3]);
        SCHEDB();
    }
    STAMP(ws, 4, 3);
    {
        float v[4] = {s1a, s1b, s2a, s2b};
#pragma unroll
        for (int i = 0; i < 4; ++i) { v[i] = dppadd<0x114>(v[i]); v[i] = dppadd<0x118>(v[i]); v[i] = swap32add(swap16add(v[i])); }
        if (lane >= 12 && lane < 16) *(f32x4*)(smem + (w * 4 + (lane & 3)) * 16) = f32x4{v[0], v[1], v[2], v[3]};
        WAITL0();
    }
    BAR();
    if (w == 0 && lane < 8) {
        float s1 = 0.f, s2 = 0.f;
#pragma unroll
        for (int ow = 0; ow < 4; ++ow) { const f32x4 v = *(const f32x4*)(smem + (ow * 4 + (lane >> 1)) * 16); s1 += (lane & 1) ? v[1] : v[0]; s2 += (lane & 1) ? v[3] : v[2]; }
        const float mean = s1 * (1.f / 640.f);
        float var = s2 * (1.f / 640.f) - mean * mean;
        var = var > 0.f ? var : 0.f;
        const float rs = 1.f / rbf(__builtin_sqrtf(rbf(rbf(var) + 1e-5f)));
        st4(wsr, WS_RS + (128 * P + 8 * S + lane) * 4, __float_as_uint(rs * rs));
    }
    const rsrc_t rs = wsr;
#pragma unroll
    for (int a = 0; a < 3; ++a) {
        const int f0 = 96 * S + 16 * (3 * wm + a);
        const int kb = f0 >> 5, oct = ((f0 & 31) >> 3) + (g >> 1);
#pragma unroll
        for (int c = 0; c < 4; c += 2) {
            u32 X[2], Y[2];
#pragma unroll
            for (int e = 0; e < 2; ++e) {
                X[e] = cvtpk(__builtin_fmaxf(acc[2 * a][c][2 * e], 0.f) * acc[2 * a + 1][c][2 * e], __builtin_fmaxf(acc[2 * a][c][2 * e + 1], 0.f) * acc[2 * a + 1][c][2 * e + 1]);
                Y[e] = cvtpk(__builtin_fmaxf(acc[2 * a][c + 1][2 * e], 0.f) * acc[2 * a + 1][c + 1][2 * e], __builtin_fmaxf(acc[2 * a][c + 1][2 * e + 1], 0.f) * acc[2 * a + 1][c + 1][2 * e + 1]);
            }
#pragma unroll
            for (int e = 0; e < 2; ++e) { auto r = __builtin_amdgcn_permlane16_swap(X[e], Y[e], false, false); X[e] = r[0]; Y[e] = r[1]; }
            const int lt = 8 * P + 4 * wn + c + (g & 1);
            st16<AUX_HM>(rs, WS_HM + ((lt * 48 + kb) * 64 + (lane & 15) + 16 * oct) * 16, u32x4{X[0], X[1], Y[0], Y[1]});
        }
    }
    STAMP(ws, 4, 4);
}

constexpr int G2_STAGE = 18432, G2_NS = 8, G2_NST = 24;
constexpr int G2_RES = G2_NS * G2_STAGE;
constexpr int G2_LDS = G2_RES + 10240;

__global__ __launch_bounds__(768, 3) void k4_gemm2(char* __restrict__ ws, u16* __restrict__ out) {
    __shared__ __attribute__((aligned(1024))) char smem[G2_LDS];
    const int b = blockIdx.x, t = threadIdx.x, lane = t & 63;
    const int w = __builtin_amdgcn_readfirstlane(t >> 6);
    const int xcd = b & 7, j = b >> 3;
    const int P = (xcd & 3) * 8 + (j & 7), S = (xcd >> 2) * 4 + (j >> 3);
    const u32 lds0 = (u32)(uintptr_t)smem;
    if (w >= 4) {
        const int lw = w - 4;
        const char* wsrc = ws + WS_W2T + S * 24 * 10240 + lw * 1024 + lane * 16;
        const char* msrc = ws + WS_HM + ((4 * P + (lw >> 1)) * 48 + (lw & 1)) * 1024 + lane * 16;
        auto issue = [&](int st, int slot) {
            const u32 base = lds0 + slot * G2_STAGE;
            glds16(msrc + st * 2048, base + 10240 + lw * 1024);
            glds16(wsrc + st * 10240, base + lw * 1024);
            if (lw < 2) glds16(wsrc + st * 10240 + 8192, base + 8192 + lw * 1024);
        };
        auto residual = [&]() {
            for (int pc = lw; pc < 10; pc += 8) {
                const int row = 8 * pc + (lane >> 3), ch = (lane & 7) ^ (((lane >> 3) & 3) << 1);
                glds16(ws + WS_HC + ((80 * S + row) * L_ + 64 * P + 8 * ch) * 2, lds0 + G2_RES + pc * 1024);
            }
        };
        if (lw < 2) {
            issue(0, 0); issue(1, 1); issue(2, 2);
            waitv<6>(); BAR();
            waitv<3>(); BAR();
            issue(3, 3);
            issue(4, 4);
            waitv<6>(); BAR();
            issue(5, 5);
            issue(6, 6);
            waitv<9>(); BAR();
            issue(7, 7);
            issue(8, 0);
            waitv<12>(); BAR();
            issue(9, 1);
            issue(10, 2);
            waitv<15>(); BAR();
            issue(11, 3);
            waitv<15>(); BAR();
            issue(12, 4);
            waitv<15>(); BAR();
            issue(13, 5);
            waitv<15>(); BAR();
            issue(14, 6);
            waitv<15>(); BAR();
            issue(15, 7);
            waitv<15>(); BAR();
            issue(16, 0);
            waitv<15>(); BAR();
            issue(17, 1);
            waitv<15>(); BAR();
            issue(18, 2);
            waitv<15>(); BAR();
            issue(19, 3);
            waitv<15>(); BAR();
            issue(20, 4);
            waitv<15>(); BAR();
            issue(21, 5);
            waitv<15>(); BAR();
            issue(22, 6);
            waitv<15>(); BAR();
            issue(23, 7);
            waitv<15>(); BAR();
            residual();
            waitv<12>(); BAR();
            waitv<9>(); BAR();
            waitv<6>(); BAR();
            waitv<3>(); BAR();
            waitv<0>(); BAR();
        } else {
            issue(0, 0); issue(1, 1); issue(2, 2);
            waitv<4>(); BAR();
            waitv<2>(); BAR();
            issue(3, 3);
            issue(4, 4);
            waitv<4>(); BAR();
            issue(5, 5);
            issue(6, 6);
            waitv<6>(); BAR();
            issue(7, 7);
            issue(8, 0);
            waitv<8>(); BAR();
            issue(9, 1);
            issue(10, 2);
            waitv<10>(); BAR();
            issue(11, 3);
            waitv<10>(); BAR();
            issue(12, 4);
            waitv<10>(); BAR();
            issue(13, 5);
            waitv<10>(); BAR();
            issue(14, 6);
            waitv<10>(); BAR();
            issue(15, 7);
            waitv<10>(); BAR();
            issue(16, 0);
            waitv<10>(); BAR();
            issue(17, 1);
            waitv<10>(); BAR();
            issue(18, 2);
            waitv<10>(); BAR();
            issue(19, 3);
            waitv<10>(); BAR();
            issue(20, 4);
            waitv<10>(); BAR();
            issue(21, 5);
            waitv<10>(); BAR();
            issue(22, 6);
            waitv<10>(); BAR();
            issue(23, 7);
            waitv<10>(); BAR();
            residual();
            waitv<8>(); BAR();
            waitv<6>(); BAR();
            waitv<4>(); BAR();
            waitv<2>(); BAR();
            waitv<0>(); BAR();
        }
        return;
    }
    STAMP(ws, 5, 0);
    const int g = lane >> 4, q = (lane & 15) >> 2, p = lane & 3;
    const int l = 64 * P + 16 * w + (lane & 15);
    const float rs2 = *((const float*)(ws + WS_RS) + l);
    const float m1 = *((const float*)(ws + WS_ST) + 2 * l);
    f32x4 acc[5];
#pragma unroll
    for (int a = 0; a < 5; ++a) acc[a] = f32x4{0.f, 0.f, 0.f, 0.f};
    const LAS char* abase = (const LAS char*)smem + lane * 16;
    const LAS char* bbase = (const LAS char*)smem + 10240 + w * 2048 + lane * 16;
    bf16x8 EA[2][5], EB[2], OA[2][5], OB[2];
    BAR();
    STAMP(ws, 5, 1);
    EB[0] = *(const LAS bf16x8*)(bbase); EB[1] = *(const LAS bf16x8*)(bbase + 1024);
#pragma unroll
    for (int a = 0; a < 5; ++a) { EA[0][a] = *(const LAS bf16x8*)(abase + a * 1024); EA[1][a] = *(const LAS bf16x8*)(abase + 5120 + a * 1024); }
    SCHEDB();
#pragma unroll 1
    for (int it = 0; it < 3; ++it) {
        if (it == 1) STAMP(ws, 5, 2); if (it == 2) STAMP(ws, 5, 3);
        BAR();
        acc[0] = MFMA16(EA[0][0], EB[0], acc[0]);
        OB[0] = *(const LAS bf16x8*)(bbase + 1*G2_STAGE);
        OA[0][0] = *(const LAS bf16x8*)(abase + 1*G2_STAGE + 0);
        SCHEDB();
        acc[1] = MFMA16(EA[0][1], EB[0], acc[1]);
        OA[0][1] = *(const LAS bf16x8*)(abase + 1*G2_STAGE + 1024);
        OA[0][2] = *(const LAS bf16x8*)(abase + 1*G2_STAGE + 2048);
        SCHEDB();
        acc[2] = MFMA16(EA[0][2], EB[0], acc[2]);
        OA[0][3] = *(const LAS bf16x8*)(abase + 1*G2_STAGE + 3072);
        SCHEDB();
        acc[3] = MFMA16(EA[0][3], EB[0], acc[3]);
        OA[0][4] = *(const LAS bf16x8*)(abase + 1*G2_STAGE + 4096);
        SCHEDB();
        acc[4] = MFMA16(EA[0][4], EB[0], acc[4]);
        OB[1] = *(const LAS bf16x8*)(bbase + 1*G2_STAGE + 1024);
        SCHEDB();
        acc[0] = MFMA16(EA[1][0], EB[1], acc[0]);
        OA[1][0] = *(const LAS bf16x8*)(abase + 1*G2_STAGE + 5120);
        SCHEDB();
        acc[1] = MFMA16(EA[1][1], EB[1], acc[1]);
        OA[1][1] = *(const LAS bf16x8*)(abase + 1*G2_STAGE + 6144);
        SCHEDB();
        acc[2] = MFMA16(EA[1][2], EB[1], acc[2]);
        OA[1][2] = *(const LAS bf16x8*)(abase + 1*G2_STAGE + 7168);
        SCHEDB();
        acc[3] = MFMA16(EA[1][3], EB[1], acc[3]);
        OA[1][3] = *(const LAS bf16x8*)(abase + 1*G2_STAGE + 8192);
        SCHEDB();
        acc[4] = MFMA16(EA[1][4], EB[1], acc[4]);
        OA[1][4] = *(const LAS bf16x8*)(abase + 1*G2_STAGE + 9216);
        SCHEDB();
        BAR();
        acc[0] = MFMA16(OA[0][0], OB[0], acc[0]);
        EB[0] = *(const LAS bf16x8*)(bbase + 2*G2_STAGE);
        EA[0][0] = *(const LAS bf16x8*)(abase + 2*G2_STAGE + 0);
        SCHEDB();
        acc[1] = MFMA16(OA[0][1], OB[0], acc[1]);
        EA[0][1] = *(const LAS bf16x8*)(abase + 2*G2_STAGE + 1024);
        EA[0][2] = *(const LAS bf16x8*)(abase + 2*G2_STAGE + 2048);
        SCHEDB();
        acc[2] = MFMA16(OA[0][2], OB[0], acc[2]);
        EA[0][3] = *(const LAS bf16x8*)(abase + 2*G2_STAGE + 3072);
        SCHEDB();
        acc[3] = MFMA16(OA[0][3], OB[0], acc[3]);
        EA[0][4] = *(const LAS bf16x8*)(abase + 2*G2_STAGE + 4096);
        SCHEDB();
        acc[4] = MFMA16(OA[0][4], OB[0], acc[4]);
        EB[1] = *(const LAS bf16x8*)(bbase + 2*G2_STAGE + 1024);
        SCHEDB();
        acc[0] = MFMA16(OA[1][0], OB[1], acc[0]);
        EA[1][0] = *(const LAS bf16x8*)(abase + 2*G2_STAGE + 5120);
        SCHEDB();
        acc[1] = MFMA16(OA[1][1], OB[1], acc[1]);
        EA[1][1] = *(const LAS bf16x8*)(abase + 2*G2_STAGE + 6144);
        SCHEDB();
        acc[2] = MFMA16(OA[1][2], OB[1], acc[2]);
        EA[1][2] = *(const LAS bf16x8*)(abase + 2*G2_STAGE + 7168);
        SCHEDB();
        acc[3] = MFMA16(OA[1][3], OB[1], acc[3]);
        EA[1][3] = *(const LAS bf16x8*)(abase + 2*G2_STAGE + 8192);
        SCHEDB();
        acc[4] = MFMA16(OA[1][4], OB[1], acc[4]);
        EA[1][4] = *(const LAS bf16x8*)(abase + 2*G2_STAGE + 9216);
        SCHEDB();
        BAR();
        acc[0] = MFMA16(EA[0][0], EB[0], acc[0]);
        OB[0] = *(const LAS bf16x8*)(bbase + 3*G2_STAGE);
        OA[0][0] = *(const LAS bf16x8*)(abase + 3*G2_STAGE + 0);
        SCHEDB();
        acc[1] = MFMA16(EA[0][1], EB[0], acc[1]);
        OA[0][1] = *(const LAS bf16x8*)(abase + 3*G2_STAGE + 1024);
        OA[0][2] = *(const LAS bf16x8*)(abase + 3*G2_STAGE + 2048);
        SCHEDB();
        acc[2] = MFMA16(EA[0][2], EB[0], acc[2]);
        OA[0][3] = *(const LAS bf16x8*)(abase + 3*G2_STAGE + 3072);
        SCHEDB();
        acc[3] = MFMA16(EA[0][3], EB[0], acc[3]);
        OA[0][4] = *(const LAS bf16x8*)(abase + 3*G2_STAGE + 4096);
        SCHEDB();
        acc[4] = MFMA16(EA[0][4], EB[0], acc[4]);
        OB[1] = *(const LAS bf16x8*)(bbase + 3*G2_STAGE + 1024);
        SCHEDB();
        acc[0] = MFMA16(EA[1][0], EB[1], acc[0]);
        OA[1][0] = *(const LAS bf16x8*)(abase + 3*G2_STAGE + 5120);
        SCHEDB();
        acc[1] = MFMA16(EA[1][1], EB[1], acc[1]);
        OA[1][1] = *(const LAS bf16x8*)(abase + 3*G2_STAGE + 6144);
        SCHEDB();
        acc[2] = MFMA16(EA[1][2], EB[1], acc[2]);
        OA[1][2] = *(const LAS bf16x8*)(abase + 3*G2_STAGE + 7168);
        SCHEDB();
        acc[3] = MFMA16(EA[1][3], EB[1], acc[3]);
        OA[1][3] = *(const LAS bf16x8*)(abase + 3*G2_STAGE + 8192);
        SCHEDB();
        acc[4] = MFMA16(EA[1][4], EB[1], acc[4]);
        OA[1][4] = *(const LAS bf16x8*)(abase + 3*G2_STAGE + 9216);
        SCHEDB();
        BAR();
        acc[0] = MFMA16(OA[0][0], OB[0], acc[0]);
        EB[0] = *(const LAS bf16x8*)(bbase + 4*G2_STAGE);
        EA[0][0] = *(const LAS bf16x8*)(abase + 4*G2_STAGE + 0);
        SCHEDB();
        acc[1] = MFMA16(OA[0][1], OB[0], acc[1]);
        EA[0][1] = *(const LAS bf16x8*)(abase + 4*G2_STAGE + 1024);
        EA[0][2] = *(const LAS bf16x8*)(abase + 4*G2_STAGE + 2048);
        SCHEDB();
        acc[2] = MFMA16(OA[0][2], OB[0], acc[2]);
        EA[0][3] = *(const LAS bf16x8*)(abase + 4*G2_STAGE + 3072);
        SCHEDB();
        acc[3] = MFMA16(OA[0][3], OB[0], acc[3]);
        EA[0][4] = *(const LAS bf16x8*)(abase + 4*G2_STAGE + 4096);
        SCHEDB();
        acc[4] = MFMA16(OA[0][4], OB[0], acc[4]);
        EB[1] = *(const LAS bf16x8*)(bbase + 4*G2_STAGE + 1024);
        SCHEDB();
        acc[0] = MFMA16(OA[1][0], OB[1], acc[0]);
        EA[1][0] = *(const LAS bf16x8*)(abase + 4*G2_STAGE + 5120);
        SCHEDB();
        acc[1] = MFMA16(OA[1][1], OB[1], acc[1]);
        EA[1][1] = *(const LAS bf16x8*)(abase + 4*G2_STAGE + 6144);
        SCHEDB();
        acc[2] = MFMA16(OA[1][2], OB[1], acc[2]);
        EA[1][2] = *(const LAS bf16x8*)(abase + 4*G2_STAGE + 7168);
        SCHEDB();
        acc[3] = MFMA16(OA[1][3], OB[1], acc[3]);
        EA[1][3] = *(const LAS bf16x8*)(abase + 4*G2_STAGE + 8192);
        SCHEDB();
        acc[4] = MFMA16(OA[1][4], OB[1], acc[4]);
        EA[1][4] = *(const LAS bf16x8*)(abase + 4*G2_STAGE + 9216);
        SCHEDB();
        BAR();
        acc[0] = MFMA16(EA[0][0], EB[0], acc[0]);
        OB[0] = *(const LAS bf16x8*)(bbase + 5*G2_STAGE);
        OA[0][0] = *(const LAS bf16x8*)(abase + 5*G2_STAGE + 0);
        SCHEDB();
        acc[1] = MFMA16(EA[0][1], EB[0], acc[1]);
        OA[0][1] = *(const LAS bf16x8*)(abase + 5*G2_STAGE + 1024);
        OA[0][2] = *(const LAS bf16x8*)(abase + 5*G2_STAGE + 2048);
        SCHEDB();
        acc[2] = MFMA16(EA[0][2], EB[0], acc[2]);
        OA[0][3] = *(const LAS bf16x8*)(abase + 5*G2_STAGE + 3072);
        SCHEDB();
        acc[3] = MFMA16(EA[0][3], EB[0], acc[3]);
        OA[0][4] = *(const LAS bf16x8*)(abase + 5*G2_STAGE + 4096);
        SCHEDB();
        acc[4] = MFMA16(EA[0][4], EB[0], acc[4]);
        OB[1] = *(const LAS bf16x8*)(bbase + 5*G2_STAGE + 1024);
        SCHEDB();
        acc[0] = MFMA16(EA[1][0], EB[1], acc[0]);
        OA[1][0] = *(const LAS bf16x8*)(abase + 5*G2_STAGE + 5120);
        SCHEDB();
        acc[1] = MFMA16(EA[1][1], EB[1], acc[1]);
        OA[1][1] = *(const LAS bf16x8*)(abase + 5*G2_STAGE + 6144);
        SCHEDB();
        acc[2] = MFMA16(EA[1][2], EB[1], acc[2]);
        OA[1][2] = *(const LAS bf16x8*)(abase + 5*G2_STAGE + 7168);
        SCHEDB();
        acc[3] = MFMA16(EA[1][3], EB[1], acc[3]);
        OA[1][3] = *(const LAS bf16x8*)(abase + 5*G2_STAGE + 8192);
        SCHEDB();
        acc[4] = MFMA16(EA[1][4], EB[1], acc[4]);
        OA[1][4] = *(const LAS bf16x8*)(abase + 5*G2_STAGE + 9216);
        SCHEDB();
        BAR();
        acc[0] = MFMA16(OA[0][0], OB[0], acc[0]);
        EB[0] = *(const LAS bf16x8*)(bbase + 6*G2_STAGE);
        EA[0][0] = *(const LAS bf16x8*)(abase + 6*G2_STAGE + 0);
        SCHEDB();
        acc[1] = MFMA16(OA[0][1], OB[0], acc[1]);
        EA[0][1] = *(const LAS bf16x8*)(abase + 6*G2_STAGE + 1024);
        EA[0][2] = *(const LAS bf16x8*)(abase + 6*G2_STAGE + 2048);
        SCHEDB();
        acc[2] = MFMA16(OA[0][2], OB[0], acc[2]);
        EA[0][3] = *(const LAS bf16x8*)(abase + 6*G2_STAGE + 3072);
        SCHEDB();
        acc[3] = MFMA16(OA[0][3], OB[0], acc[3]);
        EA[0][4] = *(const LAS bf16x8*)(abase + 6*G2_STAGE + 4096);
        SCHEDB();
        acc[4] = MFMA16(OA[0][4], OB[0], acc[4]);
        EB[1] = *(const LAS bf16x8*)(bbase + 6*G2_STAGE + 1024);
        SCHEDB();
        acc[0] = MFMA16(OA[1][0], OB[1], acc[0]);
        EA[1][0] = *(const LAS bf16x8*)(abase + 6*G2_STAGE + 5120);
        SCHEDB();
        acc[1] = MFMA16(OA[1][1], OB[1], acc[1]);
        EA[1][1] = *(const LAS bf16x8*)(abase + 6*G2_STAGE + 6144);
        SCHEDB();
        acc[2] = MFMA16(OA[1][2], OB[1], acc[2]);
        EA[1][2] = *(const LAS bf16x8*)(abase + 6*G2_STAGE + 7168);
        SCHEDB();
        acc[3] = MFMA16(OA[1][3], OB[1], acc[3]);
        EA[1][3] = *(const LAS bf16x8*)(abase + 6*G2_STAGE + 8192);
        SCHEDB();
        acc[4] = MFMA16(OA[1][4], OB[1], acc[4]);
        EA[1][4] = *(const LAS bf16x8*)(abase + 6*G2_STAGE + 9216);
        SCHEDB();
        BAR();
        acc[0] = MFMA16(EA[0][0], EB[0], acc[0]);
        OB[0] = *(const LAS bf16x8*)(bbase + 7*G2_STAGE);
        OA[0][0] = *(const LAS bf16x8*)(abase + 7*G2_STAGE + 0);
        SCHEDB();
        acc[1] = MFMA16(EA[0][1], EB[0], acc[1]);
        OA[0][1] = *(const LAS bf16x8*)(abase + 7*G2_STAGE + 1024);
        OA[0][2] = *(const LAS bf16x8*)(abase + 7*G2_STAGE + 2048);
        SCHEDB();
        acc[2] = MFMA16(EA[0][2], EB[0], acc[2]);
        OA[0][3] = *(const LAS bf16x8*)(abase + 7*G2_STAGE + 3072);
        SCHEDB();
        acc[3] = MFMA16(EA[0][3], EB[0], acc[3]);
        OA[0][4] = *(const LAS bf16x8*)(abase + 7*G2_STAGE + 4096);
        SCHEDB();
        acc[4] = MFMA16(EA[0][4], EB[0], acc[4]);
        OB[1] = *(const LAS bf16x8*)(bbase + 7*G2_STAGE + 1024);
        SCHEDB();
        acc[0] = MFMA16(EA[1][0], EB[1], acc[0]);
        OA[1][0] = *(const LAS bf16x8*)(abase + 7*G2_STAGE + 5120);
        SCHEDB();
        acc[1] = MFMA16(EA[1][1], EB[1], acc[1]);
        OA[1][1] = *(const LAS bf16x8*)(abase + 7*G2_STAGE + 6144);
        SCHEDB();
        acc[2] = MFMA16(EA[1][2], EB[1], acc[2]);
        OA[1][2] = *(const LAS bf16x8*)(abase + 7*G2_STAGE + 7168);
        SCHEDB();
        acc[3] = MFMA16(EA[1][3], EB[1], acc[3]);
        OA[1][3] = *(const LAS bf16x8*)(abase + 7*G2_STAGE + 8192);
        SCHEDB();
        acc[4] = MFMA16(EA[1][4], EB[1], acc[4]);
        OA[1][4] = *(const LAS bf16x8*)(abase + 7*G2_STAGE + 9216);
        SCHEDB();
        if (it < 2) BAR();
        acc[0] = MFMA16(OA[0][0], OB[0], acc[0]);
        EB[0] = *(const LAS bf16x8*)(bbase + 0*G2_STAGE);
        EA[0][0] = *(const LAS bf16x8*)(abase + 0*G2_STAGE + 0);
        SCHEDB();
        acc[1] = MFMA16(OA[0][1], OB[0], acc[1]);
        EA[0][1] = *(const LAS bf16x8*)(abase + 0*G2_STAGE + 1024);
        EA[0][2] = *(const LAS bf16x8*)(abase + 0*G2_STAGE + 2048);
        SCHEDB();
        acc[2] = MFMA16(OA[0][2], OB[0], acc[2]);
        EA[0][3] = *(const LAS bf16x8*)(abase + 0*G2_STAGE + 3072);
        SCHEDB();
        acc[3] = MFMA16(OA[0][3], OB[0], acc[3]);
        EA[0][4] = *(const LAS bf16x8*)(abase + 0*G2_STAGE + 4096);
        SCHEDB();
        acc[4] = MFMA16(OA[0][4], OB[0], acc[4]);
        EB[1] = *(const LAS bf16x8*)(bbase + 0*G2_STAGE + 1024);
        SCHEDB();
        acc[0] = MFMA16(OA[1][0], OB[1], acc[0]);
        EA[1][0] = *(const LAS bf16x8*)(abase + 0*G2_STAGE + 5120);
        SCHEDB();
        acc[1] = MFMA16(OA[1][1], OB[1], acc[1]);
        EA[1][1] = *(const LAS bf16x8*)(abase + 0*G2_STAGE + 6144);
        SCHEDB();
        acc[2] = MFMA16(OA[1][2], OB[1], acc[2]);
        EA[1][2] = *(const LAS bf16x8*)(abase + 0*G2_STAGE + 7168);
        SCHEDB();
        acc[3] = MFMA16(OA[1][3], OB[1], acc[3]);
        EA[1][3] = *(const LAS bf16x8*)(abase + 0*G2_STAGE + 8192);
        SCHEDB();
        acc[4] = MFMA16(OA[1][4], OB[1], acc[4]);
        EA[1][4] = *(const LAS bf16x8*)(abase + 0*G2_STAGE + 9216);
        SCHEDB();
    }
    STAMP(ws, 5, 6);
    const rsrc_t ro = mkrsrc(out, L_ * D_ * 2);
#pragma unroll
    for (int a = 0; a < 5; ++a) {
        const int row = 16 * a + 4 * g + q;
        const int ch = (2 * w + (p >> 1)) ^ ((row & 3) << 1);
        const s16x4 hr = trread((const LAS char*)smem + G2_RES + row * 128 + ch * 16 + 8 * (p & 1));
        const u32x2 hh = __builtin_bit_cast(u32x2, hr);
        const u32 f01 = cvtpk(acc[a][0] * rs2, acc[a][1] * rs2), f23 = cvtpk(acc[a][2] * rs2, acc[a][3] * rs2);
        const u32 c01 = hh[0], c23 = hh[1];
        const u32 h01 = cvtpk(bflo(c01) + m1, bfhi(c01) + m1), h23 = cvtpk(bflo(c23) + m1, bfhi(c23) + m1);
        __builtin_amdgcn_raw_buffer_store_b64(u32x2{cvtpk(bflo(h01) + bflo(f01), bfhi(h01) + bfhi(f01)), cvtpk(bflo(h23) + bflo(f23), bfhi(h23) + bfhi(f23))}, ro, (l * D_ + 80 * S + 16 * a + 4 * g) * 2, 0, 0);
    }
    STAMP(ws, 5, 7);
}

extern "C" void kernel_launch(void* const* d_in, const int* in_sizes, int n_in, void* d_out, int out_size, void* d_ws, size_t ws_size, hipStream_t stream) {
    const u16* x = (const u16*)d_in[0];
    const u16* Are = (const u16*)d_in[1]; const u16* Aim = (const u16*)d_in[2];
    const u16* Bre = (const u16*)d_in[3]; const u16* Bim = (const u16*)d_in[4];
    const u16* Cre = (const u16*)d_in[5]; const u16* Cim = (const u16*)d_in[6];
    const u16* logdt = (const u16*)d_in[7]; const u16* Dskip = (const u16*)d_in[8];
    const u16* Wg = (const u16*)d_in[10]; const u16* Wu = (const u16*)d_in[11]; const u16* Wd = (const u16*)d_in[12];
    char* ws = (char*)d_ws;
    k1_prep<<<816, 256, 0, stream>>>(x, Are, Aim, Bre, Bim, Cre, Cim, logdt, Wg, Wu, ws);
    k2_conv<<<768, 256, 0, stream>>>(Dskip, Wg, Wu, Wd, ws);
    k3_gemm1<<<256, 768, 0, stream>>>(ws);
    k4_gemm2<<<256, 768, 0, stream>>>(ws, (u16*)d_out);
}
```

```cpp
#include <hip/hip_runtime.h>
#include <stdint.h>

typedef unsigned short u16;
typedef unsigned int u32;
typedef short bf16x8 __attribute__((ext_vector_type(8)));
typedef short s16x4 __attribute__((ext_vector_type(4)));
typedef float f32x2 __attribute__((ext_vector_type(2)));
typedef float f32x4 __attribute__((ext_vector_type(4)));
typedef float f32x16 __attribute__((ext_vector_type(16)));
typedef unsigned u32x4 __attribute__((ext_vector_type(4)));
typedef unsigned u32x2 __attribute__((ext_vector_type(2)));
typedef __bf16 bf16x2_t __attribute__((ext_vector_type(2)));
#define LAS __attribute__((address_space(3)))
#define DI __device__ __forceinline__

#define STAMP(ws, k, j) do {} while (0)
#define STAMP2(ws, k, j) do {} while (0)

constexpr int L_ = 2048, D_ = 640, NM_ = 192, F_ = 1536;

constexpr u32 WS_HC  = 0;
constexpr u32 WS_HM  = WS_HC + D_ * L_ * 2;
constexpr u32 WS_ST  = WS_HM + L_ * F_ * 2;
constexpr u32 WS_RS  = WS_ST + 2048 * 4;
constexpr u32 WS_W1T = WS_RS + 2048 * 4;
constexpr u32 WS_W2T = WS_W1T + D_ * F_ * 2 * 2;
constexpr u32 WS_GR  = WS_W2T + F_ * D_ * 2;
constexpr u32 WS_AR  = WS_GR + 2048 * 8;
constexpr u32 WS_EP  = WS_AR + 1024;
constexpr u32 WS_END = WS_EP + 256;

DI float bflo(u32 v) { return __uint_as_float(v << 16); }
DI float bfhi(u32 v) { return __uint_as_float(v & 0xffff0000u); }
DI float bf2f(u16 v) { return __uint_as_float(((u32)v) << 16); }
DI u32 cvtpk(float lo, float hi) { f32x2 v = {lo, hi}; bf16x2_t r = __builtin_convertvector(v, bf16x2_t); return __builtin_bit_cast(u32, r); }
DI float rbf(float f) { return bflo(cvtpk(f, f)); }

typedef __amdgpu_buffer_rsrc_t rsrc_t;
DI rsrc_t mkrsrc(void* p, u32 bytes) { return __builtin_amdgcn_make_buffer_rsrc(p, 0, bytes, 0x00020000); }
#define AUX_KL 0
#define AUX_WT 16
#define AUX_H 16
#define AUX_HM 16
template <int AUX = 0> DI void st16(rsrc_t r, u32 off, u32x4 v) { __builtin_amdgcn_raw_buffer_store_b128(v, r, off, 0, AUX); }
template <int AUX = 0> DI void st8(rsrc_t r, u32 off, u32x2 v) { __builtin_amdgcn_raw_buffer_store_b64(v, r, off, 0, AUX); }
template <int AUX = 0> DI void st4(rsrc_t r, u32 off, u32 v) { __builtin_amdgcn_raw_buffer_store_b32(v, r, off, 0, AUX); }

template <int CTRL> DI float dppadd(float v) { return v + __builtin_bit_cast(float, __builtin_amdgcn_update_dpp(0, __builtin_bit_cast(int, v), CTRL, 0xF, 0xF, true)); }
DI float swap16add(float v) { auto r = __builtin_amdgcn_permlane16_swap(__float_as_uint(v), __float_as_uint(v), false, false); const u32 a = r[0], b = r[1]; return __uint_as_float(a) + __uint_as_float(b); }
DI float swap32add(float v) { auto r = __builtin_amdgcn_permlane32_swap(__float_as_uint(v), __float_as_uint(v), false, false); const u32 a = r[0], b = r[1]; return __uint_as_float(a) + __uint_as_float(b); }
DI float wave_sum(float v) {
    v = dppadd<0xB1>(v);
    v = dppadd<0x4E>(v);
    v = dppadd<0x141>(v);
    v = dppadd<0x140>(v);
    return swap32add(swap16add(v));
}

struct cm { f32x2 a, s; };
DI cm mkcm(f32x2 b) { return { b, f32x2{-b.y, b.x} }; }
DI f32x2 cmul(f32x2 v, const cm& m) { return v.xx * m.a + v.yy * m.s; }
DI f32x2 csq(f32x2 a) { return f32x2{ a.x * a.x - a.y * a.y, 2.f * a.x * a.y }; }
DI f32x2 cexp_p(float ar, float br, float p) {
    float e = __builtin_amdgcn_exp2f(ar * p);
    float th = __builtin_amdgcn_fractf(br * p);
    return f32x2{ e * __builtin_amdgcn_cosf(th), e * __builtin_amdgcn_sinf(th) };
}

#define WAITV(n) asm volatile("s_waitcnt vmcnt(" #n ")" ::: "memory")
#define WAITL0() asm volatile("s_waitcnt lgkmcnt(0)" ::: "memory")
template <int N> DI void waitv() { asm volatile("s_waitcnt vmcnt(%0)" :: "n"(N) : "memory"); }
#define BAR() do { asm volatile("" ::: "memory"); __builtin_amdgcn_s_barrier(); asm volatile("" ::: "memory"); } while (0)
#define LBAR() do { WAITL0(); BAR(); } while (0)
#define SGB(m, n) __builtin_amdgcn_sched_group_barrier(m, n, 0)
#define SCHEDB() __builtin_amdgcn_sched_barrier(0)
#define MFMA16(a, b, c) __builtin_amdgcn_mfma_f32_16x16x32_bf16(a, b, c, 0, 0, 0)
#define GAS __attribute__((address_space(1)))
typedef unsigned long long u64;

DI s16x4 trread(const LAS char* p) { typedef short v4i16_t __attribute__((ext_vector_type(4))); return __builtin_bit_cast(s16x4, __builtin_amdgcn_ds_read_tr16_b64_v4i16((LAS v4i16_t*)p)); }
DI bf16x8 cat8(s16x4 a, s16x4 b) { return __builtin_shufflevector(a, b, 0, 1, 2, 3, 4, 5, 6, 7); }

constexpr int KG_PITCH = 784;
constexpr int KA_A = 0, KA_ASZ = 20 * KG_PITCH;
constexpr int KA_B = 3 * KA_ASZ, KA_BSZ = 32 * KG_PITCH;
constexpr int KA_U = KA_B + 3 * KA_BSZ, KA_USZ = 4608 + 4096;
constexpr int KA_CNT = KA_U + 3 * KA_USZ;
constexpr int KA_LDS = KA_CNT + 32;
constexpr int CV_CS = 4160;
constexpr int KB_X = 32256;

#define ADDTID2(base0, v0, o0, base1, v1, o1) asm volatile("s_mov_b32 m0, %2\n\ts_nop 0\n\tds_write_addtid_b32 %0 offset:%4\n\ts_mov_b32 m0, %3\n\ts_nop 0\n\tds_write_addtid_b32 %1 offset:%5" \
    :: "v"(v0), "v"(v1), "s"(base0), "s"(base1), "i"(o0), "i"(o1) : "memory")
#define ADDTID2S(base, v0, o0, v1, o1) asm volatile("s_mov_b32 m0, %2\n\ts_nop 0\n\tds_write_addtid_b32 %0 offset:%3\n\tds_write_addtid_b32 %1 offset:%4" :: "v"(v0), "v"(v1), "s"(base), "i"(o0), "i"(o1) : "memory")

DI void lds_sync(char* smem, int cnt, u32 target, int lane) {
    LAS u32* c = (LAS u32*)((LAS char*)smem + KA_CNT) + cnt;
    WAITL0();
    if (lane == 0) __hip_atomic_fetch_add(c, 1u, __ATOMIC_RELAXED, __HIP_MEMORY_SCOPE_WORKGROUP);
    for (int spin = 0; spin < 1000000; ++spin) {
        if (__builtin_amdgcn_readfirstlane(__hip_atomic_load(c, __ATOMIC_RELAXED, __HIP_MEMORY_SCOPE_WORKGROUP)) >= target) break;
        __builtin_amdgcn_s_sleep(1);
    }
    asm volatile("" ::: "memory");
}

DI void lds_arrive(char* smem, int cnt, int lane) {
    WAITL0();
    if (lane == 0) __hip_atomic_fetch_add((LAS u32*)((LAS char*)smem + KA_CNT) + cnt, 1u, __ATOMIC_RELAXED, __HIP_MEMORY_SCOPE_WORKGROUP);
}
DI void lds_wait(char* smem, int cnt, u32 target) {
    LAS u32* c = (LAS u32*)((LAS char*)smem + KA_CNT) + cnt;
    for (int spin = 0; spin < 1000000; ++spin) {
        if (__builtin_amdgcn_readfirstlane(__hip_atomic_load(c, __ATOMIC_RELAXED, __HIP_MEMORY_SCOPE_WORKGROUP)) >= target) break;
        __builtin_amdgcn_s_sleep(1);
    }
    asm volatile("" ::: "memory");
}

struct KParams { u16 are, aim, bre, bim, cre, cim, ldt; };

DI void ka_kgen(const KParams& P, char* smem, int sA, int sB, int w, int lane) {
    const float are = bf2f(P.are), aim = bf2f(P.aim), bre = bf2f(P.bre), bim = bf2f(P.bim), cre = bf2f(P.cre), cim = bf2f(P.cim);
    const float dt = rbf(__expf(bf2f(P.ldt)));
    const float ar = (dt * are) * 1.4426950408889634f, br = (dt * aim) * 0.15915494309189535f;
    const float dbr = dt * bre, dbi = dt * bim;
    const f32x2 Wc = { cre * dbr - cim * dbi, -(cre * dbi + cim * dbr) };
    const f32x2 l1 = cexp_p(ar, br, 1.f), l8 = cexp_p(ar, br, 8.f), l256 = cexp_p(ar, br, 256.f);
    const f32x2 l2 = csq(l1), l4 = csq(l2), l16 = csq(l8);
    const cm c1 = mkcm(f32x2{l1.x, -l1.y}), c2 = mkcm(f32x2{l2.x, -l2.y}), c4 = mkcm(f32x2{l4.x, -l4.y}), c256 = mkcm(f32x2{l256.x, -l256.y}), m16 = mkcm(l16);
    const u32 lds0 = (u32)(uintptr_t)smem;
    const u32 bA = lds0 + sA + w * 256, bB = lds0 + sB + w * 256 - KB_X;
    auto pk = [](f32x2 v) { return cvtpk(v.x, v.y); };
    constexpr int R = KG_PITCH, X = KB_X;
    f32x2 a0 = Wc, x256 = cmul(Wc, c256), e0 = {1.f, 0.f}, e1 = l8;
    ADDTID2S(bA, pk(a0), 0 * R, pk(x256), 8 * R);
    ADDTID2S(bB, pk(e0), X + 0 * R, pk(e1), X + 1 * R);
    f32x2 y = x256, v = cmul(x256, c256);
#define KG_E(c) do { e0 = cmul(e0, m16); e1 = cmul(e1, m16); ADDTID2S(bB, pk(e0), X + (c) * R, pk(e1), X + ((c) + 1) * R); } while (0)
    a0 = cmul(a0, c1); y = cmul(y, c2); ADDTID2S(bA, pk(a0), 1 * R, pk(y), 9 * R); KG_E(2);
    a0 = cmul(a0, c1); y = cmul(y, c2); ADDTID2S(bA, pk(a0), 2 * R, pk(y), 10 * R); KG_E(4);
    a0 = cmul(a0, c1); y = cmul(y, c2); ADDTID2S(bA, pk(a0), 3 * R, pk(y), 11 * R); KG_E(6);
    a0 = cmul(a0, c1); y = cmul(v, c4); ADDTID2S(bA, pk(a0), 4 * R, pk(v), 12 * R); v = cmul(v, c256); KG_E(8);
    a0 = cmul(a0, c1); ADDTID2S(bA, pk(a0), 5 * R, pk(y), 13 * R); y = cmul(v, c4); KG_E(10);
    a0 = cmul(a0, c1); ADDTID2S(bA, pk(a0), 6 * R, pk(v), 14 * R); v = cmul(v, c256); KG_E(12);
    a0 = cmul(a0, c1); ADDTID2S(bA, pk(a0), 7 * R, pk(y), 15 * R); KG_E(14);
    y = cmul(v, c256); KG_E(16);
    ADDTID2S(bA, pk(v), 16 * R, pk(y), 17 * R); v = cmul(y, c256); KG_E(18);
    y = cmul(v, c256); KG_E(20);
    ADDTID2S(bA, pk(v), 18 * R, pk(y), 19 * R); v = cmul(y, c256); KG_E(22);
    const float k2048 = wave_sum(v.x);
    KG_E(24); KG_E(26);
    KG_E(28); KG_E(30);
    const int r32 = lane & 31, h = lane >> 5;
    const char* pa = smem + sA + (r32 < 20 ? r32 : 19) * R + w * 256 + h * 16;
    const char* pb = smem + sB + r32 * R + w * 256 + h * 16;
    f32x16 acc = {};
#pragma unroll
    for (int s = 0; s < 8; ++s) {
        const bf16x8 a = *(const bf16x8*)(pa + 32 * s), bb = *(const bf16x8*)(pb + 32 * s);
        acc = __builtin_amdgcn_mfma_f32_32x32x16_bf16(a, bb, acc, 0, 0, 0);
    }
    const int c = r32;
    char* sl = smem + sA + w * 256;
    char* p0 = sl + (c >> 3) * R + (8 * (c & 7) + 4 * h) * 4;
    char* p1 = sl + (h ? 6 * R + 8 * c : (4 + (c >> 4)) * R + 16 * (c & 15));
    char* p2 = sl + 8 * R + 4 * c;
#pragma unroll
    for (int q = 0; q < 4; ++q) *(float*)(p0 + 4 * q) = acc[q];
    *(float*)(p1) = acc[4]; *(float*)(p1 + 4) = acc[5];
    *(float*)(p1 + (h ? R : 8)) = acc[6]; *(float*)(p1 + (h ? R + 4 : 12)) = acc[7];
    if (h == 0) { *(float*)(p2) = acc[8]; *(float*)(p2 + 128) = acc[9]; *(float*)(p2 + R) = acc[10]; *(float*)(p2 + R + 128) = acc[11]; }
    else if (c == 0) *(float*)(sl + 10 * R) = k2048;
}

DI void ka_expand(char* smem, int sA, int sB, int w, int lane) {
    const int t = 64 * w + lane;
    auto rd = [&](int i) { const char* p = smem + sA + (i >> 6) * KG_PITCH + (i & 63) * 4; return *(const float*)p + *(const float*)(p + 256) + *(const float*)(p + 512); };
    u32 k4, k5, k6, k7, f2, f3;
    if (w == 0) {
        const int i0 = t < 32 ? 8 * t : 256 + 4 * (t - 32);
        const u32 pat = t < 32 ? 0x76543210u : 0x43222100u;
        float kv[8];
#pragma unroll
        for (int j = 0; j < 8; ++j) kv[j] = rd(i0 + ((pat >> (4 * j)) & 15));
        k4 = cvtpk(kv[0], kv[1]); k5 = cvtpk(kv[2], kv[3]); k6 = cvtpk(kv[4], kv[5]); k7 = cvtpk(kv[6], kv[7]);
        f2 = 0; f3 = 0;
    } else if (w == 1) {
        const int i0 = 384 + 2 * (t - 64);
        const float k0 = rd(i0), k1 = rd(i0 + 1), k2 = rd(i0 + 2);
        k4 = cvtpk(k0, k0); k5 = cvtpk(k0, k1); k6 = cvtpk(k1, k1); k7 = cvtpk(k2, k2);
        const float a = rd(382), b = rd(383);
        f2 = cvtpk(a, a); f3 = cvtpk(b, k0);
    } else {
        const int i0 = 512 + (t - 128);
        const float k0 = rd(i0), k1 = rd(i0 + 1);
        k4 = cvtpk(k0, k0); k5 = k4; k6 = cvtpk((t & 1) ? k1 : k0, k1); k7 = cvtpk(k1, k1);
        const float a = w == 2 ? rd(511) : k0;
        f2 = cvtpk(a, a); f3 = cvtpk(k0, k0);
    }
    const u32 k2 = __builtin_amdgcn_update_dpp(f2, k6, 0x138, 0xF, 0xF, false), k3 = __builtin_amdgcn_update_dpp(f3, k7, 0x138, 0xF, 0xF, false);
    const u32 a2 = __builtin_amdgcn_alignbit(k3, k2, 16), a3 = __builtin_amdgcn_alignbit(k4, k3, 16), a4 = __builtin_amdgcn_alignbit(k5, k4, 16),
              a5 = __builtin_amdgcn_alignbit(k6, k5, 16), a6 = __builtin_amdgcn_alignbit(k7, k6, 16);
    char* kb = smem + sB + 32 + 16 * t;
    *(u32x4*)(kb + 0 * CV_CS) = u32x4{k4, k5, k6, k7};
    *(u32x4*)(kb + 1 * CV_CS) = u32x4{a3, a4, a5, a6};
    *(u32x4*)(kb + 2 * CV_CS) = u32x4{k3, k4, k5, k6};
    *(u32x4*)(kb + 3 * CV_CS) = u32x4{a2, a3, a4, a5};
    if (t < 8) *(u32x4*)(smem + sB + (t >> 1) * CV_CS + 16 * (t & 1)) = u32x4{0, 0, 0, 0};
}

__global__ __launch_bounds__(768, 3) void ka_ssm(const u16* __restrict__ x, const u16* __restrict__ Are, const u16* __restrict__ Aim, const u16* __restrict__ Bre,
                                                 const u16* __restrict__ Bim, const u16* __restrict__ Cre, const u16* __restrict__ Cim, const u16* __restrict__ logdt,
                                                 const u16* __restrict__ Dskip, const u16* __restrict__ Wg, const u16* __restrict__ Wu, const u16* __restrict__ Wd,
                                                 char* __restrict__ ws) {
    __shared__ __attribute__((aligned(256))) char smem[KA_LDS];
    const int b = blockIdx.x, t = threadIdx.x, lane = t & 63;
    const int wv = __builtin_amdgcn_readfirstlane(t >> 6), sb = wv >> 2, w = wv & 3;
    const int xcd = b & 7, j = b >> 3;
    const int nch = j < 16 ? 3 : 2;
    const int c0 = 80 * xcd + ((xcd & 2) ? (j < 16 ? 32 + 3 * j : 2 * (j - 16)) : (j < 16 ? 3 * j : 48 + 2 * (j - 16)));
    const int xs = min((2 * c0) & ~3, ((2 * c0) | 127) + 1 - 8), e0 = (2 * c0 - xs) >> 1;
    const bool conv = sb < nch;
    const int d = c0 + sb;
    const int sA = KA_A + sb * KA_ASZ, sB = KA_B + sb * KA_BSZ, sU = KA_U + sb * KA_USZ;
    const rsrc_t wsr = mkrsrc(ws, WS_END);
    STAMP(ws, 0, 0);
    if (t < 8) *(u32*)(smem + KA_CNT + 4 * t) = 0u;

    KParams KP;
    u32 ra[4][5];
    u32x4 wv1[8], wv2[8];
    const int tt = t - 512;
    const bool w1job = (j - 16) < 12;
    float dsk = 0.f;
    if (wv == 3 || wv == 7) {
        const u32* x32 = (const u32*)x;
#pragma unroll
        for (int rr = 0; rr < 4; ++rr)
#pragma unroll
            for (int i = 0; i < 5; ++i) ra[rr][i] = x32[(8 * b + 4 * sb + rr) * 320 + lane + 64 * i];
    }
    if (conv) {
        if (w < 3) {
            const int n = d * NM_ + 64 * w + lane;
            KP.are = Are[n]; KP.aim = Aim[n]; KP.bre = Bre[n]; KP.bim = Bim[n]; KP.cre = Cre[n]; KP.cim = Cim[n]; KP.ldt = logdt[d];
        }
        dsk = bf2f(Dskip[d]);
    }
    const u32 epoch = __hip_atomic_load((GAS u32*)(ws + WS_EP), __ATOMIC_RELAXED, __HIP_MEMORY_SCOPE_AGENT);
    const rsrc_t xr0 = mkrsrc((void*)x, L_ * D_ * 2);
    LBAR();
    if (wv == 3 || wv == 7) asm volatile("" :: "v"(ra[3][4]));
    else if (conv && w < 3) asm volatile("" :: "v"((u32)KP.are), "v"((u32)KP.cim), "v"((u32)KP.ldt));
    if (conv && w == 3) {
        if (lane < 30) *(u32x4*)(smem + sU + 16 * lane) = u32x4{0, 0, 0, 0};
        lds_arrive(smem, sb, lane);
    }
    if (!conv) {
        if (w1job) {
            const int u1 = xcd * 12 + (j - 16), up = u1 >= 48, col = 32 * (up ? u1 - 48 : u1) + 8 * (tt & 3);
            const u16* src = (up ? Wu : Wg) + (8 * (tt >> 2)) * F_ + col;
#pragma unroll
            for (int r = 0; r < 8; ++r) wv1[r] = *(const u32x4*)(src + r * F_);
            if (w == 0) {
#pragma unroll
                for (int r = 0; r < 8; ++r) wv2[r] = *(const u32x4*)(src + (512 + r) * F_);
            }
        } else {
            const int n1 = 480 * (xcd * 4 + (j - 28)) + tt, n2 = n1 + 256;
            const u16* s1 = Wd + (8 * (n1 / 80)) * D_ + 8 * (n1 % 80);
#pragma unroll
            for (int r = 0; r < 8; ++r) wv1[r] = *(const u32x4*)(s1 + r * D_);
            if (tt < 224) {
                const u16* s2 = Wd + (8 * (n2 / 80)) * D_ + 8 * (n2 % 80);
#pragma unroll
                for (int r = 0; r < 8; ++r) wv2[r] = *(const u32x4*)(s2 + r * D_);
            }
        }
    }
    const u32 tag = epoch + 1u;
    if (wv == 3 || wv == 7) {
        __builtin_amdgcn_s_setprio(3);
        float s1[4], s2[4], c0v[4];
#pragma unroll
        for (int rr = 0; rr < 4; ++rr) {
            const float c = bflo(__builtin_amdgcn_readfirstlane(ra[rr][0]));
            float a1 = 0.f, a2 = 0.f;
#pragma unroll
            for (int i = 0; i < 5; ++i) { const float a = bflo(ra[rr][i]) - c, e = bfhi(ra[rr][i]) - c; a1 += a + e; a2 = __builtin_fmaf(a, a, __builtin_fmaf(e, e, a2)); }
            s1[rr] = a1; s2[rr] = a2; c0v[rr] = c;
        }
        auto red4 = [&](const float (&v)[4]) {
            auto p01 = __builtin_amdgcn_permlane32_swap(__float_as_uint(v[0]), __float_as_uint(v[1]), false, false);
            auto p23 = __builtin_amdgcn_permlane32_swap(__float_as_uint(v[2]), __float_as_uint(v[3]), false, false);
            const u32 x0 = p01[0], x1 = p01[1], y0 = p23[0], y1 = p23[1];
            const float t01 = __uint_as_float(x0) + __uint_as_float(x1);
            const float t23 = __uint_as_float(y0) + __uint_as_float(y1);
            auto q = __builtin_amdgcn_permlane16_swap(__float_as_uint(t01), __float_as_uint(t23), false, false);
            const u32 z0 = q[0], z1 = q[1];
            float r = __uint_as_float(z0) + __uint_as_float(z1);
            r = dppadd<0xB1>(r); r = dppadd<0x4E>(r); r = dppadd<0x141>(r); r = dppadd<0x140>(r);
            return r;
        };
        const float n1 = red4(s1) * (1.f / 640.f), n2 = red4(s2) * (1.f / 640.f);
        const int lr = lane >> 4, rr = (lr >> 1) | ((lr & 1) << 1);
        const float cc = rr == 0 ? c0v[0] : rr == 1 ? c0v[1] : rr == 2 ? c0v[2] : c0v[3];
        float var = n2 - n1 * n1; var = var > 0.f ? var : 0.f;
        const float sd = __builtin_sqrtf(rbf(rbf(var) + 1e-5f));
        const u32 pay = cvtpk(cc + n1, sd);
        const int row = 8 * b + 4 * sb + rr;
        if ((lane & 15) == 0) {
            __hip_atomic_store((GAS u64*)(ws + WS_GR) + row, ((u64)tag << 32) | pay, __ATOMIC_RELAXED, __HIP_MEMORY_SCOPE_AGENT);
            st4(wsr, WS_ST + row * 4, pay << 16);
        }
        asm volatile("" ::: "memory");
        if (lane == 0) __hip_atomic_fetch_add((LAS u32*)((LAS char*)smem + KA_CNT) + 4, 1u, __ATOMIC_RELAXED, __HIP_MEMORY_SCOPE_WORKGROUP);
        __builtin_amdgcn_s_setprio(0);
    }
    if (conv) {
        if (w < 3) ka_kgen(KP, smem, sA, sB, w, lane);
    } else if (w1job) {
        float* red = (float*)(smem + sA);
#pragma unroll
        for (int i = 0; i < 4; ++i) {
            float a = 0.f, c = 0.f;
#pragma unroll
            for (int r = 0; r < 8; ++r) { a += bflo(wv1[r][i]); c += bfhi(wv1[r][i]); }
            if (w == 0) {
#pragma unroll
                for (int r = 0; r < 8; ++r) { a += bflo(wv2[r][i]); c += bfhi(wv2[r][i]); }
            }
            red[(tt >> 2) * 33 + 8 * (tt & 3) + 2 * i] = a; red[(tt >> 2) * 33 + 8 * (tt & 3) + 2 * i + 1] = c;
        }
    }
    if (!conv) {
        {
            if (w1job) {
            lds_sync(smem, sb, 4, lane);
            const float* red = (const float*)(smem + sA);
            const int col = tt & 31, part = tt >> 5;
            float sm = 0.f;
#pragma unroll
            for (int r = 0; r < 8; ++r) sm += red[(8 * part + r) * 33 + col];
            sm = swap32add(sm);
            if (lane < 32) *(float*)(smem + sB + (col * 4 + w) * 4) = sm;
            lds_sync(smem, sb, 8, lane);
#define WV(r) (rnd ? wv2[r][i] : wv1[r][i])
            }
            char* stg = smem + (w == 3 ? sA + 1024 : sB + 512 + w * 8192);
            u32* dtab = (u32*)(smem + sU + w * 256);
            auto flush = [&](u32 dst, bool active, const u32x4 (&o)[8]) {
                dtab[lane] = active ? dst : 0xffffff00u;
#pragma unroll
                for (int i = 0; i < 8; ++i) *(u32x4*)(stg + lane * 128 + ((i ^ (lane & 7)) * 16)) = o[i];
#pragma unroll
                for (int i = 0; i < 8; ++i) {
                    const int sl = 8 * i + (lane >> 3), pc = lane & 7;
                    const u32x4 v = *(const u32x4*)(stg + sl * 128 + ((pc ^ (sl & 7)) * 16));
                    st16<AUX_WT>(wsr, dtab[sl] + pc * 16, v);
                }
            };
            if (w1job) {
                const int u1 = xcd * 12 + (j - 16), up = u1 >= 48, f = 32 * (up ? u1 - 48 : u1) + 8 * (tt & 3);
                float cm[8];
#pragma unroll
                for (int i = 0; i < 8; ++i) { const f32x4 v = *(const f32x4*)(smem + sB + (8 * (tt & 3) + i) * 16); cm[i] = (v[0] + v[1] + v[2] + v[3]) * (1.f / 640.f); }
                const int sl = f / 96, fl = f % 96, mt = 2 * (fl >> 4) + up;
#pragma unroll
                for (int rnd = 0; rnd < 2; ++rnd) {
                    if (rnd == 0 || w == 0) {
                        const int kg = (tt >> 2) + 64 * rnd;
                        const u32 dst = WS_W1T + ((sl * 10 + (kg >> 3)) * 24 + ((kg >> 2) & 1) * 12 + mt) * 1024 + ((fl & 15) + 16 * (kg & 3)) * 16;
                        u32x4 o[8];
#pragma unroll
                        for (int i = 0; i < 4; ++i) {
                            const float ca = cm[2 * i], cb = cm[2 * i + 1];
                            o[2 * i] = u32x4{cvtpk(bflo(WV(0)) - ca, bflo(WV(1)) - ca), cvtpk(bflo(WV(2)) - ca, bflo(WV(3)) - ca),
                                             cvtpk(bflo(WV(4)) - ca, bflo(WV(5)) - ca), cvtpk(bflo(WV(6)) - ca, bflo(WV(7)) - ca)};
                            o[2 * i + 1] = u32x4{cvtpk(bfhi(WV(0)) - cb, bfhi(WV(1)) - cb), cvtpk(bfhi(WV(2)) - cb, bfhi(WV(3)) - cb),
                                                 cvtpk(bfhi(WV(4)) - cb, bfhi(WV(5)) - cb), cvtpk(bfhi(WV(6)) - cb, bfhi(WV(7)) - cb)};
                        }
                        flush(dst, true, o);
                    }
                }
            } else {
#pragma unroll
                for (int rnd = 0; rnd < 2; ++rnd) {
                    if (rnd == 0 || w < 3 || true) {
                        const bool act = rnd == 0 || tt < 224;
                        const int n = 480 * (xcd * 4 + (j - 28)) + tt + 256 * rnd, dc = n % 80, kg = n / 80;
                        const int ds = dc / 10, dl = 8 * (dc % 10);
                        const u32 dst = WS_W2T + ((ds * 24 + (kg >> 3)) * 10 + ((kg >> 2) & 1) * 5 + (dl >> 4)) * 1024 + ((dl & 15) + 16 * (kg & 3)) * 16;
                        u32x4 o[8];
#pragma unroll
                        for (int i = 0; i < 4; ++i) {
                            o[2 * i] = u32x4{__builtin_amdgcn_perm(WV(1), WV(0), 0x05040100u), __builtin_amdgcn_perm(WV(3), WV(2), 0x05040100u),
                                             __builtin_amdgcn_perm(WV(5), WV(4), 0x05040100u), __builtin_amdgcn_perm(WV(7), WV(6), 0x05040100u)};
                            o[2 * i + 1] = u32x4{__builtin_amdgcn_perm(WV(1), WV(0), 0x07060302u), __builtin_amdgcn_perm(WV(3), WV(2), 0x07060302u),
                                                 __builtin_amdgcn_perm(WV(5), WV(4), 0x07060302u), __builtin_amdgcn_perm(WV(7), WV(6), 0x07060302u)};
                        }
                        flush(dst, act, o);
                    }
                }
            }
        }
        if (t == 512) STAMP2(ws, 6, 0);
        return;
    }
    STAMP(ws, 0, 1);
    u32x2 xg[4]; u32x4 g0 = {0, 0, 0, 0}, g1 = {0, 0, 0, 0};
    if (wv < 8) {
        lds_wait(smem, 4, 2);
#pragma unroll
        for (int r = 0; r < 4; ++r) xg[r] = __builtin_amdgcn_raw_buffer_load_b64(xr0, (4 * t + r) * (D_ * 2) + xs, 0, 0);
        g0 = __builtin_amdgcn_raw_buffer_load_b128(wsr, WS_GR + t * 32, 0, 16);
        g1 = __builtin_amdgcn_raw_buffer_load_b128(wsr, WS_GR + t * 32 + 16, 0, 16);
    }
    if (w < 3) lds_sync(smem, sb, 4, lane); else lds_wait(smem, sb, 4);
    STAMP(ws, 0, 2);
    ka_expand(smem, sA, sB, w, lane);
    if (wv < 8) {
        for (int spin = 0; spin < 100000; ++spin) {
            if (__all(g0[1] == tag && g0[3] == tag && g1[1] == tag && g1[3] == tag)) break;
            __builtin_amdgcn_s_sleep(4);
            asm volatile("" ::: "memory");
            g0 = __builtin_amdgcn_raw_buffer_load_b128(wsr, WS_GR + t * 32, 0, 16);
            g1 = __builtin_amdgcn_raw_buffer_load_b128(wsr, WS_GR + t * 32 + 16, 0, 16);
        }
        const u32 pr[4] = {g0[0], g0[2], g1[0], g1[2]};
        float m[4], rs[4];
#pragma unroll
        for (int r = 0; r < 4; ++r) { m[r] = bflo(pr[r]); rs[r] = __builtin_amdgcn_rcpf(bfhi(pr[r])); }
#pragma unroll
        for (int s = 0; s < 3; ++s) {
            if (s < nch) {
                const int e = e0 + s;
                float xv[4];
#pragma unroll
                for (int r = 0; r < 4; ++r) { const u32 dw = e < 2 ? xg[r][0] : xg[r][1]; xv[r] = (e & 1) ? bfhi(dw) : bflo(dw); }
                const u32 q01 = cvtpk(xv[0] - m[0], xv[1] - m[1]), q23 = cvtpk(xv[2] - m[2], xv[3] - m[3]);
                const u32 u01 = cvtpk(bflo(q01) * rs[0], bfhi(q01) * rs[1]), u23 = cvtpk(bflo(q23) * rs[2], bfhi(q23) * rs[3]);
                char* ub = smem + KA_U + s * KA_USZ;
                *(u32x2*)(ub + 480 + 8 * t) = u32x2{u01, u23};
                *(u32x2*)(ub + 4608 + 8 * t) = u32x2{q01, q23};
            }
        }
    }
    STAMP(ws, 0, 3);
    asm volatile("" :: "v"(tag));
    lds_sync(smem, 3, 4 * nch, lane);
    STAMP(ws, 0, 4);
    if (t == 0) __hip_atomic_store((GAS u32*)(ws + WS_AR) + b, tag, __ATOMIC_RELAXED, __HIP_MEMORY_SCOPE_AGENT);
    const int al = lane & 15, g = lane >> 4;
    f32x4 acc[8];
#pragma unroll
    for (int i = 0; i < 8; ++i) acc[i] = f32x4{0.f, 0.f, 0.f, 0.f};
    {
        const LAS char* lds = (const LAS char*)smem;
        const int q = (lane & 15) >> 2, p = lane & 3;
        const LAS char* ub0 = lds + sU + 448 + 32 * al + 16 * g - 128 * w;
        const LAS char* ub1 = ub0 - 64;
        const LAS char* kb0 = lds + sB + q * CV_CS + 56 - 16 * g + 8 * p + 128 * w;
        const LAS char* kb1 = kb0 + 64;
        bf16x8 U0[8], U1[8]; s16x4 A0[8][2], A1[8][2];
        A0[0][0] = trread(kb0 + 8); A0[0][1] = trread(kb0 + 0); U0[0] = *(const LAS bf16x8*)(ub0 + 0); U0[1] = *(const LAS bf16x8*)(ub0 + 512); U0[2] = *(const LAS bf16x8*)(ub0 + 1024); U0[3] = *(const LAS bf16x8*)(ub0 + 1536); U0[4] = *(const LAS bf16x8*)(ub0 + 2048); U0[5] = *(const LAS bf16x8*)(ub0 + 2560); U0[6] = *(const LAS bf16x8*)(ub0 + 3072); U0[7] = *(const LAS bf16x8*)(ub0 + 3584); A0[1][0] = trread(kb0 + 520); A0[1][1] = trread(kb0 + 512); A0[2][0] = trread(kb0 + 1032); A0[2][1] = trread(kb0 + 1024); A0[3][0] = trread(kb0 + 1544); A0[3][1] = trread(kb0 + 1536); A0[4][0] = trread(kb0 + 2056); A0[4][1] = trread(kb0 + 2048); A0[5][0] = trread(kb0 + 2568); A0[5][1] = trread(kb0 + 2560); A0[6][0] = trread(kb0 + 3080); A0[6][1] = trread(kb0 + 3072); A0[7][0] = trread(kb0 + 3592); A0[7][1] = trread(kb0 + 3584);
        SCHEDB();
        acc[0] = MFMA16(cat8(A0[0][0], A0[0][1]), U0[0], acc[0]);
        SCHEDB();
        acc[1] = MFMA16(cat8(A0[0][0], A0[0][1]), U0[1], acc[1]);
        SCHEDB();
        acc[2] = MFMA16(cat8(A0[0][0], A0[0][1]), U0[2], acc[2]);
        SCHEDB();
        acc[3] = MFMA16(cat8(A0[0][0], A0[0][1]), U0[3], acc[3]);
        SCHEDB();
        acc[4] = MFMA16(cat8(A0[0][0], A0[0][1]), U0[4], acc[4]);
        SCHEDB();
        acc[5] = MFMA16(cat8(A0[0][0], A0[0][1]), U0[5], acc[5]);
        SCHEDB();
        acc[6] = MFMA16(cat8(A0[0][0], A0[0][1]), U0[6], acc[6]);
        SCHEDB();
        acc[7] = MFMA16(cat8(A0[0][0], A0[0][1]), U0[7], acc[7]);
        SCHEDB();
        acc[1] = MFMA16(cat8(A0[1][0], A0[1][1]), U0[0], acc[1]);
        A1[0][0] = trread(kb1 + 8);
        SCHEDB();
        acc[2] = MFMA16(cat8(A0[1][0], A0[1][1]), U0[1], acc[2]);
        A1[0][1] = trread(kb1 + 0);
        SCHEDB();
        acc[3] = MFMA16(cat8(A0[1][0], A0[1][1]), U0[2], acc[3]);
        U1[0] = *(const LAS bf16x8*)(ub1 + 0);
        SCHEDB();
        acc[4] = MFMA16(cat8(A0[1][0], A0[1][1]), U0[3], acc[4]);
        U1[1] = *(const LAS bf16x8*)(ub1 + 512);
        SCHEDB();
        acc[5] = MFMA16(cat8(A0[1][0], A0[1][1]), U0[4], acc[5]);
        U1[2] = *(const LAS bf16x8*)(ub1 + 1024);
        SCHEDB();
        acc[6] = MFMA16(cat8(A0[1][0], A0[1][1]), U0[5], acc[6]);
        U1[3] = *(const LAS bf16x8*)(ub1 + 1536);
        SCHEDB();
        acc[7] = MFMA16(cat8(A0[1][0], A0[1][1]), U0[6], acc[7]);
        U1[4] = *(const LAS bf16x8*)(ub1 + 2048);
        SCHEDB();
        acc[2] = MFMA16(cat8(A0[2][0], A0[2][1]), U0[0], acc[2]);
        U1[5] = *(const LAS bf16x8*)(ub1 + 2560);
        SCHEDB();
        acc[3] = MFMA16(cat8(A0[2][0], A0[2][1]), U0[1], acc[3]);
        U1[6] = *(const LAS bf16x8*)(ub1 + 3072);
        SCHEDB();
        acc[4] = MFMA16(cat8(A0[2][0], A0[2][1]), U0[2], acc[4]);
        U1[7] = *(const LAS bf16x8*)(ub1 + 3584);
        SCHEDB();
        acc[5] = MFMA16(cat8(A0[2][0], A0[2][1]), U0[3], acc[5]);
        A1[1][0] = trread(kb1 + 520);
        SCHEDB();
        acc[6] = MFMA16(cat8(A0[2][0], A0[2][1]), U0[4], acc[6]);
        A1[1][1] = trread(kb1 + 512);
        SCHEDB();
        acc[7] = MFMA16(cat8(A0[2][0], A0[2][1]), U0[5], acc[7]);
        A1[2][0] = trread(kb1 + 1032);
        SCHEDB();
        acc[3] = MFMA16(cat8(A0[3][0], A0[3][1]), U0[0], acc[3]);
        A1[2][1] = trread(kb1 + 1024);
        SCHEDB();
        acc[4] = MFMA16(cat8(A0[3][0], A0[3][1]), U0[1], acc[4]);
        A1[3][0] = trread(kb1 + 1544);
        SCHEDB();
        acc[5] = MFMA16(cat8(A0[3][0], A0[3][1]), U0[2], acc[5]);
        A1[3][1] = trread(kb1 + 1536);
        SCHEDB();
        acc[6] = MFMA16(cat8(A0[3][0], A0[3][1]), U0[3], acc[6]);
        A1[4][0] = trread(kb1 + 2056);
        SCHEDB();
        acc[7] = MFMA16(cat8(A0[3][0], A0[3][1]), U0[4], acc[7]);
        A1[4][1] = trread(kb1 + 2048);
        SCHEDB();
        acc[4] = MFMA16(cat8(A0[4][0], A0[4][1]), U0[0], acc[4]);
        A1[5][0] = trread(kb1 + 2568);
        SCHEDB();
        acc[5] = MFMA16(cat8(A0[4][0], A0[4][1]), U0[1], acc[5]);
        A1[5][1] = trread(kb1 + 2560);
        SCHEDB();
        acc[6] = MFMA16(cat8(A0[4][0], A0[4][1]), U0[2], acc[6]);
        A1[6][0] = trread(kb1 + 3080);
        SCHEDB();
        acc[7] = MFMA16(cat8(A0[4][0], A0[4][1]), U0[3], acc[7]);
        A1[6][1] = trread(kb1 + 3072);
        SCHEDB();
        acc[5] = MFMA16(cat8(A0[5][0], A0[5][1]), U0[0], acc[5]);
        A1[7][0] = trread(kb1 + 3592);
        SCHEDB();
        acc[6] = MFMA16(cat8(A0[5][0], A0[5][1]), U0[1], acc[6]);
        A1[7][1] = trread(kb1 + 3584);
        SCHEDB();
        acc[7] = MFMA16(cat8(A0[5][0], A0[5][1]), U0[2], acc[7]);
        SCHEDB();
        acc[6] = MFMA16(cat8(A0[6][0], A0[6][1]), U0[0], acc[6]);
        SCHEDB();
        acc[7] = MFMA16(cat8(A0[6][0], A0[6][1]), U0[1], acc[7]);
        SCHEDB();
        acc[7] = MFMA16(cat8(A0[7][0], A0[7][1]), U0[0], acc[7]);
        SCHEDB();
        acc[0] = MFMA16(cat8(A1[0][0], A1[0][1]), U1[0], acc[0]); SCHEDB();
        acc[1] = MFMA16(cat8(A1[0][0], A1[0][1]), U1[1], acc[1]); SCHEDB();
        acc[2] = MFMA16(cat8(A1[0][0], A1[0][1]), U1[2], acc[2]); SCHEDB();
        acc[3] = MFMA16(cat8(A1[0][0], A1[0][1]), U1[3], acc[3]); SCHEDB();
        acc[4] = MFMA16(cat8(A1[0][0], A1[0][1]), U1[4], acc[4]); SCHEDB();
        acc[5] = MFMA16(cat8(A1[0][0], A1[0][1]), U1[5], acc[5]); SCHEDB();
        acc[6] = MFMA16(cat8(A1[0][0], A1[0][1]), U1[6], acc[6]); SCHEDB();
        acc[7] = MFMA16(cat8(A1[0][0], A1[0][1]), U1[7], acc[7]); SCHEDB();
        acc[1] = MFMA16(cat8(A1[1][0], A1[1][1]), U1[0], acc[1]); SCHEDB();
        acc[2] = MFMA16(cat8(A1[1][0], A1[1][1]), U1[1], acc[2]); SCHEDB();
        acc[3] = MFMA16(cat8(A1[1][0], A1[1][1]), U1[2], acc[3]); SCHEDB();
        acc[4] = MFMA16(cat8(A1[1][0], A1[1][1]), U1[3], acc[4]); SCHEDB();
        acc[5] = MFMA16(cat8(A1[1][0], A1[1][1]), U1[4], acc[5]); SCHEDB();
        acc[6] = MFMA16(cat8(A1[1][0], A1[1][1]), U1[5], acc[6]); SCHEDB();
        acc[7] = MFMA16(cat8(A1[1][0], A1[1][1]), U1[6], acc[7]); SCHEDB();
        acc[2] = MFMA16(cat8(A1[2][0], A1[2][1]), U1[0], acc[2]); SCHEDB();
        acc[3] = MFMA16(cat8(A1[2][0], A1[2][1]), U1[1], acc[3]); SCHEDB();
        acc[4] = MFMA16(cat8(A1[2][0], A1[2][1]), U1[2], acc[4]); SCHEDB();
        acc[5] = MFMA16(cat8(A1[2][0], A1[2][1]), U1[3], acc[5]); SCHEDB();
        acc[6] = MFMA16(cat8(A1[2][0], A1[2][1]), U1[4], acc[6]); SCHEDB();
        acc[7] = MFMA16(cat8(A1[2][0], A1[2][1]), U1[5], acc[7]); SCHEDB();
        acc[3] = MFMA16(cat8(A1[3][0], A1[3][1]), U1[0], acc[3]); SCHEDB();
        acc[4] = MFMA16(cat8(A1[3][0], A1[3][1]), U1[1], acc[4]); SCHEDB();
        acc[5] = MFMA16(cat8(A1[3][0], A1[3][1]), U1[2], acc[5]); SCHEDB();
        acc[6] = MFMA16(cat8(A1[3][0], A1[3][1]), U1[3], acc[6]); SCHEDB();
        acc[7] = MFMA16(cat8(A1[3][0], A1[3][1]), U1[4], acc[7]); SCHEDB();
        acc[4] = MFMA16(cat8(A1[4][0], A1[4][1]), U1[0], acc[4]); SCHEDB();
        acc[5] = MFMA16(cat8(A1[4][0], A1[4][1]), U1[1], acc[5]); SCHEDB();
        acc[6] = MFMA16(cat8(A1[4][0], A1[4][1]), U1[2], acc[6]); SCHEDB();
        acc[7] = MFMA16(cat8(A1[4][0], A1[4][1]), U1[3], acc[7]); SCHEDB();
        acc[5] = MFMA16(cat8(A1[5][0], A1[5][1]), U1[0], acc[5]); SCHEDB();
        acc[6] = MFMA16(cat8(A1[5][0], A1[5][1]), U1[1], acc[6]); SCHEDB();
        acc[7] = MFMA16(cat8(A1[5][0], A1[5][1]), U1[2], acc[7]); SCHEDB();
        acc[6] = MFMA16(cat8(A1[6][0], A1[6][1]), U1[0], acc[6]); SCHEDB();
        acc[7] = MFMA16(cat8(A1[6][0], A1[6][1]), U1[1], acc[7]); SCHEDB();
        acc[7] = MFMA16(cat8(A1[7][0], A1[7][1]), U1[0], acc[7]); SCHEDB();
    }
    STAMP(ws, 0, 5);
    lds_sync(smem, sb, 8, lane);
    {
        char* rb = smem + sB + w * 6144 + lane * 16;
        int slot = 0;
#pragma unroll
        for (int tau = 0; tau < 8; ++tau) { if ((tau >> 1) != w) { *(f32x4*)(rb + slot * 1024) = acc[tau]; ++slot; } }
    }
    lds_sync(smem, sb, 12, lane);
    {
#pragma unroll
        for (int i = 0; i < 2; ++i) {
            f32x4 y;
            if (w == 0) y = acc[i]; else if (w == 1) y = acc[2 + i]; else if (w == 2) y = acc[4 + i]; else y = acc[6 + i];
            const int tau = 2 * w + i;
#pragma unroll
            for (int ow = 0; ow < 4; ++ow) {
                if (ow != w) {
                    const int sl = (tau < 2 * ow) ? tau : tau - 2;
                    y += *(const f32x4*)(smem + sB + ow * 6144 + sl * 1024 + lane * 16);
                }
            }
            const int l0 = 256 * tau + 16 * al + 4 * g;
            const u32x2 uv = *(const u32x2*)(smem + sU + 480 + 2 * l0), qv = *(const u32x2*)(smem + sU + 4608 + 2 * l0);
            float hv[4];
#pragma unroll
            for (int r = 0; r < 4; ++r) {
                const u32 uu = uv[r >> 1], qq = qv[r >> 1];
                const float u = (r & 1) ? bfhi(uu) : bflo(uu), q0 = (r & 1) ? bfhi(qq) : bflo(qq);
                const u32 yk = cvtpk(y[r], u * dsk);
                hv[r] = q0 + rbf(bflo(yk) + bfhi(yk));
            }
            st8<AUX_H>(wsr, WS_HC + (d * L_ + l0) * 2, u32x2{cvtpk(hv[0], hv[1]), cvtpk(hv[2], hv[3])});
        }
    }
    STAMP(ws, 0, 6);
    if (b == 0 && wv == 0) {
        for (int spin = 0; spin < 100000; ++spin) {
            asm volatile("" ::: "memory");
            const u32x4 a = __builtin_amdgcn_raw_buffer_load_b128(wsr, WS_AR + lane * 16, 0, 16);
            if (__all(a[0] == tag && a[1] == tag && a[2] == tag && a[3] == tag)) break;
            __builtin_amdgcn_s_sleep(8);
        }
        if (lane == 0) __hip_atomic_store((GAS u32*)(ws + WS_EP), tag, __ATOMIC_RELAXED, __HIP_MEMORY_SCOPE_AGENT);
    }
}

#define G1_UNROLL 1
constexpr int G1_STAGE = 40960, G1_NS = 4, G1_NST = 10;
constexpr int G1_LDS = G1_NS * G1_STAGE;

DI void glds16(const void* g, u32 lds_base) {
    u32 sv; asm volatile("s_mov_b32 %0, m0\n\ts_mov_b32 m0, %2\n\ts_nop 0\n\tglobal_load_lds_dwordx4 %1, off\n\ts_mov_b32 m0, %0" : "=&s"(sv) : "v"(g), "s"(lds_base) : "memory");
}

__global__ __launch_bounds__(768, 3) void k3_gemm1(char* __restrict__ ws) {
    __shared__ __attribute__((aligned(1024))) char smem[G1_LDS];
    const int b = blockIdx.x, t = threadIdx.x, lane = t & 63;
    const int w = __builtin_amdgcn_readfirstlane(t >> 6);
    const int xcd = b & 7, j = b >> 3;
    const int P = (xcd & 1) * 8 + (j & 7), S = (xcd >> 1) * 4 + (j >> 3);
    const u32 lds0 = (u32)(uintptr_t)smem;
    const rsrc_t wsr = mkrsrc(ws, WS_END);
    if (w >= 4) {
        const int lw = w - 4;
        const char* wsrc = ws + WS_W1T + S * 10 * 24576 + lw * 1024 + lane * 16;
        const int hk = 16 * ((lw >> 2) & 1) + 4 * ((lw >> 1) & 1) + 8 * ((lane >> 3) & 1) + ((lane >> 1) & 3);
        const char* hsrc = ws + WS_HC + (hk * L_ + 128 * P + 64 * (lw & 1) + 16 * (lane >> 4) + 8 * (lane & 1)) * 2;
        auto issue = [&](int st, int slot) {
            const u32 base = lds0 + slot * G1_STAGE;
#pragma unroll
            for (int i = 0; i < 2; ++i) glds16(hsrc + (st * 64 + 32 * i) * L_ * 2, base + 24576 + (lw + 8 * i) * 1024);
#pragma unroll
            for (int i = 0; i < 3; ++i) glds16(wsrc + st * 24576 + i * 8192, base + lw * 1024 + i * 8192);
        };
#define ISSUE(a, b) issue(a, b)
        issue(0, 0); issue(1, 1);
        waitv<5>(); BAR();
        issue(2, 2);
        waitv<5>(); BAR();
        ISSUE(3, 3);
        waitv<5>(); BAR();
        ISSUE(4, 0);
        waitv<5>(); BAR();
        ISSUE(5, 1);
        waitv<5>(); BAR();
        ISSUE(6, 2);
        waitv<5>(); BAR();
        ISSUE(7, 3);
        waitv<5>(); BAR();
        ISSUE(8, 0);
        waitv<5>(); BAR();
        ISSUE(9, 1);
        waitv<5>(); BAR();
        waitv<0>(); BAR();
        BAR();
        BAR();
        return;
    }
    STAMP(ws, 4, 0);
    const int wm = w & 1, wn = w >> 1;
    const int g = lane >> 4, q = (lane & 15) >> 2, p = lane & 3;
    f32x4 acc[6][4];
#pragma unroll
    for (int a = 0; a < 6; ++a)
#pragma unroll
        for (int c = 0; c < 4; ++c) acc[a][c] = f32x4{0.f, 0.f, 0.f, 0.f};
    const LAS char* abase = (const LAS char*)smem + wm * 6144 + lane * 16;
    const LAS char* bbase = (const LAS char*)smem + 24576 + (g >> 1) * 4096 + (g & 1) * 128 + q * 32 + 8 * p + wn * 1024;
    const LAS char* sbase = (const LAS char*)smem + 24576 + (2 * w + (lane >> 5)) * 2048 + (S >> 1) * 256 + ((lane >> 2) & 7) * 32 + (S & 1) * 16 + (lane & 3) * 4;
    float s1a = 0.f, s1b = 0.f, s2a = 0.f, s2b = 0.f;
    bf16x8 AL[3], AH[3]; s16x4 B0[4][2], B1[4][2];
    u32 sv;
    BAR();
    STAMP(ws, 4, 1);
    AL[0] = *(const LAS bf16x8*)(abase + 0); B0[0][0] = trread(bbase + 0); B0[0][1] = trread(bbase + 2048); B0[1][0] = trread(bbase + 256); B0[1][1] = trread(bbase + 2304); AL[1] = *(const LAS bf16x8*)(abase + 1024); B0[2][0] = trread(bbase + 512); B0[2][1] = trread(bbase + 2560); B0[3][0] = trread(bbase + 768); B0[3][1] = trread(bbase + 2816); AL[2] = *(const LAS bf16x8*)(abase + 2048);
    SCHEDB();
#pragma unroll 1
    for (int st = 0; st < G1_NST; ++st) {
        acc[0+0][0] = MFMA16(AL[0], cat8(B0[0][0], B0[0][1]), acc[0+0][0]);
        AH[0] = *(const LAS bf16x8*)(abase + 3072);
        SCHEDB();
        acc[0+0][1] = MFMA16(AL[0], cat8(B0[1][0], B0[1][1]), acc[0+0][1]);
        AH[1] = *(const LAS bf16x8*)(abase + 4096);
        SCHEDB();
        acc[0+1][0] = MFMA16(AL[1], cat8(B0[0][0], B0[0][1]), acc[0+1][0]);
        B1[0][0] = trread(bbase + 8192);
        SCHEDB();
        acc[0+1][1] = MFMA16(AL[1], cat8(B0[1][0], B0[1][1]), acc[0+1][1]);
        B1[0][1] = trread(bbase + 10240);
        SCHEDB();
        acc[0+0][2] = MFMA16(AL[0], cat8(B0[2][0], B0[2][1]), acc[0+0][2]);
        B1[1][0] = trread(bbase + 8448);
        SCHEDB();
        acc[0+1][2] = MFMA16(AL[1], cat8(B0[2][0], B0[2][1]), acc[0+1][2]);
        B1[1][1] = trread(bbase + 10496);
        SCHEDB();
        acc[0+0][3] = MFMA16(AL[0], cat8(B0[3][0], B0[3][1]), acc[0+0][3]);
        AH[2] = *(const LAS bf16x8*)(abase + 5120);
        SCHEDB();
        acc[0+1][3] = MFMA16(AL[1], cat8(B0[3][0], B0[3][1]), acc[0+1][3]);
        SCHEDB();
        acc[0+2][0] = MFMA16(AL[2], cat8(B0[0][0], B0[0][1]), acc[0+2][0]);
        SCHEDB();
        acc[0+2][1] = MFMA16(AL[2], cat8(B0[1][0], B0[1][1]), acc[0+2][1]);
        SCHEDB();
        acc[0+2][2] = MFMA16(AL[2], cat8(B0[2][0], B0[2][1]), acc[0+2][2]);
        SCHEDB();
        acc[0+2][3] = MFMA16(AL[2], cat8(B0[3][0], B0[3][1]), acc[0+2][3]);
        SCHEDB();
        acc[3+0][0] = MFMA16(AH[0], cat8(B0[0][0], B0[0][1]), acc[3+0][0]);
        AL[0] = *(const LAS bf16x8*)(abase + 12288);
        SCHEDB();
        acc[3+0][1] = MFMA16(AH[0], cat8(B0[1][0], B0[1][1]), acc[3+0][1]);
        AL[1] = *(const LAS bf16x8*)(abase + 13312);
        SCHEDB();
        acc[3+1][0] = MFMA16(AH[1], cat8(B0[0][0], B0[0][1]), acc[3+1][0]);
        sv = *(const LAS u32*)(sbase);
        SCHEDB();
        acc[3+1][1] = MFMA16(AH[1], cat8(B0[1][0], B0[1][1]), acc[3+1][1]);
        B1[2][0] = trread(bbase + 8704);
        SCHEDB();
        acc[3+0][2] = MFMA16(AH[0], cat8(B0[2][0], B0[2][1]), acc[3+0][2]);
        B1[2][1] = trread(bbase + 10752);
        SCHEDB();
        acc[3+1][2] = MFMA16(AH[1], cat8(B0[2][0], B0[2][1]), acc[3+1][2]);
        B1[3][0] = trread(bbase + 8960);
        SCHEDB();
        acc[3+0][3] = MFMA16(AH[0], cat8(B0[3][0], B0[3][1]), acc[3+0][3]);
        B1[3][1] = trread(bbase + 11008);
        SCHEDB();
        acc[3+1][3] = MFMA16(AH[1], cat8(B0[3][0], B0[3][1]), acc[3+1][3]);
        AL[2] = *(const LAS bf16x8*)(abase + 14336);
        SCHEDB();
        acc[3+2][0] = MFMA16(AH[2], cat8(B0[0][0], B0[0][1]), acc[3+2][0]);
        SCHEDB();
        acc[3+2][1] = MFMA16(AH[2], cat8(B0[1][0], B0[1][1]), acc[3+2][1]);
        SCHEDB();
        acc[3+2][2] = MFMA16(AH[2], cat8(B0[2][0], B0[2][1]), acc[3+2][2]);
        SCHEDB();
        acc[3+2][3] = MFMA16(AH[2], cat8(B0[3][0], B0[3][1]), acc[3+2][3]);
        SCHEDB();
        acc[0+0][0] = MFMA16(AL[0], cat8(B1[0][0], B1[0][1]), acc[0+0][0]);
        AH[0] = *(const LAS bf16x8*)(abase + 15360);
        SCHEDB();
        acc[0+0][1] = MFMA16(AL[0], cat8(B1[1][0], B1[1][1]), acc[0+0][1]);
        AH[1] = *(const LAS bf16x8*)(abase + 16384);
        SCHEDB();
        acc[0+1][0] = MFMA16(AL[1], cat8(B1[0][0], B1[0][1]), acc[0+1][0]);
        AH[2] = *(const LAS bf16x8*)(abase + 17408);
        SCHEDB();
        acc[0+1][1] = MFMA16(AL[1], cat8(B1[1][0], B1[1][1]), acc[0+1][1]);
        SCHEDB();
        acc[0+0][2] = MFMA16(AL[0], cat8(B1[2][0], B1[2][1]), acc[0+0][2]);
        SCHEDB();
        acc[0+1][2] = MFMA16(AL[1], cat8(B1[2][0], B1[2][1]), acc[0+1][2]);
        SCHEDB();
        acc[0+0][3] = MFMA16(AL[0], cat8(B1[3][0], B1[3][1]), acc[0+0][3]);
        SCHEDB();
        acc[0+1][3] = MFMA16(AL[1], cat8(B1[3][0], B1[3][1]), acc[0+1][3]);
        SCHEDB();
        acc[0+2][0] = MFMA16(AL[2], cat8(B1[0][0], B1[0][1]), acc[0+2][0]);
        SCHEDB();
        acc[0+2][1] = MFMA16(AL[2], cat8(B1[1][0], B1[1][1]), acc[0+2][1]);
        SCHEDB();
        acc[0+2][2] = MFMA16(AL[2], cat8(B1[2][0], B1[2][1]), acc[0+2][2]);
        SCHEDB();
        acc[0+2][3] = MFMA16(AL[2], cat8(B1[3][0], B1[3][1]), acc[0+2][3]);
        SCHEDB();
        { const float a = bflo(sv), c = bfhi(sv); s1a += a; s1b += c; s2a = __builtin_fmaf(a, a, s2a); s2b = __builtin_fmaf(c, c, s2b); }
        WAITL0();
        BAR();
        { const int dlt = (st & 3) == 3 ? -3 * G1_STAGE : G1_STAGE; abase += dlt; bbase += dlt; sbase += dlt; }
        acc[3+0][0] = MFMA16(AH[0], cat8(B1[0][0], B1[0][1]), acc[3+0][0]);
        AL[0] = *(const LAS bf16x8*)(abase + 0);
        SCHEDB();
        acc[3+0][1] = MFMA16(AH[0], cat8(B1[1][0], B1[1][1]), acc[3+0][1]);
        B0[0][0] = trread(bbase + 0);
        SCHEDB();
        acc[3+1][0] = MFMA16(AH[1], cat8(B1[0][0], B1[0][1]), acc[3+1][0]);
        B0[0][1] = trread(bbase + 2048);
        SCHEDB();
        acc[3+1][1] = MFMA16(AH[1], cat8(B1[1][0], B1[1][1]), acc[3+1][1]);
        B0[1][0] = trread(bbase + 256);
        SCHEDB();
        acc[3+0][2] = MFMA16(AH[0], cat8(B1[2][0], B1[2][1]), acc[3+0][2]);
        B0[1][1] = trread(bbase + 2304);
        SCHEDB();
        acc[3+1][2] = MFMA16(AH[1], cat8(B1[2][0], B1[2][1]), acc[3+1][2]);
        AL[1] = *(const LAS bf16x8*)(abase + 1024);
        SCHEDB();
        acc[3+0][3] = MFMA16(AH[0], cat8(B1[3][0], B1[3][1]), acc[3+0][3]);
        B0[2][0] = trread(bbase + 512);
        SCHEDB();
        acc[3+1][3] = MFMA16(AH[1], cat8(B1[3][0], B1[3][1]), acc[3+1][3]);
        B0[2][1] = trread(bbase + 2560);
        SCHEDB();
        acc[3+2][0] = MFMA16(AH[2], cat8(B1[0][0], B1[0][1]), acc[3+2][0]);
        B0[3][0] = trread(bbase + 768);
        SCHEDB();
        acc[3+2][1] = MFMA16(AH[2], cat8(B1[1][0], B1[1][1]), acc[3+2][1]);
        B0[3][1] = trread(bbase + 2816);
        SCHEDB();
        acc[3+2][2] = MFMA16(AH[2], cat8(B1[2][0], B1[2][1]), acc[3+2][2]);
        AL[2] = *(const LAS bf16x8*)(abase + 2048);
        SCHEDB();
        acc[3+2][3] = MFMA16(AH[2], cat8(B1[3][0], B1[3][1]), acc[3+2][3]);
        SCHEDB();
    }
    STAMP(ws, 4, 3);
    {
        float v[4] = {s1a, s1b, s2a, s2b};
#pragma unroll
        for (int i = 0; i < 4; ++i) { v[i] = dppadd<0x114>(v[i]); v[i] = dppadd<0x118>(v[i]); v[i] = swap32add(swap16add(v[i])); }
        if (lane >= 12 && lane < 16) *(f32x4*)(smem + (w * 4 + (lane & 3)) * 16) = f32x4{v[0], v[1], v[2], v[3]};
        WAITL0();
    }
    BAR();
    if (w == 0 && lane < 8) {
        float s1 = 0.f, s2 = 0.f;
#pragma unroll
        for (int ow = 0; ow < 4; ++ow) { const f32x4 v = *(const f32x4*)(smem + (ow * 4 + (lane >> 1)) * 16); s1 += (lane & 1) ? v[1] : v[0]; s2 += (lane & 1) ? v[3] : v[2]; }
        const float mean = s1 * (1.f / 640.f);
        float var = s2 * (1.f / 640.f) - mean * mean;
        var = var > 0.f ? var : 0.f;
        const float rs = 1.f / rbf(__builtin_sqrtf(rbf(rbf(var) + 1e-5f)));
        st4(wsr, WS_RS + (128 * P + 8 * S + lane) * 4, __float_as_uint(rs * rs));
    }
    const rsrc_t rs = wsr;
#pragma unroll
    for (int a = 0; a < 3; ++a) {
        const int f0 = 96 * S + 16 * (3 * wm + a);
        const int kb = f0 >> 5, oct = ((f0 & 31) >> 3) + (g >> 1);
#pragma unroll
        for (int c = 0; c < 4; c += 2) {
            u32 X[2], Y[2];
#pragma unroll
            for (int e = 0; e < 2; ++e) {
                X[e] = cvtpk(__builtin_fmaxf(acc[2 * a][c][2 * e], 0.f) * acc[2 * a + 1][c][2 * e], __builtin_fmaxf(acc[2 * a][c][2 * e + 1], 0.f) * acc[2 * a + 1][c][2 * e + 1]);
                Y[e] = cvtpk(__builtin_fmaxf(acc[2 * a][c + 1][2 * e], 0.f) * acc[2 * a + 1][c + 1][2 * e], __builtin_fmaxf(acc[2 * a][c + 1][2 * e + 1], 0.f) * acc[2 * a + 1][c + 1][2 * e + 1]);
            }
#pragma unroll
            for (int e = 0; e < 2; ++e) { auto r = __builtin_amdgcn_permlane16_swap(X[e], Y[e], false, false); X[e] = r[0]; Y[e] = r[1]; }
            const int lt = 8 * P + 4 * wn + c + (g & 1);
            st16<AUX_HM>(rs, WS_HM + ((lt * 48 + kb) * 64 + (lane & 15) + 16 * oct) * 16, u32x4{X[0], X[1], Y[0], Y[1]});
        }
    }
    STAMP(ws, 4, 4);
}

constexpr int G2_STAGE = 18432, G2_NS = 8, G2_NST = 24;
constexpr int G2_RES = G2_NS * G2_STAGE;
constexpr int G2_LDS = G2_RES + 10240;

__global__ __launch_bounds__(768, 3) void k4_gemm2(char* __restrict__ ws, u16* __restrict__ out) {
    __shared__ __attribute__((aligned(1024))) char smem[G2_LDS];
    const int b = blockIdx.x, t = threadIdx.x, lane = t & 63;
    const int w = __builtin_amdgcn_readfirstlane(t >> 6);
    const int xcd = b & 7, j = b >> 3;
    const int P = (xcd & 3) * 8 + (j & 7), S = (xcd >> 2) * 4 + (j >> 3);
    const u32 lds0 = (u32)(uintptr_t)smem;
    if (w >= 4) {
        const int lw = w - 4;
        const char* wsrc = ws + WS_W2T + S * 24 * 10240 + lw * 1024 + lane * 16;
        const char* msrc = ws + WS_HM + ((4 * P + (lw >> 1)) * 48 + (lw & 1)) * 1024 + lane * 16;
        auto issue = [&](int st, int slot) {
            const u32 base = lds0 + slot * G2_STAGE;
            glds16(msrc + st * 2048, base + 10240 + lw * 1024);
            glds16(wsrc + st * 10240, base + lw * 1024);
            if (lw < 2) glds16(wsrc + st * 10240 + 8192, base + 8192 + lw * 1024);
        };
        auto residual = [&]() {
            for (int pc = lw; pc < 10; pc += 8) {
                const int row = 8 * pc + (lane >> 3), ch = (lane & 7) ^ (((lane >> 3) & 3) << 1);
                glds16(ws + WS_HC + ((80 * S + row) * L_ + 64 * P + 8 * ch) * 2, lds0 + G2_RES + pc * 1024);
            }
        };
        if (lw < 2) {
            issue(0, 0); issue(1, 1); issue(2, 2);
            waitv<6>(); BAR();
            waitv<3>(); BAR();
            issue(3, 3);
            issue(4, 4);
            waitv<6>(); BAR();
            issue(5, 5);
            issue(6, 6);
            waitv<9>(); BAR();
            issue(7, 7);
            issue(8, 0);
            waitv<12>(); BAR();
            issue(9, 1);
            issue(10, 2);
            waitv<15>(); BAR();
            issue(11, 3);
            waitv<15>(); BAR();
            issue(12, 4);
            waitv<15>(); BAR();
            issue(13, 5);
            waitv<15>(); BAR();
            issue(14, 6);
            waitv<15>(); BAR();
            issue(15, 7);
            waitv<15>(); BAR();
            issue(16, 0);
            waitv<15>(); BAR();
            issue(17, 1);
            waitv<15>(); BAR();
            issue(18, 2);
            waitv<15>(); BAR();
            issue(19, 3);
            waitv<15>(); BAR();
            issue(20, 4);
            waitv<15>(); BAR();
            issue(21, 5);
            waitv<15>(); BAR();
            issue(22, 6);
            waitv<15>(); BAR();
            issue(23, 7);
            waitv<15>(); BAR();
            residual();
            waitv<12>(); BAR();
            waitv<9>(); BAR();
            waitv<6>(); BAR();
            waitv<3>(); BAR();
            waitv<0>(); BAR();
        } else {
            issue(0, 0); issue(1, 1); issue(2, 2);
            waitv<4>(); BAR();
            waitv<2>(); BAR();
            issue(3, 3);
            issue(4, 4);
            waitv<4>(); BAR();
            issue(5, 5);
            issue(6, 6);
            waitv<6>(); BAR();
            issue(7, 7);
            issue(8, 0);
            waitv<8>(); BAR();
            issue(9, 1);
            issue(10, 2);
            waitv<10>(); BAR();
            issue(11, 3);
            waitv<10>(); BAR();
            issue(12, 4);
            waitv<10>(); BAR();
            issue(13, 5);
            waitv<10>(); BAR();
            issue(14, 6);
            waitv<10>(); BAR();
            issue(15, 7);
            waitv<10>(); BAR();
            issue(16, 0);
            waitv<10>(); BAR();
            issue(17, 1);
            waitv<10>(); BAR();
            issue(18, 2);
            waitv<10>(); BAR();
            issue(19, 3);
            waitv<10>(); BAR();
            issue(20, 4);
            waitv<10>(); BAR();
            issue(21, 5);
            waitv<10>(); BAR();
            issue(22, 6);
            waitv<10>(); BAR();
            issue(23, 7);
            waitv<10>(); BAR();
            residual();
            waitv<8>(); BAR();
            waitv<6>(); BAR();
            waitv<4>(); BAR();
            waitv<2>(); BAR();
            waitv<0>(); BAR();
        }
        return;
    }
    STAMP(ws, 5, 0);
    const int g = lane >> 4, q = (lane & 15) >> 2, p = lane & 3;
    const int l = 64 * P + 16 * w + (lane & 15);
    const float rs2 = *((const float*)(ws + WS_RS) + l);
    const float m1 = *((const float*)(ws + WS_ST) + l);
    f32x4 acc[5];
#pragma unroll
    for (int a = 0; a < 5; ++a) acc[a] = f32x4{0.f, 0.f, 0.f, 0.f};
    const LAS char* abase = (const LAS char*)smem + lane * 16;
    const LAS char* bbase = (const LAS char*)smem + 10240 + w * 2048 + lane * 16;
    bf16x8 EA[2][5], EB[2], OA[2][5], OB[2];
    BAR();
    STAMP(ws, 5, 1);
    EB[0] = *(const LAS bf16x8*)(bbase); EB[1] = *(const LAS bf16x8*)(bbase + 1024);
#pragma unroll
    for (int a = 0; a < 5; ++a) { EA[0][a] = *(const LAS bf16x8*)(abase + a * 1024); EA[1][a] = *(const LAS bf16x8*)(abase + 5120 + a * 1024); }
    SCHEDB();
#pragma unroll 1
    for (int it = 0; it < 3; ++it) {
        if (it == 1) STAMP(ws, 5, 2); if (it == 2) STAMP(ws, 5, 3);
        BAR();
        acc[0] = MFMA16(EA[0][0], EB[0], acc[0]);
        OB[0] = *(const LAS bf16x8*)(bbase + 1*G2_STAGE);
        OA[0][0] = *(const LAS bf16x8*)(abase + 1*G2_STAGE + 0);
        SCHEDB();
        acc[1] = MFMA16(EA[0][1], EB[0], acc[1]);
        OA[0][1] = *(const LAS bf16x8*)(abase + 1*G2_STAGE + 1024);
        OA[0][2] = *(const LAS bf16x8*)(abase + 1*G2_STAGE + 2048);
        SCHEDB();
        acc[2] = MFMA16(EA[0][2], EB[0], acc[2]);
        OA[0][3] = *(const LAS bf16x8*)(abase + 1*G2_STAGE + 3072);
        SCHEDB();
        acc[3] = MFMA16(EA[0][3], EB[0], acc[3]);
        OA[0][4] = *(const LAS bf16x8*)(abase + 1*G2_STAGE + 4096);
        SCHEDB();
        acc[4] = MFMA16(EA[0][4], EB[0], acc[4]);
        OB[1] = *(const LAS bf16x8*)(bbase + 1*G2_STAGE + 1024);
        SCHEDB();
        acc[0] = MFMA16(EA[1][0], EB[1], acc[0]);
        OA[1][0] = *(const LAS bf16x8*)(abase + 1*G2_STAGE + 5120);
        SCHEDB();
        acc[1] = MFMA16(EA[1][1], EB[1], acc[1]);
        OA[1][1] = *(const LAS bf16x8*)(abase + 1*G2_STAGE + 6144);
        SCHEDB();
        acc[2] = MFMA16(EA[1][2], EB[1], acc[2]);
        OA[1][2] = *(const LAS bf16x8*)(abase + 1*G2_STAGE + 7168);
        SCHEDB();
        acc[3] = MFMA16(EA[1][3], EB[1], acc[3]);
        OA[1][3] = *(const LAS bf16x8*)(abase + 1*G2_STAGE + 8192);
        SCHEDB();
        acc[4] = MFMA16(EA[1][4], EB[1], acc[4]);
        OA[1][4] = *(const LAS bf16x8*)(abase + 1*G2_STAGE + 9216);
        SCHEDB();
        BAR();
        acc[0] = MFMA16(OA[0][0], OB[0], acc[0]);
        EB[0] = *(const LAS bf16x8*)(bbase + 2*G2_STAGE);
        EA[0][0] = *(const LAS bf16x8*)(abase + 2*G2_STAGE + 0);
        SCHEDB();
        acc[1] = MFMA16(OA[0][1], OB[0], acc[1]);
        EA[0][1] = *(const LAS bf16x8*)(abase + 2*G2_STAGE + 1024);
        EA[0][2] = *(const LAS bf16x8*)(abase + 2*G2_STAGE + 2048);
        SCHEDB();
        acc[2] = MFMA16(OA[0][2], OB[0], acc[2]);
        EA[0][3] = *(const LAS bf16x8*)(abase + 2*G2_STAGE + 3072);
        SCHEDB();
        acc[3] = MFMA16(OA[0][3], OB[0], acc[3]);
        EA[0][4] = *(const LAS bf16x8*)(abase + 2*G2_STAGE + 4096);
        SCHEDB();
        acc[4] = MFMA16(OA[0][4], OB[0], acc[4]);
        EB[1] = *(const LAS bf16x8*)(bbase + 2*G2_STAGE + 1024);
        SCHEDB();
        acc[0] = MFMA16(OA[1][0], OB[1], acc[0]);
        EA[1][0] = *(const LAS bf16x8*)(abase + 2*G2_STAGE + 5120);
        SCHEDB();
        acc[1] = MFMA16(OA[1][1], OB[1], acc[1]);
        EA[1][1] = *(const LAS bf16x8*)(abase + 2*G2_STAGE + 6144);
        SCHEDB();
        acc[2] = MFMA16(OA[1][2], OB[1], acc[2]);
        EA[1][2] = *(const LAS bf16x8*)(abase + 2*G2_STAGE + 7168);
        SCHEDB();
        acc[3] = MFMA16(OA[1][3], OB[1], acc[3]);
        EA[1][3] = *(const LAS bf16x8*)(abase + 2*G2_STAGE + 8192);
        SCHEDB();
        acc[4] = MFMA16(OA[1][4], OB[1], acc[4]);
        EA[1][4] = *(const LAS bf16x8*)(abase + 2*G2_STAGE + 9216);
        SCHEDB();
        BAR();
        acc[0] = MFMA16(EA[0][0], EB[0], acc[0]);
        OB[0] = *(const LAS bf16x8*)(bbase + 3*G2_STAGE);
        OA[0][0] = *(const LAS bf16x8*)(abase + 3*G2_STAGE + 0);
        SCHEDB();
        acc[1] = MFMA16(EA[0][1], EB[0], acc[1]);
        OA[0][1] = *(const LAS bf16x8*)(abase + 3*G2_STAGE + 1024);
        OA[0][2] = *(const LAS bf16x8*)(abase + 3*G2_STAGE + 2048);
        SCHEDB();
        acc[2] = MFMA16(EA[0][2], EB[0], acc[2]);
        OA[0][3] = *(const LAS bf16x8*)(abase + 3*G2_STAGE + 3072);
        SCHEDB();
        acc[3] = MFMA16(EA[0][3], EB[0], acc[3]);
        OA[0][4] = *(const LAS bf16x8*)(abase + 3*G2_STAGE + 4096);
        SCHEDB();
        acc[4] = MFMA16(EA[0][4], EB[0], acc[4]);
        OB[1] = *(const LAS bf16x8*)(bbase + 3*G2_STAGE + 1024);
        SCHEDB();
        acc[0] = MFMA16(EA[1][0], EB[1], acc[0]);
        OA[1][0] = *(const LAS bf16x8*)(abase + 3*G2_STAGE + 5120);
        SCHEDB();
        acc[1] = MFMA16(EA[1][1], EB[1], acc[1]);
        OA[1][1] = *(const LAS bf16x8*)(abase + 3*G2_STAGE + 6144);
        SCHEDB();
        acc[2] = MFMA16(EA[1][2], EB[1], acc[2]);
        OA[1][2] = *(const LAS bf16x8*)(abase + 3*G2_STAGE + 7168);
        SCHEDB();
        acc[3] = MFMA16(EA[1][3], EB[1], acc[3]);
        OA[1][3] = *(const LAS bf16x8*)(abase + 3*G2_STAGE + 8192);
        SCHEDB();
        acc[4] = MFMA16(EA[1][4], EB[1], acc[4]);
        OA[1][4] = *(const LAS bf16x8*)(abase + 3*G2_STAGE + 9216);
        SCHEDB();
        BAR();
        acc[0] = MFMA16(OA[0][0], OB[0], acc[0]);
        EB[0] = *(const LAS bf16x8*)(bbase + 4*G2_STAGE);
        EA[0][0] = *(const LAS bf16x8*)(abase + 4*G2_STAGE + 0);
        SCHEDB();
        acc[1] = MFMA16(OA[0][1], OB[0], acc[1]);
        EA[0][1] = *(const LAS bf16x8*)(abase + 4*G2_STAGE + 1024);
        EA[0][2] = *(const LAS bf16x8*)(abase + 4*G2_STAGE + 2048);
        SCHEDB();
        acc[2] = MFMA16(OA[0][2], OB[0], acc[2]);
        EA[0][3] = *(const LAS bf16x8*)(abase + 4*G2_STAGE + 3072);
        SCHEDB();
        acc[3] = MFMA16(OA[0][3], OB[0], acc[3]);
        EA[0][4] = *(const LAS bf16x8*)(abase + 4*G2_STAGE + 4096);
        SCHEDB();
        acc[4] = MFMA16(OA[0][4], OB[0], acc[4]);
        EB[1] = *(const LAS bf16x8*)(bbase + 4*G2_STAGE + 1024);
        SCHEDB();
        acc[0] = MFMA16(OA[1][0], OB[1], acc[0]);
        EA[1][0] = *(const LAS bf16x8*)(abase + 4*G2_STAGE + 5120);
        SCHEDB();
        acc[1] = MFMA16(OA[1][1], OB[1], acc[1]);
        EA[1][1] = *(const LAS bf16x8*)(abase + 4*G2_STAGE + 6144);
        SCHEDB();
        acc[2] = MFMA16(OA[1][2], OB[1], acc[2]);
        EA[1][2] = *(const LAS bf16x8*)(abase + 4*G2_STAGE + 7168);
        SCHEDB();
        acc[3] = MFMA16(OA[1][3], OB[1], acc[3]);
        EA[1][3] = *(const LAS bf16x8*)(abase + 4*G2_STAGE + 8192);
        SCHEDB();
        acc[4] = MFMA16(OA[1][4], OB[1], acc[4]);
        EA[1][4] = *(const LAS bf16x8*)(abase + 4*G2_STAGE + 9216);
        SCHEDB();
        BAR();
        acc[0] = MFMA16(EA[0][0], EB[0], acc[0]);
        OB[0] = *(const LAS bf16x8*)(bbase + 5*G2_STAGE);
        OA[0][0] = *(const LAS bf16x8*)(abase + 5*G2_STAGE + 0);
        SCHEDB();
        acc[1] = MFMA16(EA[0][1], EB[0], acc[1]);
        OA[0][1] = *(const LAS bf16x8*)(abase + 5*G2_STAGE + 1024);
        OA[0][2] = *(const LAS bf16x8*)(abase + 5*G2_STAGE + 2048);
        SCHEDB();
        acc[2] = MFMA16(EA[0][2], EB[0], acc[2]);
        OA[0][3] = *(const LAS bf16x8*)(abase + 5*G2_STAGE + 3072);
        SCHEDB();
        acc[3] = MFMA16(EA[0][3], EB[0], acc[3]);
        OA[0][4] = *(const LAS bf16x8*)(abase + 5*G2_STAGE + 4096);
        SCHEDB();
        acc[4] = MFMA16(EA[0][4], EB[0], acc[4]);
        OB[1] = *(const LAS bf16x8*)(bbase + 5*G2_STAGE + 1024);
        SCHEDB();
        acc[0] = MFMA16(EA[1][0], EB[1], acc[0]);
        OA[1][0] = *(const LAS bf16x8*)(abase + 5*G2_STAGE + 5120);
        SCHEDB();
        acc[1] = MFMA16(EA[1][1], EB[1], acc[1]);
        OA[1][1] = *(const LAS bf16x8*)(abase + 5*G2_STAGE + 6144);
        SCHEDB();
        acc[2] = MFMA16(EA[1][2], EB[1], acc[2]);
        OA[1][2] = *(const LAS bf16x8*)(abase + 5*G2_STAGE + 7168);
        SCHEDB();
        acc[3] = MFMA16(EA[1][3], EB[1], acc[3]);
        OA[1][3] = *(const LAS bf16x8*)(abase + 5*G2_STAGE + 8192);
        SCHEDB();
        acc[4] = MFMA16(EA[1][4], EB[1], acc[4]);
        OA[1][4] = *(const LAS bf16x8*)(abase + 5*G2_STAGE + 9216);
        SCHEDB();
        BAR();
        acc[0] = MFMA16(OA[0][0], OB[0], acc[0]);
        EB[0] = *(const LAS bf16x8*)(bbase + 6*G2_STAGE);
        EA[0][0] = *(const LAS bf16x8*)(abase + 6*G2_STAGE + 0);
        SCHEDB();
        acc[1] = MFMA16(OA[0][1], OB[0], acc[1]);
        EA[0][1] = *(const LAS bf16x8*)(abase + 6*G2_STAGE + 1024);
        EA[0][2] = *(const LAS bf16x8*)(abase + 6*G2_STAGE + 2048);
        SCHEDB();
        acc[2] = MFMA16(OA[0][2], OB[0], acc[2]);
        EA[0][3] = *(const LAS bf16x8*)(abase + 6*G2_STAGE + 3072);
        SCHEDB();
        acc[3] = MFMA16(OA[0][3], OB[0], acc[3]);
        EA[0][4] = *(const LAS bf16x8*)(abase + 6*G2_STAGE + 4096);
        SCHEDB();
        acc[4] = MFMA16(OA[0][4], OB[0], acc[4]);
        EB[1] = *(const LAS bf16x8*)(bbase + 6*G2_STAGE + 1024);
        SCHEDB();
        acc[0] = MFMA16(OA[1][0], OB[1], acc[0]);
        EA[1][0] = *(const LAS bf16x8*)(abase + 6*G2_STAGE + 5120);
        SCHEDB();
        acc[1] = MFMA16(OA[1][1], OB[1], acc[1]);
        EA[1][1] = *(const LAS bf16x8*)(abase + 6*G2_STAGE + 6144);
        SCHEDB();
        acc[2] = MFMA16(OA[1][2], OB[1], acc[2]);
        EA[1][2] = *(const LAS bf16x8*)(abase + 6*G2_STAGE + 7168);
        SCHEDB();
        acc[3] = MFMA16(OA[1][3], OB[1], acc[3]);
        EA[1][3] = *(const LAS bf16x8*)(abase + 6*G2_STAGE + 8192);
        SCHEDB();
        acc[4] = MFMA16(OA[1][4], OB[1], acc[4]);
        EA[1][4] = *(const LAS bf16x8*)(abase + 6*G2_STAGE + 9216);
        SCHEDB();
        BAR();
        acc[0] = MFMA16(EA[0][0], EB[0], acc[0]);
        OB[0] = *(const LAS bf16x8*)(bbase + 7*G2_STAGE);
        OA[0][0] = *(const LAS bf16x8*)(abase + 7*G2_STAGE + 0);
        SCHEDB();
        acc[1] = MFMA16(EA[0][1], EB[0], acc[1]);
        OA[0][1] = *(const LAS bf16x8*)(abase + 7*G2_STAGE + 1024);
        OA[0][2] = *(const LAS bf16x8*)(abase + 7*G2_STAGE + 2048);
        SCHEDB();
        acc[2] = MFMA16(EA[0][2], EB[0], acc[2]);
        OA[0][3] = *(const LAS bf16x8*)(abase + 7*G2_STAGE + 3072);
        SCHEDB();
        acc[3] = MFMA16(EA[0][3], EB[0], acc[3]);
        OA[0][4] = *(const LAS bf16x8*)(abase + 7*G2_STAGE + 4096);
        SCHEDB();
        acc[4] = MFMA16(EA[0][4], EB[0], acc[4]);
        OB[1] = *(const LAS bf16x8*)(bbase + 7*G2_STAGE + 1024);
        SCHEDB();
        acc[0] = MFMA16(EA[1][0], EB[1], acc[0]);
        OA[1][0] = *(const LAS bf16x8*)(abase + 7*G2_STAGE + 5120);
        SCHEDB();
        acc[1] = MFMA16(EA[1][1], EB[1], acc[1]);
        OA[1][1] = *(const LAS bf16x8*)(abase + 7*G2_STAGE + 6144);
        SCHEDB();
        acc[2] = MFMA16(EA[1][2], EB[1], acc[2]);
        OA[1][2] = *(const LAS bf16x8*)(abase + 7*G2_STAGE + 7168);
        SCHEDB();
        acc[3] = MFMA16(EA[1][3], EB[1], acc[3]);
        OA[1][3] = *(const LAS bf16x8*)(abase + 7*G2_STAGE + 8192);
        SCHEDB();
        acc[4] = MFMA16(EA[1][4], EB[1], acc[4]);
        OA[1][4] = *(const LAS bf16x8*)(abase + 7*G2_STAGE + 9216);
        SCHEDB();
        if (it < 2) BAR();
        acc[0] = MFMA16(OA[0][0], OB[0], acc[0]);
        EB[0] = *(const LAS bf16x8*)(bbase + 0*G2_STAGE);
        EA[0][0] = *(const LAS bf16x8*)(abase + 0*G2_STAGE + 0);
        SCHEDB();
        acc[1] = MFMA16(OA[0][1], OB[0], acc[1]);
        EA[0][1] = *(const LAS bf16x8*)(abase + 0*G2_STAGE + 1024);
        EA[0][2] = *(const LAS bf16x8*)(abase + 0*G2_STAGE + 2048);
        SCHEDB();
        acc[2] = MFMA16(OA[0][2], OB[0], acc[2]);
        EA[0][3] = *(const LAS bf16x8*)(abase + 0*G2_STAGE + 3072);
        SCHEDB();
        acc[3] = MFMA16(OA[0][3], OB[0], acc[3]);
        EA[0][4] = *(const LAS bf16x8*)(abase + 0*G2_STAGE + 4096);
        SCHEDB();
        acc[4] = MFMA16(OA[0][4], OB[0], acc[4]);
        EB[1] = *(const LAS bf16x8*)(bbase + 0*G2_STAGE + 1024);
        SCHEDB();
        acc[0] = MFMA16(OA[1][0], OB[1], acc[0]);
        EA[1][0] = *(const LAS bf16x8*)(abase + 0*G2_STAGE + 5120);
        SCHEDB();
        acc[1] = MFMA16(OA[1][1], OB[1], acc[1]);
        EA[1][1] = *(const LAS bf16x8*)(abase + 0*G2_STAGE + 6144);
        SCHEDB();
        acc[2] = MFMA16(OA[1][2], OB[1], acc[2]);
        EA[1][2] = *(const LAS bf16x8*)(abase + 0*G2_STAGE + 7168);
        SCHEDB();
        acc[3] = MFMA16(OA[1][3], OB[1], acc[3]);
        EA[1][3] = *(const LAS bf16x8*)(abase + 0*G2_STAGE + 8192);
        SCHEDB();
        acc[4] = MFMA16(OA[1][4], OB[1], acc[4]);
        EA[1][4] = *(const LAS bf16x8*)(abase + 0*G2_STAGE + 9216);
        SCHEDB();
    }
    STAMP(ws, 5, 6);
    const rsrc_t ro = mkrsrc(out, L_ * D_ * 2);
#pragma unroll
    for (int a = 0; a < 5; ++a) {
        const int row = 16 * a + 4 * g + q;
        const int ch = (2 * w + (p >> 1)) ^ ((row & 3) << 1);
        const s16x4 hr = trread((const LAS char*)smem + G2_RES + row * 128 + ch * 16 + 8 * (p & 1));
        const u32x2 hh = __builtin_bit_cast(u32x2, hr);
        const u32 f01 = cvtpk(acc[a][0] * rs2, acc[a][1] * rs2), f23 = cvtpk(acc[a][2] * rs2, acc[a][3] * rs2);
        const u32 c01 = hh[0], c23 = hh[1];
        const u32 h01 = cvtpk(bflo(c01) + m1, bfhi(c01) + m1), h23 = cvtpk(bflo(c23) + m1, bfhi(c23) + m1);
        __builtin_amdgcn_raw_buffer_store_b64(u32x2{cvtpk(bflo(h01) + bflo(f01), bfhi(h01) + bfhi(f01)), cvtpk(bflo(h23) + bflo(f23), bfhi(h23) + bfhi(f23))}, ro, (l * D_ + 80 * S + 16 * a + 4 * g) * 2, 0, 0);
    }
    STAMP(ws, 5, 7);
}

extern "C" void kernel_launch(void* const* d_in, const int* in_sizes, int n_in, void* d_out, int out_size, void* d_ws, size_t ws_size, hipStream_t stream) {
    const u16* x = (const u16*)d_in[0];
    const u16* Are = (const u16*)d_in[1]; const u16* Aim = (const u16*)d_in[2];
    const u16* Bre = (const u16*)d_in[3]; const u16* Bim = (const u16*)d_in[4];
    const u16* Cre = (const u16*)d_in[5]; const u16* Cim = (const u16*)d_in[6];
    const u16* logdt = (const u16*)d_in[7]; const u16* Dskip = (const u16*)d_in[8];
    const u16* Wg = (const u16*)d_in[10]; const u16* Wu = (const u16*)d_in[11]; const u16* Wd = (const u16*)d_in[12];
    char* ws = (char*)d_ws;
    ka_ssm<<<256, 768, 0, stream>>>(x, Are, Aim, Bre, Bim, Cre, Cim, logdt, Dskip, Wg, Wu, Wd, ws);
    k3_gemm1<<<256, 768, 0, stream>>>(ws);
    k4_gemm2<<<256, 768, 0, stream>>>(ws, (u16*)d_out);
}
```

```cpp
#include <hip/hip_runtime.h>
#include <stdint.h>

typedef unsigned short u16;
typedef unsigned int u32;
typedef short bf16x8 __attribute__((ext_vector_type(8)));
typedef short s16x4 __attribute__((ext_vector_type(4)));
typedef float f32x2 __attribute__((ext_vector_type(2)));
typedef float f32x4 __attribute__((ext_vector_type(4)));
typedef float f32x16 __attribute__((ext_vector_type(16)));
typedef unsigned u32x4 __attribute__((ext_vector_type(4)));
typedef unsigned u32x2 __attribute__((ext_vector_type(2)));
typedef __bf16 bf16x2_t __attribute__((ext_vector_type(2)));
#define LAS __attribute__((address_space(3)))
#define DI __device__ __forceinline__

#define STAMP(ws, k, j) do {} while (0)
#define STAMP2(ws, k, j) do {} while (0)

constexpr int L_ = 2048, D_ = 640, NM_ = 192, F_ = 1536;

constexpr u32 WS_HC  = 0;
constexpr u32 WS_HM  = WS_HC + D_ * L_ * 2;
constexpr u32 WS_ST  = WS_HM + L_ * F_ * 2;
constexpr u32 WS_RS  = WS_ST + 2048 * 4;
constexpr u32 WS_W1T = WS_RS + 2048 * 4;
constexpr u32 WS_W2T = WS_W1T + D_ * F_ * 2 * 2;
constexpr u32 WS_GR  = WS_W2T + F_ * D_ * 2;
constexpr u32 WS_AR  = WS_GR + 2048 * 8;
constexpr u32 WS_EP  = WS_AR + 1024;
constexpr u32 WS_END = WS_EP + 256;

DI float bflo(u32 v) { return __uint_as_float(v << 16); }
DI float bfhi(u32 v) { return __uint_as_float(v & 0xffff0000u); }
DI float bf2f(u16 v) { return __uint_as_float(((u32)v) << 16); }
DI u32 cvtpk(float lo, float hi) { f32x2 v = {lo, hi}; bf16x2_t r = __builtin_convertvector(v, bf16x2_t); return __builtin_bit_cast(u32, r); }
DI float rbf(float f) { return bflo(cvtpk(f, f)); }

typedef __amdgpu_buffer_rsrc_t rsrc_t;
DI rsrc_t mkrsrc(void* p, u32 bytes) { return __builtin_amdgcn_make_buffer_rsrc(p, 0, bytes, 0x00020000); }
#define AUX_KL 0
#define AUX_WT 16
#define AUX_H 16
#define AUX_HM 16
template <int AUX = 0> DI void st16(rsrc_t r, u32 off, u32x4 v) { __builtin_amdgcn_raw_buffer_store_b128(v, r, off, 0, AUX); }
template <int AUX = 0> DI void st8(rsrc_t r, u32 off, u32x2 v) { __builtin_amdgcn_raw_buffer_store_b64(v, r, off, 0, AUX); }
template <int AUX = 0> DI void st4(rsrc_t r, u32 off, u32 v) { __builtin_amdgcn_raw_buffer_store_b32(v, r, off, 0, AUX); }

template <int CTRL> DI float dppadd(float v) { return v + __builtin_bit_cast(float, __builtin_amdgcn_update_dpp(0, __builtin_bit_cast(int, v), CTRL, 0xF, 0xF, true)); }
DI float swap16add(float v) { auto r = __builtin_amdgcn_permlane16_swap(__float_as_uint(v), __float_as_uint(v), false, false); const u32 a = r[0], b = r[1]; return __uint_as_float(a) + __uint_as_float(b); }
DI float swap32add(float v) { auto r = __builtin_amdgcn_permlane32_swap(__float_as_uint(v), __float_as_uint(v), false, false); const u32 a = r[0], b = r[1]; return __uint_as_float(a) + __uint_as_float(b); }
DI float wave_sum(float v) {
    v = dppadd<0xB1>(v);
    v = dppadd<0x4E>(v);
    v = dppadd<0x141>(v);
    v = dppadd<0x140>(v);
    return swap32add(swap16add(v));
}

struct cm { f32x2 a, s; };
DI cm mkcm(f32x2 b) { return { b, f32x2{-b.y, b.x} }; }
DI f32x2 cmul(f32x2 v, const cm& m) { return v.xx * m.a + v.yy * m.s; }
DI f32x2 csq(f32x2 a) { return f32x2{ a.x * a.x - a.y * a.y, 2.f * a.x * a.y }; }
DI f32x2 cexp_p(float ar, float br, float p) {
    float e = __builtin_amdgcn_exp2f(ar * p);
    float th = __builtin_amdgcn_fractf(br * p);
    return f32x2{ e * __builtin_amdgcn_cosf(th), e * __builtin_amdgcn_sinf(th) };
}

#define WAITV(n) asm volatile("s_waitcnt vmcnt(" #n ")" ::: "memory")
#define WAITL0() asm volatile("s_waitcnt lgkmcnt(0)" ::: "memory")
template <int N> DI void waitv() { asm volatile("s_waitcnt vmcnt(%0)" :: "n"(N) : "memory"); }
#define BAR() do { asm volatile("" ::: "memory"); __builtin_amdgcn_s_barrier(); asm volatile("" ::: "memory"); } while (0)
#define LBAR() do { WAITL0(); BAR(); } while (0)
#define SGB(m, n) __builtin_amdgcn_sched_group_barrier(m, n, 0)
#define SCHEDB() __builtin_amdgcn_sched_barrier(0)
#define MFMA16(a, b, c) __builtin_amdgcn_mfma_f32_16x16x32_bf16(a, b, c, 0, 0, 0)
#define GAS __attribute__((address_space(1)))
typedef unsigned long long u64;

DI s16x4 trread(const LAS char* p) { typedef short v4i16_t __attribute__((ext_vector_type(4))); return __builtin_bit_cast(s16x4, __builtin_amdgcn_ds_read_tr16_b64_v4i16((LAS v4i16_t*)p)); }
DI bf16x8 cat8(s16x4 a, s16x4 b) { return __builtin_shufflevector(a, b, 0, 1, 2, 3, 4, 5, 6, 7); }

constexpr int KG_PITCH = 784;
constexpr int KA_A = 0, KA_ASZ = 20 * KG_PITCH;
constexpr int KA_B = 3 * KA_ASZ, KA_BSZ = 32 * KG_PITCH;
constexpr int KA_U = KA_B + 3 * KA_BSZ, KA_USZ = 4608 + 4096;
constexpr int KA_CNT = KA_U + 3 * KA_USZ;
constexpr int KA_LDS = KA_CNT + 32;
constexpr int CV_CS = 4160;
constexpr int KB_X = 32256;

#define ADDTID2(base0, v0, o0, base1, v1, o1) asm volatile("s_mov_b32 m0, %2\n\ts_nop 0\n\tds_write_addtid_b32 %0 offset:%4\n\ts_mov_b32 m0, %3\n\ts_nop 0\n\tds_write_addtid_b32 %1 offset:%5" \
    :: "v"(v0), "v"(v1), "s"(base0), "s"(base1), "i"(o0), "i"(o1) : "memory")
#define ADDTID2S(base, v0, o0, v1, o1) asm volatile("s_mov_b32 m0, %2\n\ts_nop 0\n\tds_write_addtid_b32 %0 offset:%3\n\tds_write_addtid_b32 %1 offset:%4" :: "v"(v0), "v"(v1), "s"(base), "i"(o0), "i"(o1) : "memory")

DI void lds_sync(char* smem, int cnt, u32 target, int lane) {
    LAS u32* c = (LAS u32*)((LAS char*)smem + KA_CNT) + cnt;
    WAITL0();
    if (lane == 0) __hip_atomic_fetch_add(c, 1u, __ATOMIC_RELAXED, __HIP_MEMORY_SCOPE_WORKGROUP);
    for (int spin = 0; spin < 1000000; ++spin) {
        if (__builtin_amdgcn_readfirstlane(__hip_atomic_load(c, __ATOMIC_RELAXED, __HIP_MEMORY_SCOPE_WORKGROUP)) >= target) break;
        __builtin_amdgcn_s_sleep(1);
    }
    asm volatile("" ::: "memory");
}

DI void lds_arrive(char* smem, int cnt, int lane) {
    WAITL0();
    if (lane == 0) __hip_atomic_fetch_add((LAS u32*)((LAS char*)smem + KA_CNT) + cnt, 1u, __ATOMIC_RELAXED, __HIP_MEMORY_SCOPE_WORKGROUP);
}
DI void lds_wait(char* smem, int cnt, u32 target) {
    LAS u32* c = (LAS u32*)((LAS char*)smem + KA_CNT) + cnt;
    for (int spin = 0; spin < 1000000; ++spin) {
        if (__builtin_amdgcn_readfirstlane(__hip_atomic_load(c, __ATOMIC_RELAXED, __HIP_MEMORY_SCOPE_WORKGROUP)) >= target) break;
        __builtin_amdgcn_s_sleep(1);
    }
    asm volatile("" ::: "memory");
}

struct KParams { u16 are, aim, bre, bim, cre, cim, ldt; };

DI void ka_kgen(const KParams& P, char* smem, int sA, int sB, int w, int lane) {
    const float are = bf2f(P.are), aim = bf2f(P.aim), bre = bf2f(P.bre), bim = bf2f(P.bim), cre = bf2f(P.cre), cim = bf2f(P.cim);
    const float dt = rbf(__expf(bf2f(P.ldt)));
    const float ar = (dt * are) * 1.4426950408889634f, br = (dt * aim) * 0.15915494309189535f;
    const float dbr = dt * bre, dbi = dt * bim;
    const f32x2 Wc = { cre * dbr - cim * dbi, -(cre * dbi + cim * dbr) };
    const f32x2 l1 = cexp_p(ar, br, 1.f), l8 = cexp_p(ar, br, 8.f), l256 = cexp_p(ar, br, 256.f);
    const f32x2 l2 = csq(l1), l4 = csq(l2), l16 = csq(l8);
    const cm c1 = mkcm(f32x2{l1.x, -l1.y}), c2 = mkcm(f32x2{l2.x, -l2.y}), c4 = mkcm(f32x2{l4.x, -l4.y}), c256 = mkcm(f32x2{l256.x, -l256.y}), m16 = mkcm(l16);
    const u32 lds0 = (u32)(uintptr_t)smem;
    const u32 bA = lds0 + sA + w * 256, bB = lds0 + sB + w * 256 - KB_X;
    auto pk = [](f32x2 v) { return cvtpk(v.x, v.y); };
    constexpr int R = KG_PITCH, X = KB_X;
    f32x2 a0 = Wc, x256 = cmul(Wc, c256), e0 = {1.f, 0.f}, e1 = l8;
    ADDTID2S(bA, pk(a0), 0 * R, pk(x256), 8 * R);
    ADDTID2S(bB, pk(e0), X + 0 * R, pk(e1), X + 1 * R);
    f32x2 y = x256, v = cmul(x256, c256);
#define KG_E(c) do { e0 = cmul(e0, m16); e1 = cmul(e1, m16); ADDTID2S(bB, pk(e0), X + (c) * R, pk(e1), X + ((c) + 1) * R); } while (0)
    a0 = cmul(a0, c1); y = cmul(y, c2); ADDTID2S(bA, pk(a0), 1 * R, pk(y), 9 * R); KG_E(2);
    a0 = cmul(a0, c1); y = cmul(y, c2); ADDTID2S(bA, pk(a0), 2 * R, pk(y), 10 * R); KG_E(4);
    a0 = cmul(a0, c1); y = cmul(y, c2); ADDTID2S(bA, pk(a0), 3 * R, pk(y), 11 * R); KG_E(6);
    a0 = cmul(a0, c1); y = cmul(v, c4); ADDTID2S(bA, pk(a0), 4 * R, pk(v), 12 * R); v = cmul(v, c256); KG_E(8);
    a0 = cmul(a0, c1); ADDTID2S(bA, pk(a0), 5 * R, pk(y), 13 * R); y = cmul(v, c4); KG_E(10);
    a0 = cmul(a0, c1); ADDTID2S(bA, pk(a0), 6 * R, pk(v), 14 * R); v = cmul(v, c256); KG_E(12);
    a0 = cmul(a0, c1); ADDTID2S(bA, pk(a0), 7 * R, pk(y), 15 * R); KG_E(14);
    y = cmul(v, c256); KG_E(16);
    ADDTID2S(bA, pk(v), 16 * R, pk(y), 17 * R); v = cmul(y, c256); KG_E(18);
    y = cmul(v, c256); KG_E(20);
    ADDTID2S(bA, pk(v), 18 * R, pk(y), 19 * R); v = cmul(y, c256); KG_E(22);
    const float k2048 = wave_sum(v.x);
    KG_E(24); KG_E(26);
    KG_E(28); KG_E(30);
    const int r32 = lane & 31, h = lane >> 5;
    const char* pa = smem + sA + (r32 < 20 ? r32 : 19) * R + w * 256 + h * 16;
    const char* pb = smem + sB + r32 * R + w * 256 + h * 16;
    f32x16 acc = {};
#pragma unroll
    for (int s = 0; s < 8; ++s) {
        const bf16x8 a = *(const bf16x8*)(pa + 32 * s), bb = *(const bf16x8*)(pb + 32 * s);
        acc = __builtin_amdgcn_mfma_f32_32x32x16_bf16(a, bb, acc, 0, 0, 0);
    }
    const int c = r32;
    char* sl = smem + sA + w * 256;
    char* p0 = sl + (c >> 3) * R + (8 * (c & 7) + 4 * h) * 4;
    char* p1 = sl + (h ? 6 * R + 8 * c : (4 + (c >> 4)) * R + 16 * (c & 15));
    char* p2 = sl + 8 * R + 4 * c;
#pragma unroll
    for (int q = 0; q < 4; ++q) *(float*)(p0 + 4 * q) = acc[q];
    *(float*)(p1) = acc[4]; *(float*)(p1 + 4) = acc[5];
    *(float*)(p1 + (h ? R : 8)) = acc[6]; *(float*)(p1 + (h ? R + 4 : 12)) = acc[7];
    if (h == 0) { *(float*)(p2) = acc[8]; *(float*)(p2 + 128) = acc[9]; *(float*)(p2 + R) = acc[10]; *(float*)(p2 + R + 128) = acc[11]; }
    else if (c == 0) *(float*)(sl + 10 * R) = k2048;
}

DI void ka_expand(char* smem, int sA, int sB, int w, int lane) {
    const int t = 64 * w + lane;
    auto rd = [&](int i) { const char* p = smem + sA + (i >> 6) * KG_PITCH + (i & 63) * 4; return *(const float*)p + *(const float*)(p + 256) + *(const float*)(p + 512); };
    u32 k4, k5, k6, k7, f2, f3;
    if (w == 0) {
        const int i0 = t < 32 ? 8 * t : 256 + 4 * (t - 32);
        const u32 pat = t < 32 ? 0x76543210u : 0x43222100u;
        float kv[8];
#pragma unroll
        for (int j = 0; j < 8; ++j) kv[j] = rd(i0 + ((pat >> (4 * j)) & 15));
        k4 = cvtpk(kv[0], kv[1]); k5 = cvtpk(kv[2], kv[3]); k6 = cvtpk(kv[4], kv[5]); k7 = cvtpk(kv[6], kv[7]);
        f2 = 0; f3 = 0;
    } else if (w == 1) {
        const int i0 = 384 + 2 * (t - 64);
        const float k0 = rd(i0), k1 = rd(i0 + 1), k2 = rd(i0 + 2);
        k4 = cvtpk(k0, k0); k5 = cvtpk(k0, k1); k6 = cvtpk(k1, k1); k7 = cvtpk(k2, k2);
        const float a = rd(382), b = rd(383);
        f2 = cvtpk(a, a); f3 = cvtpk(b, k0);
    } else {
        const int i0 = 512 + (t - 128);
        const float k0 = rd(i0), k1 = rd(i0 + 1);
        k4 = cvtpk(k0, k0); k5 = k4; k6 = cvtpk((t & 1) ? k1 : k0, k1); k7 = cvtpk(k1, k1);
        const float a = w == 2 ? rd(511) : k0;
        f2 = cvtpk(a, a); f3 = cvtpk(k0, k0);
    }
    const u32 k2 = __builtin_amdgcn_update_dpp(f2, k6, 0x138, 0xF, 0xF, false), k3 = __builtin_amdgcn_update_dpp(f3, k7, 0x138, 0xF, 0xF, false);
    const u32 a2 = __builtin_amdgcn_alignbit(k3, k2, 16), a3 = __builtin_amdgcn_alignbit(k4, k3, 16), a4 = __builtin_amdgcn_alignbit(k5, k4, 16),
              a5 = __builtin_amdgcn_alignbit(k6, k5, 16), a6 = __builtin_amdgcn_alignbit(k7, k6, 16);
    char* kb = smem + sB + 32 + 16 * t;
    *(u32x4*)(kb + 0 * CV_CS) = u32x4{k4, k5, k6, k7};
    *(u32x4*)(kb + 1 * CV_CS) = u32x4{a3, a4, a5, a6};
    *(u32x4*)(kb + 2 * CV_CS) = u32x4{k3, k4, k5, k6};
    *(u32x4*)(kb + 3 * CV_CS) = u32x4{a2, a3, a4, a5};
    if (t < 8) *(u32x4*)(smem + sB + (t >> 1) * CV_CS + 16 * (t & 1)) = u32x4{0, 0, 0, 0};
}

__global__ __launch_bounds__(768, 3) void ka_ssm(const u16* __restrict__ x, const u16* __restrict__ Are, const u16* __restrict__ Aim, const u16* __restrict__ Bre,
                                                 const u16* __restrict__ Bim, const u16* __restrict__ Cre, const u16* __restrict__ Cim, const u16* __restrict__ logdt,
                                                 const u16* __restrict__ Dskip, const u16* __restrict__ Wg, const u16* __restrict__ Wu, const u16* __restrict__ Wd,
                                                 char* __restrict__ ws) {
    __shared__ __attribute__((aligned(256))) char smem[KA_LDS];
    const int b = blockIdx.x, t = threadIdx.x, lane = t & 63;
    const int wv = __builtin_amdgcn_readfirstlane(t >> 6), sb = wv >> 2, w = wv & 3;
    const int xcd = b & 7, j = b >> 3;
    const int nch = j < 16 ? 3 : 2;
    const int c0 = 80 * xcd + ((xcd & 2) ? (j < 16 ? 32 + 3 * j : 2 * (j - 16)) : (j < 16 ? 3 * j : 48 + 2 * (j - 16)));
    const int xs = min((2 * c0) & ~3, ((2 * c0) | 127) + 1 - 8), e0 = (2 * c0 - xs) >> 1;
    const bool conv = sb < nch;
    const int d = c0 + sb;
    const int sA = KA_A + sb * KA_ASZ, sB = KA_B + sb * KA_BSZ, sU = KA_U + sb * KA_USZ;
    const rsrc_t wsr = mkrsrc(ws, WS_END);
    STAMP(ws, 0, 0);
    if (t < 8) *(u32*)(smem + KA_CNT + 4 * t) = 0u;

    KParams KP;
    u32 ra[4][5];
    u32x4 wv1[8], wv2[8];
    const int tt = t - 512;
    const bool w1job = (j - 16) < 12;
    float dsk = 0.f;
    if (wv == 3 || wv == 7) {
        const u32* x32 = (const u32*)x;
#pragma unroll
        for (int rr = 0; rr < 4; ++rr)
#pragma unroll
            for (int i = 0; i < 5; ++i) ra[rr][i] = x32[(8 * b + 4 * sb + rr) * 320 + lane + 64 * i];
    }
    if (conv) {
        if (w < 3) {
            const int n = d * NM_ + 64 * w + lane;
            KP.are = Are[n]; KP.aim = Aim[n]; KP.bre = Bre[n]; KP.bim = Bim[n]; KP.cre = Cre[n]; KP.cim = Cim[n]; KP.ldt = logdt[d];
        }
        dsk = bf2f(Dskip[d]);
    }
    const u32 epoch = __hip_atomic_load((GAS u32*)(ws + WS_EP), __ATOMIC_RELAXED, __HIP_MEMORY_SCOPE_AGENT);
    const rsrc_t xr0 = mkrsrc((void*)x, L_ * D_ * 2);
    LBAR();
    u32x2 xg[4];
    if (wv < 8) {
        xg[0] = __builtin_amdgcn_raw_buffer_load_b64(xr0, (4 * t + 0) * (D_ * 2) + xs, 0, 0);
        xg[1] = __builtin_amdgcn_raw_buffer_load_b64(xr0, (4 * t + 1) * (D_ * 2) + xs, 0, 0);
    }
    if (conv && w == 3) {
        if (lane < 30) *(u32x4*)(smem + sU + 16 * lane) = u32x4{0, 0, 0, 0};
        lds_arrive(smem, sb, lane);
    }
    if (!conv) {
        if (w1job) {
            const int u1 = xcd * 12 + (j - 16), up = u1 >= 48, col = 32 * (up ? u1 - 48 : u1) + 8 * (tt & 3);
            const u16* src = (up ? Wu : Wg) + (8 * (tt >> 2)) * F_ + col;
#pragma unroll
            for (int r = 0; r < 8; ++r) wv1[r] = *(const u32x4*)(src + r * F_);
            if (w == 0) {
#pragma unroll
                for (int r = 0; r < 8; ++r) wv2[r] = *(const u32x4*)(src + (512 + r) * F_);
            }
        } else {
            const int n1 = 480 * (xcd * 4 + (j - 28)) + tt, n2 = n1 + 256;
            const u16* s1 = Wd + (8 * (n1 / 80)) * D_ + 8 * (n1 % 80);
#pragma unroll
            for (int r = 0; r < 8; ++r) wv1[r] = *(const u32x4*)(s1 + r * D_);
            if (tt < 224) {
                const u16* s2 = Wd + (8 * (n2 / 80)) * D_ + 8 * (n2 % 80);
#pragma unroll
                for (int r = 0; r < 8; ++r) wv2[r] = *(const u32x4*)(s2 + r * D_);
            }
        }
    }
    const u32 tag = epoch + 1u;
    if (wv == 3 || wv == 7) {
        __builtin_amdgcn_s_setprio(3);
        float s1[4], s2[4], c0v[4];
#pragma unroll
        for (int rr = 0; rr < 4; ++rr) {
            const float c = bflo(__builtin_amdgcn_readfirstlane(ra[rr][0]));
            float a1 = 0.f, a2 = 0.f;
#pragma unroll
            for (int i = 0; i < 5; ++i) { const float a = bflo(ra[rr][i]) - c, e = bfhi(ra[rr][i]) - c; a1 += a + e; a2 = __builtin_fmaf(a, a, __builtin_fmaf(e, e, a2)); }
            s1[rr] = a1; s2[rr] = a2; c0v[rr] = c;
        }
        auto red4 = [&](const float (&v)[4]) {
            auto p01 = __builtin_amdgcn_permlane32_swap(__float_as_uint(v[0]), __float_as_uint(v[1]), false, false);
            auto p23 = __builtin_amdgcn_permlane32_swap(__float_as_uint(v[2]), __float_as_uint(v[3]), false, false);
            const u32 x0 = p01[0], x1 = p01[1], y0 = p23[0], y1 = p23[1];
            const float t01 = __uint_as_float(x0) + __uint_as_float(x1);
            const float t23 = __uint_as_float(y0) + __uint_as_float(y1);
            auto q = __builtin_amdgcn_permlane16_swap(__float_as_uint(t01), __float_as_uint(t23), false, false);
            const u32 z0 = q[0], z1 = q[1];
            float r = __uint_as_float(z0) + __uint_as_float(z1);
            r = dppadd<0xB1>(r); r = dppadd<0x4E>(r); r = dppadd<0x141>(r); r = dppadd<0x140>(r);
            return r;
        };
        const float n1 = red4(s1) * (1.f / 640.f), n2 = red4(s2) * (1.f / 640.f);
        const int lr = lane >> 4, rr = (lr >> 1) | ((lr & 1) << 1);
        const float cc = rr == 0 ? c0v[0] : rr == 1 ? c0v[1] : rr == 2 ? c0v[2] : c0v[3];
        float var = n2 - n1 * n1; var = var > 0.f ? var : 0.f;
        const float sd = __builtin_sqrtf(rbf(rbf(var) + 1e-5f));
        const u32 pay = cvtpk(cc + n1, sd);
        const int row = 8 * b + 4 * sb + rr;
        if ((lane & 15) == 0) {
            __hip_atomic_store((GAS u64*)(ws + WS_GR) + row, ((u64)tag << 32) | pay, __ATOMIC_RELAXED, __HIP_MEMORY_SCOPE_AGENT);
            st4(wsr, WS_ST + row * 4, pay << 16);
        }
        asm volatile("" ::: "memory");
        if (lane == 0) __hip_atomic_fetch_add((LAS u32*)((LAS char*)smem + KA_CNT) + 4, 1u, __ATOMIC_RELAXED, __HIP_MEMORY_SCOPE_WORKGROUP);
        __builtin_amdgcn_s_setprio(0);
    }
    if (conv) {
        if (w < 3) ka_kgen(KP, smem, sA, sB, w, lane);
    } else if (w1job) {
        float* red = (float*)(smem + sA);
#pragma unroll
        for (int i = 0; i < 4; ++i) {
            float a = 0.f, c = 0.f;
#pragma unroll
            for (int r = 0; r < 8; ++r) { a += bflo(wv1[r][i]); c += bfhi(wv1[r][i]); }
            if (w == 0) {
#pragma unroll
                for (int r = 0; r < 8; ++r) { a += bflo(wv2[r][i]); c += bfhi(wv2[r][i]); }
            }
            red[(tt >> 2) * 33 + 8 * (tt & 3) + 2 * i] = a; red[(tt >> 2) * 33 + 8 * (tt & 3) + 2 * i + 1] = c;
        }
    }
    if (!conv) {
        {
            if (w1job) {
            lds_sync(smem, sb, 4, lane);
            const float* red = (const float*)(smem + sA);
            const int col = tt & 31, part = tt >> 5;
            float sm = 0.f;
#pragma unroll
            for (int r = 0; r < 8; ++r) sm += red[(8 * part + r) * 33 + col];
            sm = swap32add(sm);
            if (lane < 32) *(float*)(smem + sB + (col * 4 + w) * 4) = sm;
            lds_sync(smem, sb, 8, lane);
#define WV(r) (rnd ? wv2[r][i] : wv1[r][i])
            }
            char* stg = smem + (w == 3 ? sA + 1024 : sB + 512 + w * 8192);
            u32* dtab = (u32*)(smem + sU + w * 256);
            auto flush = [&](u32 dst, bool active, const u32x4 (&o)[8]) {
                dtab[lane] = active ? dst : 0xffffff00u;
#pragma unroll
                for (int i = 0; i < 8; ++i) *(u32x4*)(stg + lane * 128 + ((i ^ (lane & 7)) * 16)) = o[i];
#pragma unroll
                for (int i = 0; i < 8; ++i) {
                    const int sl = 8 * i + (lane >> 3), pc = lane & 7;
                    const u32x4 v = *(const u32x4*)(stg + sl * 128 + ((pc ^ (sl & 7)) * 16));
                    st16<AUX_WT>(wsr, dtab[sl] + pc * 16, v);
                }
            };
            if (w1job) {
                const int u1 = xcd * 12 + (j - 16), up = u1 >= 48, f = 32 * (up ? u1 - 48 : u1) + 8 * (tt & 3);
                float cm[8];
#pragma unroll
                for (int i = 0; i < 8; ++i) { const f32x4 v = *(const f32x4*)(smem + sB + (8 * (tt & 3) + i) * 16); cm[i] = (v[0] + v[1] + v[2] + v[3]) * (1.f / 640.f); }
                const int sl = f / 96, fl = f % 96, mt = 2 * (fl >> 4) + up;
#pragma unroll
                for (int rnd = 0; rnd < 2; ++rnd) {
                    if (rnd == 0 || w == 0) {
                        const int kg = (tt >> 2) + 64 * rnd;
                        const u32 dst = WS_W1T + ((sl * 10 + (kg >> 3)) * 24 + ((kg >> 2) & 1) * 12 + mt) * 1024 + ((fl & 15) + 16 * (kg & 3)) * 16;
                        u32x4 o[8];
#pragma unroll
                        for (int i = 0; i < 4; ++i) {
                            const float ca = cm[2 * i], cb = cm[2 * i + 1];
                            o[2 * i] = u32x4{cvtpk(bflo(WV(0)) - ca, bflo(WV(1)) - ca), cvtpk(bflo(WV(2)) - ca, bflo(WV(3)) - ca),
                                             cvtpk(bflo(WV(4)) - ca, bflo(WV(5)) - ca), cvtpk(bflo(WV(6)) - ca, bflo(WV(7)) - ca)};
                            o[2 * i + 1] = u32x4{cvtpk(bfhi(WV(0)) - cb, bfhi(WV(1)) - cb), cvtpk(bfhi(WV(2)) - cb, bfhi(WV(3)) - cb),
                                                 cvtpk(bfhi(WV(4)) - cb, bfhi(WV(5)) - cb), cvtpk(bfhi(WV(6)) - cb, bfhi(WV(7)) - cb)};
                        }
                        flush(dst, true, o);
                    }
                }
            } else {
#pragma unroll
                for (int rnd = 0; rnd < 2; ++rnd) {
                    if (rnd == 0 || w < 3 || true) {
                        const bool act = rnd == 0 || tt < 224;
                        const int n = 480 * (xcd * 4 + (j - 28)) + tt + 256 * rnd, dc = n % 80, kg = n / 80;
                        const int ds = dc / 10, dl = 8 * (dc % 10);
                        const u32 dst = WS_W2T + ((ds * 24 + (kg >> 3)) * 10 + ((kg >> 2) & 1) * 5 + (dl >> 4)) * 1024 + ((dl & 15) + 16 * (kg & 3)) * 16;
                        u32x4 o[8];
#pragma unroll
                        for (int i = 0; i < 4; ++i) {
                            o[2 * i] = u32x4{__builtin_amdgcn_perm(WV(1), WV(0), 0x05040100u), __builtin_amdgcn_perm(WV(3), WV(2), 0x05040100u),
                                             __builtin_amdgcn_perm(WV(5), WV(4), 0x05040100u), __builtin_amdgcn_perm(WV(7), WV(6), 0x05040100u)};
                            o[2 * i + 1] = u32x4{__builtin_amdgcn_perm(WV(1), WV(0), 0x07060302u), __builtin_amdgcn_perm(WV(3), WV(2), 0x07060302u),
                                                 __builtin_amdgcn_perm(WV(5), WV(4), 0x07060302u), __builtin_amdgcn_perm(WV(7), WV(6), 0x07060302u)};
                        }
                        flush(dst, act, o);
                    }
                }
            }
        }
        if (t == 512) STAMP2(ws, 6, 0);
        return;
    }
    STAMP(ws, 0, 1);
    u32x4 g0 = {0, 0, 0, 0}, g1 = {0, 0, 0, 0};
    if (wv < 8) {
        lds_wait(smem, 4, 2);
        xg[2] = __builtin_amdgcn_raw_buffer_load_b64(xr0, (4 * t + 2) * (D_ * 2) + xs, 0, 0);
        xg[3] = __builtin_amdgcn_raw_buffer_load_b64(xr0, (4 * t + 3) * (D_ * 2) + xs, 0, 0);
        g0 = __builtin_amdgcn_raw_buffer_load_b128(wsr, WS_GR + t * 32, 0, 16);
        g1 = __builtin_amdgcn_raw_buffer_load_b128(wsr, WS_GR + t * 32 + 16, 0, 16);
    }
    if (w < 3) lds_sync(smem, sb, 4, lane); else lds_wait(smem, sb, 4);
    STAMP(ws, 0, 2);
    ka_expand(smem, sA, sB, w, lane);
    if (wv < 8) {
        for (int spin = 0; spin < 100000; ++spin) {
            if (__all(g0[1] == tag && g0[3] == tag && g1[1] == tag && g1[3] == tag)) break;
            __builtin_amdgcn_s_sleep(4);
            asm volatile("" ::: "memory");
            g0 = __builtin_amdgcn_raw_buffer_load_b128(wsr, WS_GR + t * 32, 0, 16);
            g1 = __builtin_amdgcn_raw_buffer_load_b128(wsr, WS_GR + t * 32 + 16, 0, 16);
        }
        const u32 pr[4] = {g0[0], g0[2], g1[0], g1[2]};
        float m[4], rs[4];
#pragma unroll
        for (int r = 0; r < 4; ++r) { m[r] = bflo(pr[r]); rs[r] = __builtin_amdgcn_rcpf(bfhi(pr[r])); }
#pragma unroll
        for (int s = 0; s < 3; ++s) {
            if (s < nch) {
                const int e = e0 + s;
                float xv[4];
#pragma unroll
                for (int r = 0; r < 4; ++r) { const u32 dw = e < 2 ? xg[r][0] : xg[r][1]; xv[r] = (e & 1) ? bfhi(dw) : bflo(dw); }
                const u32 q01 = cvtpk(xv[0] - m[0], xv[1] - m[1]), q23 = cvtpk(xv[2] - m[2], xv[3] - m[3]);
                const u32 u01 = cvtpk(bflo(q01) * rs[0], bfhi(q01) * rs[1]), u23 = cvtpk(bflo(q23) * rs[2], bfhi(q23) * rs[3]);
                char* ub = smem + KA_U + s * KA_USZ;
                *(u32x2*)(ub + 480 + 8 * t) = u32x2{u01, u23};
                *(u32x2*)(ub + 4608 + 8 * t) = u32x2{q01, q23};
            }
        }
    }
    STAMP(ws, 0, 3);
    asm volatile("" :: "v"(tag));
    lds_sync(smem, 3, 4 * nch, lane);
    STAMP(ws, 0, 4);
    if (t == 0) __hip_atomic_store((GAS u32*)(ws + WS_AR) + b, tag, __ATOMIC_RELAXED, __HIP_MEMORY_SCOPE_AGENT);
    const int al = lane & 15, g = lane >> 4;
    f32x4 acc[8];
#pragma unroll
    for (int i = 0; i < 8; ++i) acc[i] = f32x4{0.f, 0.f, 0.f, 0.f};
    {
        const LAS char* lds = (const LAS char*)smem;
        const int q = (lane & 15) >> 2, p = lane & 3;
        const LAS char* ub0 = lds + sU + 448 + 32 * al + 16 * g - 128 * w;
        const LAS char* ub1 = ub0 - 64;
        const LAS char* kb0 = lds + sB + q * CV_CS + 56 - 16 * g + 8 * p + 128 * w;
        const LAS char* kb1 = kb0 + 64;
        bf16x8 U0[8], U1[8]; s16x4 A0[8][2], A1[8][2];
        A0[0][0] = trread(kb0 + 8); A0[0][1] = trread(kb0 + 0); U0[0] = *(const LAS bf16x8*)(ub0 + 0); U0[1] = *(const LAS bf16x8*)(ub0 + 512); U0[2] = *(const LAS bf16x8*)(ub0 + 1024); U0[3] = *(const LAS bf16x8*)(ub0 + 1536); U0[4] = *(const LAS bf16x8*)(ub0 + 2048); U0[5] = *(const LAS bf16x8*)(ub0 + 2560); U0[6] = *(const LAS bf16x8*)(ub0 + 3072); U0[7] = *(const LAS bf16x8*)(ub0 + 3584); A0[1][0] = trread(kb0 + 520); A0[1][1] = trread(kb0 + 512); A0[2][0] = trread(kb0 + 1032); A0[2][1] = trread(kb0 + 1024); A0[3][0] = trread(kb0 + 1544); A0[3][1] = trread(kb0 + 1536); A0[4][0] = trread(kb0 + 2056); A0[4][1] = trread(kb0 + 2048); A0[5][0] = trread(kb0 + 2568); A0[5][1] = trread(kb0 + 2560); A0[6][0] = trread(kb0 + 3080); A0[6][1] = trread(kb0 + 3072); A0[7][0] = trread(kb0 + 3592); A0[7][1] = trread(kb0 + 3584);
        SCHEDB();
        acc[0] = MFMA16(cat8(A0[0][0], A0[0][1]), U0[0], acc[0]);
        SCHEDB();
        acc[1] = MFMA16(cat8(A0[0][0], A0[0][1]), U0[1], acc[1]);
        SCHEDB();
        acc[2] = MFMA16(cat8(A0[0][0], A0[0][1]), U0[2], acc[2]);
        SCHEDB();
        acc[3] = MFMA16(cat8(A0[0][0], A0[0][1]), U0[3], acc[3]);
        SCHEDB();
        acc[4] = MFMA16(cat8(A0[0][0], A0[0][1]), U0[4], acc[4]);
        SCHEDB();
        acc[5] = MFMA16(cat8(A0[0][0], A0[0][1]), U0[5], acc[5]);
        SCHEDB();
        acc[6] = MFMA16(cat8(A0[0][0], A0[0][1]), U0[6], acc[6]);
        SCHEDB();
        acc[7] = MFMA16(cat8(A0[0][0], A0[0][1]), U0[7], acc[7]);
        SCHEDB();
        acc[1] = MFMA16(cat8(A0[1][0], A0[1][1]), U0[0], acc[1]);
        A1[0][0] = trread(kb1 + 8);
        SCHEDB();
        acc[2] = MFMA16(cat8(A0[1][0], A0[1][1]), U0[1], acc[2]);
        A1[0][1] = trread(kb1 + 0);
        SCHEDB();
        acc[3] = MFMA16(cat8(A0[1][0], A0[1][1]), U0[2], acc[3]);
        U1[0] = *(const LAS bf16x8*)(ub1 + 0);
        SCHEDB();
        acc[4] = MFMA16(cat8(A0[1][0], A0[1][1]), U0[3], acc[4]);
        U1[1] = *(const LAS bf16x8*)(ub1 + 512);
        SCHEDB();
        acc[5] = MFMA16(cat8(A0[1][0], A0[1][1]), U0[4], acc[5]);
        U1[2] = *(const LAS bf16x8*)(ub1 + 1024);
        SCHEDB();
        acc[6] = MFMA16(cat8(A0[1][0], A0[1][1]), U0[5], acc[6]);
        U1[3] = *(const LAS bf16x8*)(ub1 + 1536);
        SCHEDB();
        acc[7] = MFMA16(cat8(A0[1][0], A0[1][1]), U0[6], acc[7]);
        U1[4] = *(const LAS bf16x8*)(ub1 + 2048);
        SCHEDB();
        acc[2] = MFMA16(cat8(A0[2][0], A0[2][1]), U0[0], acc[2]);
        U1[5] = *(const LAS bf16x8*)(ub1 + 2560);
        SCHEDB();
        acc[3] = MFMA16(cat8(A0[2][0], A0[2][1]), U0[1], acc[3]);
        U1[6] = *(const LAS bf16x8*)(ub1 + 3072);
        SCHEDB();
        acc[4] = MFMA16(cat8(A0[2][0], A0[2][1]), U0[2], acc[4]);
        U1[7] = *(const LAS bf16x8*)(ub1 + 3584);
        SCHEDB();
        acc[5] = MFMA16(cat8(A0[2][0], A0[2][1]), U0[3], acc[5]);
        A1[1][0] = trread(kb1 + 520);
        SCHEDB();
        acc[6] = MFMA16(cat8(A0[2][0], A0[2][1]), U0[4], acc[6]);
        A1[1][1] = trread(kb1 + 512);
        SCHEDB();
        acc[7] = MFMA16(cat8(A0[2][0], A0[2][1]), U0[5], acc[7]);
        A1[2][0] = trread(kb1 + 1032);
        SCHEDB();
        acc[3] = MFMA16(cat8(A0[3][0], A0[3][1]), U0[0], acc[3]);
        A1[2][1] = trread(kb1 + 1024);
        SCHEDB();
        acc[4] = MFMA16(cat8(A0[3][0], A0[3][1]), U0[1], acc[4]);
        A1[3][0] = trread(kb1 + 1544);
        SCHEDB();
        acc[5] = MFMA16(cat8(A0[3][0], A0[3][1]), U0[2], acc[5]);
        A1[3][1] = trread(kb1 + 1536);
        SCHEDB();
        acc[6] = MFMA16(cat8(A0[3][0], A0[3][1]), U0[3], acc[6]);
        A1[4][0] = trread(kb1 + 2056);
        SCHEDB();
        acc[7] = MFMA16(cat8(A0[3][0], A0[3][1]), U0[4], acc[7]);
        A1[4][1] = trread(kb1 + 2048);
        SCHEDB();
        acc[4] = MFMA16(cat8(A0[4][0], A0[4][1]), U0[0], acc[4]);
        A1[5][0] = trread(kb1 + 2568);
        SCHEDB();
        acc[5] = MFMA16(cat8(A0[4][0], A0[4][1]), U0[1], acc[5]);
        A1[5][1] = trread(kb1 + 2560);
        SCHEDB();
        acc[6] = MFMA16(cat8(A0[4][0], A0[4][1]), U0[2], acc[6]);
        A1[6][0] = trread(kb1 + 3080);
        SCHEDB();
        acc[7] = MFMA16(cat8(A0[4][0], A0[4][1]), U0[3], acc[7]);
        A1[6][1] = trread(kb1 + 3072);
        SCHEDB();
        acc[5] = MFMA16(cat8(A0[5][0], A0[5][1]), U0[0], acc[5]);
        A1[7][0] = trread(kb1 + 3592);
        SCHEDB();
        acc[6] = MFMA16(cat8(A0[5][0], A0[5][1]), U0[1], acc[6]);
        A1[7][1] = trread(kb1 + 3584);
        SCHEDB();
        acc[7] = MFMA16(cat8(A0[5][0], A0[5][1]), U0[2], acc[7]);
        SCHEDB();
        acc[6] = MFMA16(cat8(A0[6][0], A0[6][1]), U0[0], acc[6]);
        SCHEDB();
        acc[7] = MFMA16(cat8(A0[6][0], A0[6][1]), U0[1], acc[7]);
        SCHEDB();
        acc[7] = MFMA16(cat8(A0[7][0], A0[7][1]), U0[0], acc[7]);
        SCHEDB();
        acc[0] = MFMA16(cat8(A1[0][0], A1[0][1]), U1[0], acc[0]); SCHEDB();
        acc[1] = MFMA16(cat8(A1[0][0], A1[0][1]), U1[1], acc[1]); SCHEDB();
        acc[2] = MFMA16(cat8(A1[0][0], A1[0][1]), U1[2], acc[2]); SCHEDB();
        acc[3] = MFMA16(cat8(A1[0][0], A1[0][1]), U1[3], acc[3]); SCHEDB();
        acc[4] = MFMA16(cat8(A1[0][0], A1[0][1]), U1[4], acc[4]); SCHEDB();
        acc[5] = MFMA16(cat8(A1[0][0], A1[0][1]), U1[5], acc[5]); SCHEDB();
        acc[6] = MFMA16(cat8(A1[0][0], A1[0][1]), U1[6], acc[6]); SCHEDB();
        acc[7] = MFMA16(cat8(A1[0][0], A1[0][1]), U1[7], acc[7]); SCHEDB();
        acc[1] = MFMA16(cat8(A1[1][0], A1[1][1]), U1[0], acc[1]); SCHEDB();
        acc[2] = MFMA16(cat8(A1[1][0], A1[1][1]), U1[1], acc[2]); SCHEDB();
        acc[3] = MFMA16(cat8(A1[1][0], A1[1][1]), U1[2], acc[3]); SCHEDB();
        acc[4] = MFMA16(cat8(A1[1][0], A1[1][1]), U1[3], acc[4]); SCHEDB();
        acc[5] = MFMA16(cat8(A1[1][0], A1[1][1]), U1[4], acc[5]); SCHEDB();
        acc[6] = MFMA16(cat8(A1[1][0], A1[1][1]), U1[5], acc[6]); SCHEDB();
        acc[7] = MFMA16(cat8(A1[1][0], A1[1][1]), U1[6], acc[7]); SCHEDB();
        acc[2] = MFMA16(cat8(A1[2][0], A1[2][1]), U1[0], acc[2]); SCHEDB();
        acc[3] = MFMA16(cat8(A1[2][0], A1[2][1]), U1[1], acc[3]); SCHEDB();
        acc[4] = MFMA16(cat8(A1[2][0], A1[2][1]), U1[2], acc[4]); SCHEDB();
        acc[5] = MFMA16(cat8(A1[2][0], A1[2][1]), U1[3], acc[5]); SCHEDB();
        acc[6] = MFMA16(cat8(A1[2][0], A1[2][1]), U1[4], acc[6]); SCHEDB();
        acc[7] = MFMA16(cat8(A1[2][0], A1[2][1]), U1[5], acc[7]); SCHEDB();
        acc[3] = MFMA16(cat8(A1[3][0], A1[3][1]), U1[0], acc[3]); SCHEDB();
        acc[4] = MFMA16(cat8(A1[3][0], A1[3][1]), U1[1], acc[4]); SCHEDB();
        acc[5] = MFMA16(cat8(A1[3][0], A1[3][1]), U1[2], acc[5]); SCHEDB();
        acc[6] = MFMA16(cat8(A1[3][0], A1[3][1]), U1[3], acc[6]); SCHEDB();
        acc[7] = MFMA16(cat8(A1[3][0], A1[3][1]), U1[4], acc[7]); SCHEDB();
        acc[4] = MFMA16(cat8(A1[4][0], A1[4][1]), U1[0], acc[4]); SCHEDB();
        acc[5] = MFMA16(cat8(A1[4][0], A1[4][1]), U1[1], acc[5]); SCHEDB();
        acc[6] = MFMA16(cat8(A1[4][0], A1[4][1]), U1[2], acc[6]); SCHEDB();
        acc[7] = MFMA16(cat8(A1[4][0], A1[4][1]), U1[3], acc[7]); SCHEDB();
        acc[5] = MFMA16(cat8(A1[5][0], A1[5][1]), U1[0], acc[5]); SCHEDB();
        acc[6] = MFMA16(cat8(A1[5][0], A1[5][1]), U1[1], acc[6]); SCHEDB();
        acc[7] = MFMA16(cat8(A1[5][0], A1[5][1]), U1[2], acc[7]); SCHEDB();
        acc[6] = MFMA16(cat8(A1[6][0], A1[6][1]), U1[0], acc[6]); SCHEDB();
        acc[7] = MFMA16(cat8(A1[6][0], A1[6][1]), U1[1], acc[7]); SCHEDB();
        acc[7] = MFMA16(cat8(A1[7][0], A1[7][1]), U1[0], acc[7]); SCHEDB();
    }
    STAMP(ws, 0, 5);
    u32x4 arr = {0, 0, 0, 0};
    if (b == 0 && wv == 0) arr = __builtin_amdgcn_raw_buffer_load_b128(wsr, WS_AR + lane * 16, 0, 16);
    lds_sync(smem, sb, 8, lane);
    {
        char* rb = smem + sB + w * 6144 + lane * 16;
        int slot = 0;
#pragma unroll
        for (int tau = 0; tau < 8; ++tau) { if ((tau >> 1) != w) { *(f32x4*)(rb + slot * 1024) = acc[tau]; ++slot; } }
    }
    lds_sync(smem, sb, 12, lane);
    {
#pragma unroll
        for (int i = 0; i < 2; ++i) {
            f32x4 y;
            if (w == 0) y = acc[i]; else if (w == 1) y = acc[2 + i]; else if (w == 2) y = acc[4 + i]; else y = acc[6 + i];
            const int tau = 2 * w + i;
#pragma unroll
            for (int ow = 0; ow < 4; ++ow) {
                if (ow != w) {
                    const int sl = (tau < 2 * ow) ? tau : tau - 2;
                    y += *(const f32x4*)(smem + sB + ow * 6144 + sl * 1024 + lane * 16);
                }
            }
            const int l0 = 256 * tau + 16 * al + 4 * g;
            const u32x2 uv = *(const u32x2*)(smem + sU + 480 + 2 * l0), qv = *(const u32x2*)(smem + sU + 4608 + 2 * l0);
            float hv[4];
#pragma unroll
            for (int r = 0; r < 4; ++r) {
                const u32 uu = uv[r >> 1], qq = qv[r >> 1];
                const float u = (r & 1) ? bfhi(uu) : bflo(uu), q0 = (r & 1) ? bfhi(qq) : bflo(qq);
                const u32 yk = cvtpk(y[r], u * dsk);
                hv[r] = q0 + rbf(bflo(yk) + bfhi(yk));
            }
            st8<AUX_H>(wsr, WS_HC + (d * L_ + l0) * 2, u32x2{cvtpk(hv[0], hv[1]), cvtpk(hv[2], hv[3])});
        }
    }
    STAMP(ws, 0, 6);
    if (b == 0 && wv == 0) {
        for (int spin = 0; spin < 100000; ++spin) {
            if (__all(arr[0] == tag && arr[1] == tag && arr[2] == tag && arr[3] == tag)) break;
            asm volatile("" ::: "memory");
            arr = __builtin_amdgcn_raw_buffer_load_b128(wsr, WS_AR + lane * 16, 0, 16);
        }
        if (lane == 0) __hip_atomic_store((GAS u32*)(ws + WS_EP), tag, __ATOMIC_RELAXED, __HIP_MEMORY_SCOPE_AGENT);
    }
}

#define G1_UNROLL 1
constexpr int G1_STAGE = 40960, G1_NS = 4, G1_NST = 10;
constexpr int G1_LDS = G1_NS * G1_STAGE;

DI void glds16(const void* g, u32 lds_base) {
    u32 sv; asm volatile("s_mov_b32 %0, m0\n\ts_mov_b32 m0, %2\n\ts_nop 0\n\tglobal_load_lds_dwordx4 %1, off\n\ts_mov_b32 m0, %0" : "=&s"(sv) : "v"(g), "s"(lds_base) : "memory");
}

__global__ __launch_bounds__(768, 3) void k3_gemm1(char* __restrict__ ws) {
    __shared__ __attribute__((aligned(1024))) char smem[G1_LDS];
    const int b = blockIdx.x, t = threadIdx.x, lane = t & 63;
    const int w = __builtin_amdgcn_readfirstlane(t >> 6);
    const int xcd = b & 7, j = b >> 3;
    const int P = (xcd & 1) * 8 + (j & 7), S = (xcd >> 1) * 4 + (j >> 3);
    const u32 lds0 = (u32)(uintptr_t)smem;
    const rsrc_t wsr = mkrsrc(ws, WS_END);
    if (w >= 4) {
        const int lw = w - 4;
        const char* wsrc = ws + WS_W1T + S * 10 * 24576 + lw * 1024 + lane * 16;
        const int hk = 16 * ((lw >> 2) & 1) + 4 * ((lw >> 1) & 1) + 8 * ((lane >> 3) & 1) + ((lane >> 1) & 3);
        const char* hsrc = ws + WS_HC + (hk * L_ + 128 * P + 64 * (lw & 1) + 16 * (lane >> 4) + 8 * (lane & 1)) * 2;
        auto issue = [&](int st, int slot) {
            const u32 base = lds0 + slot * G1_STAGE;
#pragma unroll
            for (int i = 0; i < 2; ++i) glds16(hsrc + (st * 64 + 32 * i) * L_ * 2, base + 24576 + (lw + 8 * i) * 1024);
#pragma unroll
            for (int i = 0; i < 3; ++i) glds16(wsrc + st * 24576 + i * 8192, base + lw * 1024 + i * 8192);
        };
#define ISSUE(a, b) issue(a, b)
        issue(0, 0); issue(1, 1);
        waitv<5>(); BAR();
        issue(2, 2);
        waitv<5>(); BAR();
        ISSUE(3, 3);
        waitv<5>(); BAR();
        ISSUE(4, 0);
        waitv<5>(); BAR();
        ISSUE(5, 1);
        waitv<5>(); BAR();
        ISSUE(6, 2);
        waitv<5>(); BAR();
        ISSUE(7, 3);
        waitv<5>(); BAR();
        ISSUE(8, 0);
        waitv<5>(); BAR();
        ISSUE(9, 1);
        waitv<5>(); BAR();
        waitv<0>(); BAR();
        BAR();
        BAR();
        return;
    }
    STAMP(ws, 4, 0);
    const int wm = w & 1, wn = w >> 1;
    const int g = lane >> 4, q = (lane & 15) >> 2, p = lane & 3;
    f32x4 acc[6][4];
#pragma unroll
    for (int a = 0; a < 6; ++a)
#pragma unroll
        for (int c = 0; c < 4; ++c) acc[a][c] = f32x4{0.f, 0.f, 0.f, 0.f};
    const LAS char* abase = (const LAS char*)smem + wm * 6144 + lane * 16;
    const LAS char* bbase = (const LAS char*)smem + 24576 + (g >> 1) * 4096 + (g & 1) * 128 + q * 32 + 8 * p + wn * 1024;
    const LAS char* sbase = (const LAS char*)smem + 24576 + (2 * w + (lane >> 5)) * 2048 + (S >> 1) * 256 + ((lane >> 2) & 7) * 32 + (S & 1) * 16 + (lane & 3) * 4;
    float s1a = 0.f, s1b = 0.f, s2a = 0.f, s2b = 0.f;
    bf16x8 AL[3], AH[3]; s16x4 B0[4][2], B1[4][2];
    u32 sv;
    BAR();
    STAMP(ws, 4, 1);
    AL[0] = *(const LAS bf16x8*)(abase + 0); B0[0][0] = trread(bbase + 0); B0[0][1] = trread(bbase + 2048); B0[1][0] = trread(bbase + 256); B0[1][1] = trread(bbase + 2304); AL[1] = *(const LAS bf16x8*)(abase + 1024); B0[2][0] = trread(bbase + 512); B0[2][1] = trread(bbase + 2560); B0[3][0] = trread(bbase + 768); B0[3][1] = trread(bbase + 2816); AL[2] = *(const LAS bf16x8*)(abase + 2048);
    SCHEDB();
#pragma unroll 1
    for (int st = 0; st < G1_NST; ++st) {
        acc[0+0][0] = MFMA16(AL[0], cat8(B0[0][0], B0[0][1]), acc[0+0][0]);
        AH[0] = *(const LAS bf16x8*)(abase + 3072);
        SCHEDB();
        acc[0+0][1] = MFMA16(AL[0], cat8(B0[1][0], B0[1][1]), acc[0+0][1]);
        AH[1] = *(const LAS bf16x8*)(abase + 4096);
        SCHEDB();
        acc[0+1][0] = MFMA16(AL[1], cat8(B0[0][0], B0[0][1]), acc[0+1][0]);
        B1[0][0] = trread(bbase + 8192);
        SCHEDB();
        acc[0+1][1] = MFMA16(AL[1], cat8(B0[1][0], B0[1][1]), acc[0+1][1]);
        B1[0][1] = trread(bbase + 10240);
        SCHEDB();
        acc[0+0][2] = MFMA16(AL[0], cat8(B0[2][0], B0[2][1]), acc[0+0][2]);
        B1[1][0] = trread(bbase + 8448);
        SCHEDB();
        acc[0+1][2] = MFMA16(AL[1], cat8(B0[2][0], B0[2][1]), acc[0+1][2]);
        B1[1][1] = trread(bbase + 10496);
        SCHEDB();
        acc[0+0][3] = MFMA16(AL[0], cat8(B0[3][0], B0[3][1]), acc[0+0][3]);
        AH[2] = *(const LAS bf16x8*)(abase + 5120);
        SCHEDB();
        acc[0+1][3] = MFMA16(AL[1], cat8(B0[3][0], B0[3][1]), acc[0+1][3]);
        SCHEDB();
        acc[0+2][0] = MFMA16(AL[2], cat8(B0[0][0], B0[0][1]), acc[0+2][0]);
        SCHEDB();
        acc[0+2][1] = MFMA16(AL[2], cat8(B0[1][0], B0[1][1]), acc[0+2][1]);
        SCHEDB();
        acc[0+2][2] = MFMA16(AL[2], cat8(B0[2][0], B0[2][1]), acc[0+2][2]);
        SCHEDB();
        acc[0+2][3] = MFMA16(AL[2], cat8(B0[3][0], B0[3][1]), acc[0+2][3]);
        SCHEDB();
        acc[3+0][0] = MFMA16(AH[0], cat8(B0[0][0], B0[0][1]), acc[3+0][0]);
        AL[0] = *(const LAS bf16x8*)(abase + 12288);
        SCHEDB();
        acc[3+0][1] = MFMA16(AH[0], cat8(B0[1][0], B0[1][1]), acc[3+0][1]);
        AL[1] = *(const LAS bf16x8*)(abase + 13312);
        SCHEDB();
        acc[3+1][0] = MFMA16(AH[1], cat8(B0[0][0], B0[0][1]), acc[3+1][0]);
        sv = *(const LAS u32*)(sbase);
        SCHEDB();
        acc[3+1][1] = MFMA16(AH[1], cat8(B0[1][0], B0[1][1]), acc[3+1][1]);
        B1[2][0] = trread(bbase + 8704);
        SCHEDB();
        acc[3+0][2] = MFMA16(AH[0], cat8(B0[2][0], B0[2][1]), acc[3+0][2]);
        B1[2][1] = trread(bbase + 10752);
        SCHEDB();
        acc[3+1][2] = MFMA16(AH[1], cat8(B0[2][0], B0[2][1]), acc[3+1][2]);
        B1[3][0] = trread(bbase + 8960);
        SCHEDB();
        acc[3+0][3] = MFMA16(AH[0], cat8(B0[3][0], B0[3][1]), acc[3+0][3]);
        B1[3][1] = trread(bbase + 11008);
        SCHEDB();
        acc[3+1][3] = MFMA16(AH[1], cat8(B0[3][0], B0[3][1]), acc[3+1][3]);
        AL[2] = *(const LAS bf16x8*)(abase + 14336);
        SCHEDB();
        acc[3+2][0] = MFMA16(AH[2], cat8(B0[0][0], B0[0][1]), acc[3+2][0]);
        SCHEDB();
        acc[3+2][1] = MFMA16(AH[2], cat8(B0[1][0], B0[1][1]), acc[3+2][1]);
        SCHEDB();
        acc[3+2][2] = MFMA16(AH[2], cat8(B0[2][0], B0[2][1]), acc[3+2][2]);
        SCHEDB();
        acc[3+2][3] = MFMA16(AH[2], cat8(B0[3][0], B0[3][1]), acc[3+2][3]);
        SCHEDB();
        acc[0+0][0] = MFMA16(AL[0], cat8(B1[0][0], B1[0][1]), acc[0+0][0]);
        AH[0] = *(const LAS bf16x8*)(abase + 15360);
        SCHEDB();
        acc[0+0][1] = MFMA16(AL[0], cat8(B1[1][0], B1[1][1]), acc[0+0][1]);
        AH[1] = *(const LAS bf16x8*)(abase + 16384);
        SCHEDB();
        acc[0+1][0] = MFMA16(AL[1], cat8(B1[0][0], B1[0][1]), acc[0+1][0]);
        AH[2] = *(const LAS bf16x8*)(abase + 17408);
        SCHEDB();
        acc[0+1][1] = MFMA16(AL[1], cat8(B1[1][0], B1[1][1]), acc[0+1][1]);
        SCHEDB();
        acc[0+0][2] = MFMA16(AL[0], cat8(B1[2][0], B1[2][1]), acc[0+0][2]);
        SCHEDB();
        acc[0+1][2] = MFMA16(AL[1], cat8(B1[2][0], B1[2][1]), acc[0+1][2]);
        SCHEDB();
        acc[0+0][3] = MFMA16(AL[0], cat8(B1[3][0], B1[3][1]), acc[0+0][3]);
        SCHEDB();
        acc[0+1][3] = MFMA16(AL[1], cat8(B1[3][0], B1[3][1]), acc[0+1][3]);
        SCHEDB();
        acc[0+2][0] = MFMA16(AL[2], cat8(B1[0][0], B1[0][1]), acc[0+2][0]);
        SCHEDB();
        acc[0+2][1] = MFMA16(AL[2], cat8(B1[1][0], B1[1][1]), acc[0+2][1]);
        SCHEDB();
        acc[0+2][2] = MFMA16(AL[2], cat8(B1[2][0], B1[2][1]), acc[0+2][2]);
        SCHEDB();
        acc[0+2][3] = MFMA16(AL[2], cat8(B1[3][0], B1[3][1]), acc[0+2][3]);
        SCHEDB();
        { const float a = bflo(sv), c = bfhi(sv); s1a += a; s1b += c; s2a = __builtin_fmaf(a, a, s2a); s2b = __builtin_fmaf(c, c, s2b); }
        WAITL0();
        BAR();
        { const int dlt = (st & 3) == 3 ? -3 * G1_STAGE : G1_STAGE; abase += dlt; bbase += dlt; sbase += dlt; }
        acc[3+0][0] = MFMA16(AH[0], cat8(B1[0][0], B1[0][1]), acc[3+0][0]);
        AL[0] = *(const LAS bf16x8*)(abase + 0);
        SCHEDB();
        acc[3+0][1] = MFMA16(AH[0], cat8(B1[1][0], B1[1][1]), acc[3+0][1]);
        B0[0][0] = trread(bbase + 0);
        SCHEDB();
        acc[3+1][0] = MFMA16(AH[1], cat8(B1[0][0], B1[0][1]), acc[3+1][0]);
        B0[0][1] = trread(bbase + 2048);
        SCHEDB();
        acc[3+1][1] = MFMA16(AH[1], cat8(B1[1][0], B1[1][1]), acc[3+1][1]);
        B0[1][0] = trread(bbase + 256);
        SCHEDB();
        acc[3+0][2] = MFMA16(AH[0], cat8(B1[2][0], B1[2][1]), acc[3+0][2]);
        B0[1][1] = trread(bbase + 2304);
        SCHEDB();
        acc[3+1][2] = MFMA16(AH[1], cat8(B1[2][0], B1[2][1]), acc[3+1][2]);
        AL[1] = *(const LAS bf16x8*)(abase + 1024);
        SCHEDB();
        acc[3+0][3] = MFMA16(AH[0], cat8(B1[3][0], B1[3][1]), acc[3+0][3]);
        B0[2][0] = trread(bbase + 512);
        SCHEDB();
        acc[3+1][3] = MFMA16(AH[1], cat8(B1[3][0], B1[3][1]), acc[3+1][3]);
        B0[2][1] = trread(bbase + 2560);
        SCHEDB();
        acc[3+2][0] = MFMA16(AH[2], cat8(B1[0][0], B1[0][1]), acc[3+2][0]);
        B0[3][0] = trread(bbase + 768);
        SCHEDB();
        acc[3+2][1] = MFMA16(AH[2], cat8(B1[1][0], B1[1][1]), acc[3+2][1]);
        B0[3][1] = trread(bbase + 2816);
        SCHEDB();
        acc[3+2][2] = MFMA16(AH[2], cat8(B1[2][0], B1[2][1]), acc[3+2][2]);
        AL[2] = *(const LAS bf16x8*)(abase + 2048);
        SCHEDB();
        acc[3+2][3] = MFMA16(AH[2], cat8(B1[3][0], B1[3][1]), acc[3+2][3]);
        SCHEDB();
    }
    STAMP(ws, 4, 3);
    {
        float v[4] = {s1a, s1b, s2a, s2b};
#pragma unroll
        for (int i = 0; i < 4; ++i) { v[i] = dppadd<0x114>(v[i]); v[i] = dppadd<0x118>(v[i]); v[i] = swap32add(swap16add(v[i])); }
        if (lane >= 12 && lane < 16) *(f32x4*)(smem + (w * 4 + (lane & 3)) * 16) = f32x4{v[0], v[1], v[2], v[3]};
        WAITL0();
    }
    BAR();
    if (w == 0 && lane < 8) {
        float s1 = 0.f, s2 = 0.f;
#pragma unroll
        for (int ow = 0; ow < 4; ++ow) { const f32x4 v = *(const f32x4*)(smem + (ow * 4 + (lane >> 1)) * 16); s1 += (lane & 1) ? v[1] : v[0]; s2 += (lane & 1) ? v[3] : v[2]; }
        const float mean = s1 * (1.f / 640.f);
        float var = s2 * (1.f / 640.f) - mean * mean;
        var = var > 0.f ? var : 0.f;
        const float rs = 1.f / rbf(__builtin_sqrtf(rbf(rbf(var) + 1e-5f)));
        st4(wsr, WS_RS + (128 * P + 8 * S + lane) * 4, __float_as_uint(rs * rs));
    }
    const rsrc_t rs = wsr;
#pragma unroll
    for (int a = 0; a < 3; ++a) {
        const int f0 = 96 * S + 16 * (3 * wm + a);
        const int kb = f0 >> 5, oct = ((f0 & 31) >> 3) + (g >> 1);
#pragma unroll
        for (int c = 0; c < 4; c += 2) {
            u32 X[2], Y[2];
#pragma unroll
            for (int e = 0; e < 2; ++e) {
                X[e] = cvtpk(__builtin_fmaxf(acc[2 * a][c][2 * e], 0.f) * acc[2 * a + 1][c][2 * e], __builtin_fmaxf(acc[2 * a][c][2 * e + 1], 0.f) * acc[2 * a + 1][c][2 * e + 1]);
                Y[e] = cvtpk(__builtin_fmaxf(acc[2 * a][c + 1][2 * e], 0.f) * acc[2 * a + 1][c + 1][2 * e], __builtin_fmaxf(acc[2 * a][c + 1][2 * e + 1], 0.f) * acc[2 * a + 1][c + 1][2 * e + 1]);
            }
#pragma unroll
            for (int e = 0; e < 2; ++e) { auto r = __builtin_amdgcn_permlane16_swap(X[e], Y[e], false, false); X[e] = r[0]; Y[e] = r[1]; }
            const int lt = 8 * P + 4 * wn + c + (g & 1);
            st16<AUX_HM>(rs, WS_HM + ((lt * 48 + kb) * 64 + (lane & 15) + 16 * oct) * 16, u32x4{X[0], X[1], Y[0], Y[1]});
        }
    }
    STAMP(ws, 4, 4);
}

constexpr int G2_STAGE = 18432, G2_NS = 8, G2_NST = 24;
constexpr int G2_RES = G2_NS * G2_STAGE;
constexpr int G2_LDS = G2_RES + 10240;

__global__ __launch_bounds__(768, 3) void k4_gemm2(char* __restrict__ ws, u16* __restrict__ out) {
    __shared__ __attribute__((aligned(1024))) char smem[G2_LDS];
    const int b = blockIdx.x, t = threadIdx.x, lane = t & 63;
    const int w = __builtin_amdgcn_readfirstlane(t >> 6);
    const int xcd = b & 7, j = b >> 3;
    const int P = (xcd & 3) * 8 + (j & 7), S = (xcd >> 2) * 4 + (j >> 3);
    const u32 lds0 = (u32)(uintptr_t)smem;
    if (w >= 4) {
        const int lw = w - 4;
        const char* wsrc = ws + WS_W2T + S * 24 * 10240 + lw * 1024 + lane * 16;
        const char* msrc = ws + WS_HM + ((4 * P + (lw >> 1)) * 48 + (lw & 1)) * 1024 + lane * 16;
        auto issue = [&](int st, int slot) {
            const u32 base = lds0 + slot * G2_STAGE;
            glds16(msrc + st * 2048, base + 10240 + lw * 1024);
            glds16(wsrc + st * 10240, base + lw * 1024);
            if (lw < 2) glds16(wsrc + st * 10240 + 8192, base + 8192 + lw * 1024);
        };
        auto residual = [&]() {
            for (int pc = lw; pc < 10; pc += 8) {
                const int row = 8 * pc + (lane >> 3), ch = (lane & 7) ^ (((lane >> 3) & 3) << 1);
                glds16(ws + WS_HC + ((80 * S + row) * L_ + 64 * P + 8 * ch) * 2, lds0 + G2_RES + pc * 1024);
            }
        };
        if (lw < 2) {
            issue(0, 0); issue(1, 1); issue(2, 2);
            waitv<6>(); BAR();
            waitv<3>(); BAR();
            issue(3, 3);
            issue(4, 4);
            waitv<6>(); BAR();
            issue(5, 5);
            issue(6, 6);
            waitv<9>(); BAR();
            issue(7, 7);
            issue(8, 0);
            waitv<12>(); BAR();
            issue(9, 1);
            issue(10, 2);
            waitv<15>(); BAR();
            issue(11, 3);
            waitv<15>(); BAR();
            issue(12, 4);
            waitv<15>(); BAR();
            issue(13, 5);
            waitv<15>(); BAR();
            issue(14, 6);
            waitv<15>(); BAR();
            issue(15, 7);
            waitv<15>(); BAR();
            issue(16, 0);
            waitv<15>(); BAR();
            issue(17, 1);
            waitv<15>(); BAR();
            issue(18, 2);
            waitv<15>(); BAR();
            issue(19, 3);
            waitv<15>(); BAR();
            issue(20, 4);
            waitv<15>(); BAR();
            issue(21, 5);
            waitv<15>(); BAR();
            issue(22, 6);
            waitv<15>(); BAR();
            issue(23, 7);
            waitv<15>(); BAR();
            residual();
            waitv<12>(); BAR();
            waitv<9>(); BAR();
            waitv<6>(); BAR();
            waitv<3>(); BAR();
            waitv<0>(); BAR();
        } else {
            issue(0, 0); issue(1, 1); issue(2, 2);
            waitv<4>(); BAR();
            waitv<2>(); BAR();
            issue(3, 3);
            issue(4, 4);
            waitv<4>(); BAR();
            issue(5, 5);
            issue(6, 6);
            waitv<6>(); BAR();
            issue(7, 7);
            issue(8, 0);
            waitv<8>(); BAR();
            issue(9, 1);
            issue(10, 2);
            waitv<10>(); BAR();
            issue(11, 3);
            waitv<10>(); BAR();
            issue(12, 4);
            waitv<10>(); BAR();
            issue(13, 5);
            waitv<10>(); BAR();
            issue(14, 6);
            waitv<10>(); BAR();
            issue(15, 7);
            waitv<10>(); BAR();
            issue(16, 0);
            waitv<10>(); BAR();
            issue(17, 1);
            waitv<10>(); BAR();
            issue(18, 2);
            waitv<10>(); BAR();
            issue(19, 3);
            waitv<10>(); BAR();
            issue(20, 4);
            waitv<10>(); BAR();
            issue(21, 5);
            waitv<10>(); BAR();
            issue(22, 6);
            waitv<10>(); BAR();
            issue(23, 7);
            waitv<10>(); BAR();
            residual();
            waitv<8>(); BAR();
            waitv<6>(); BAR();
            waitv<4>(); BAR();
            waitv<2>(); BAR();
            waitv<0>(); BAR();
        }
        return;
    }
    STAMP(ws, 5, 0);
    const int g = lane >> 4, q = (lane & 15) >> 2, p = lane & 3;
    const int l = 64 * P + 16 * w + (lane & 15);
    const float rs2 = *((const float*)(ws + WS_RS) + l);
    const float m1 = *((const float*)(ws + WS_ST) + l);
    f32x4 acc[5];
#pragma unroll
    for (int a = 0; a < 5; ++a) acc[a] = f32x4{0.f, 0.f, 0.f, 0.f};
    const LAS char* abase = (const LAS char*)smem + lane * 16;
    const LAS char* bbase = (const LAS char*)smem + 10240 + w * 2048 + lane * 16;
    bf16x8 EA[2][5], EB[2], OA[2][5], OB[2];
    BAR();
    STAMP(ws, 5, 1);
    EB[0] = *(const LAS bf16x8*)(bbase); EB[1] = *(const LAS bf16x8*)(bbase + 1024);
#pragma unroll
    for (int a = 0; a < 5; ++a) { EA[0][a] = *(const LAS bf16x8*)(abase + a * 1024); EA[1][a] = *(const LAS bf16x8*)(abase + 5120 + a * 1024); }
    SCHEDB();
#pragma unroll 1
    for (int it = 0; it < 3; ++it) {
        if (it == 1) STAMP(ws, 5, 2); if (it == 2) STAMP(ws, 5, 3);
        BAR();
        acc[0] = MFMA16(EA[0][0], EB[0], acc[0]);
        OB[0] = *(const LAS bf16x8*)(bbase + 1*G2_STAGE);
        OA[0][0] = *(const LAS bf16x8*)(abase + 1*G2_STAGE + 0);
        SCHEDB();
        acc[1] = MFMA16(EA[0][1], EB[0], acc[1]);
        OA[0][1] = *(const LAS bf16x8*)(abase + 1*G2_STAGE + 1024);
        OA[0][2] = *(const LAS bf16x8*)(abase + 1*G2_STAGE + 2048);
        SCHEDB();
        acc[2] = MFMA16(EA[0][2], EB[0], acc[2]);
        OA[0][3] = *(const LAS bf16x8*)(abase + 1*G2_STAGE + 3072);
        SCHEDB();
        acc[3] = MFMA16(EA[0][3], EB[0], acc[3]);
        OA[0][4] = *(const LAS bf16x8*)(abase + 1*G2_STAGE + 4096);
        SCHEDB();
        acc[4] = MFMA16(EA[0][4], EB[0], acc[4]);
        OB[1] = *(const LAS bf16x8*)(bbase + 1*G2_STAGE + 1024);
        SCHEDB();
        acc[0] = MFMA16(EA[1][0], EB[1], acc[0]);
        OA[1][0] = *(const LAS bf16x8*)(abase + 1*G2_STAGE + 5120);
        SCHEDB();
        acc[1] = MFMA16(EA[1][1], EB[1], acc[1]);
        OA[1][1] = *(const LAS bf16x8*)(abase + 1*G2_STAGE + 6144);
        SCHEDB();
        acc[2] = MFMA16(EA[1][2], EB[1], acc[2]);
        OA[1][2] = *(const LAS bf16x8*)(abase + 1*G2_STAGE + 7168);
        SCHEDB();
        acc[3] = MFMA16(EA[1][3], EB[1], acc[3]);
        OA[1][3] = *(const LAS bf16x8*)(abase + 1*G2_STAGE + 8192);
        SCHEDB();
        acc[4] = MFMA16(EA[1][4], EB[1], acc[4]);
        OA[1][4] = *(const LAS bf16x8*)(abase + 1*G2_STAGE + 9216);
        SCHEDB();
        BAR();
        acc[0] = MFMA16(OA[0][0], OB[0], acc[0]);
        EB[0] = *(const LAS bf16x8*)(bbase + 2*G2_STAGE);
        EA[0][0] = *(const LAS bf16x8*)(abase + 2*G2_STAGE + 0);
        SCHEDB();
        acc[1] = MFMA16(OA[0][1], OB[0], acc[1]);
        EA[0][1] = *(const LAS bf16x8*)(abase + 2*G2_STAGE + 1024);
        EA[0][2] = *(const LAS bf16x8*)(abase + 2*G2_STAGE + 2048);
        SCHEDB();
        acc[2] = MFMA16(OA[0][2], OB[0], acc[2]);
        EA[0][3] = *(const LAS bf16x8*)(abase + 2*G2_STAGE + 3072);
        SCHEDB();
        acc[3] = MFMA16(OA[0][3], OB[0], acc[3]);
        EA[0][4] = *(const LAS bf16x8*)(abase + 2*G2_STAGE + 4096);
        SCHEDB();
        acc[4] = MFMA16(OA[0][4], OB[0], acc[4]);
        EB[1] = *(const LAS bf16x8*)(bbase + 2*G2_STAGE + 1024);
        SCHEDB();
        acc[0] = MFMA16(OA[1][0], OB[1], acc[0]);
        EA[1][0] = *(const LAS bf16x8*)(abase + 2*G2_STAGE + 5120);
        SCHEDB();
        acc[1] = MFMA16(OA[1][1], OB[1], acc[1]);
        EA[1][1] = *(const LAS bf16x8*)(abase + 2*G2_STAGE + 6144);
        SCHEDB();
        acc[2] = MFMA16(OA[1][2], OB[1], acc[2]);
        EA[1][2] = *(const LAS bf16x8*)(abase + 2*G2_STAGE + 7168);
        SCHEDB();
        acc[3] = MFMA16(OA[1][3], OB[1], acc[3]);
        EA[1][3] = *(const LAS bf16x8*)(abase + 2*G2_STAGE + 8192);
        SCHEDB();
        acc[4] = MFMA16(OA[1][4], OB[1], acc[4]);
        EA[1][4] = *(const LAS bf16x8*)(abase + 2*G2_STAGE + 9216);
        SCHEDB();
        BAR();
        acc[0] = MFMA16(EA[0][0], EB[0], acc[0]);
        OB[0] = *(const LAS bf16x8*)(bbase + 3*G2_STAGE);
        OA[0][0] = *(const LAS bf16x8*)(abase + 3*G2_STAGE + 0);
        SCHEDB();
        acc[1] = MFMA16(EA[0][1], EB[0], acc[1]);
        OA[0][1] = *(const LAS bf16x8*)(abase + 3*G2_STAGE + 1024);
        OA[0][2] = *(const LAS bf16x8*)(abase + 3*G2_STAGE + 2048);
        SCHEDB();
        acc[2] = MFMA16(EA[0][2], EB[0], acc[2]);
        OA[0][3] = *(const LAS bf16x8*)(abase + 3*G2_STAGE + 3072);
        SCHEDB();
        acc[3] = MFMA16(EA[0][3], EB[0], acc[3]);
        OA[0][4] = *(const LAS bf16x8*)(abase + 3*G2_STAGE + 4096);
        SCHEDB();
        acc[4] = MFMA16(EA[0][4], EB[0], acc[4]);
        OB[1] = *(const LAS bf16x8*)(bbase + 3*G2_STAGE + 1024);
        SCHEDB();
        acc[0] = MFMA16(EA[1][0], EB[1], acc[0]);
        OA[1][0] = *(const LAS bf16x8*)(abase + 3*G2_STAGE + 5120);
        SCHEDB();
        acc[1] = MFMA16(EA[1][1], EB[1], acc[1]);
        OA[1][1] = *(const LAS bf16x8*)(abase + 3*G2_STAGE + 6144);
        SCHEDB();
        acc[2] = MFMA16(EA[1][2], EB[1], acc[2]);
        OA[1][2] = *(const LAS bf16x8*)(abase + 3*G2_STAGE + 7168);
        SCHEDB();
        acc[3] = MFMA16(EA[1][3], EB[1], acc[3]);
        OA[1][3] = *(const LAS bf16x8*)(abase + 3*G2_STAGE + 8192);
        SCHEDB();
        acc[4] = MFMA16(EA[1][4], EB[1], acc[4]);
        OA[1][4] = *(const LAS bf16x8*)(abase + 3*G2_STAGE + 9216);
        SCHEDB();
        BAR();
        acc[0] = MFMA16(OA[0][0], OB[0], acc[0]);
        EB[0] = *(const LAS bf16x8*)(bbase + 4*G2_STAGE);
        EA[0][0] = *(const LAS bf16x8*)(abase + 4*G2_STAGE + 0);
        SCHEDB();
        acc[1] = MFMA16(OA[0][1], OB[0], acc[1]);
        EA[0][1] = *(const LAS bf16x8*)(abase + 4*G2_STAGE + 1024);
        EA[0][2] = *(const LAS bf16x8*)(abase + 4*G2_STAGE + 2048);
        SCHEDB();
        acc[2] = MFMA16(OA[0][2], OB[0], acc[2]);
        EA[0][3] = *(const LAS bf16x8*)(abase + 4*G2_STAGE + 3072);
        SCHEDB();
        acc[3] = MFMA16(OA[0][3], OB[0], acc[3]);
        EA[0][4] = *(const LAS bf16x8*)(abase + 4*G2_STAGE + 4096);
        SCHEDB();
        acc[4] = MFMA16(OA[0][4], OB[0], acc[4]);
        EB[1] = *(const LAS bf16x8*)(bbase + 4*G2_STAGE + 1024);
        SCHEDB();
        acc[0] = MFMA16(OA[1][0], OB[1], acc[0]);
        EA[1][0] = *(const LAS bf16x8*)(abase + 4*G2_STAGE + 5120);
        SCHEDB();
        acc[1] = MFMA16(OA[1][1], OB[1], acc[1]);
        EA[1][1] = *(const LAS bf16x8*)(abase + 4*G2_STAGE + 6144);
        SCHEDB();
        acc[2] = MFMA16(OA[1][2], OB[1], acc[2]);
        EA[1][2] = *(const LAS bf16x8*)(abase + 4*G2_STAGE + 7168);
        SCHEDB();
        acc[3] = MFMA16(OA[1][3], OB[1], acc[3]);
        EA[1][3] = *(const LAS bf16x8*)(abase + 4*G2_STAGE + 8192);
        SCHEDB();
        acc[4] = MFMA16(OA[1][4], OB[1], acc[4]);
        EA[1][4] = *(const LAS bf16x8*)(abase + 4*G2_STAGE + 9216);
        SCHEDB();
        BAR();
        acc[0] = MFMA16(EA[0][0], EB[0], acc[0]);
        OB[0] = *(const LAS bf16x8*)(bbase + 5*G2_STAGE);
        OA[0][0] = *(const LAS bf16x8*)(abase + 5*G2_STAGE + 0);
        SCHEDB();
        acc[1] = MFMA16(EA[0][1], EB[0], acc[1]);
        OA[0][1] = *(const LAS bf16x8*)(abase + 5*G2_STAGE + 1024);
        OA[0][2] = *(const LAS bf16x8*)(abase + 5*G2_STAGE + 2048);
        SCHEDB();
        acc[2] = MFMA16(EA[0][2], EB[0], acc[2]);
        OA[0][3] = *(const LAS bf16x8*)(abase + 5*G2_STAGE + 3072);
        SCHEDB();
        acc[3] = MFMA16(EA[0][3], EB[0], acc[3]);
        OA[0][4] = *(const LAS bf16x8*)(abase + 5*G2_STAGE + 4096);
        SCHEDB();
        acc[4] = MFMA16(EA[0][4], EB[0], acc[4]);
        OB[1] = *(const LAS bf16x8*)(bbase + 5*G2_STAGE + 1024);
        SCHEDB();
        acc[0] = MFMA16(EA[1][0], EB[1], acc[0]);
        OA[1][0] = *(const LAS bf16x8*)(abase + 5*G2_STAGE + 5120);
        SCHEDB();
        acc[1] = MFMA16(EA[1][1], EB[1], acc[1]);
        OA[1][1] = *(const LAS bf16x8*)(abase + 5*G2_STAGE + 6144);
        SCHEDB();
        acc[2] = MFMA16(EA[1][2], EB[1], acc[2]);
        OA[1][2] = *(const LAS bf16x8*)(abase + 5*G2_STAGE + 7168);
        SCHEDB();
        acc[3] = MFMA16(EA[1][3], EB[1], acc[3]);
        OA[1][3] = *(const LAS bf16x8*)(abase + 5*G2_STAGE + 8192);
        SCHEDB();
        acc[4] = MFMA16(EA[1][4], EB[1], acc[4]);
        OA[1][4] = *(const LAS bf16x8*)(abase + 5*G2_STAGE + 9216);
        SCHEDB();
        BAR();
        acc[0] = MFMA16(OA[0][0], OB[0], acc[0]);
        EB[0] = *(const LAS bf16x8*)(bbase + 6*G2_STAGE);
        EA[0][0] = *(const LAS bf16x8*)(abase + 6*G2_STAGE + 0);
        SCHEDB();
        acc[1] = MFMA16(OA[0][1], OB[0], acc[1]);
        EA[0][1] = *(const LAS bf16x8*)(abase + 6*G2_STAGE + 1024);
        EA[0][2] = *(const LAS bf16x8*)(abase + 6*G2_STAGE + 2048);
        SCHEDB();
        acc[2] = MFMA16(OA[0][2], OB[0], acc[2]);
        EA[0][3] = *(const LAS bf16x8*)(abase + 6*G2_STAGE + 3072);
        SCHEDB();
        acc[3] = MFMA16(OA[0][3], OB[0], acc[3]);
        EA[0][4] = *(const LAS bf16x8*)(abase + 6*G2_STAGE + 4096);
        SCHEDB();
        acc[4] = MFMA16(OA[0][4], OB[0], acc[4]);
        EB[1] = *(const LAS bf16x8*)(bbase + 6*G2_STAGE + 1024);
        SCHEDB();
        acc[0] = MFMA16(OA[1][0], OB[1], acc[0]);
        EA[1][0] = *(const LAS bf16x8*)(abase + 6*G2_STAGE + 5120);
        SCHEDB();
        acc[1] = MFMA16(OA[1][1], OB[1], acc[1]);
        EA[1][1] = *(const LAS bf16x8*)(abase + 6*G2_STAGE + 6144);
        SCHEDB();
        acc[2] = MFMA16(OA[1][2], OB[1], acc[2]);
        EA[1][2] = *(const LAS bf16x8*)(abase + 6*G2_STAGE + 7168);
        SCHEDB();
        acc[3] = MFMA16(OA[1][3], OB[1], acc[3]);
        EA[1][3] = *(const LAS bf16x8*)(abase + 6*G2_STAGE + 8192);
        SCHEDB();
        acc[4] = MFMA16(OA[1][4], OB[1], acc[4]);
        EA[1][4] = *(const LAS bf16x8*)(abase + 6*G2_STAGE + 9216);
        SCHEDB();
        BAR();
        acc[0] = MFMA16(EA[0][0], EB[0], acc[0]);
        OB[0] = *(const LAS bf16x8*)(bbase + 7*G2_STAGE);
        OA[0][0] = *(const LAS bf16x8*)(abase + 7*G2_STAGE + 0);
        SCHEDB();
        acc[1] = MFMA16(EA[0][1], EB[0], acc[1]);
        OA[0][1] = *(const LAS bf16x8*)(abase + 7*G2_STAGE + 1024);
        OA[0][2] = *(const LAS bf16x8*)(abase + 7*G2_STAGE + 2048);
        SCHEDB();
        acc[2] = MFMA16(EA[0][2], EB[0], acc[2]);
        OA[0][3] = *(const LAS bf16x8*)(abase + 7*G2_STAGE + 3072);
        SCHEDB();
        acc[3] = MFMA16(EA[0][3], EB[0], acc[3]);
        OA[0][4] = *(const LAS bf16x8*)(abase + 7*G2_STAGE + 4096);
        SCHEDB();
        acc[4] = MFMA16(EA[0][4], EB[0], acc[4]);
        OB[1] = *(const LAS bf16x8*)(bbase + 7*G2_STAGE + 1024);
        SCHEDB();
        acc[0] = MFMA16(EA[1][0], EB[1], acc[0]);
        OA[1][0] = *(const LAS bf16x8*)(abase + 7*G2_STAGE + 5120);
        SCHEDB();
        acc[1] = MFMA16(EA[1][1], EB[1], acc[1]);
        OA[1][1] = *(const LAS bf16x8*)(abase + 7*G2_STAGE + 6144);
        SCHEDB();
        acc[2] = MFMA16(EA[1][2], EB[1], acc[2]);
        OA[1][2] = *(const LAS bf16x8*)(abase + 7*G2_STAGE + 7168);
        SCHEDB();
        acc[3] = MFMA16(EA[1][3], EB[1], acc[3]);
        OA[1][3] = *(const LAS bf16x8*)(abase + 7*G2_STAGE + 8192);
        SCHEDB();
        acc[4] = MFMA16(EA[1][4], EB[1], acc[4]);
        OA[1][4] = *(const LAS bf16x8*)(abase + 7*G2_STAGE + 9216);
        SCHEDB();
        if (it < 2) BAR();
        acc[0] = MFMA16(OA[0][0], OB[0], acc[0]);
        EB[0] = *(const LAS bf16x8*)(bbase + 0*G2_STAGE);
        EA[0][0] = *(const LAS bf16x8*)(abase + 0*G2_STAGE + 0);
        SCHEDB();
        acc[1] = MFMA16(OA[0][1], OB[0], acc[1]);
        EA[0][1] = *(const LAS bf16x8*)(abase + 0*G2_STAGE + 1024);
        EA[0][2] = *(const LAS bf16x8*)(abase + 0*G2_STAGE + 2048);
        SCHEDB();
        acc[2] = MFMA16(OA[0][2], OB[0], acc[2]);
        EA[0][3] = *(const LAS bf16x8*)(abase + 0*G2_STAGE + 3072);
        SCHEDB();
        acc[3] = MFMA16(OA[0][3], OB[0], acc[3]);
        EA[0][4] = *(const LAS bf16x8*)(abase + 0*G2_STAGE + 4096);
        SCHEDB();
        acc[4] = MFMA16(OA[0][4], OB[0], acc[4]);
        EB[1] = *(const LAS bf16x8*)(bbase + 0*G2_STAGE + 1024);
        SCHEDB();
        acc[0] = MFMA16(OA[1][0], OB[1], acc[0]);
        EA[1][0] = *(const LAS bf16x8*)(abase + 0*G2_STAGE + 5120);
        SCHEDB();
        acc[1] = MFMA16(OA[1][1], OB[1], acc[1]);
        EA[1][1] = *(const LAS bf16x8*)(abase + 0*G2_STAGE + 6144);
        SCHEDB();
        acc[2] = MFMA16(OA[1][2], OB[1], acc[2]);
        EA[1][2] = *(const LAS bf16x8*)(abase + 0*G2_STAGE + 7168);
        SCHEDB();
        acc[3] = MFMA16(OA[1][3], OB[1], acc[3]);
        EA[1][3] = *(const LAS bf16x8*)(abase + 0*G2_STAGE + 8192);
        SCHEDB();
        acc[4] = MFMA16(OA[1][4], OB[1], acc[4]);
        EA[1][4] = *(const LAS bf16x8*)(abase + 0*G2_STAGE + 9216);
        SCHEDB();
    }
    STAMP(ws, 5, 6);
    const rsrc_t ro = mkrsrc(out, L_ * D_ * 2);
#pragma unroll
    for (int a = 0; a < 5; ++a) {
        const int row = 16 * a + 4 * g + q;
        const int ch = (2 * w + (p >> 1)) ^ ((row & 3) << 1);
        const s16x4 hr = trread((const LAS char*)smem + G2_RES + row * 128 + ch * 16 + 8 * (p & 1));
        const u32x2 hh = __builtin_bit_cast(u32x2, hr);
        const u32 f01 = cvtpk(acc[a][0] * rs2, acc[a][1] * rs2), f23 = cvtpk(acc[a][2] * rs2, acc[a][3] * rs2);
        const u32 c01 = hh[0], c23 = hh[1];
        const u32 h01 = cvtpk(bflo(c01) + m1, bfhi(c01) + m1), h23 = cvtpk(bflo(c23) + m1, bfhi(c23) + m1);
        __builtin_amdgcn_raw_buffer_store_b64(u32x2{cvtpk(bflo(h01) + bflo(f01), bfhi(h01) + bfhi(f01)), cvtpk(bflo(h23) + bflo(f23), bfhi(h23) + bfhi(f23))}, ro, (l * D_ + 80 * S + 16 * a + 4 * g) * 2, 0, 0);
    }
    STAMP(ws, 5, 7);
}

extern "C" void kernel_launch(void* const* d_in, const int* in_sizes, int n_in, void* d_out, int out_size, void* d_ws, size_t ws_size, hipStream_t stream) {
    const u16* x = (const u16*)d_in[0];
    const u16* Are = (const u16*)d_in[1]; const u16* Aim = (const u16*)d_in[2];
    const u16* Bre = (const u16*)d_in[3]; const u16* Bim = (const u16*)d_in[4];
    const u16* Cre = (const u16*)d_in[5]; const u16* Cim = (const u16*)d_in[6];
    const u16* logdt = (const u16*)d_in[7]; const u16* Dskip = (const u16*)d_in[8];
    const u16* Wg = (const u16*)d_in[10]; const u16* Wu = (const u16*)d_in[11]; const u16* Wd = (const u16*)d_in[12];
    char* ws = (char*)d_ws;
    ka_ssm<<<256, 768, 0, stream>>>(x, Are, Aim, Bre, Bim, Cre, Cim, logdt, Dskip, Wg, Wu, Wd, ws);
    k3_gemm1<<<256, 768, 0, stream>>>(ws);
    k4_gemm2<<<256, 768, 0, stream>>>(ws, (u16*)d_out);
}
```

```cpp
#include <hip/hip_runtime.h>
#include <stdint.h>

typedef unsigned short u16;
typedef unsigned int u32;
typedef short bf16x8 __attribute__((ext_vector_type(8)));
typedef short s16x4 __attribute__((ext_vector_type(4)));
typedef float f32x2 __attribute__((ext_vector_type(2)));
typedef float f32x4 __attribute__((ext_vector_type(4)));
typedef float f32x16 __attribute__((ext_vector_type(16)));
typedef unsigned u32x4 __attribute__((ext_vector_type(4)));
typedef unsigned u32x2 __attribute__((ext_vector_type(2)));
typedef __bf16 bf16x2_t __attribute__((ext_vector_type(2)));
#define LAS __attribute__((address_space(3)))
#define DI __device__ __forceinline__

#define STAMP(ws, k, j) do {} while (0)
#define STAMP2(ws, k, j) do {} while (0)

constexpr int L_ = 2048, D_ = 640, NM_ = 192, F_ = 1536;

constexpr u32 WS_HC  = 0;
constexpr u32 WS_HM  = WS_HC + D_ * L_ * 2;
constexpr u32 WS_ST  = WS_HM + L_ * F_ * 2;
constexpr u32 WS_RS  = WS_ST + 2048 * 4;
constexpr u32 WS_W1T = WS_RS + 2048 * 4;
constexpr u32 WS_W2T = WS_W1T + D_ * F_ * 2 * 2;
constexpr u32 WS_GR  = WS_W2T + F_ * D_ * 2;
constexpr u32 WS_F2  = WS_GR + 2048 * 8;
constexpr u32 WS_FW  = WS_F2 + 256 * 64;
constexpr u32 WS_F3  = WS_FW + 128 * 64;
constexpr u32 WS_EP  = WS_F3 + 256 * 64;
constexpr u32 WS_END = WS_EP + 256;

DI float bflo(u32 v) { return __uint_as_float(v << 16); }
DI float bfhi(u32 v) { return __uint_as_float(v & 0xffff0000u); }
DI float bf2f(u16 v) { return __uint_as_float(((u32)v) << 16); }
DI u32 cvtpk(float lo, float hi) { f32x2 v = {lo, hi}; bf16x2_t r = __builtin_convertvector(v, bf16x2_t); return __builtin_bit_cast(u32, r); }
DI float rbf(float f) { return bflo(cvtpk(f, f)); }

typedef __amdgpu_buffer_rsrc_t rsrc_t;
DI rsrc_t mkrsrc(void* p, u32 bytes) { return __builtin_amdgcn_make_buffer_rsrc(p, 0, bytes, 0x00020000); }
#define AUX_KL 0
#define AUX_WT 0
#define AUX_H 0
#define AUX_HM 0
template <int AUX = 0> DI void st16(rsrc_t r, u32 off, u32x4 v) { __builtin_amdgcn_raw_buffer_store_b128(v, r, off, 0, AUX); }
template <int AUX = 0> DI void st8(rsrc_t r, u32 off, u32x2 v) { __builtin_amdgcn_raw_buffer_store_b64(v, r, off, 0, AUX); }
template <int AUX = 0> DI void st4(rsrc_t r, u32 off, u32 v) { __builtin_amdgcn_raw_buffer_store_b32(v, r, off, 0, AUX); }

template <int CTRL> DI float dppadd(float v) { return v + __builtin_bit_cast(float, __builtin_amdgcn_update_dpp(0, __builtin_bit_cast(int, v), CTRL, 0xF, 0xF, true)); }
DI float swap16add(float v) { auto r = __builtin_amdgcn_permlane16_swap(__float_as_uint(v), __float_as_uint(v), false, false); const u32 a = r[0], b = r[1]; return __uint_as_float(a) + __uint_as_float(b); }
DI float swap32add(float v) { auto r = __builtin_amdgcn_permlane32_swap(__float_as_uint(v), __float_as_uint(v), false, false); const u32 a = r[0], b = r[1]; return __uint_as_float(a) + __uint_as_float(b); }
DI float wave_sum(float v) {
    v = dppadd<0xB1>(v);
    v = dppadd<0x4E>(v);
    v = dppadd<0x141>(v);
    v = dppadd<0x140>(v);
    return swap32add(swap16add(v));
}

struct cm { f32x2 a, s; };
DI cm mkcm(f32x2 b) { return { b, f32x2{-b.y, b.x} }; }
DI f32x2 cmul(f32x2 v, const cm& m) { return v.xx * m.a + v.yy * m.s; }
DI f32x2 csq(f32x2 a) { return f32x2{ a.x * a.x - a.y * a.y, 2.f * a.x * a.y }; }
DI f32x2 cexp_p(float ar, float br, float p) {
    float e = __builtin_amdgcn_exp2f(ar * p);
    float th = __builtin_amdgcn_fractf(br * p);
    return f32x2{ e * __builtin_amdgcn_cosf(th), e * __builtin_amdgcn_sinf(th) };
}

#define WAITV(n) asm volatile("s_waitcnt vmcnt(" #n ")" ::: "memory")
#define WAITL0() asm volatile("s_waitcnt lgkmcnt(0)" ::: "memory")
template <int N> DI void waitv() { asm volatile("s_waitcnt vmcnt(%0)" :: "n"(N) : "memory"); }
#define BAR() do { asm volatile("" ::: "memory"); __builtin_amdgcn_s_barrier(); asm volatile("" ::: "memory"); } while (0)
#define LBAR() do { WAITL0(); BAR(); } while (0)
#define SGB(m, n) __builtin_amdgcn_sched_group_barrier(m, n, 0)
#define SCHEDB() __builtin_amdgcn_sched_barrier(0)
#define MFMA16(a, b, c) __builtin_amdgcn_mfma_f32_16x16x32_bf16(a, b, c, 0, 0, 0)
#define GAS __attribute__((address_space(1)))
typedef unsigned long long u64;

DI void glds16(const void* g, u32 lds_base) {
    u32 sv; asm volatile("s_mov_b32 %0, m0\n\ts_mov_b32 m0, %2\n\ts_nop 0\n\tglobal_load_lds_dwordx4 %1, off sc1\n\ts_mov_b32 m0, %0" : "=&s"(sv) : "v"(g), "s"(lds_base) : "memory");
}
DI void poll_flags(rsrc_t wsr, u32 off, int n, u32 tag, int lane) {
    const u32 o = off + (lane < n ? lane : 0) * 64;
#define POLL() __builtin_amdgcn_raw_buffer_load_b32(wsr, o, 0, 16)
#define FRESH() do { __builtin_amdgcn_s_sleep(3); asm volatile("" ::: "memory"); } while (0)
    u32 v0 = POLL(); FRESH(); u32 v1 = POLL(); FRESH(); u32 v2 = POLL(); FRESH(); u32 v3 = POLL();
    for (int spin = 0; spin < 100000; ++spin) {
        if (__all(v0 == tag)) break; FRESH(); v0 = POLL();
        if (__all(v1 == tag)) break; FRESH(); v1 = POLL();
        if (__all(v2 == tag)) break; FRESH(); v2 = POLL();
        if (__all(v3 == tag)) break; FRESH(); v3 = POLL();
    }
    asm volatile("" ::: "memory");
#undef POLL
#undef FRESH
}
DI void st_flag(char* ws, u32 off, u32 tag) { __hip_atomic_store((GAS u32*)(ws + off), tag, __ATOMIC_RELAXED, __HIP_MEMORY_SCOPE_AGENT); }
DI s16x4 trread(const LAS char* p) { typedef short v4i16_t __attribute__((ext_vector_type(4))); return __builtin_bit_cast(s16x4, __builtin_amdgcn_ds_read_tr16_b64_v4i16((LAS v4i16_t*)p)); }
DI bf16x8 cat8(s16x4 a, s16x4 b) { return __builtin_shufflevector(a, b, 0, 1, 2, 3, 4, 5, 6, 7); }

constexpr int KG_PITCH = 784;
constexpr int KA_A = 0, KA_ASZ = 20 * KG_PITCH;
constexpr int KA_B = 3 * KA_ASZ, KA_BSZ = 32 * KG_PITCH;
constexpr int KA_U = KA_B + 3 * KA_BSZ, KA_USZ = 4608 + 4096;
constexpr int KA_CNT = KA_U + 3 * KA_USZ;
constexpr int KA_LDS = KA_CNT + 32;
constexpr int CV_CS = 4160;
constexpr int KB_X = 32256;

#define ADDTID2(base0, v0, o0, base1, v1, o1) asm volatile("s_mov_b32 m0, %2\n\ts_nop 0\n\tds_write_addtid_b32 %0 offset:%4\n\ts_mov_b32 m0, %3\n\ts_nop 0\n\tds_write_addtid_b32 %1 offset:%5" \
    :: "v"(v0), "v"(v1), "s"(base0), "s"(base1), "i"(o0), "i"(o1) : "memory")
#define ADDTID2S(base, v0, o0, v1, o1) asm volatile("s_mov_b32 m0, %2\n\ts_nop 0\n\tds_write_addtid_b32 %0 offset:%3\n\tds_write_addtid_b32 %1 offset:%4" :: "v"(v0), "v"(v1), "s"(base), "i"(o0), "i"(o1) : "memory")

DI void lds_sync(char* smem, int cnt, u32 target, int lane) {
    LAS u32* c = (LAS u32*)((LAS char*)smem + KA_CNT) + cnt;
    WAITL0();
    if (lane == 0) __hip_atomic_fetch_add(c, 1u, __ATOMIC_RELAXED, __HIP_MEMORY_SCOPE_WORKGROUP);
    for (int spin = 0; spin < 1000000; ++spin) {
        if (__builtin_amdgcn_readfirstlane(__hip_atomic_load(c, __ATOMIC_RELAXED, __HIP_MEMORY_SCOPE_WORKGROUP)) >= target) break;
        __builtin_amdgcn_s_sleep(1);
    }
    asm volatile("" ::: "memory");
}

DI void lds_arrive(char* smem, int cnt, int lane) {
    WAITL0();
    if (lane == 0) __hip_atomic_fetch_add((LAS u32*)((LAS char*)smem + KA_CNT) + cnt, 1u, __ATOMIC_RELAXED, __HIP_MEMORY_SCOPE_WORKGROUP);
}
DI void lds_wait(char* smem, int cnt, u32 target) {
    LAS u32* c = (LAS u32*)((LAS char*)smem + KA_CNT) + cnt;
    for (int spin = 0; spin < 1000000; ++spin) {
        if (__builtin_amdgcn_readfirstlane(__hip_atomic_load(c, __ATOMIC_RELAXED, __HIP_MEMORY_SCOPE_WORKGROUP)) >= target) break;
        __builtin_amdgcn_s_sleep(1);
    }
    asm volatile("" ::: "memory");
}

struct KParams { u16 are, aim, bre, bim, cre, cim, ldt; };

DI void ka_kgen(const KParams& P, char* smem, int sA, int sB, int w, int lane) {
    const float are = bf2f(P.are), aim = bf2f(P.aim), bre = bf2f(P.bre), bim = bf2f(P.bim), cre = bf2f(P.cre), cim = bf2f(P.cim);
    const float dt = rbf(__expf(bf2f(P.ldt)));
    const float ar = (dt * are) * 1.4426950408889634f, br = (dt * aim) * 0.15915494309189535f;
    const float dbr = dt * bre, dbi = dt * bim;
    const f32x2 Wc = { cre * dbr - cim * dbi, -(cre * dbi + cim * dbr) };
    const f32x2 l1 = cexp_p(ar, br, 1.f), l8 = cexp_p(ar, br, 8.f), l256 = cexp_p(ar, br, 256.f);
    const f32x2 l2 = csq(l1), l4 = csq(l2), l16 = csq(l8);
    const cm c1 = mkcm(f32x2{l1.x, -l1.y}), c2 = mkcm(f32x2{l2.x, -l2.y}), c4 = mkcm(f32x2{l4.x, -l4.y}), c256 = mkcm(f32x2{l256.x, -l256.y}), m16 = mkcm(l16);
    const u32 lds0 = (u32)(uintptr_t)smem;
    const u32 bA = lds0 + sA + w * 256, bB = lds0 + sB + w * 256 - KB_X;
    auto pk = [](f32x2 v) { return cvtpk(v.x, v.y); };
    constexpr int R = KG_PITCH, X = KB_X;
    f32x2 a0 = Wc, x256 = cmul(Wc, c256), e0 = {1.f, 0.f}, e1 = l8;
    ADDTID2S(bA, pk(a0), 0 * R, pk(x256), 8 * R);
    ADDTID2S(bB, pk(e0), X + 0 * R, pk(e1), X + 1 * R);
    f32x2 y = x256, v = cmul(x256, c256);
#define KG_E(c) do { e0 = cmul(e0, m16); e1 = cmul(e1, m16); ADDTID2S(bB, pk(e0), X + (c) * R, pk(e1), X + ((c) + 1) * R); } while (0)
    a0 = cmul(a0, c1); y = cmul(y, c2); ADDTID2S(bA, pk(a0), 1 * R, pk(y), 9 * R); KG_E(2);
    a0 = cmul(a0, c1); y = cmul(y, c2); ADDTID2S(bA, pk(a0), 2 * R, pk(y), 10 * R); KG_E(4);
    a0 = cmul(a0, c1); y = cmul(y, c2); ADDTID2S(bA, pk(a0), 3 * R, pk(y), 11 * R); KG_E(6);
    a0 = cmul(a0, c1); y = cmul(v, c4); ADDTID2S(bA, pk(a0), 4 * R, pk(v), 12 * R); v = cmul(v, c256); KG_E(8);
    a0 = cmul(a0, c1); ADDTID2S(bA, pk(a0), 5 * R, pk(y), 13 * R); y = cmul(v, c4); KG_E(10);
    a0 = cmul(a0, c1); ADDTID2S(bA, pk(a0), 6 * R, pk(v), 14 * R); v = cmul(v, c256); KG_E(12);
    a0 = cmul(a0, c1); ADDTID2S(bA, pk(a0), 7 * R, pk(y), 15 * R); KG_E(14);
    y = cmul(v, c256); KG_E(16);
    ADDTID2S(bA, pk(v), 16 * R, pk(y), 17 * R); v = cmul(y, c256); KG_E(18);
    y = cmul(v, c256); KG_E(20);
    ADDTID2S(bA, pk(v), 18 * R, pk(y), 19 * R); v = cmul(y, c256); KG_E(22);
    const float k2048 = wave_sum(v.x);
    KG_E(24); KG_E(26);
    KG_E(28); KG_E(30);
    const int r32 = lane & 31, h = lane >> 5;
    const char* pa = smem + sA + (r32 < 20 ? r32 : 19) * R + w * 256 + h * 16;
    const char* pb = smem + sB + r32 * R + w * 256 + h * 16;
    f32x16 acc = {};
#pragma unroll
    for (int s = 0; s < 8; ++s) {
        const bf16x8 a = *(const bf16x8*)(pa + 32 * s), bb = *(const bf16x8*)(pb + 32 * s);
        acc = __builtin_amdgcn_mfma_f32_32x32x16_bf16(a, bb, acc, 0, 0, 0);
    }
    const int c = r32;
    char* sl = smem + sA + w * 256;
    char* p0 = sl + (c >> 3) * R + (8 * (c & 7) + 4 * h) * 4;
    char* p1 = sl + (h ? 6 * R + 8 * c : (4 + (c >> 4)) * R + 16 * (c & 15));
    char* p2 = sl + 8 * R + 4 * c;
#pragma unroll
    for (int q = 0; q < 4; ++q) *(float*)(p0 + 4 * q) = acc[q];
    *(float*)(p1) = acc[4]; *(float*)(p1 + 4) = acc[5];
    *(float*)(p1 + (h ? R : 8)) = acc[6]; *(float*)(p1 + (h ? R + 4 : 12)) = acc[7];
    if (h == 0) { *(float*)(p2) = acc[8]; *(float*)(p2 + 128) = acc[9]; *(float*)(p2 + R) = acc[10]; *(float*)(p2 + R + 128) = acc[11]; }
    else if (c == 0) *(float*)(sl + 10 * R) = k2048;
}

DI void ka_expand(char* smem, int sA, int sB, int w, int lane) {
    const int t = 64 * w + lane;
    auto rd = [&](int i) { const char* p = smem + sA + (i >> 6) * KG_PITCH + (i & 63) * 4; return *(const float*)p + *(const float*)(p + 256) + *(const float*)(p + 512); };
    u32 k4, k5, k6, k7, f2, f3;
    if (w == 0) {
        const int i0 = t < 32 ? 8 * t : 256 + 4 * (t - 32);
        const u32 pat = t < 32 ? 0x76543210u : 0x43222100u;
        float kv[8];
#pragma unroll
        for (int j = 0; j < 8; ++j) kv[j] = rd(i0 + ((pat >> (4 * j)) & 15));
        k4 = cvtpk(kv[0], kv[1]); k5 = cvtpk(kv[2], kv[3]); k6 = cvtpk(kv[4], kv[5]); k7 = cvtpk(kv[6], kv[7]);
        f2 = 0; f3 = 0;
    } else if (w == 1) {
        const int i0 = 384 + 2 * (t - 64);
        const float k0 = rd(i0), k1 = rd(i0 + 1), k2 = rd(i0 + 2);
        k4 = cvtpk(k0, k0); k5 = cvtpk(k0, k1); k6 = cvtpk(k1, k1); k7 = cvtpk(k2, k2);
        const float a = rd(382), b = rd(383);
        f2 = cvtpk(a, a); f3 = cvtpk(b, k0);
    } else {
        const int i0 = 512 + (t - 128);
        const float k0 = rd(i0), k1 = rd(i0 + 1);
        k4 = cvtpk(k0, k0); k5 = k4; k6 = cvtpk((t & 1) ? k1 : k0, k1); k7 = cvtpk(k1, k1);
        const float a = w == 2 ? rd(511) : k0;
        f2 = cvtpk(a, a); f3 = cvtpk(k0, k0);
    }
    const u32 k2 = __builtin_amdgcn_update_dpp(f2, k6, 0x138, 0xF, 0xF, false), k3 = __builtin_amdgcn_update_dpp(f3, k7, 0x138, 0xF, 0xF, false);
    const u32 a2 = __builtin_amdgcn_alignbit(k3, k2, 16), a3 = __builtin_amdgcn_alignbit(k4, k3, 16), a4 = __builtin_amdgcn_alignbit(k5, k4, 16),
              a5 = __builtin_amdgcn_alignbit(k6, k5, 16), a6 = __builtin_amdgcn_alignbit(k7, k6, 16);
    char* kb = smem + sB + 32 + 16 * t;
    *(u32x4*)(kb + 0 * CV_CS) = u32x4{k4, k5, k6, k7};
    *(u32x4*)(kb + 1 * CV_CS) = u32x4{a3, a4, a5, a6};
    *(u32x4*)(kb + 2 * CV_CS) = u32x4{k3, k4, k5, k6};
    *(u32x4*)(kb + 3 * CV_CS) = u32x4{a2, a3, a4, a5};
    if (t < 8) *(u32x4*)(smem + sB + (t >> 1) * CV_CS + 16 * (t & 1)) = u32x4{0, 0, 0, 0};
}

DI u32 phase_ssm(const u16* __restrict__ x, const u16* __restrict__ Are, const u16* __restrict__ Aim, const u16* __restrict__ Bre,
                  const u16* __restrict__ Bim, const u16* __restrict__ Cre, const u16* __restrict__ Cim, const u16* __restrict__ logdt,
                  const u16* __restrict__ Dskip, const u16* __restrict__ Wg, const u16* __restrict__ Wu, const u16* __restrict__ Wd,
                  char* __restrict__ ws, char* smem) {
    const int b = blockIdx.x, t = threadIdx.x, lane = t & 63;
    const int wv = __builtin_amdgcn_readfirstlane(t >> 6), sb = wv >> 2, w = wv & 3;
    const int xcd = b & 7, j = b >> 3;
    const int nch = j < 16 ? 3 : 2;
    const int c0 = 80 * xcd + ((xcd & 2) ? (j < 16 ? 32 + 3 * j : 2 * (j - 16)) : (j < 16 ? 3 * j : 48 + 2 * (j - 16)));
    const int xs = min((2 * c0) & ~3, ((2 * c0) | 127) + 1 - 8), e0 = (2 * c0 - xs) >> 1;
    const bool conv = sb < nch;
    const int d = c0 + sb;
    const int sA = KA_A + sb * KA_ASZ, sB = KA_B + sb * KA_BSZ, sU = KA_U + sb * KA_USZ;
    const rsrc_t wsr = mkrsrc(ws, WS_END);
    STAMP(ws, 0, 0);
    if (t < 8) *(u32*)(smem + KA_CNT + 4 * t) = 0u;

    KParams KP;
    u32 ra[4][5];
    u32x4 wv1[8], wv2[8];
    const int tt = t - 512;
    const bool w1job = (j - 16) < 12;
    float dsk = 0.f;
    if (wv == 3 || wv == 7) {
        const u32* x32 = (const u32*)x;
#pragma unroll
        for (int rr = 0; rr < 4; ++rr)
#pragma unroll
            for (int i = 0; i < 5; ++i) ra[rr][i] = x32[(8 * b + 4 * sb + rr) * 320 + lane + 64 * i];
    }
    if (conv) {
        if (w < 3) {
            const int n = d * NM_ + 64 * w + lane;
            KP.are = Are[n]; KP.aim = Aim[n]; KP.bre = Bre[n]; KP.bim = Bim[n]; KP.cre = Cre[n]; KP.cim = Cim[n]; KP.ldt = logdt[d];
        }
        dsk = bf2f(Dskip[d]);
    }
    const u32 epoch = __hip_atomic_load((GAS u32*)(ws + WS_EP), __ATOMIC_RELAXED, __HIP_MEMORY_SCOPE_AGENT);
    const rsrc_t xr0 = mkrsrc((void*)x, L_ * D_ * 2);
    LBAR();
    u32x2 xg[4];
    if (wv < 8) {
        xg[0] = __builtin_amdgcn_raw_buffer_load_b64(xr0, (4 * t + 0) * (D_ * 2) + xs, 0, 0);
        xg[1] = __builtin_amdgcn_raw_buffer_load_b64(xr0, (4 * t + 1) * (D_ * 2) + xs, 0, 0);
    }
    if (conv && w == 3) {
        if (lane < 30) *(u32x4*)(smem + sU + 16 * lane) = u32x4{0, 0, 0, 0};
        lds_arrive(smem, sb, lane);
    }
    if (!conv) {
        if (w1job) {
            const int u1 = xcd * 12 + (j - 16), up = u1 >= 48, col = 32 * (up ? u1 - 48 : u1) + 8 * (tt & 3);
            const u16* src = (up ? Wu : Wg) + (8 * (tt >> 2)) * F_ + col;
#pragma unroll
            for (int r = 0; r < 8; ++r) wv1[r] = *(const u32x4*)(src + r * F_);
            if (w == 0) {
#pragma unroll
                for (int r = 0; r < 8; ++r) wv2[r] = *(const u32x4*)(src + (512 + r) * F_);
            }
        } else {
            const int n1 = 480 * (xcd * 4 + (j - 28)) + tt, n2 = n1 + 256;
            const u16* s1 = Wd + (8 * (n1 / 80)) * D_ + 8 * (n1 % 80);
#pragma unroll
            for (int r = 0; r < 8; ++r) wv1[r] = *(const u32x4*)(s1 + r * D_);
            if (tt < 224) {
                const u16* s2 = Wd + (8 * (n2 / 80)) * D_ + 8 * (n2 % 80);
#pragma unroll
                for (int r = 0; r < 8; ++r) wv2[r] = *(const u32x4*)(s2 + r * D_);
            }
        }
    }
    const u32 tag = epoch + 1u;
    if (wv == 3 || wv == 7) {
        __builtin_amdgcn_s_setprio(3);
        float s1[4], s2[4], c0v[4];
#pragma unroll
        for (int rr = 0; rr < 4; ++rr) {
            const float c = bflo(__builtin_amdgcn_readfirstlane(ra[rr][0]));
            float a1 = 0.f, a2 = 0.f;
#pragma unroll
            for (int i = 0; i < 5; ++i) { const float a = bflo(ra[rr][i]) - c, e = bfhi(ra[rr][i]) - c; a1 += a + e; a2 = __builtin_fmaf(a, a, __builtin_fmaf(e, e, a2)); }
            s1[rr] = a1; s2[rr] = a2; c0v[rr] = c;
        }
        auto red4 = [&](const float (&v)[4]) {
            auto p01 = __builtin_amdgcn_permlane32_swap(__float_as_uint(v[0]), __float_as_uint(v[1]), false, false);
            auto p23 = __builtin_amdgcn_permlane32_swap(__float_as_uint(v[2]), __float_as_uint(v[3]), false, false);
            const u32 x0 = p01[0], x1 = p01[1], y0 = p23[0], y1 = p23[1];
            const float t01 = __uint_as_float(x0) + __uint_as_float(x1);
            const float t23 = __uint_as_float(y0) + __uint_as_float(y1);
            auto q = __builtin_amdgcn_permlane16_swap(__float_as_uint(t01), __float_as_uint(t23), false, false);
            const u32 z0 = q[0], z1 = q[1];
            float r = __uint_as_float(z0) + __uint_as_float(z1);
            r = dppadd<0xB1>(r); r = dppadd<0x4E>(r); r = dppadd<0x141>(r); r = dppadd<0x140>(r);
            return r;
        };
        const float n1 = red4(s1) * (1.f / 640.f), n2 = red4(s2) * (1.f / 640.f);
        const int lr = lane >> 4, rr = (lr >> 1) | ((lr & 1) << 1);
        const float cc = rr == 0 ? c0v[0] : rr == 1 ? c0v[1] : rr == 2 ? c0v[2] : c0v[3];
        float var = n2 - n1 * n1; var = var > 0.f ? var : 0.f;
        const float sd = __builtin_sqrtf(rbf(rbf(var) + 1e-5f));
        const u32 pay = cvtpk(cc + n1, sd);
        const int row = 8 * b + 4 * sb + rr;
        if ((lane & 15) == 0) {
            __hip_atomic_store((GAS u64*)(ws + WS_GR) + row, ((u64)tag << 32) | pay, __ATOMIC_RELAXED, __HIP_MEMORY_SCOPE_AGENT);
            st4<16>(wsr, WS_ST + row * 4, pay << 16);
        }
        asm volatile("" ::: "memory");
        if (lane == 0) __hip_atomic_fetch_add((LAS u32*)((LAS char*)smem + KA_CNT) + 4, 1u, __ATOMIC_RELAXED, __HIP_MEMORY_SCOPE_WORKGROUP);
        __builtin_amdgcn_s_setprio(0);
    }
    if (conv) {
        if (w < 3) ka_kgen(KP, smem, sA, sB, w, lane);
    } else if (w1job) {
        float* red = (float*)(smem + sA);
#pragma unroll
        for (int i = 0; i < 4; ++i) {
            float a = 0.f, c = 0.f;
#pragma unroll
            for (int r = 0; r < 8; ++r) { a += bflo(wv1[r][i]); c += bfhi(wv1[r][i]); }
            if (w == 0) {
#pragma unroll
                for (int r = 0; r < 8; ++r) { a += bflo(wv2[r][i]); c += bfhi(wv2[r][i]); }
            }
            red[(tt >> 2) * 33 + 8 * (tt & 3) + 2 * i] = a; red[(tt >> 2) * 33 + 8 * (tt & 3) + 2 * i + 1] = c;
        }
    }
    if (!conv) {
        __builtin_amdgcn_s_setprio(2);
        {
            if (w1job) {
            lds_sync(smem, sb, 4, lane);
            const float* red = (const float*)(smem + sA);
            const int col = tt & 31, part = tt >> 5;
            float sm = 0.f;
#pragma unroll
            for (int r = 0; r < 8; ++r) sm += red[(8 * part + r) * 33 + col];
            sm = swap32add(sm);
            if (lane < 32) *(float*)(smem + sB + (col * 4 + w) * 4) = sm;
            lds_sync(smem, sb, 8, lane);
#define WV(r) (rnd ? wv2[r][i] : wv1[r][i])
            }
            char* stg = smem + (w == 3 ? sA + 1024 : sB + 512 + w * 8192);
            u32* dtab = (u32*)(smem + sU + w * 256);
            auto flush = [&](u32 dst, bool active, const u32x4 (&o)[8]) {
                dtab[lane] = active ? dst : 0xffffff00u;
#pragma unroll
                for (int i = 0; i < 8; ++i) *(u32x4*)(stg + lane * 128 + ((i ^ (lane & 7)) * 16)) = o[i];
#pragma unroll
                for (int i = 0; i < 8; ++i) {
                    const int sl = 8 * i + (lane >> 3), pc = lane & 7;
                    const u32x4 v = *(const u32x4*)(stg + sl * 128 + ((pc ^ (sl & 7)) * 16));
                    st16<16>(wsr, dtab[sl] + pc * 16, v);
                }
            };
            if (w1job) {
                const int u1 = xcd * 12 + (j - 16), up = u1 >= 48, f = 32 * (up ? u1 - 48 : u1) + 8 * (tt & 3);
                float cm[8];
#pragma unroll
                for (int i = 0; i < 8; ++i) { const f32x4 v = *(const f32x4*)(smem + sB + (8 * (tt & 3) + i) * 16); cm[i] = (v[0] + v[1] + v[2] + v[3]) * (1.f / 640.f); }
                const int sl = f / 96, fl = f % 96, mt = 2 * (fl >> 4) + up;
#pragma unroll
                for (int rnd = 0; rnd < 2; ++rnd) {
                    if (rnd == 0 || w == 0) {
                        const int kg = (tt >> 2) + 64 * rnd;
                        const u32 dst = WS_W1T + ((sl * 10 + (kg >> 3)) * 24 + ((kg >> 2) & 1) * 12 + mt) * 1024 + ((fl & 15) + 16 * (kg & 3)) * 16;
                        u32x4 o[8];
#pragma unroll
                        for (int i = 0; i < 4; ++i) {
                            const float ca = cm[2 * i], cb = cm[2 * i + 1];
                            o[2 * i] = u32x4{cvtpk(bflo(WV(0)) - ca, bflo(WV(1)) - ca), cvtpk(bflo(WV(2)) - ca, bflo(WV(3)) - ca),
                                             cvtpk(bflo(WV(4)) - ca, bflo(WV(5)) - ca), cvtpk(bflo(WV(6)) - ca, bflo(WV(7)) - ca)};
                            o[2 * i + 1] = u32x4{cvtpk(bfhi(WV(0)) - cb, bfhi(WV(1)) - cb), cvtpk(bfhi(WV(2)) - cb, bfhi(WV(3)) - cb),
                                                 cvtpk(bfhi(WV(4)) - cb, bfhi(WV(5)) - cb), cvtpk(bfhi(WV(6)) - cb, bfhi(WV(7)) - cb)};
                        }
                        flush(dst, true, o);
                    }
                }
            } else {
#pragma unroll
                for (int rnd = 0; rnd < 2; ++rnd) {
                    if (rnd == 0 || w < 3 || true) {
                        const bool act = rnd == 0 || tt < 224;
                        const int n = 480 * (xcd * 4 + (j - 28)) + tt + 256 * rnd, dc = n % 80, kg = n / 80;
                        const int ds = dc / 10, dl = 8 * (dc % 10);
                        const u32 dst = WS_W2T + ((ds * 24 + (kg >> 3)) * 10 + ((kg >> 2) & 1) * 5 + (dl >> 4)) * 1024 + ((dl & 15) + 16 * (kg & 3)) * 16;
                        u32x4 o[8];
#pragma unroll
                        for (int i = 0; i < 4; ++i) {
                            o[2 * i] = u32x4{__builtin_amdgcn_perm(WV(1), WV(0), 0x05040100u), __builtin_amdgcn_perm(WV(3), WV(2), 0x05040100u),
                                             __builtin_amdgcn_perm(WV(5), WV(4), 0x05040100u), __builtin_amdgcn_perm(WV(7), WV(6), 0x05040100u)};
                            o[2 * i + 1] = u32x4{__builtin_amdgcn_perm(WV(1), WV(0), 0x07060302u), __builtin_amdgcn_perm(WV(3), WV(2), 0x07060302u),
                                                 __builtin_amdgcn_perm(WV(5), WV(4), 0x07060302u), __builtin_amdgcn_perm(WV(7), WV(6), 0x07060302u)};
                        }
                        flush(dst, act, o);
                    }
                }
            }
        }
        __builtin_amdgcn_s_setprio(0);
        WAITV(0);
        lds_sync(smem, sb, w1job ? 12 : 4, lane);
        if (t == 512) st_flag(ws, WS_FW + (xcd * 16 + (j - 16)) * 64, tag);
        if (t == 512) STAMP2(ws, 6, 0);
        return tag;
    }
    STAMP(ws, 0, 1);
    u32x4 g0 = {0, 0, 0, 0}, g1 = {0, 0, 0, 0};
    if (wv < 8) {
        lds_wait(smem, 4, 2);
        xg[2] = __builtin_amdgcn_raw_buffer_load_b64(xr0, (4 * t + 2) * (D_ * 2) + xs, 0, 0);
        xg[3] = __builtin_amdgcn_raw_buffer_load_b64(xr0, (4 * t + 3) * (D_ * 2) + xs, 0, 0);
        g0 = __builtin_amdgcn_raw_buffer_load_b128(wsr, WS_GR + t * 32, 0, 16);
        g1 = __builtin_amdgcn_raw_buffer_load_b128(wsr, WS_GR + t * 32 + 16, 0, 16);
    }
    if (w < 3) lds_sync(smem, sb, 4, lane); else lds_wait(smem, sb, 4);
    STAMP(ws, 0, 2);
    ka_expand(smem, sA, sB, w, lane);
    if (wv < 8) {
        for (int spin = 0; spin < 100000; ++spin) {
            if (__all(g0[1] == tag && g0[3] == tag && g1[1] == tag && g1[3] == tag)) break;
            __builtin_amdgcn_s_sleep(4);
            asm volatile("" ::: "memory");
            g0 = __builtin_amdgcn_raw_buffer_load_b128(wsr, WS_GR + t * 32, 0, 16);
            g1 = __builtin_amdgcn_raw_buffer_load_b128(wsr, WS_GR + t * 32 + 16, 0, 16);
        }
        const u32 pr[4] = {g0[0], g0[2], g1[0], g1[2]};
        float m[4], rs[4];
#pragma unroll
        for (int r = 0; r < 4; ++r) { m[r] = bflo(pr[r]); rs[r] = __builtin_amdgcn_rcpf(bfhi(pr[r])); }
#pragma unroll
        for (int s = 0; s < 3; ++s) {
            if (s < nch) {
                const int e = e0 + s;
                float xv[4];
#pragma unroll
                for (int r = 0; r < 4; ++r) { const u32 dw = e < 2 ? xg[r][0] : xg[r][1]; xv[r] = (e & 1) ? bfhi(dw) : bflo(dw); }
                const u32 q01 = cvtpk(xv[0] - m[0], xv[1] - m[1]), q23 = cvtpk(xv[2] - m[2], xv[3] - m[3]);
                const u32 u01 = cvtpk(bflo(q01) * rs[0], bfhi(q01) * rs[1]), u23 = cvtpk(bflo(q23) * rs[2], bfhi(q23) * rs[3]);
                char* ub = smem + KA_U + s * KA_USZ;
                *(u32x2*)(ub + 480 + 8 * t) = u32x2{u01, u23};
                *(u32x2*)(ub + 4608 + 8 * t) = u32x2{q01, q23};
            }
        }
    }
    STAMP(ws, 0, 3);
    asm volatile("" :: "v"(tag));
    lds_sync(smem, 3, 4 * nch, lane);
    STAMP(ws, 0, 4);
    const int al = lane & 15, g = lane >> 4;
    f32x4 acc[8];
#pragma unroll
    for (int i = 0; i < 8; ++i) acc[i] = f32x4{0.f, 0.f, 0.f, 0.f};
    {
        const LAS char* lds = (const LAS char*)smem;
        const int q = (lane & 15) >> 2, p = lane & 3;
        const LAS char* ub0 = lds + sU + 448 + 32 * al + 16 * g - 128 * w;
        const LAS char* ub1 = ub0 - 64;
        const LAS char* kb0 = lds + sB + q * CV_CS + 56 - 16 * g + 8 * p + 128 * w;
        const LAS char* kb1 = kb0 + 64;
        bf16x8 U0[8], U1[8]; s16x4 A0[8][2], A1[8][2];
        A0[0][0] = trread(kb0 + 8); A0[0][1] = trread(kb0 + 0); U0[0] = *(const LAS bf16x8*)(ub0 + 0); U0[1] = *(const LAS bf16x8*)(ub0 + 512); U0[2] = *(const LAS bf16x8*)(ub0 + 1024); U0[3] = *(const LAS bf16x8*)(ub0 + 1536); U0[4] = *(const LAS bf16x8*)(ub0 + 2048); U0[5] = *(const LAS bf16x8*)(ub0 + 2560); U0[6] = *(const LAS bf16x8*)(ub0 + 3072); U0[7] = *(const LAS bf16x8*)(ub0 + 3584); A0[1][0] = trread(kb0 + 520); A0[1][1] = trread(kb0 + 512); A0[2][0] = trread(kb0 + 1032); A0[2][1] = trread(kb0 + 1024); A0[3][0] = trread(kb0 + 1544); A0[3][1] = trread(kb0 + 1536); A0[4][0] = trread(kb0 + 2056); A0[4][1] = trread(kb0 + 2048); A0[5][0] = trread(kb0 + 2568); A0[5][1] = trread(kb0 + 2560); A0[6][0] = trread(kb0 + 3080); A0[6][1] = trread(kb0 + 3072); A0[7][0] = trread(kb0 + 3592); A0[7][1] = trread(kb0 + 3584);
        SCHEDB();
        acc[0] = MFMA16(cat8(A0[0][0], A0[0][1]), U0[0], acc[0]);
        SCHEDB();
        acc[1] = MFMA16(cat8(A0[0][0], A0[0][1]), U0[1], acc[1]);
        SCHEDB();
        acc[2] = MFMA16(cat8(A0[0][0], A0[0][1]), U0[2], acc[2]);
        SCHEDB();
        acc[3] = MFMA16(cat8(A0[0][0], A0[0][1]), U0[3], acc[3]);
        SCHEDB();
        acc[4] = MFMA16(cat8(A0[0][0], A0[0][1]), U0[4], acc[4]);
        SCHEDB();
        acc[5] = MFMA16(cat8(A0[0][0], A0[0][1]), U0[5], acc[5]);
        SCHEDB();
        acc[6] = MFMA16(cat8(A0[0][0], A0[0][1]), U0[6], acc[6]);
        SCHEDB();
        acc[7] = MFMA16(cat8(A0[0][0], A0[0][1]), U0[7], acc[7]);
        SCHEDB();
        acc[1] = MFMA16(cat8(A0[1][0], A0[1][1]), U0[0], acc[1]);
        A1[0][0] = trread(kb1 + 8);
        SCHEDB();
        acc[2] = MFMA16(cat8(A0[1][0], A0[1][1]), U0[1], acc[2]);
        A1[0][1] = trread(kb1 + 0);
        SCHEDB();
        acc[3] = MFMA16(cat8(A0[1][0], A0[1][1]), U0[2], acc[3]);
        U1[0] = *(const LAS bf16x8*)(ub1 + 0);
        SCHEDB();
        acc[4] = MFMA16(cat8(A0[1][0], A0[1][1]), U0[3], acc[4]);
        U1[1] = *(const LAS bf16x8*)(ub1 + 512);
        SCHEDB();
        acc[5] = MFMA16(cat8(A0[1][0], A0[1][1]), U0[4], acc[5]);
        U1[2] = *(const LAS bf16x8*)(ub1 + 1024);
        SCHEDB();
        acc[6] = MFMA16(cat8(A0[1][0], A0[1][1]), U0[5], acc[6]);
        U1[3] = *(const LAS bf16x8*)(ub1 + 1536);
        SCHEDB();
        acc[7] = MFMA16(cat8(A0[1][0], A0[1][1]), U0[6], acc[7]);
        U1[4] = *(const LAS bf16x8*)(ub1 + 2048);
        SCHEDB();
        acc[2] = MFMA16(cat8(A0[2][0], A0[2][1]), U0[0], acc[2]);
        U1[5] = *(const LAS bf16x8*)(ub1 + 2560);
        SCHEDB();
        acc[3] = MFMA16(cat8(A0[2][0], A0[2][1]), U0[1], acc[3]);
        U1[6] = *(const LAS bf16x8*)(ub1 + 3072);
        SCHEDB();
        acc[4] = MFMA16(cat8(A0[2][0], A0[2][1]), U0[2], acc[4]);
        U1[7] = *(const LAS bf16x8*)(ub1 + 3584);
        SCHEDB();
        acc[5] = MFMA16(cat8(A0[2][0], A0[2][1]), U0[3], acc[5]);
        A1[1][0] = trread(kb1 + 520);
        SCHEDB();
        acc[6] = MFMA16(cat8(A0[2][0], A0[2][1]), U0[4], acc[6]);
        A1[1][1] = trread(kb1 + 512);
        SCHEDB();
        acc[7] = MFMA16(cat8(A0[2][0], A0[2][1]), U0[5], acc[7]);
        A1[2][0] = trread(kb1 + 1032);
        SCHEDB();
        acc[3] = MFMA16(cat8(A0[3][0], A0[3][1]), U0[0], acc[3]);
        A1[2][1] = trread(kb1 + 1024);
        SCHEDB();
        acc[4] = MFMA16(cat8(A0[3][0], A0[3][1]), U0[1], acc[4]);
        A1[3][0] = trread(kb1 + 1544);
        SCHEDB();
        acc[5] = MFMA16(cat8(A0[3][0], A0[3][1]), U0[2], acc[5]);
        A1[3][1] = trread(kb1 + 1536);
        SCHEDB();
        acc[6] = MFMA16(cat8(A0[3][0], A0[3][1]), U0[3], acc[6]);
        A1[4][0] = trread(kb1 + 2056);
        SCHEDB();
        acc[7] = MFMA16(cat8(A0[3][0], A0[3][1]), U0[4], acc[7]);
        A1[4][1] = trread(kb1 + 2048);
        SCHEDB();
        acc[4] = MFMA16(cat8(A0[4][0], A0[4][1]), U0[0], acc[4]);
        A1[5][0] = trread(kb1 + 2568);
        SCHEDB();
        acc[5] = MFMA16(cat8(A0[4][0], A0[4][1]), U0[1], acc[5]);
        A1[5][1] = trread(kb1 + 2560);
        SCHEDB();
        acc[6] = MFMA16(cat8(A0[4][0], A0[4][1]), U0[2], acc[6]);
        A1[6][0] = trread(kb1 + 3080);
        SCHEDB();
        acc[7] = MFMA16(cat8(A0[4][0], A0[4][1]), U0[3], acc[7]);
        A1[6][1] = trread(kb1 + 3072);
        SCHEDB();
        acc[5] = MFMA16(cat8(A0[5][0], A0[5][1]), U0[0], acc[5]);
        A1[7][0] = trread(kb1 + 3592);
        SCHEDB();
        acc[6] = MFMA16(cat8(A0[5][0], A0[5][1]), U0[1], acc[6]);
        A1[7][1] = trread(kb1 + 3584);
        SCHEDB();
        acc[7] = MFMA16(cat8(A0[5][0], A0[5][1]), U0[2], acc[7]);
        SCHEDB();
        acc[6] = MFMA16(cat8(A0[6][0], A0[6][1]), U0[0], acc[6]);
        SCHEDB();
        acc[7] = MFMA16(cat8(A0[6][0], A0[6][1]), U0[1], acc[7]);
        SCHEDB();
        acc[7] = MFMA16(cat8(A0[7][0], A0[7][1]), U0[0], acc[7]);
        SCHEDB();
        acc[0] = MFMA16(cat8(A1[0][0], A1[0][1]), U1[0], acc[0]); SCHEDB();
        acc[1] = MFMA16(cat8(A1[0][0], A1[0][1]), U1[1], acc[1]); SCHEDB();
        acc[2] = MFMA16(cat8(A1[0][0], A1[0][1]), U1[2], acc[2]); SCHEDB();
        acc[3] = MFMA16(cat8(A1[0][0], A1[0][1]), U1[3], acc[3]); SCHEDB();
        acc[4] = MFMA16(cat8(A1[0][0], A1[0][1]), U1[4], acc[4]); SCHEDB();
        acc[5] = MFMA16(cat8(A1[0][0], A1[0][1]), U1[5], acc[5]); SCHEDB();
        acc[6] = MFMA16(cat8(A1[0][0], A1[0][1]), U1[6], acc[6]); SCHEDB();
        acc[7] = MFMA16(cat8(A1[0][0], A1[0][1]), U1[7], acc[7]); SCHEDB();
        acc[1] = MFMA16(cat8(A1[1][0], A1[1][1]), U1[0], acc[1]); SCHEDB();
        acc[2] = MFMA16(cat8(A1[1][0], A1[1][1]), U1[1], acc[2]); SCHEDB();
        acc[3] = MFMA16(cat8(A1[1][0], A1[1][1]), U1[2], acc[3]); SCHEDB();
        acc[4] = MFMA16(cat8(A1[1][0], A1[1][1]), U1[3], acc[4]); SCHEDB();
        acc[5] = MFMA16(cat8(A1[1][0], A1[1][1]), U1[4], acc[5]); SCHEDB();
        acc[6] = MFMA16(cat8(A1[1][0], A1[1][1]), U1[5], acc[6]); SCHEDB();
        acc[7] = MFMA16(cat8(A1[1][0], A1[1][1]), U1[6], acc[7]); SCHEDB();
        acc[2] = MFMA16(cat8(A1[2][0], A1[2][1]), U1[0], acc[2]); SCHEDB();
        acc[3] = MFMA16(cat8(A1[2][0], A1[2][1]), U1[1], acc[3]); SCHEDB();
        acc[4] = MFMA16(cat8(A1[2][0], A1[2][1]), U1[2], acc[4]); SCHEDB();
        acc[5] = MFMA16(cat8(A1[2][0], A1[2][1]), U1[3], acc[5]); SCHEDB();
        acc[6] = MFMA16(cat8(A1[2][0], A1[2][1]), U1[4], acc[6]); SCHEDB();
        acc[7] = MFMA16(cat8(A1[2][0], A1[2][1]), U1[5], acc[7]); SCHEDB();
        acc[3] = MFMA16(cat8(A1[3][0], A1[3][1]), U1[0], acc[3]); SCHEDB();
        acc[4] = MFMA16(cat8(A1[3][0], A1[3][1]), U1[1], acc[4]); SCHEDB();
        acc[5] = MFMA16(cat8(A1[3][0], A1[3][1]), U1[2], acc[5]); SCHEDB();
        acc[6] = MFMA16(cat8(A1[3][0], A1[3][1]), U1[3], acc[6]); SCHEDB();
        acc[7] = MFMA16(cat8(A1[3][0], A1[3][1]), U1[4], acc[7]); SCHEDB();
        acc[4] = MFMA16(cat8(A1[4][0], A1[4][1]), U1[0], acc[4]); SCHEDB();
        acc[5] = MFMA16(cat8(A1[4][0], A1[4][1]), U1[1], acc[5]); SCHEDB();
        acc[6] = MFMA16(cat8(A1[4][0], A1[4][1]), U1[2], acc[6]); SCHEDB();
        acc[7] = MFMA16(cat8(A1[4][0], A1[4][1]), U1[3], acc[7]); SCHEDB();
        acc[5] = MFMA16(cat8(A1[5][0], A1[5][1]), U1[0], acc[5]); SCHEDB();
        acc[6] = MFMA16(cat8(A1[5][0], A1[5][1]), U1[1], acc[6]); SCHEDB();
        acc[7] = MFMA16(cat8(A1[5][0], A1[5][1]), U1[2], acc[7]); SCHEDB();
        acc[6] = MFMA16(cat8(A1[6][0], A1[6][1]), U1[0], acc[6]); SCHEDB();
        acc[7] = MFMA16(cat8(A1[6][0], A1[6][1]), U1[1], acc[7]); SCHEDB();
        acc[7] = MFMA16(cat8(A1[7][0], A1[7][1]), U1[0], acc[7]); SCHEDB();
    }
    STAMP(ws, 0, 5);
    lds_sync(smem, sb, 8, lane);
    {
        char* rb = smem + sB + w * 6144 + lane * 16;
        int slot = 0;
#pragma unroll
        for (int tau = 0; tau < 8; ++tau) { if ((tau >> 1) != w) { *(f32x4*)(rb + slot * 1024) = acc[tau]; ++slot; } }
    }
    lds_sync(smem, sb, 12, lane);
    {
#pragma unroll
        for (int i = 0; i < 2; ++i) {
            f32x4 y;
            if (w == 0) y = acc[i]; else if (w == 1) y = acc[2 + i]; else if (w == 2) y = acc[4 + i]; else y = acc[6 + i];
            const int tau = 2 * w + i;
#pragma unroll
            for (int ow = 0; ow < 4; ++ow) {
                if (ow != w) {
                    const int sl = (tau < 2 * ow) ? tau : tau - 2;
                    y += *(const f32x4*)(smem + sB + ow * 6144 + sl * 1024 + lane * 16);
                }
            }
            const int l0 = 256 * tau + 16 * al + 4 * g;
            const u32x2 uv = *(const u32x2*)(smem + sU + 480 + 2 * l0), qv = *(const u32x2*)(smem + sU + 4608 + 2 * l0);
            float hv[4];
#pragma unroll
            for (int r = 0; r < 4; ++r) {
                const u32 uu = uv[r >> 1], qq = qv[r >> 1];
                const float u = (r & 1) ? bfhi(uu) : bflo(uu), q0 = (r & 1) ? bfhi(qq) : bflo(qq);
                const u32 yk = cvtpk(y[r], u * dsk);
                hv[r] = q0 + rbf(bflo(yk) + bfhi(yk));
            }
            st8<16>(wsr, WS_HC + (d * L_ + l0) * 2, u32x2{cvtpk(hv[0], hv[1]), cvtpk(hv[2], hv[3])});
        }
    }
    STAMP(ws, 0, 6);
    return tag;
}

#define G1_UNROLL 1
constexpr int G1_STAGE = 40960, G1_NS = 4, G1_NST = 10;
constexpr int G1_LDS = G1_NS * G1_STAGE;

DI void phase_gemm1(char* __restrict__ ws, char* smem, u32 tag) {
    const int b = blockIdx.x, t = threadIdx.x, lane = t & 63;
    const int w = __builtin_amdgcn_readfirstlane(t >> 6);
    const int xcd = b & 7, j = b >> 3;
    const int P = (xcd & 1) * 8 + (j & 7), S = (xcd >> 1) * 4 + (j >> 3);
    const u32 lds0 = (u32)(uintptr_t)smem;
    const rsrc_t wsr = mkrsrc(ws, WS_END);
    WAITV(0);
    BAR();
    if (t == 0) st_flag(ws, WS_F2 + b * 64, tag);
    if (w == 8 || w == 9) poll_flags(wsr, WS_FW + (w - 8) * 64 * 64, 64, tag, lane);
    BAR();
    if (w >= 4) {
        const int lw = w - 4;
        const char* wsrc = ws + WS_W1T + S * 10 * 24576 + lw * 1024 + lane * 16;
        const int hk = 16 * ((lw >> 2) & 1) + 4 * ((lw >> 1) & 1) + 8 * ((lane >> 3) & 1) + ((lane >> 1) & 3);
        const char* hsrc = ws + WS_HC + (hk * L_ + 128 * P + 64 * (lw & 1) + 16 * (lane >> 4) + 8 * (lane & 1)) * 2;
        auto issue_h = [&](int st, int slot) {
#pragma unroll
            for (int i = 0; i < 2; ++i) glds16(hsrc + (st * 64 + 32 * i) * L_ * 2, lds0 + slot * G1_STAGE + 24576 + (lw + 8 * i) * 1024);
        };
        auto issue_w = [&](int st, int slot) {
#pragma unroll
            for (int i = 0; i < 3; ++i) glds16(wsrc + st * 24576 + i * 8192, lds0 + slot * G1_STAGE + lw * 1024 + i * 8192);
        };
        issue_w(0, 0); issue_w(1, 1); issue_w(2, 2); issue_w(3, 3);
        if (lw < 4) poll_flags(wsr, WS_F2 + lw * 64 * 64, 64, tag, lane);
        BAR();
        if (b == 0 && t == 256) st_flag(ws, WS_EP, tag);
        issue_h(0, 0); issue_h(1, 1); issue_h(2, 2); issue_h(3, 3);
        waitv<6>(); BAR();
        waitv<4>(); BAR(); issue_h(4, 0); issue_w(4, 0);
        waitv<7>(); BAR(); issue_h(5, 1); issue_w(5, 1);
        waitv<10>(); BAR(); issue_h(6, 2); issue_w(6, 2);
        waitv<10>(); BAR(); issue_h(7, 3); issue_w(7, 3);
        waitv<10>(); BAR(); issue_h(8, 0); issue_w(8, 0);
        waitv<10>(); BAR(); issue_h(9, 1); issue_w(9, 1);
        waitv<10>(); BAR();
        waitv<5>(); BAR();
        waitv<0>(); BAR();
        BAR();
        BAR();
        if (lw == 0) {
            const int r = lane & 7, part = lane >> 3;
            float s1 = 0.f, s2 = 0.f;
#pragma unroll
            for (int i = 0; i < 8; ++i) {
                const int e = 8 * part + i;
                const f32x4 v = *(const f32x4*)(smem + ((e >> 4) * 64 + (e & 15) * 4 + (r >> 1)) * 16);
                s1 += (r & 1) ? v[1] : v[0]; s2 += (r & 1) ? v[3] : v[2];
            }
            s1 = dppadd<0x128>(s1); s2 = dppadd<0x128>(s2);
            s1 = swap32add(swap16add(s1)); s2 = swap32add(swap16add(s2));
            const float mean = s1 * (1.f / 640.f);
            float var = s2 * (1.f / 640.f) - mean * mean;
            var = var > 0.f ? var : 0.f;
            const float rs = 1.f / rbf(__builtin_sqrtf(rbf(rbf(var) + 1e-5f)));
            if (lane < 8) st4<16>(wsr, WS_RS + (128 * P + 8 * S + lane) * 4, __float_as_uint(rs * rs));
        }
        return;
    }
    BAR();
    STAMP(ws, 4, 0);
    const int wm = w & 1, wn = w >> 1;
    const int g = lane >> 4, q = (lane & 15) >> 2, p = lane & 3;
    f32x4 acc[6][4];
#pragma unroll
    for (int a = 0; a < 6; ++a)
#pragma unroll
        for (int c = 0; c < 4; ++c) acc[a][c] = f32x4{0.f, 0.f, 0.f, 0.f};
    const LAS char* abase = (const LAS char*)smem + wm * 6144 + lane * 16;
    const LAS char* bbase = (const LAS char*)smem + 24576 + (g >> 1) * 4096 + (g & 1) * 128 + q * 32 + 8 * p + wn * 1024;
    const LAS char* sbase = (const LAS char*)smem + 24576 + (2 * w + (lane >> 5)) * 2048 + (S >> 1) * 256 + ((lane >> 2) & 7) * 32 + (S & 1) * 16 + (lane & 3) * 4;
    float s1a = 0.f, s1b = 0.f, s2a = 0.f, s2b = 0.f;
    bf16x8 AL[3], AH[3]; s16x4 B0[4][2], B1[4][2];
    u32 sv;
    BAR();
    STAMP(ws, 4, 1);
    AL[0] = *(const LAS bf16x8*)(abase + 0); B0[0][0] = trread(bbase + 0); B0[0][1] = trread(bbase + 2048); B0[1][0] = trread(bbase + 256); B0[1][1] = trread(bbase + 2304); AL[1] = *(const LAS bf16x8*)(abase + 1024); B0[2][0] = trread(bbase + 512); B0[2][1] = trread(bbase + 2560); B0[3][0] = trread(bbase + 768); B0[3][1] = trread(bbase + 2816); AL[2] = *(const LAS bf16x8*)(abase + 2048);
    SCHEDB();
#pragma unroll 1
    for (int st = 0; st < G1_NST; ++st) {
        acc[0+0][0] = MFMA16(AL[0], cat8(B0[0][0], B0[0][1]), acc[0+0][0]);
        AH[0] = *(const LAS bf16x8*)(abase + 3072);
        SCHEDB();
        acc[0+0][1] = MFMA16(AL[0], cat8(B0[1][0], B0[1][1]), acc[0+0][1]);
        AH[1] = *(const LAS bf16x8*)(abase + 4096);
        SCHEDB();
        acc[0+1][0] = MFMA16(AL[1], cat8(B0[0][0], B0[0][1]), acc[0+1][0]);
        B1[0][0] = trread(bbase + 8192);
        SCHEDB();
        acc[0+1][1] = MFMA16(AL[1], cat8(B0[1][0], B0[1][1]), acc[0+1][1]);
        B1[0][1] = trread(bbase + 10240);
        SCHEDB();
        acc[0+0][2] = MFMA16(AL[0], cat8(B0[2][0], B0[2][1]), acc[0+0][2]);
        B1[1][0] = trread(bbase + 8448);
        SCHEDB();
        acc[0+1][2] = MFMA16(AL[1], cat8(B0[2][0], B0[2][1]), acc[0+1][2]);
        B1[1][1] = trread(bbase + 10496);
        SCHEDB();
        acc[0+0][3] = MFMA16(AL[0], cat8(B0[3][0], B0[3][1]), acc[0+0][3]);
        AH[2] = *(const LAS bf16x8*)(abase + 5120);
        SCHEDB();
        acc[0+1][3] = MFMA16(AL[1], cat8(B0[3][0], B0[3][1]), acc[0+1][3]);
        SCHEDB();
        acc[0+2][0] = MFMA16(AL[2], cat8(B0[0][0], B0[0][1]), acc[0+2][0]);
        SCHEDB();
        acc[0+2][1] = MFMA16(AL[2], cat8(B0[1][0], B0[1][1]), acc[0+2][1]);
        SCHEDB();
        acc[0+2][2] = MFMA16(AL[2], cat8(B0[2][0], B0[2][1]), acc[0+2][2]);
        SCHEDB();
        acc[0+2][3] = MFMA16(AL[2], cat8(B0[3][0], B0[3][1]), acc[0+2][3]);
        SCHEDB();
        acc[3+0][0] = MFMA16(AH[0], cat8(B0[0][0], B0[0][1]), acc[3+0][0]);
        AL[0] = *(const LAS bf16x8*)(abase + 12288);
        SCHEDB();
        acc[3+0][1] = MFMA16(AH[0], cat8(B0[1][0], B0[1][1]), acc[3+0][1]);
        AL[1] = *(const LAS bf16x8*)(abase + 13312);
        SCHEDB();
        acc[3+1][0] = MFMA16(AH[1], cat8(B0[0][0], B0[0][1]), acc[3+1][0]);
        sv = *(const LAS u32*)(sbase);
        SCHEDB();
        acc[3+1][1] = MFMA16(AH[1], cat8(B0[1][0], B0[1][1]), acc[3+1][1]);
        B1[2][0] = trread(bbase + 8704);
        SCHEDB();
        acc[3+0][2] = MFMA16(AH[0], cat8(B0[2][0], B0[2][1]), acc[3+0][2]);
        B1[2][1] = trread(bbase + 10752);
        SCHEDB();
        acc[3+1][2] = MFMA16(AH[1], cat8(B0[2][0], B0[2][1]), acc[3+1][2]);
        B1[3][0] = trread(bbase + 8960);
        SCHEDB();
        acc[3+0][3] = MFMA16(AH[0], cat8(B0[3][0], B0[3][1]), acc[3+0][3]);
        B1[3][1] = trread(bbase + 11008);
        SCHEDB();
        acc[3+1][3] = MFMA16(AH[1], cat8(B0[3][0], B0[3][1]), acc[3+1][3]);
        AL[2] = *(const LAS bf16x8*)(abase + 14336);
        SCHEDB();
        acc[3+2][0] = MFMA16(AH[2], cat8(B0[0][0], B0[0][1]), acc[3+2][0]);
        SCHEDB();
        acc[3+2][1] = MFMA16(AH[2], cat8(B0[1][0], B0[1][1]), acc[3+2][1]);
        SCHEDB();
        acc[3+2][2] = MFMA16(AH[2], cat8(B0[2][0], B0[2][1]), acc[3+2][2]);
        SCHEDB();
        acc[3+2][3] = MFMA16(AH[2], cat8(B0[3][0], B0[3][1]), acc[3+2][3]);
        SCHEDB();
        acc[0+0][0] = MFMA16(AL[0], cat8(B1[0][0], B1[0][1]), acc[0+0][0]);
        AH[0] = *(const LAS bf16x8*)(abase + 15360);
        SCHEDB();
        acc[0+0][1] = MFMA16(AL[0], cat8(B1[1][0], B1[1][1]), acc[0+0][1]);
        AH[1] = *(const LAS bf16x8*)(abase + 16384);
        SCHEDB();
        acc[0+1][0] = MFMA16(AL[1], cat8(B1[0][0], B1[0][1]), acc[0+1][0]);
        AH[2] = *(const LAS bf16x8*)(abase + 17408);
        SCHEDB();
        acc[0+1][1] = MFMA16(AL[1], cat8(B1[1][0], B1[1][1]), acc[0+1][1]);
        SCHEDB();
        acc[0+0][2] = MFMA16(AL[0], cat8(B1[2][0], B1[2][1]), acc[0+0][2]);
        SCHEDB();
        acc[0+1][2] = MFMA16(AL[1], cat8(B1[2][0], B1[2][1]), acc[0+1][2]);
        SCHEDB();
        acc[0+0][3] = MFMA16(AL[0], cat8(B1[3][0], B1[3][1]), acc[0+0][3]);
        SCHEDB();
        acc[0+1][3] = MFMA16(AL[1], cat8(B1[3][0], B1[3][1]), acc[0+1][3]);
        SCHEDB();
        acc[0+2][0] = MFMA16(AL[2], cat8(B1[0][0], B1[0][1]), acc[0+2][0]);
        SCHEDB();
        acc[0+2][1] = MFMA16(AL[2], cat8(B1[1][0], B1[1][1]), acc[0+2][1]);
        SCHEDB();
        acc[0+2][2] = MFMA16(AL[2], cat8(B1[2][0], B1[2][1]), acc[0+2][2]);
        SCHEDB();
        acc[0+2][3] = MFMA16(AL[2], cat8(B1[3][0], B1[3][1]), acc[0+2][3]);
        SCHEDB();
        { const float a = bflo(sv), c = bfhi(sv); s1a += a; s1b += c; s2a = __builtin_fmaf(a, a, s2a); s2b = __builtin_fmaf(c, c, s2b); }
        WAITL0();
        BAR();
        { const int dlt = (st & 3) == 3 ? -3 * G1_STAGE : G1_STAGE; abase += dlt; bbase += dlt; sbase += dlt; }
        acc[3+0][0] = MFMA16(AH[0], cat8(B1[0][0], B1[0][1]), acc[3+0][0]);
        AL[0] = *(const LAS bf16x8*)(abase + 0);
        SCHEDB();
        acc[3+0][1] = MFMA16(AH[0], cat8(B1[1][0], B1[1][1]), acc[3+0][1]);
        B0[0][0] = trread(bbase + 0);
        SCHEDB();
        acc[3+1][0] = MFMA16(AH[1], cat8(B1[0][0], B1[0][1]), acc[3+1][0]);
        B0[0][1] = trread(bbase + 2048);
        SCHEDB();
        acc[3+1][1] = MFMA16(AH[1], cat8(B1[1][0], B1[1][1]), acc[3+1][1]);
        B0[1][0] = trread(bbase + 256);
        SCHEDB();
        acc[3+0][2] = MFMA16(AH[0], cat8(B1[2][0], B1[2][1]), acc[3+0][2]);
        B0[1][1] = trread(bbase + 2304);
        SCHEDB();
        acc[3+1][2] = MFMA16(AH[1], cat8(B1[2][0], B1[2][1]), acc[3+1][2]);
        AL[1] = *(const LAS bf16x8*)(abase + 1024);
        SCHEDB();
        acc[3+0][3] = MFMA16(AH[0], cat8(B1[3][0], B1[3][1]), acc[3+0][3]);
        B0[2][0] = trread(bbase + 512);
        SCHEDB();
        acc[3+1][3] = MFMA16(AH[1], cat8(B1[3][0], B1[3][1]), acc[3+1][3]);
        B0[2][1] = trread(bbase + 2560);
        SCHEDB();
        acc[3+2][0] = MFMA16(AH[2], cat8(B1[0][0], B1[0][1]), acc[3+2][0]);
        B0[3][0] = trread(bbase + 768);
        SCHEDB();
        acc[3+2][1] = MFMA16(AH[2], cat8(B1[1][0], B1[1][1]), acc[3+2][1]);
        B0[3][1] = trread(bbase + 2816);
        SCHEDB();
        acc[3+2][2] = MFMA16(AH[2], cat8(B1[2][0], B1[2][1]), acc[3+2][2]);
        AL[2] = *(const LAS bf16x8*)(abase + 2048);
        SCHEDB();
        acc[3+2][3] = MFMA16(AH[2], cat8(B1[3][0], B1[3][1]), acc[3+2][3]);
        SCHEDB();
    }
    STAMP(ws, 4, 3);
    *(f32x4*)(smem + (w * 64 + lane) * 16) = f32x4{s1a, s1b, s2a, s2b};
    WAITL0();
    BAR();
    const rsrc_t rs = wsr;
#pragma unroll
    for (int a = 0; a < 3; ++a) {
        const int f0 = 96 * S + 16 * (3 * wm + a);
        const int kb = f0 >> 5, oct = ((f0 & 31) >> 3) + (g >> 1);
#pragma unroll
        for (int c = 0; c < 4; c += 2) {
            u32 X[2], Y[2];
#pragma unroll
            for (int e = 0; e < 2; ++e) {
                X[e] = cvtpk(__builtin_fmaxf(acc[2 * a][c][2 * e], 0.f) * acc[2 * a + 1][c][2 * e], __builtin_fmaxf(acc[2 * a][c][2 * e + 1], 0.f) * acc[2 * a + 1][c][2 * e + 1]);
                Y[e] = cvtpk(__builtin_fmaxf(acc[2 * a][c + 1][2 * e], 0.f) * acc[2 * a + 1][c + 1][2 * e], __builtin_fmaxf(acc[2 * a][c + 1][2 * e + 1], 0.f) * acc[2 * a + 1][c + 1][2 * e + 1]);
            }
#pragma unroll
            for (int e = 0; e < 2; ++e) { auto r = __builtin_amdgcn_permlane16_swap(X[e], Y[e], false, false); X[e] = r[0]; Y[e] = r[1]; }
            const int lt = 8 * P + 4 * wn + c + (g & 1);
            st16<16>(rs, WS_HM + ((lt * 48 + kb) * 64 + (lane & 15) + 16 * oct) * 16, u32x4{X[0], X[1], Y[0], Y[1]});
        }
    }
    STAMP(ws, 4, 4);
}

constexpr int G2_STAGE = 18432, G2_NS = 8, G2_NST = 24;
constexpr int G2_RES = G2_NS * G2_STAGE;
constexpr int G2_LDS = G2_RES + 10240;

DI void phase_gemm2(char* __restrict__ ws, u16* __restrict__ out, char* smem, u32 tag) {
    const int b = blockIdx.x, t = threadIdx.x, lane = t & 63;
    const int w = __builtin_amdgcn_readfirstlane(t >> 6);
    const int xcd = b & 7, j = b >> 3;
    const int P = (xcd & 3) * 8 + (j & 7), S = (xcd >> 2) * 4 + (j >> 3);
    const u32 lds0 = (u32)(uintptr_t)smem;
    const rsrc_t wsr = mkrsrc(ws, WS_END);
    WAITV(0);
    BAR();
    if (t == 0) { const int P1 = (xcd & 1) * 8 + (j & 7), S1 = (xcd >> 1) * 4 + (j >> 3); st_flag(ws, WS_F3 + (P1 * 16 + S1) * 64, tag); }
    if (w >= 4) {
        const int lw = w - 4;
        const char* wsrc = ws + WS_W2T + S * 24 * 10240 + lw * 1024 + lane * 16;
        const char* msrc = ws + WS_HM + ((4 * P + (lw >> 1)) * 48 + (lw & 1)) * 1024 + lane * 16;
        auto issue_m = [&](int st, int slot) { glds16(msrc + st * 2048, lds0 + slot * G2_STAGE + 10240 + lw * 1024); };
        auto issue_w = [&](int st, int slot) {
            glds16(wsrc + st * 10240, lds0 + slot * G2_STAGE + lw * 1024);
            if (lw < 2) glds16(wsrc + st * 10240 + 8192, lds0 + slot * G2_STAGE + 8192 + lw * 1024);
        };
        auto residual = [&]() {
            for (int pc = lw; pc < 10; pc += 8) {
                const int row = 8 * pc + (lane >> 3), ch = (lane & 7) ^ (((lane >> 3) & 3) << 1);
                glds16(ws + WS_HC + ((80 * S + row) * L_ + 64 * P + 8 * ch) * 2, lds0 + G2_RES + pc * 1024);
            }
        };
        if (lw < 2) {
            issue_w(0, 0);
            issue_w(1, 1);
            issue_w(2, 2);
            issue_w(3, 3);
            issue_w(4, 4);
            issue_w(5, 5);
            issue_w(6, 6);
            issue_w(7, 7);
            if (lw == 0) poll_flags(wsr, WS_F3 + (P >> 1) * 16 * 64, 16, tag, lane);
            BAR();
            issue_m(0, 0);
            issue_m(1, 1);
            issue_m(2, 2);
            issue_m(3, 3);
            issue_m(4, 4);
            issue_m(5, 5);
            issue_m(6, 6);
            issue_m(7, 7);
            waitv<7>(); BAR();
            waitv<6>(); BAR();
            waitv<5>(); BAR();
            issue_m(8, 0); issue_w(8, 0);
            waitv<7>(); BAR();
            issue_m(9, 1); issue_w(9, 1);
            waitv<9>(); BAR();
            issue_m(10, 2); issue_w(10, 2);
            waitv<11>(); BAR();
            issue_m(11, 3); issue_w(11, 3);
            waitv<13>(); BAR();
            issue_m(12, 4); issue_w(12, 4);
            waitv<15>(); BAR();
            issue_m(13, 5); issue_w(13, 5);
            waitv<15>(); BAR();
            issue_m(14, 6); issue_w(14, 6);
            waitv<15>(); BAR();
            issue_m(15, 7); issue_w(15, 7);
            waitv<15>(); BAR();
            issue_m(16, 0); issue_w(16, 0);
            waitv<15>(); BAR();
            issue_m(17, 1); issue_w(17, 1);
            waitv<15>(); BAR();
            issue_m(18, 2); issue_w(18, 2);
            waitv<15>(); BAR();
            issue_m(19, 3); issue_w(19, 3);
            waitv<15>(); BAR();
            issue_m(20, 4); issue_w(20, 4);
            waitv<15>(); BAR();
            issue_m(21, 5); issue_w(21, 5);
            waitv<15>(); BAR();
            issue_m(22, 6); issue_w(22, 6);
            waitv<15>(); BAR();
            issue_m(23, 7); issue_w(23, 7);
            waitv<15>(); BAR();
            residual();
            waitv<12>(); BAR();
            waitv<9>(); BAR();
            waitv<6>(); BAR();
            waitv<3>(); BAR();
            waitv<0>(); BAR();
        } else {
            issue_w(0, 0);
            issue_w(1, 1);
            issue_w(2, 2);
            issue_w(3, 3);
            issue_w(4, 4);
            issue_w(5, 5);
            issue_w(6, 6);
            issue_w(7, 7);
            if (lw == 0) poll_flags(wsr, WS_F3 + (P >> 1) * 16 * 64, 16, tag, lane);
            BAR();
            issue_m(0, 0);
            issue_m(1, 1);
            issue_m(2, 2);
            issue_m(3, 3);
            issue_m(4, 4);
            issue_m(5, 5);
            issue_m(6, 6);
            issue_m(7, 7);
            waitv<7>(); BAR();
            waitv<6>(); BAR();
            waitv<5>(); BAR();
            issue_m(8, 0); issue_w(8, 0);
            waitv<6>(); BAR();
            issue_m(9, 1); issue_w(9, 1);
            waitv<7>(); BAR();
            issue_m(10, 2); issue_w(10, 2);
            waitv<8>(); BAR();
            issue_m(11, 3); issue_w(11, 3);
            waitv<9>(); BAR();
            issue_m(12, 4); issue_w(12, 4);
            waitv<10>(); BAR();
            issue_m(13, 5); issue_w(13, 5);
            waitv<10>(); BAR();
            issue_m(14, 6); issue_w(14, 6);
            waitv<10>(); BAR();
            issue_m(15, 7); issue_w(15, 7);
            waitv<10>(); BAR();
            issue_m(16, 0); issue_w(16, 0);
            waitv<10>(); BAR();
            issue_m(17, 1); issue_w(17, 1);
            waitv<10>(); BAR();
            issue_m(18, 2); issue_w(18, 2);
            waitv<10>(); BAR();
            issue_m(19, 3); issue_w(19, 3);
            waitv<10>(); BAR();
            issue_m(20, 4); issue_w(20, 4);
            waitv<10>(); BAR();
            issue_m(21, 5); issue_w(21, 5);
            waitv<10>(); BAR();
            issue_m(22, 6); issue_w(22, 6);
            waitv<10>(); BAR();
            issue_m(23, 7); issue_w(23, 7);
            waitv<10>(); BAR();
            residual();
            waitv<8>(); BAR();
            waitv<6>(); BAR();
            waitv<4>(); BAR();
            waitv<2>(); BAR();
            waitv<0>(); BAR();
        }
        return;
    }
    BAR();
    STAMP(ws, 5, 0);
    const int g = lane >> 4, q = (lane & 15) >> 2, p = lane & 3;
    const int l = 64 * P + 16 * w + (lane & 15);
    const float rs2 = __uint_as_float(__builtin_amdgcn_raw_buffer_load_b32(wsr, WS_RS + l * 4, 0, 16));
    const float m1 = __uint_as_float(__builtin_amdgcn_raw_buffer_load_b32(wsr, WS_ST + l * 4, 0, 16));
    f32x4 acc[5];
#pragma unroll
    for (int a = 0; a < 5; ++a) acc[a] = f32x4{0.f, 0.f, 0.f, 0.f};
    const LAS char* abase = (const LAS char*)smem + lane * 16;
    const LAS char* bbase = (const LAS char*)smem + 10240 + w * 2048 + lane * 16;
    bf16x8 EA[2][5], EB[2], OA[2][5], OB[2];
    BAR();
    STAMP(ws, 5, 1);
    EB[0] = *(const LAS bf16x8*)(bbase); EB[1] = *(const LAS bf16x8*)(bbase + 1024);
#pragma unroll
    for (int a = 0; a < 5; ++a) { EA[0][a] = *(const LAS bf16x8*)(abase + a * 1024); EA[1][a] = *(const LAS bf16x8*)(abase + 5120 + a * 1024); }
    SCHEDB();
#pragma unroll 1
    for (int it = 0; it < 3; ++it) {
        if (it == 1) STAMP(ws, 5, 2); if (it == 2) STAMP(ws, 5, 3);
        BAR();
        acc[0] = MFMA16(EA[0][0], EB[0], acc[0]);
        OB[0] = *(const LAS bf16x8*)(bbase + 1*G2_STAGE);
        OA[0][0] = *(const LAS bf16x8*)(abase + 1*G2_STAGE + 0);
        SCHEDB();
        acc[1] = MFMA16(EA[0][1], EB[0], acc[1]);
        OA[0][1] = *(const LAS bf16x8*)(abase + 1*G2_STAGE + 1024);
        OA[0][2] = *(const LAS bf16x8*)(abase + 1*G2_STAGE + 2048);
        SCHEDB();
        acc[2] = MFMA16(EA[0][2], EB[0], acc[2]);
        OA[0][3] = *(const LAS bf16x8*)(abase + 1*G2_STAGE + 3072);
        SCHEDB();
        acc[3] = MFMA16(EA[0][3], EB[0], acc[3]);
        OA[0][4] = *(const LAS bf16x8*)(abase + 1*G2_STAGE + 4096);
        SCHEDB();
        acc[4] = MFMA16(EA[0][4], EB[0], acc[4]);
        OB[1] = *(const LAS bf16x8*)(bbase + 1*G2_STAGE + 1024);
        SCHEDB();
        acc[0] = MFMA16(EA[1][0], EB[1], acc[0]);
        OA[1][0] = *(const LAS bf16x8*)(abase + 1*G2_STAGE + 5120);
        SCHEDB();
        acc[1] = MFMA16(EA[1][1], EB[1], acc[1]);
        OA[1][1] = *(const LAS bf16x8*)(abase + 1*G2_STAGE + 6144);
        SCHEDB();
        acc[2] = MFMA16(EA[1][2], EB[1], acc[2]);
        OA[1][2] = *(const LAS bf16x8*)(abase + 1*G2_STAGE + 7168);
        SCHEDB();
        acc[3] = MFMA16(EA[1][3], EB[1], acc[3]);
        OA[1][3] = *(const LAS bf16x8*)(abase + 1*G2_STAGE + 8192);
        SCHEDB();
        acc[4] = MFMA16(EA[1][4], EB[1], acc[4]);
        OA[1][4] = *(const LAS bf16x8*)(abase + 1*G2_STAGE + 9216);
        SCHEDB();
        BAR();
        acc[0] = MFMA16(OA[0][0], OB[0], acc[0]);
        EB[0] = *(const LAS bf16x8*)(bbase + 2*G2_STAGE);
        EA[0][0] = *(const LAS bf16x8*)(abase + 2*G2_STAGE + 0);
        SCHEDB();
        acc[1] = MFMA16(OA[0][1], OB[0], acc[1]);
        EA[0][1] = *(const LAS bf16x8*)(abase + 2*G2_STAGE + 1024);
        EA[0][2] = *(const LAS bf16x8*)(abase + 2*G2_STAGE + 2048);
        SCHEDB();
        acc[2] = MFMA16(OA[0][2], OB[0], acc[2]);
        EA[0][3] = *(const LAS bf16x8*)(abase + 2*G2_STAGE + 3072);
        SCHEDB();
        acc[3] = MFMA16(OA[0][3], OB[0], acc[3]);
        EA[0][4] = *(const LAS bf16x8*)(abase + 2*G2_STAGE + 4096);
        SCHEDB();
        acc[4] = MFMA16(OA[0][4], OB[0], acc[4]);
        EB[1] = *(const LAS bf16x8*)(bbase + 2*G2_STAGE + 1024);
        SCHEDB();
        acc[0] = MFMA16(OA[1][0], OB[1], acc[0]);
        EA[1][0] = *(const LAS bf16x8*)(abase + 2*G2_STAGE + 5120);
        SCHEDB();
        acc[1] = MFMA16(OA[1][1], OB[1], acc[1]);
        EA[1][1] = *(const LAS bf16x8*)(abase + 2*G2_STAGE + 6144);
        SCHEDB();
        acc[2] = MFMA16(OA[1][2], OB[1], acc[2]);
        EA[1][2] = *(const LAS bf16x8*)(abase + 2*G2_STAGE + 7168);
        SCHEDB();
        acc[3] = MFMA16(OA[1][3], OB[1], acc[3]);
        EA[1][3] = *(const LAS bf16x8*)(abase + 2*G2_STAGE + 8192);
        SCHEDB();
        acc[4] = MFMA16(OA[1][4], OB[1], acc[4]);
        EA[1][4] = *(const LAS bf16x8*)(abase + 2*G2_STAGE + 9216);
        SCHEDB();
        BAR();
        acc[0] = MFMA16(EA[0][0], EB[0], acc[0]);
        OB[0] = *(const LAS bf16x8*)(bbase + 3*G2_STAGE);
        OA[0][0] = *(const LAS bf16x8*)(abase + 3*G2_STAGE + 0);
        SCHEDB();
        acc[1] = MFMA16(EA[0][1], EB[0], acc[1]);
        OA[0][1] = *(const LAS bf16x8*)(abase + 3*G2_STAGE + 1024);
        OA[0][2] = *(const LAS bf16x8*)(abase + 3*G2_STAGE + 2048);
        SCHEDB();
        acc[2] = MFMA16(EA[0][2], EB[0], acc[2]);
        OA[0][3] = *(const LAS bf16x8*)(abase + 3*G2_STAGE + 3072);
        SCHEDB();
        acc[3] = MFMA16(EA[0][3], EB[0], acc[3]);
        OA[0][4] = *(const LAS bf16x8*)(abase + 3*G2_STAGE + 4096);
        SCHEDB();
        acc[4] = MFMA16(EA[0][4], EB[0], acc[4]);
        OB[1] = *(const LAS bf16x8*)(bbase + 3*G2_STAGE + 1024);
        SCHEDB();
        acc[0] = MFMA16(EA[1][0], EB[1], acc[0]);
        OA[1][0] = *(const LAS bf16x8*)(abase + 3*G2_STAGE + 5120);
        SCHEDB();
        acc[1] = MFMA16(EA[1][1], EB[1], acc[1]);
        OA[1][1] = *(const LAS bf16x8*)(abase + 3*G2_STAGE + 6144);
        SCHEDB();
        acc[2] = MFMA16(EA[1][2], EB[1], acc[2]);
        OA[1][2] = *(const LAS bf16x8*)(abase + 3*G2_STAGE + 7168);
        SCHEDB();
        acc[3] = MFMA16(EA[1][3], EB[1], acc[3]);
        OA[1][3] = *(const LAS bf16x8*)(abase + 3*G2_STAGE + 8192);
        SCHEDB();
        acc[4] = MFMA16(EA[1][4], EB[1], acc[4]);
        OA[1][4] = *(const LAS bf16x8*)(abase + 3*G2_STAGE + 9216);
        SCHEDB();
        BAR();
        acc[0] = MFMA16(OA[0][0], OB[0], acc[0]);
        EB[0] = *(const LAS bf16x8*)(bbase + 4*G2_STAGE);
        EA[0][0] = *(const LAS bf16x8*)(abase + 4*G2_STAGE + 0);
        SCHEDB();
        acc[1] = MFMA16(OA[0][1], OB[0], acc[1]);
        EA[0][1] = *(const LAS bf16x8*)(abase + 4*G2_STAGE + 1024);
        EA[0][2] = *(const LAS bf16x8*)(abase + 4*G2_STAGE + 2048);
        SCHEDB();
        acc[2] = MFMA16(OA[0][2], OB[0], acc[2]);
        EA[0][3] = *(const LAS bf16x8*)(abase + 4*G2_STAGE + 3072);
        SCHEDB();
        acc[3] = MFMA16(OA[0][3], OB[0], acc[3]);
        EA[0][4] = *(const LAS bf16x8*)(abase + 4*G2_STAGE + 4096);
        SCHEDB();
        acc[4] = MFMA16(OA[0][4], OB[0], acc[4]);
        EB[1] = *(const LAS bf16x8*)(bbase + 4*G2_STAGE + 1024);
        SCHEDB();
        acc[0] = MFMA16(OA[1][0], OB[1], acc[0]);
        EA[1][0] = *(const LAS bf16x8*)(abase + 4*G2_STAGE + 5120);
        SCHEDB();
        acc[1] = MFMA16(OA[1][1], OB[1], acc[1]);
        EA[1][1] = *(const LAS bf16x8*)(abase + 4*G2_STAGE + 6144);
        SCHEDB();
        acc[2] = MFMA16(OA[1][2], OB[1], acc[2]);
        EA[1][2] = *(const LAS bf16x8*)(abase + 4*G2_STAGE + 7168);
        SCHEDB();
        acc[3] = MFMA16(OA[1][3], OB[1], acc[3]);
        EA[1][3] = *(const LAS bf16x8*)(abase + 4*G2_STAGE + 8192);
        SCHEDB();
        acc[4] = MFMA16(OA[1][4], OB[1], acc[4]);
        EA[1][4] = *(const LAS bf16x8*)(abase + 4*G2_STAGE + 9216);
        SCHEDB();
        BAR();
        acc[0] = MFMA16(EA[0][0], EB[0], acc[0]);
        OB[0] = *(const LAS bf16x8*)(bbase + 5*G2_STAGE);
        OA[0][0] = *(const LAS bf16x8*)(abase + 5*G2_STAGE + 0);
        SCHEDB();
        acc[1] = MFMA16(EA[0][1], EB[0], acc[1]);
        OA[0][1] = *(const LAS bf16x8*)(abase + 5*G2_STAGE + 1024);
        OA[0][2] = *(const LAS bf16x8*)(abase + 5*G2_STAGE + 2048);
        SCHEDB();
        acc[2] = MFMA16(EA[0][2], EB[0], acc[2]);
        OA[0][3] = *(const LAS bf16x8*)(abase + 5*G2_STAGE + 3072);
        SCHEDB();
        acc[3] = MFMA16(EA[0][3], EB[0], acc[3]);
        OA[0][4] = *(const LAS bf16x8*)(abase + 5*G2_STAGE + 4096);
        SCHEDB();
        acc[4] = MFMA16(EA[0][4], EB[0], acc[4]);
        OB[1] = *(const LAS bf16x8*)(bbase + 5*G2_STAGE + 1024);
        SCHEDB();
        acc[0] = MFMA16(EA[1][0], EB[1], acc[0]);
        OA[1][0] = *(const LAS bf16x8*)(abase + 5*G2_STAGE + 5120);
        SCHEDB();
        acc[1] = MFMA16(EA[1][1], EB[1], acc[1]);
        OA[1][1] = *(const LAS bf16x8*)(abase + 5*G2_STAGE + 6144);
        SCHEDB();
        acc[2] = MFMA16(EA[1][2], EB[1], acc[2]);
        OA[1][2] = *(const LAS bf16x8*)(abase + 5*G2_STAGE + 7168);
        SCHEDB();
        acc[3] = MFMA16(EA[1][3], EB[1], acc[3]);
        OA[1][3] = *(const LAS bf16x8*)(abase + 5*G2_STAGE + 8192);
        SCHEDB();
        acc[4] = MFMA16(EA[1][4], EB[1], acc[4]);
        OA[1][4] = *(const LAS bf16x8*)(abase + 5*G2_STAGE + 9216);
        SCHEDB();
        BAR();
        acc[0] = MFMA16(OA[0][0], OB[0], acc[0]);
        EB[0] = *(const LAS bf16x8*)(bbase + 6*G2_STAGE);
        EA[0][0] = *(const LAS bf16x8*)(abase + 6*G2_STAGE + 0);
        SCHEDB();
        acc[1] = MFMA16(OA[0][1], OB[0], acc[1]);
        EA[0][1] = *(const LAS bf16x8*)(abase + 6*G2_STAGE + 1024);
        EA[0][2] = *(const LAS bf16x8*)(abase + 6*G2_STAGE + 2048);
        SCHEDB();
        acc[2] = MFMA16(OA[0][2], OB[0], acc[2]);
        EA[0][3] = *(const LAS bf16x8*)(abase + 6*G2_STAGE + 3072);
        SCHEDB();
        acc[3] = MFMA16(OA[0][3], OB[0], acc[3]);
        EA[0][4] = *(const LAS bf16x8*)(abase + 6*G2_STAGE + 4096);
        SCHEDB();
        acc[4] = MFMA16(OA[0][4], OB[0], acc[4]);
        EB[1] = *(const LAS bf16x8*)(bbase + 6*G2_STAGE + 1024);
        SCHEDB();
        acc[0] = MFMA16(OA[1][0], OB[1], acc[0]);
        EA[1][0] = *(const LAS bf16x8*)(abase + 6*G2_STAGE + 5120);
        SCHEDB();
        acc[1] = MFMA16(OA[1][1], OB[1], acc[1]);
        EA[1][1] = *(const LAS bf16x8*)(abase + 6*G2_STAGE + 6144);
        SCHEDB();
        acc[2] = MFMA16(OA[1][2], OB[1], acc[2]);
        EA[1][2] = *(const LAS bf16x8*)(abase + 6*G2_STAGE + 7168);
        SCHEDB();
        acc[3] = MFMA16(OA[1][3], OB[1], acc[3]);
        EA[1][3] = *(const LAS bf16x8*)(abase + 6*G2_STAGE + 8192);
        SCHEDB();
        acc[4] = MFMA16(OA[1][4], OB[1], acc[4]);
        EA[1][4] = *(const LAS bf16x8*)(abase + 6*G2_STAGE + 9216);
        SCHEDB();
        BAR();
        acc[0] = MFMA16(EA[0][0], EB[0], acc[0]);
        OB[0] = *(const LAS bf16x8*)(bbase + 7*G2_STAGE);
        OA[0][0] = *(const LAS bf16x8*)(abase + 7*G2_STAGE + 0);
        SCHEDB();
        acc[1] = MFMA16(EA[0][1], EB[0], acc[1]);
        OA[0][1] = *(const LAS bf16x8*)(abase + 7*G2_STAGE + 1024);
        OA[0][2] = *(const LAS bf16x8*)(abase + 7*G2_STAGE + 2048);
        SCHEDB();
        acc[2] = MFMA16(EA[0][2], EB[0], acc[2]);
        OA[0][3] = *(const LAS bf16x8*)(abase + 7*G2_STAGE + 3072);
        SCHEDB();
        acc[3] = MFMA16(EA[0][3], EB[0], acc[3]);
        OA[0][4] = *(const LAS bf16x8*)(abase + 7*G2_STAGE + 4096);
        SCHEDB();
        acc[4] = MFMA16(EA[0][4], EB[0], acc[4]);
        OB[1] = *(const LAS bf16x8*)(bbase + 7*G2_STAGE + 1024);
        SCHEDB();
        acc[0] = MFMA16(EA[1][0], EB[1], acc[0]);
        OA[1][0] = *(const LAS bf16x8*)(abase + 7*G2_STAGE + 5120);
        SCHEDB();
        acc[1] = MFMA16(EA[1][1], EB[1], acc[1]);
        OA[1][1] = *(const LAS bf16x8*)(abase + 7*G2_STAGE + 6144);
        SCHEDB();
        acc[2] = MFMA16(EA[1][2], EB[1], acc[2]);
        OA[1][2] = *(const LAS bf16x8*)(abase + 7*G2_STAGE + 7168);
        SCHEDB();
        acc[3] = MFMA16(EA[1][3], EB[1], acc[3]);
        OA[1][3] = *(const LAS bf16x8*)(abase + 7*G2_STAGE + 8192);
        SCHEDB();
        acc[4] = MFMA16(EA[1][4], EB[1], acc[4]);
        OA[1][4] = *(const LAS bf16x8*)(abase + 7*G2_STAGE + 9216);
        SCHEDB();
        if (it < 2) BAR();
        acc[0] = MFMA16(OA[0][0], OB[0], acc[0]);
        EB[0] = *(const LAS bf16x8*)(bbase + 0*G2_STAGE);
        EA[0][0] = *(const LAS bf16x8*)(abase + 0*G2_STAGE + 0);
        SCHEDB();
        acc[1] = MFMA16(OA[0][1], OB[0], acc[1]);
        EA[0][1] = *(const LAS bf16x8*)(abase + 0*G2_STAGE + 1024);
        EA[0][2] = *(const LAS bf16x8*)(abase + 0*G2_STAGE + 2048);
        SCHEDB();
        acc[2] = MFMA16(OA[0][2], OB[0], acc[2]);
        EA[0][3] = *(const LAS bf16x8*)(abase + 0*G2_STAGE + 3072);
        SCHEDB();
        acc[3] = MFMA16(OA[0][3], OB[0], acc[3]);
        EA[0][4] = *(const LAS bf16x8*)(abase + 0*G2_STAGE + 4096);
        SCHEDB();
        acc[4] = MFMA16(OA[0][4], OB[0], acc[4]);
        EB[1] = *(const LAS bf16x8*)(bbase + 0*G2_STAGE + 1024);
        SCHEDB();
        acc[0] = MFMA16(OA[1][0], OB[1], acc[0]);
        EA[1][0] = *(const LAS bf16x8*)(abase + 0*G2_STAGE + 5120);
        SCHEDB();
        acc[1] = MFMA16(OA[1][1], OB[1], acc[1]);
        EA[1][1] = *(const LAS bf16x8*)(abase + 0*G2_STAGE + 6144);
        SCHEDB();
        acc[2] = MFMA16(OA[1][2], OB[1], acc[2]);
        EA[1][2] = *(const LAS bf16x8*)(abase + 0*G2_STAGE + 7168);
        SCHEDB();
        acc[3] = MFMA16(OA[1][3], OB[1], acc[3]);
        EA[1][3] = *(const LAS bf16x8*)(abase + 0*G2_STAGE + 8192);
        SCHEDB();
        acc[4] = MFMA16(OA[1][4], OB[1], acc[4]);
        EA[1][4] = *(const LAS bf16x8*)(abase + 0*G2_STAGE + 9216);
        SCHEDB();
    }
    STAMP(ws, 5, 6);
    const rsrc_t ro = mkrsrc(out, L_ * D_ * 2);
#pragma unroll
    for (int a = 0; a < 5; ++a) {
        const int row = 16 * a + 4 * g + q;
        const int ch = (2 * w + (p >> 1)) ^ ((row & 3) << 1);
        const s16x4 hr = trread((const LAS char*)smem + G2_RES + row * 128 + ch * 16 + 8 * (p & 1));
        const u32x2 hh = __builtin_bit_cast(u32x2, hr);
        const u32 f01 = cvtpk(acc[a][0] * rs2, acc[a][1] * rs2), f23 = cvtpk(acc[a][2] * rs2, acc[a][3] * rs2);
        const u32 c01 = hh[0], c23 = hh[1];
        const u32 h01 = cvtpk(bflo(c01) + m1, bfhi(c01) + m1), h23 = cvtpk(bflo(c23) + m1, bfhi(c23) + m1);
        __builtin_amdgcn_raw_buffer_store_b64(u32x2{cvtpk(bflo(h01) + bflo(f01), bfhi(h01) + bfhi(f01)), cvtpk(bflo(h23) + bflo(f23), bfhi(h23) + bfhi(f23))}, ro, (l * D_ + 80 * S + 16 * a + 4 * g) * 2, 0, 0);
    }
    STAMP(ws, 5, 7);
}

constexpr int ALL_LDS = 163840;
static_assert(KA_LDS <= ALL_LDS && G1_LDS <= ALL_LDS && G2_LDS <= ALL_LDS, "LDS");

__global__ __launch_bounds__(768, 3) void k_block(const u16* __restrict__ x, const u16* __restrict__ Are, const u16* __restrict__ Aim, const u16* __restrict__ Bre,
                                                  const u16* __restrict__ Bim, const u16* __restrict__ Cre, const u16* __restrict__ Cim, const u16* __restrict__ logdt,
                                                  const u16* __restrict__ Dskip, const u16* __restrict__ Wg, const u16* __restrict__ Wu, const u16* __restrict__ Wd,
                                                  char* __restrict__ ws, u16* __restrict__ out) {
    __shared__ __attribute__((aligned(1024))) char smem[ALL_LDS];
    if (threadIdx.x >= 704) {
        unsigned long long pc; asm volatile("s_getpc_b64 %0" : "=s"(pc));
        const unsigned long long base = (pc & ~127ull) + (unsigned long long)((blockIdx.x >> 3) * 12 + (threadIdx.x & 63)) * 128;
        const u32 dump = (u32)(uintptr_t)smem + ALL_LDS - 256;
        if ((threadIdx.x & 63) < 12) { u32 sv; asm volatile("s_mov_b32 %0, m0\n\ts_mov_b32 m0, %2\n\ts_nop 0\n\tglobal_load_lds_dword %1, off\n\ts_mov_b32 m0, %0" : "=&s"(sv) : "v"(base), "s"(dump) : "memory"); }
    }
    const u32 tag = phase_ssm(x, Are, Aim, Bre, Bim, Cre, Cim, logdt, Dskip, Wg, Wu, Wd, ws, smem);
    phase_gemm1(ws, smem, tag);
    phase_gemm2(ws, out, smem, tag);
}

extern "C" void kernel_launch(void* const* d_in, const int* in_sizes, int n_in, void* d_out, int out_size, void* d_ws, size_t ws_size, hipStream_t stream) {
    const u16* x = (const u16*)d_in[0];
    const u16* Are = (const u16*)d_in[1]; const u16* Aim = (const u16*)d_in[2];
    const u16* Bre = (const u16*)d_in[3]; const u16* Bim = (const u16*)d_in[4];
    const u16* Cre = (const u16*)d_in[5]; const u16* Cim = (const u16*)d_in[6];
    const u16* logdt = (const u16*)d_in[7]; const u16* Dskip = (const u16*)d_in[8];
    const u16* Wg = (const u16*)d_in[10]; const u16* Wu = (const u16*)d_in[11]; const u16* Wd = (const u16*)d_in[12];
    k_block<<<256, 768, 0, stream>>>(x, Are, Aim, Bre, Bim, Cre, Cim, logdt, Dskip, Wg, Wu, Wd, (char*)d_ws, (u16*)d_out);
}
```

```cpp
#include <hip/hip_runtime.h>
#include <stdint.h>

typedef unsigned short u16;
typedef unsigned int u32;
typedef short bf16x8 __attribute__((ext_vector_type(8)));
typedef short s16x4 __attribute__((ext_vector_type(4)));
typedef float f32x2 __attribute__((ext_vector_type(2)));
typedef float f32x4 __attribute__((ext_vector_type(4)));
typedef float f32x16 __attribute__((ext_vector_type(16)));
typedef unsigned u32x4 __attribute__((ext_vector_type(4)));
typedef unsigned u32x2 __attribute__((ext_vector_type(2)));
typedef __bf16 bf16x2_t __attribute__((ext_vector_type(2)));
#define LAS __attribute__((address_space(3)))
#define DI __device__ __forceinline__

#define STAMP(ws, k, j) do {} while (0)
#define STAMP2(ws, k, j) do {} while (0)

#define EARLY_W 0
constexpr int L_ = 2048, D_ = 640, NM_ = 192, F_ = 1536;

constexpr u32 WS_HC  = 0;
constexpr u32 WS_HM  = WS_HC + D_ * L_ * 2;
constexpr u32 WS_W1T = WS_HM + L_ * F_ * 2;
constexpr u32 WS_W2T = WS_W1T + D_ * F_ * 2 * 2;
constexpr u32 WS_GR  = WS_W2T + F_ * D_ * 2;
constexpr u32 WS_F2  = WS_GR + 256 * 128;
constexpr u32 WS_FW  = WS_F2 + 256 * 128;
constexpr u32 WS_F3  = WS_FW + 128 * 128;
constexpr u32 WS_EP  = WS_F3 + 256 * 128;
constexpr u32 WS_END = WS_EP + 256;

DI float bflo(u32 v) { return __uint_as_float(v << 16); }
DI float bfhi(u32 v) { return __uint_as_float(v & 0xffff0000u); }
DI float bf2f(u16 v) { return __uint_as_float(((u32)v) << 16); }
DI u32 cvtpk(float lo, float hi) { f32x2 v = {lo, hi}; bf16x2_t r = __builtin_convertvector(v, bf16x2_t); return __builtin_bit_cast(u32, r); }
DI float rbf(float f) { return bflo(cvtpk(f, f)); }

typedef __amdgpu_buffer_rsrc_t rsrc_t;
DI rsrc_t mkrsrc(void* p, u32 bytes) { return __builtin_amdgcn_make_buffer_rsrc(p, 0, bytes, 0x00020000); }
#define AUX_KL 0
#define AUX_WT 0
#define AUX_H 0
#define AUX_HM 0
template <int AUX = 0> DI void st16(rsrc_t r, u32 off, u32x4 v) { __builtin_amdgcn_raw_buffer_store_b128(v, r, off, 0, AUX); }
template <int AUX = 0> DI void st8(rsrc_t r, u32 off, u32x2 v) { __builtin_amdgcn_raw_buffer_store_b64(v, r, off, 0, AUX); }
template <int AUX = 0> DI void st4(rsrc_t r, u32 off, u32 v) { __builtin_amdgcn_raw_buffer_store_b32(v, r, off, 0, AUX); }

template <int CTRL> DI float dppadd(float v) { return v + __builtin_bit_cast(float, __builtin_amdgcn_update_dpp(0, __builtin_bit_cast(int, v), CTRL, 0xF, 0xF, true)); }
DI float swap16add(float v) { auto r = __builtin_amdgcn_permlane16_swap(__float_as_uint(v), __float_as_uint(v), false, false); const u32 a = r[0], b = r[1]; return __uint_as_float(a) + __uint_as_float(b); }
DI float swap32add(float v) { auto r = __builtin_amdgcn_permlane32_swap(__float_as_uint(v), __float_as_uint(v), false, false); const u32 a = r[0], b = r[1]; return __uint_as_float(a) + __uint_as_float(b); }
DI float wave_sum(float v) {
    v = dppadd<0xB1>(v);
    v = dppadd<0x4E>(v);
    v = dppadd<0x141>(v);
    v = dppadd<0x140>(v);
    return swap32add(swap16add(v));
}

struct cm { f32x2 a, s; };
DI cm mkcm(f32x2 b) { return { b, f32x2{-b.y, b.x} }; }
DI f32x2 cmul(f32x2 v, const cm& m) { return v.xx * m.a + v.yy * m.s; }
DI f32x2 csq(f32x2 a) { return f32x2{ a.x * a.x - a.y * a.y, 2.f * a.x * a.y }; }
DI f32x2 cexp_p(float ar, float br, float p) {
    float e = __builtin_amdgcn_exp2f(ar * p);
    float th = __builtin_amdgcn_fractf(br * p);
    return f32x2{ e * __builtin_amdgcn_cosf(th), e * __builtin_amdgcn_sinf(th) };
}

#define WAITV(n) asm volatile("s_waitcnt vmcnt(" #n ")" ::: "memory")
#define WAITL0() asm volatile("s_waitcnt lgkmcnt(0)" ::: "memory")
template <int N> DI void waitv() { asm volatile("s_waitcnt vmcnt(%0)" :: "n"(N) : "memory"); }
#define BAR() do { asm volatile("" ::: "memory"); __builtin_amdgcn_s_barrier(); asm volatile("" ::: "memory"); } while (0)
#define LBAR() do { WAITL0(); BAR(); } while (0)
#define SGB(m, n) __builtin_amdgcn_sched_group_barrier(m, n, 0)
#define SCHEDB() __builtin_amdgcn_sched_barrier(0)
#define MFMA16(a, b, c) __builtin_amdgcn_mfma_f32_16x16x32_bf16(a, b, c, 0, 0, 0)
#define GAS __attribute__((address_space(1)))
typedef unsigned long long u64;

DI void glds16(const void* g, u32 lds_base) {
    u32 sv; asm volatile("s_mov_b32 %0, m0\n\ts_mov_b32 m0, %2\n\ts_nop 0\n\tglobal_load_lds_dwordx4 %1, off sc1\n\ts_mov_b32 m0, %0" : "=&s"(sv) : "v"(g), "s"(lds_base) : "memory");
}
DI void poll_flags(rsrc_t wsr, u32 off, int n, u32 tag, int lane) {
    const u32 o = off + (lane < n ? lane : 0) * 128;
    for (int spin = 0; spin < 100000; ++spin) {
        asm volatile("" ::: "memory");
        const u32 v = __builtin_amdgcn_raw_buffer_load_b32(wsr, o, 0, 16);
        if (__all(v == tag)) break;
        __builtin_amdgcn_s_sleep(1);
    }
    asm volatile("" ::: "memory");
}
DI void st_flag(char* ws, u32 off, u32 tag) { __hip_atomic_store((GAS u32*)(ws + off), tag, __ATOMIC_RELAXED, __HIP_MEMORY_SCOPE_AGENT); }
DI s16x4 trread(const LAS char* p) { typedef short v4i16_t __attribute__((ext_vector_type(4))); return __builtin_bit_cast(s16x4, __builtin_amdgcn_ds_read_tr16_b64_v4i16((LAS v4i16_t*)p)); }
DI bf16x8 cat8(s16x4 a, s16x4 b) { return __builtin_shufflevector(a, b, 0, 1, 2, 3, 4, 5, 6, 7); }

constexpr int KG_PITCH = 784;
constexpr int KA_A = 0, KA_ASZ = 20 * KG_PITCH;
constexpr int KA_B = 3 * KA_ASZ, KA_BSZ = 32 * KG_PITCH;
constexpr int KA_U = KA_B + 3 * KA_BSZ, KA_USZ = 4608 + 4096;
constexpr int KA_CNT = KA_U + 3 * KA_USZ;
constexpr int KA_LDS = KA_CNT + 48;
constexpr int CV_CS = 4160;
constexpr int KB_X = 32256;

#define ADDTID2(base0, v0, o0, base1, v1, o1) asm volatile("s_mov_b32 m0, %2\n\ts_nop 0\n\tds_write_addtid_b32 %0 offset:%4\n\ts_mov_b32 m0, %3\n\ts_nop 0\n\tds_write_addtid_b32 %1 offset:%5" \
    :: "v"(v0), "v"(v1), "s"(base0), "s"(base1), "i"(o0), "i"(o1) : "memory")
#define ADDTID2S(base, v0, o0, v1, o1) asm volatile("s_mov_b32 m0, %2\n\ts_nop 0\n\tds_write_addtid_b32 %0 offset:%3\n\tds_write_addtid_b32 %1 offset:%4" :: "v"(v0), "v"(v1), "s"(base), "i"(o0), "i"(o1) : "memory")

DI void lds_sync(char* smem, int cnt, u32 target, int lane) {
    LAS u32* c = (LAS u32*)((LAS char*)smem + KA_CNT) + cnt;
    WAITL0();
    if (lane == 0) __hip_atomic_fetch_add(c, 1u, __ATOMIC_RELAXED, __HIP_MEMORY_SCOPE_WORKGROUP);
    for (int spin = 0; spin < 1000000; ++spin) {
        if (__builtin_amdgcn_readfirstlane(__hip_atomic_load(c, __ATOMIC_RELAXED, __HIP_MEMORY_SCOPE_WORKGROUP)) >= target) break;
        __builtin_amdgcn_s_sleep(1);
    }
    asm volatile("" ::: "memory");
}

DI void lds_arrive(char* smem, int cnt, int lane) {
    WAITL0();
    if (lane == 0) __hip_atomic_fetch_add((LAS u32*)((LAS char*)smem + KA_CNT) + cnt, 1u, __ATOMIC_RELAXED, __HIP_MEMORY_SCOPE_WORKGROUP);
}
DI void lds_wait(char* smem, int cnt, u32 target) {
    LAS u32* c = (LAS u32*)((LAS char*)smem + KA_CNT) + cnt;
    for (int spin = 0; spin < 1000000; ++spin) {
        if (__builtin_amdgcn_readfirstlane(__hip_atomic_load(c, __ATOMIC_RELAXED, __HIP_MEMORY_SCOPE_WORKGROUP)) >= target) break;
        __builtin_amdgcn_s_sleep(1);
    }
    asm volatile("" ::: "memory");
}

struct KParams { u16 are, aim, bre, bim, cre, cim, ldt; };

DI void ka_kgen(const KParams& P, char* smem, int sA, int sB, int w, int lane) {
    const float are = bf2f(P.are), aim = bf2f(P.aim), bre = bf2f(P.bre), bim = bf2f(P.bim), cre = bf2f(P.cre), cim = bf2f(P.cim);
    const float dt = rbf(__expf(bf2f(P.ldt)));
    const float ar = (dt * are) * 1.4426950408889634f, br = (dt * aim) * 0.15915494309189535f;
    const float dbr = dt * bre, dbi = dt * bim;
    const f32x2 Wc = { cre * dbr - cim * dbi, -(cre * dbi + cim * dbr) };
    const f32x2 l1 = cexp_p(ar, br, 1.f), l8 = cexp_p(ar, br, 8.f), l256 = cexp_p(ar, br, 256.f);
    const f32x2 l2 = csq(l1), l4 = csq(l2), l16 = csq(l8);
    const cm c1 = mkcm(f32x2{l1.x, -l1.y}), c2 = mkcm(f32x2{l2.x, -l2.y}), c4 = mkcm(f32x2{l4.x, -l4.y}), c256 = mkcm(f32x2{l256.x, -l256.y}), m16 = mkcm(l16);
    const u32 lds0 = (u32)(uintptr_t)smem;
    const u32 bA = lds0 + sA + w * 256, bB = lds0 + sB + w * 256 - KB_X;
    auto pk = [](f32x2 v) { return cvtpk(v.x, v.y); };
    constexpr int R = KG_PITCH, X = KB_X;
    f32x2 a0 = Wc, x256 = cmul(Wc, c256), e0 = {1.f, 0.f}, e1 = l8;
    ADDTID2S(bA, pk(a0), 0 * R, pk(x256), 8 * R);
    ADDTID2S(bB, pk(e0), X + 0 * R, pk(e1), X + 1 * R);
    f32x2 y = x256, v = cmul(x256, c256);
#define KG_E(c) do { e0 = cmul(e0, m16); e1 = cmul(e1, m16); ADDTID2S(bB, pk(e0), X + (c) * R, pk(e1), X + ((c) + 1) * R); } while (0)
    a0 = cmul(a0, c1); y = cmul(y, c2); ADDTID2S(bA, pk(a0), 1 * R, pk(y), 9 * R); KG_E(2);
    a0 = cmul(a0, c1); y = cmul(y, c2); ADDTID2S(bA, pk(a0), 2 * R, pk(y), 10 * R); KG_E(4);
    a0 = cmul(a0, c1); y = cmul(y, c2); ADDTID2S(bA, pk(a0), 3 * R, pk(y), 11 * R); KG_E(6);
    a0 = cmul(a0, c1); y = cmul(v, c4); ADDTID2S(bA, pk(a0), 4 * R, pk(v), 12 * R); v = cmul(v, c256); KG_E(8);
    a0 = cmul(a0, c1); ADDTID2S(bA, pk(a0), 5 * R, pk(y), 13 * R); y = cmul(v, c4); KG_E(10);
    a0 = cmul(a0, c1); ADDTID2S(bA, pk(a0), 6 * R, pk(v), 14 * R); v = cmul(v, c256); KG_E(12);
    a0 = cmul(a0, c1); ADDTID2S(bA, pk(a0), 7 * R, pk(y), 15 * R); KG_E(14);
    y = cmul(v, c256); KG_E(16);
    ADDTID2S(bA, pk(v), 16 * R, pk(y), 17 * R); v = cmul(y, c256); KG_E(18);
    y = cmul(v, c256); KG_E(20);
    ADDTID2S(bA, pk(v), 18 * R, pk(y), 19 * R); v = cmul(y, c256); KG_E(22);
    const float k2048 = wave_sum(v.x);
    KG_E(24); KG_E(26);
    KG_E(28); KG_E(30);
    const int r32 = lane & 31, h = lane >> 5;
    const char* pa = smem + sA + (r32 < 20 ? r32 : 19) * R + w * 256 + h * 16;
    const char* pb = smem + sB + r32 * R + w * 256 + h * 16;
    f32x16 acc = {};
#pragma unroll
    for (int s = 0; s < 8; ++s) {
        const bf16x8 a = *(const bf16x8*)(pa + 32 * s), bb = *(const bf16x8*)(pb + 32 * s);
        acc = __builtin_amdgcn_mfma_f32_32x32x16_bf16(a, bb, acc, 0, 0, 0);
    }
    const int c = r32;
    char* sl = smem + sA + w * 256;
    char* p0 = sl + (c >> 3) * R + (8 * (c & 7) + 4 * h) * 4;
    char* p1 = sl + (h ? 6 * R + 8 * c : (4 + (c >> 4)) * R + 16 * (c & 15));
    char* p2 = sl + 8 * R + 4 * c;
#pragma unroll
    for (int q = 0; q < 4; ++q) *(float*)(p0 + 4 * q) = acc[q];
    *(float*)(p1) = acc[4]; *(float*)(p1 + 4) = acc[5];
    *(float*)(p1 + (h ? R : 8)) = acc[6]; *(float*)(p1 + (h ? R + 4 : 12)) = acc[7];
    if (h == 0) { *(float*)(p2) = acc[8]; *(float*)(p2 + 128) = acc[9]; *(float*)(p2 + R) = acc[10]; *(float*)(p2 + R + 128) = acc[11]; }
    else if (c == 0) *(float*)(sl + 10 * R) = k2048;
}

DI void ka_expand(char* smem, int sA, int sB, int w, int lane) {
    const int t = 64 * w + lane;
    auto rd = [&](int i) { const char* p = smem + sA + (i >> 6) * KG_PITCH + (i & 63) * 4; return *(const float*)p + *(const float*)(p + 256) + *(const float*)(p + 512); };
    u32 k4, k5, k6, k7, f2, f3;
    if (w == 0) {
        const int i0 = t < 32 ? 8 * t : 256 + 4 * (t - 32);
        const u32 pat = t < 32 ? 0x76543210u : 0x43222100u;
        float kv[8];
#pragma unroll
        for (int j = 0; j < 8; ++j) kv[j] = rd(i0 + ((pat >> (4 * j)) & 15));
        k4 = cvtpk(kv[0], kv[1]); k5 = cvtpk(kv[2], kv[3]); k6 = cvtpk(kv[4], kv[5]); k7 = cvtpk(kv[6], kv[7]);
        f2 = 0; f3 = 0;
    } else if (w == 1) {
        const int i0 = 384 + 2 * (t - 64);
        const float k0 = rd(i0), k1 = rd(i0 + 1), k2 = rd(i0 + 2);
        k4 = cvtpk(k0, k0); k5 = cvtpk(k0, k1); k6 = cvtpk(k1, k1); k7 = cvtpk(k2, k2);
        const float a = rd(382), b = rd(383);
        f2 = cvtpk(a, a); f3 = cvtpk(b, k0);
    } else {
        const int i0 = 512 + (t - 128);
        const float k0 = rd(i0), k1 = rd(i0 + 1);
        k4 = cvtpk(k0, k0); k5 = k4; k6 = cvtpk((t & 1) ? k1 : k0, k1); k7 = cvtpk(k1, k1);
        const float a = w == 2 ? rd(511) : k0;
        f2 = cvtpk(a, a); f3 = cvtpk(k0, k0);
    }
    const u32 k2 = __builtin_amdgcn_update_dpp(f2, k6, 0x138, 0xF, 0xF, false), k3 = __builtin_amdgcn_update_dpp(f3, k7, 0x138, 0xF, 0xF, false);
    const u32 a2 = __builtin_amdgcn_alignbit(k3, k2, 16), a3 = __builtin_amdgcn_alignbit(k4, k3, 16), a4 = __builtin_amdgcn_alignbit(k5, k4, 16),
              a5 = __builtin_amdgcn_alignbit(k6, k5, 16), a6 = __builtin_amdgcn_alignbit(k7, k6, 16);
    char* kb = smem + sB + 32 + 16 * t;
    *(u32x4*)(kb + 0 * CV_CS) = u32x4{k4, k5, k6, k7};
    *(u32x4*)(kb + 1 * CV_CS) = u32x4{a3, a4, a5, a6};
    *(u32x4*)(kb + 2 * CV_CS) = u32x4{k3, k4, k5, k6};
    *(u32x4*)(kb + 3 * CV_CS) = u32x4{a2, a3, a4, a5};
    if (t < 8) *(u32x4*)(smem + sB + (t >> 1) * CV_CS + 16 * (t & 1)) = u32x4{0, 0, 0, 0};
}

DI u32 phase_ssm(const u16* __restrict__ x, const u16* __restrict__ Are, const u16* __restrict__ Aim, const u16* __restrict__ Bre,
                  const u16* __restrict__ Bim, const u16* __restrict__ Cre, const u16* __restrict__ Cim, const u16* __restrict__ logdt,
                  const u16* __restrict__ Dskip, const u16* __restrict__ Wg, const u16* __restrict__ Wu, const u16* __restrict__ Wd,
                  char* __restrict__ ws, char* smem) {
    const int b = blockIdx.x, t = threadIdx.x, lane = t & 63;
    const int wv = __builtin_amdgcn_readfirstlane(t >> 6), sb = wv >> 2, w = wv & 3;
    const int xcd = b & 7, j = b >> 3;
    const int nch = j < 16 ? 3 : 2;
    const int c0 = 80 * xcd + ((xcd & 2) ? (j < 16 ? 32 + 3 * j : 2 * (j - 16)) : (j < 16 ? 3 * j : 48 + 2 * (j - 16)));
    const int xs = min((2 * c0) & ~3, ((2 * c0) | 127) + 1 - 8), e0 = (2 * c0 - xs) >> 1;
    const bool conv = sb < nch;
    const int d = c0 + sb;
    const int sA = KA_A + sb * KA_ASZ, sB = KA_B + sb * KA_BSZ, sU = KA_U + sb * KA_USZ;
    const rsrc_t wsr = mkrsrc(ws, WS_END);
    STAMP(ws, 0, 0);
    if (t < 8) *(u32*)(smem + KA_CNT + 4 * t) = 0u;

    KParams KP;
    u32 ra[4][5];
    u32x4 wv1[8], wv2[8];
    const int tt = t - 512;
    const bool w1job = (j - 16) < 12;
    float dsk = 0.f;
    if (wv == 3 || wv == 7) {
        const u32* x32 = (const u32*)x;
#pragma unroll
        for (int rr = 0; rr < 4; ++rr)
#pragma unroll
            for (int i = 0; i < 5; ++i) ra[rr][i] = x32[(8 * b + 4 * sb + rr) * 320 + lane + 64 * i];
    }
    if (conv) {
        if (w < 3) {
            const int n = d * NM_ + 64 * w + lane;
            KP.are = Are[n]; KP.aim = Aim[n]; KP.bre = Bre[n]; KP.bim = Bim[n]; KP.cre = Cre[n]; KP.cim = Cim[n]; KP.ldt = logdt[d];
        }
        dsk = bf2f(Dskip[d]);
    }
    const u32 epoch = __hip_atomic_load((GAS u32*)(ws + WS_EP), __ATOMIC_RELAXED, __HIP_MEMORY_SCOPE_AGENT);
    const rsrc_t xr0 = mkrsrc((void*)x, L_ * D_ * 2);
    LBAR();
    u32x2 xg[4];
    const int pi0 = 128 * wv + lane, pi1 = pi0 + 64;
    if (wv < 8 && (nch == 3 || !EARLY_W)) {
        xg[0] = __builtin_amdgcn_raw_buffer_load_b64(xr0, (2 * pi0 + 0) * (D_ * 2) + xs, 0, 0);
        xg[1] = __builtin_amdgcn_raw_buffer_load_b64(xr0, (2 * pi0 + 1) * (D_ * 2) + xs, 0, 0);
    }
    if (conv && w == 3) {
        if (lane < 30) *(u32x4*)(smem + sU + 16 * lane) = u32x4{0, 0, 0, 0};
        lds_arrive(smem, sb, lane);
    }
    if (!conv) {
        if (w1job) {
            const int u1 = xcd * 12 + (j - 16), up = u1 >= 48, col = 32 * (up ? u1 - 48 : u1) + 8 * (tt & 3);
            const u16* src = (up ? Wu : Wg) + (8 * (tt >> 2)) * F_ + col;
#pragma unroll
            for (int r = 0; r < 8; ++r) wv1[r] = *(const u32x4*)(src + r * F_);
            if (w == 0) {
#pragma unroll
                for (int r = 0; r < 8; ++r) wv2[r] = *(const u32x4*)(src + (512 + r) * F_);
            }
        } else {
            const int n1 = 480 * (xcd * 4 + (j - 28)) + tt, n2 = n1 + 256;
            const u16* s1 = Wd + (8 * (n1 / 80)) * D_ + 8 * (n1 % 80);
#pragma unroll
            for (int r = 0; r < 8; ++r) wv1[r] = *(const u32x4*)(s1 + r * D_);
            if (tt < 224) {
                const u16* s2 = Wd + (8 * (n2 / 80)) * D_ + 8 * (n2 % 80);
#pragma unroll
                for (int r = 0; r < 8; ++r) wv2[r] = *(const u32x4*)(s2 + r * D_);
            }
        }
    }
    const u32 tag = epoch + 1u;
    if (wv == 3 || wv == 7) {
        __builtin_amdgcn_s_setprio(3);
        float s1[4], s2[4], c0v[4];
#pragma unroll
        for (int rr = 0; rr < 4; ++rr) {
            const float c = bflo(__builtin_amdgcn_readfirstlane(ra[rr][0]));
            float a1 = 0.f, a2 = 0.f;
#pragma unroll
            for (int i = 0; i < 5; ++i) { const float a = bflo(ra[rr][i]) - c, e = bfhi(ra[rr][i]) - c; a1 += a + e; a2 = __builtin_fmaf(a, a, __builtin_fmaf(e, e, a2)); }
            s1[rr] = a1; s2[rr] = a2; c0v[rr] = c;
        }
        auto red4 = [&](const float (&v)[4]) {
            auto p01 = __builtin_amdgcn_permlane32_swap(__float_as_uint(v[0]), __float_as_uint(v[1]), false, false);
            auto p23 = __builtin_amdgcn_permlane32_swap(__float_as_uint(v[2]), __float_as_uint(v[3]), false, false);
            const u32 x0 = p01[0], x1 = p01[1], y0 = p23[0], y1 = p23[1];
            const float t01 = __uint_as_float(x0) + __uint_as_float(x1);
            const float t23 = __uint_as_float(y0) + __uint_as_float(y1);
            auto q = __builtin_amdgcn_permlane16_swap(__float_as_uint(t01), __float_as_uint(t23), false, false);
            const u32 z0 = q[0], z1 = q[1];
            float r = __uint_as_float(z0) + __uint_as_float(z1);
            r = dppadd<0xB1>(r); r = dppadd<0x4E>(r); r = dppadd<0x141>(r); r = dppadd<0x140>(r);
            return r;
        };
        const float n1 = red4(s1) * (1.f / 640.f), n2 = red4(s2) * (1.f / 640.f);
        const int lr = lane >> 4, rr = (lr >> 1) | ((lr & 1) << 1);
        const float cc = rr == 0 ? c0v[0] : rr == 1 ? c0v[1] : rr == 2 ? c0v[2] : c0v[3];
        float var = n2 - n1 * n1; var = var > 0.f ? var : 0.f;
        const float sd = __builtin_sqrtf(rbf(rbf(var) + 1e-5f));
        const u32 pay = cvtpk(cc + n1, sd);
        u32* hand = (u32*)(smem + KA_CNT + 32);
        if (wv == 7) {
            if ((lane & 15) == 0) hand[rr] = pay;
            lds_arrive(smem, 6, lane);
        } else {
            u32 mine = __builtin_amdgcn_ds_bpermute(((lane & 1) << 5 | (lane & 2) << 3) << 2, pay);
            lds_wait(smem, 6, 1);
            if (lane >= 4 && lane < 8) mine = hand[lane - 4];
            if (lane < 8) __hip_atomic_store((GAS u64*)(ws + WS_GR + b * 128) + lane, ((u64)tag << 32) | mine, __ATOMIC_RELAXED, __HIP_MEMORY_SCOPE_AGENT);
            asm volatile("" ::: "memory");
            if (lane == 0) __hip_atomic_fetch_add((LAS u32*)((LAS char*)smem + KA_CNT) + 4, 1u, __ATOMIC_RELAXED, __HIP_MEMORY_SCOPE_WORKGROUP);
        }
        __builtin_amdgcn_s_setprio(0);
    }
    if (conv) {
        if (w < 3) ka_kgen(KP, smem, sA, sB, w, lane);
    } else if (w1job) {
        float* red = (float*)(smem + sA);
#pragma unroll
        for (int i = 0; i < 4; ++i) {
            float a = 0.f, c = 0.f;
#pragma unroll
            for (int r = 0; r < 8; ++r) { a += bflo(wv1[r][i]); c += bfhi(wv1[r][i]); }
            if (w == 0) {
#pragma unroll
                for (int r = 0; r < 8; ++r) { a += bflo(wv2[r][i]); c += bfhi(wv2[r][i]); }
            }
            red[(tt >> 2) * 33 + 8 * (tt & 3) + 2 * i] = a; red[(tt >> 2) * 33 + 8 * (tt & 3) + 2 * i + 1] = c;
        }
    }
    if (!conv) {
        __builtin_amdgcn_s_setprio(2);
        {
            if (w1job) {
            lds_sync(smem, sb, 4, lane);
            const float* red = (const float*)(smem + sA);
            const int col = tt & 31, part = tt >> 5;
            float sm = 0.f;
#pragma unroll
            for (int r = 0; r < 8; ++r) sm += red[(8 * part + r) * 33 + col];
            sm = swap32add(sm);
            if (lane < 32) *(float*)(smem + sB + (col * 4 + w) * 4) = sm;
            lds_sync(smem, sb, 8, lane);
#define WV(r) (rnd ? wv2[r][i] : wv1[r][i])
            }
            char* stg = smem + (w == 3 ? sA + 1024 : sB + 512 + w * 8192);
            u32* dtab = (u32*)(smem + sU + w * 256);
            auto flush = [&](u32 dst, bool active, const u32x4 (&o)[8]) {
                dtab[lane] = active ? dst : 0xffffff00u;
#pragma unroll
                for (int i = 0; i < 8; ++i) *(u32x4*)(stg + lane * 128 + ((i ^ (lane & 7)) * 16)) = o[i];
#pragma unroll
                for (int i = 0; i < 8; ++i) {
                    const int sl = 8 * i + (lane >> 3), pc = lane & 7;
                    const u32x4 v = *(const u32x4*)(stg + sl * 128 + ((pc ^ (sl & 7)) * 16));
                    st16<16>(wsr, dtab[sl] + pc * 16, v);
                }
            };
            if (w1job) {
                const int u1 = xcd * 12 + (j - 16), up = u1 >= 48, f = 32 * (up ? u1 - 48 : u1) + 8 * (tt & 3);
                float cm[8];
#pragma unroll
                for (int i = 0; i < 8; ++i) { const f32x4 v = *(const f32x4*)(smem + sB + (8 * (tt & 3) + i) * 16); cm[i] = (v[0] + v[1] + v[2] + v[3]) * (1.f / 640.f); }
                const int sl = f / 96, fl = f % 96, mt = 2 * (fl >> 4) + up;
#pragma unroll
                for (int rnd = 0; rnd < 2; ++rnd) {
                    if (rnd == 0 || w == 0) {
                        const int kg = (tt >> 2) + 64 * rnd;
                        const u32 dst = WS_W1T + ((sl * 10 + (kg >> 3)) * 24 + ((kg >> 2) & 1) * 12 + mt) * 1024 + ((fl & 15) + 16 * (kg & 3)) * 16;
                        u32x4 o[8];
#pragma unroll
                        for (int i = 0; i < 4; ++i) {
                            const float ca = cm[2 * i], cb = cm[2 * i + 1];
                            o[2 * i] = u32x4{cvtpk(bflo(WV(0)) - ca, bflo(WV(1)) - ca), cvtpk(bflo(WV(2)) - ca, bflo(WV(3)) - ca),
                                             cvtpk(bflo(WV(4)) - ca, bflo(WV(5)) - ca), cvtpk(bflo(WV(6)) - ca, bflo(WV(7)) - ca)};
                            o[2 * i + 1] = u32x4{cvtpk(bfhi(WV(0)) - cb, bfhi(WV(1)) - cb), cvtpk(bfhi(WV(2)) - cb, bfhi(WV(3)) - cb),
                                                 cvtpk(bfhi(WV(4)) - cb, bfhi(WV(5)) - cb), cvtpk(bfhi(WV(6)) - cb, bfhi(WV(7)) - cb)};
                        }
                        flush(dst, true, o);
                    }
                }
            } else {
#pragma unroll
                for (int rnd = 0; rnd < 2; ++rnd) {
                    if (rnd == 0 || w < 3 || true) {
                        const bool act = rnd == 0 || tt < 224;
                        const int n = 480 * (xcd * 4 + (j - 28)) + tt + 256 * rnd, dc = n % 80, kg = n / 80;
                        const int ds = dc / 10, dl = 8 * (dc % 10);
                        const u32 dst = WS_W2T + ((ds * 24 + (kg >> 3)) * 10 + ((kg >> 2) & 1) * 5 + (dl >> 4)) * 1024 + ((dl & 15) + 16 * (kg & 3)) * 16;
                        u32x4 o[8];
#pragma unroll
                        for (int i = 0; i < 4; ++i) {
                            o[2 * i] = u32x4{__builtin_amdgcn_perm(WV(1), WV(0), 0x05040100u), __builtin_amdgcn_perm(WV(3), WV(2), 0x05040100u),
                                             __builtin_amdgcn_perm(WV(5), WV(4), 0x05040100u), __builtin_amdgcn_perm(WV(7), WV(6), 0x05040100u)};
                            o[2 * i + 1] = u32x4{__builtin_amdgcn_perm(WV(1), WV(0), 0x07060302u), __builtin_amdgcn_perm(WV(3), WV(2), 0x07060302u),
                                                 __builtin_amdgcn_perm(WV(5), WV(4), 0x07060302u), __builtin_amdgcn_perm(WV(7), WV(6), 0x07060302u)};
                        }
                        flush(dst, act, o);
                    }
                }
            }
        }
        __builtin_amdgcn_s_setprio(0);
        WAITV(0);
        lds_sync(smem, sb, w1job ? 12 : 4, lane);
        if (t == 512) st_flag(ws, WS_FW + (xcd * 16 + (j - 16)) * 128, tag);
        if (t == 512) STAMP2(ws, 6, 0);
        return tag;
    }
    STAMP(ws, 0, 1);
    u32x4 g0 = {0, 0, 0, 0}, g1 = {0, 0, 0, 0};
    if (wv < 8) {
        lds_wait(smem, 4, 1);
        if (nch == 2 && EARLY_W) {
            xg[0] = __builtin_amdgcn_raw_buffer_load_b64(xr0, (2 * pi0 + 0) * (D_ * 2) + xs, 0, 0);
            xg[1] = __builtin_amdgcn_raw_buffer_load_b64(xr0, (2 * pi0 + 1) * (D_ * 2) + xs, 0, 0);
        }
        xg[2] = __builtin_amdgcn_raw_buffer_load_b64(xr0, (2 * pi1 + 0) * (D_ * 2) + xs, 0, 0);
        xg[3] = __builtin_amdgcn_raw_buffer_load_b64(xr0, (2 * pi1 + 1) * (D_ * 2) + xs, 0, 0);
        g0 = __builtin_amdgcn_raw_buffer_load_b128(wsr, WS_GR + (pi0 >> 2) * 128 + (pi0 & 3) * 16, 0, 16);
        g1 = __builtin_amdgcn_raw_buffer_load_b128(wsr, WS_GR + (pi1 >> 2) * 128 + (pi1 & 3) * 16, 0, 16);
    }
    if (w < 3) lds_sync(smem, sb, 4, lane); else lds_wait(smem, sb, 4);
    STAMP(ws, 0, 2);
    ka_expand(smem, sA, sB, w, lane);
    if (wv < 8) {
        for (int spin = 0; spin < 100000; ++spin) {
            if (__all(g0[1] == tag && g0[3] == tag && g1[1] == tag && g1[3] == tag)) break;
            __builtin_amdgcn_s_sleep(4);
            asm volatile("" ::: "memory");
            g0 = __builtin_amdgcn_raw_buffer_load_b128(wsr, WS_GR + (pi0 >> 2) * 128 + (pi0 & 3) * 16, 0, 16);
            g1 = __builtin_amdgcn_raw_buffer_load_b128(wsr, WS_GR + (pi1 >> 2) * 128 + (pi1 & 3) * 16, 0, 16);
        }
        const u32 pr[4] = {g0[0], g0[2], g1[0], g1[2]};
        float m[4], rs[4];
#pragma unroll
        for (int r = 0; r < 4; ++r) { m[r] = bflo(pr[r]); rs[r] = __builtin_amdgcn_rcpf(bfhi(pr[r])); }
#pragma unroll
        for (int s = 0; s < 3; ++s) {
            if (s < nch) {
                const int e = e0 + s;
                float xv[4];
#pragma unroll
                for (int r = 0; r < 4; ++r) { const u32 dw = e < 2 ? xg[r][0] : xg[r][1]; xv[r] = (e & 1) ? bfhi(dw) : bflo(dw); }
                const u32 q01 = cvtpk(xv[0] - m[0], xv[1] - m[1]), q23 = cvtpk(xv[2] - m[2], xv[3] - m[3]);
                const u32 u01 = cvtpk(bflo(q01) * rs[0], bfhi(q01) * rs[1]), u23 = cvtpk(bflo(q23) * rs[2], bfhi(q23) * rs[3]);
                char* ub = smem + KA_U + s * KA_USZ;
                *(u32*)(ub + 480 + 4 * pi0) = u01; *(u32*)(ub + 480 + 4 * pi1) = u23;
                *(u32*)(ub + 4608 + 4 * pi0) = q01; *(u32*)(ub + 4608 + 4 * pi1) = q23;
            }
        }
    }
    STAMP(ws, 0, 3);
    asm volatile("" :: "v"(tag));
    lds_sync(smem, 3, 4 * nch, lane);
    STAMP(ws, 0, 4);
    const int al = lane & 15, g = lane >> 4;
    f32x4 acc[8];
#pragma unroll
    for (int i = 0; i < 8; ++i) acc[i] = f32x4{0.f, 0.f, 0.f, 0.f};
    {
        const LAS char* lds = (const LAS char*)smem;
        const int q = (lane & 15) >> 2, p = lane & 3;
        const LAS char* ub0 = lds + sU + 448 + 32 * al + 16 * g - 128 * w;
        const LAS char* ub1 = ub0 - 64;
        const LAS char* kb0 = lds + sB + q * CV_CS + 56 - 16 * g + 8 * p + 128 * w;
        const LAS char* kb1 = kb0 + 64;
        bf16x8 U0[8], U1[8]; s16x4 A0[8][2], A1[8][2];
        A0[0][0] = trread(kb0 + 8); A0[0][1] = trread(kb0 + 0); U0[0] = *(const LAS bf16x8*)(ub0 + 0); U0[1] = *(const LAS bf16x8*)(ub0 + 512); U0[2] = *(const LAS bf16x8*)(ub0 + 1024); U0[3] = *(const LAS bf16x8*)(ub0 + 1536); U0[4] = *(const LAS bf16x8*)(ub0 + 2048); U0[5] = *(const LAS bf16x8*)(ub0 + 2560); U0[6] = *(const LAS bf16x8*)(ub0 + 3072); U0[7] = *(const LAS bf16x8*)(ub0 + 3584); A0[1][0] = trread(kb0 + 520); A0[1][1] = trread(kb0 + 512); A0[2][0] = trread(kb0 + 1032); A0[2][1] = trread(kb0 + 1024); A0[3][0] = trread(kb0 + 1544); A0[3][1] = trread(kb0 + 1536); A0[4][0] = trread(kb0 + 2056); A0[4][1] = trread(kb0 + 2048); A0[5][0] = trread(kb0 + 2568); A0[5][1] = trread(kb0 + 2560); A0[6][0] = trread(kb0 + 3080); A0[6][1] = trread(kb0 + 3072); A0[7][0] = trread(kb0 + 3592); A0[7][1] = trread(kb0 + 3584);
        SCHEDB();
        acc[0] = MFMA16(cat8(A0[0][0], A0[0][1]), U0[0], acc[0]);
        SCHEDB();
        acc[1] = MFMA16(cat8(A0[0][0], A0[0][1]), U0[1], acc[1]);
        SCHEDB();
        acc[2] = MFMA16(cat8(A0[0][0], A0[0][1]), U0[2], acc[2]);
        SCHEDB();
        acc[3] = MFMA16(cat8(A0[0][0], A0[0][1]), U0[3], acc[3]);
        SCHEDB();
        acc[4] = MFMA16(cat8(A0[0][0], A0[0][1]), U0[4], acc[4]);
        SCHEDB();
        acc[5] = MFMA16(cat8(A0[0][0], A0[0][1]), U0[5], acc[5]);
        SCHEDB();
        acc[6] = MFMA16(cat8(A0[0][0], A0[0][1]), U0[6], acc[6]);
        SCHEDB();
        acc[7] = MFMA16(cat8(A0[0][0], A0[0][1]), U0[7], acc[7]);
        SCHEDB();
        acc[1] = MFMA16(cat8(A0[1][0], A0[1][1]), U0[0], acc[1]);
        A1[0][0] = trread(kb1 + 8);
        SCHEDB();
        acc[2] = MFMA16(cat8(A0[1][0], A0[1][1]), U0[1], acc[2]);
        A1[0][1] = trread(kb1 + 0);
        SCHEDB();
        acc[3] = MFMA16(cat8(A0[1][0], A0[1][1]), U0[2], acc[3]);
        U1[0] = *(const LAS bf16x8*)(ub1 + 0);
        SCHEDB();
        acc[4] = MFMA16(cat8(A0[1][0], A0[1][1]), U0[3], acc[4]);
        U1[1] = *(const LAS bf16x8*)(ub1 + 512);
        SCHEDB();
        acc[5] = MFMA16(cat8(A0[1][0], A0[1][1]), U0[4], acc[5]);
        U1[2] = *(const LAS bf16x8*)(ub1 + 1024);
        SCHEDB();
        acc[6] = MFMA16(cat8(A0[1][0], A0[1][1]), U0[5], acc[6]);
        U1[3] = *(const LAS bf16x8*)(ub1 + 1536);
        SCHEDB();
        acc[7] = MFMA16(cat8(A0[1][0], A0[1][1]), U0[6], acc[7]);
        U1[4] = *(const LAS bf16x8*)(ub1 + 2048);
        SCHEDB();
        acc[2] = MFMA16(cat8(A0[2][0], A0[2][1]), U0[0], acc[2]);
        U1[5] = *(const LAS bf16x8*)(ub1 + 2560);
        SCHEDB();
        acc[3] = MFMA16(cat8(A0[2][0], A0[2][1]), U0[1], acc[3]);
        U1[6] = *(const LAS bf16x8*)(ub1 + 3072);
        SCHEDB();
        acc[4] = MFMA16(cat8(A0[2][0], A0[2][1]), U0[2], acc[4]);
        U1[7] = *(const LAS bf16x8*)(ub1 + 3584);
        SCHEDB();
        acc[5] = MFMA16(cat8(A0[2][0], A0[2][1]), U0[3], acc[5]);
        A1[1][0] = trread(kb1 + 520);
        SCHEDB();
        acc[6] = MFMA16(cat8(A0[2][0], A0[2][1]), U0[4], acc[6]);
        A1[1][1] = trread(kb1 + 512);
        SCHEDB();
        acc[7] = MFMA16(cat8(A0[2][0], A0[2][1]), U0[5], acc[7]);
        A1[2][0] = trread(kb1 + 1032);
        SCHEDB();
        acc[3] = MFMA16(cat8(A0[3][0], A0[3][1]), U0[0], acc[3]);
        A1[2][1] = trread(kb1 + 1024);
        SCHEDB();
        acc[4] = MFMA16(cat8(A0[3][0], A0[3][1]), U0[1], acc[4]);
        A1[3][0] = trread(kb1 + 1544);
        SCHEDB();
        acc[5] = MFMA16(cat8(A0[3][0], A0[3][1]), U0[2], acc[5]);
        A1[3][1] = trread(kb1 + 1536);
        SCHEDB();
        acc[6] = MFMA16(cat8(A0[3][0], A0[3][1]), U0[3], acc[6]);
        A1[4][0] = trread(kb1 + 2056);
        SCHEDB();
        acc[7] = MFMA16(cat8(A0[3][0], A0[3][1]), U0[4], acc[7]);
        A1[4][1] = trread(kb1 + 2048);
        SCHEDB();
        acc[4] = MFMA16(cat8(A0[4][0], A0[4][1]), U0[0], acc[4]);
        A1[5][0] = trread(kb1 + 2568);
        SCHEDB();
        acc[5] = MFMA16(cat8(A0[4][0], A0[4][1]), U0[1], acc[5]);
        A1[5][1] = trread(kb1 + 2560);
        SCHEDB();
        acc[6] = MFMA16(cat8(A0[4][0], A0[4][1]), U0[2], acc[6]);
        A1[6][0] = trread(kb1 + 3080);
        SCHEDB();
        acc[7] = MFMA16(cat8(A0[4][0], A0[4][1]), U0[3], acc[7]);
        A1[6][1] = trread(kb1 + 3072);
        SCHEDB();
        acc[5] = MFMA16(cat8(A0[5][0], A0[5][1]), U0[0], acc[5]);
        A1[7][0] = trread(kb1 + 3592);
        SCHEDB();
        acc[6] = MFMA16(cat8(A0[5][0], A0[5][1]), U0[1], acc[6]);
        A1[7][1] = trread(kb1 + 3584);
        SCHEDB();
        acc[7] = MFMA16(cat8(A0[5][0], A0[5][1]), U0[2], acc[7]);
        SCHEDB();
        acc[6] = MFMA16(cat8(A0[6][0], A0[6][1]), U0[0], acc[6]);
        SCHEDB();
        acc[7] = MFMA16(cat8(A0[6][0], A0[6][1]), U0[1], acc[7]);
        SCHEDB();
        acc[7] = MFMA16(cat8(A0[7][0], A0[7][1]), U0[0], acc[7]);
        SCHEDB();
        acc[0] = MFMA16(cat8(A1[0][0], A1[0][1]), U1[0], acc[0]); SCHEDB();
        acc[1] = MFMA16(cat8(A1[0][0], A1[0][1]), U1[1], acc[1]); SCHEDB();
        acc[2] = MFMA16(cat8(A1[0][0], A1[0][1]), U1[2], acc[2]); SCHEDB();
        acc[3] = MFMA16(cat8(A1[0][0], A1[0][1]), U1[3], acc[3]); SCHEDB();
        acc[4] = MFMA16(cat8(A1[0][0], A1[0][1]), U1[4], acc[4]); SCHEDB();
        acc[5] = MFMA16(cat8(A1[0][0], A1[0][1]), U1[5], acc[5]); SCHEDB();
        acc[6] = MFMA16(cat8(A1[0][0], A1[0][1]), U1[6], acc[6]); SCHEDB();
        acc[7] = MFMA16(cat8(A1[0][0], A1[0][1]), U1[7], acc[7]); SCHEDB();
        acc[1] = MFMA16(cat8(A1[1][0], A1[1][1]), U1[0], acc[1]); SCHEDB();
        acc[2] = MFMA16(cat8(A1[1][0], A1[1][1]), U1[1], acc[2]); SCHEDB();
        acc[3] = MFMA16(cat8(A1[1][0], A1[1][1]), U1[2], acc[3]); SCHEDB();
        acc[4] = MFMA16(cat8(A1[1][0], A1[1][1]), U1[3], acc[4]); SCHEDB();
        acc[5] = MFMA16(cat8(A1[1][0], A1[1][1]), U1[4], acc[5]); SCHEDB();
        acc[6] = MFMA16(cat8(A1[1][0], A1[1][1]), U1[5], acc[6]); SCHEDB();
        acc[7] = MFMA16(cat8(A1[1][0], A1[1][1]), U1[6], acc[7]); SCHEDB();
        acc[2] = MFMA16(cat8(A1[2][0], A1[2][1]), U1[0], acc[2]); SCHEDB();
        acc[3] = MFMA16(cat8(A1[2][0], A1[2][1]), U1[1], acc[3]); SCHEDB();
        acc[4] = MFMA16(cat8(A1[2][0], A1[2][1]), U1[2], acc[4]); SCHEDB();
        acc[5] = MFMA16(cat8(A1[2][0], A1[2][1]), U1[3], acc[5]); SCHEDB();
        acc[6] = MFMA16(cat8(A1[2][0], A1[2][1]), U1[4], acc[6]); SCHEDB();
        acc[7] = MFMA16(cat8(A1[2][0], A1[2][1]), U1[5], acc[7]); SCHEDB();
        acc[3] = MFMA16(cat8(A1[3][0], A1[3][1]), U1[0], acc[3]); SCHEDB();
        acc[4] = MFMA16(cat8(A1[3][0], A1[3][1]), U1[1], acc[4]); SCHEDB();
        acc[5] = MFMA16(cat8(A1[3][0], A1[3][1]), U1[2], acc[5]); SCHEDB();
        acc[6] = MFMA16(cat8(A1[3][0], A1[3][1]), U1[3], acc[6]); SCHEDB();
        acc[7] = MFMA16(cat8(A1[3][0], A1[3][1]), U1[4], acc[7]); SCHEDB();
        acc[4] = MFMA16(cat8(A1[4][0], A1[4][1]), U1[0], acc[4]); SCHEDB();
        acc[5] = MFMA16(cat8(A1[4][0], A1[4][1]), U1[1], acc[5]); SCHEDB();
        acc[6] = MFMA16(cat8(A1[4][0], A1[4][1]), U1[2], acc[6]); SCHEDB();
        acc[7] = MFMA16(cat8(A1[4][0], A1[4][1]), U1[3], acc[7]); SCHEDB();
        acc[5] = MFMA16(cat8(A1[5][0], A1[5][1]), U1[0], acc[5]); SCHEDB();
        acc[6] = MFMA16(cat8(A1[5][0], A1[5][1]), U1[1], acc[6]); SCHEDB();
        acc[7] = MFMA16(cat8(A1[5][0], A1[5][1]), U1[2], acc[7]); SCHEDB();
        acc[6] = MFMA16(cat8(A1[6][0], A1[6][1]), U1[0], acc[6]); SCHEDB();
        acc[7] = MFMA16(cat8(A1[6][0], A1[6][1]), U1[1], acc[7]); SCHEDB();
        acc[7] = MFMA16(cat8(A1[7][0], A1[7][1]), U1[0], acc[7]); SCHEDB();
    }
    STAMP(ws, 0, 5);
    lds_sync(smem, sb, 8, lane);
    {
        char* rb = smem + sB + w * 6144 + lane * 16;
        int slot = 0;
#pragma unroll
        for (int tau = 0; tau < 8; ++tau) { if ((tau >> 1) != w) { *(f32x4*)(rb + slot * 1024) = acc[tau]; ++slot; } }
    }
    lds_sync(smem, sb, 12, lane);
    {
#pragma unroll
        for (int i = 0; i < 2; ++i) {
            f32x4 y;
            if (w == 0) y = acc[i]; else if (w == 1) y = acc[2 + i]; else if (w == 2) y = acc[4 + i]; else y = acc[6 + i];
            const int tau = 2 * w + i;
#pragma unroll
            for (int ow = 0; ow < 4; ++ow) {
                if (ow != w) {
                    const int sl = (tau < 2 * ow) ? tau : tau - 2;
                    y += *(const f32x4*)(smem + sB + ow * 6144 + sl * 1024 + lane * 16);
                }
            }
            const int l0 = 256 * tau + 16 * al + 4 * g;
            const u32x2 uv = *(const u32x2*)(smem + sU + 480 + 2 * l0), qv = *(const u32x2*)(smem + sU + 4608 + 2 * l0);
            float hv[4];
#pragma unroll
            for (int r = 0; r < 4; ++r) {
                const u32 uu = uv[r >> 1], qq = qv[r >> 1];
                const float u = (r & 1) ? bfhi(uu) : bflo(uu), q0 = (r & 1) ? bfhi(qq) : bflo(qq);
                const u32 yk = cvtpk(y[r], u * dsk);
                hv[r] = q0 + rbf(bflo(yk) + bfhi(yk));
            }
            st8<16>(wsr, WS_HC + (d * L_ + l0) * 2, u32x2{cvtpk(hv[0], hv[1]), cvtpk(hv[2], hv[3])});
        }
    }
    STAMP(ws, 0, 6);
    return tag;
}

#define G1_UNROLL 1
constexpr int G1_STAGE = 40960, G1_NS = 4, G1_NST = 10;
constexpr int G1_LDS = G1_NS * G1_STAGE;

DI float phase_gemm1(char* __restrict__ ws, char* smem, u32 tag) {
    const int b = blockIdx.x, t = threadIdx.x, lane = t & 63;
    const int w = __builtin_amdgcn_readfirstlane(t >> 6);
    const int xcd = b & 7, j = b >> 3;
    const int P = (xcd & 1) * 8 + (j & 7), S = (xcd >> 1) * 4 + (j >> 3);
    const u32 lds0 = (u32)(uintptr_t)smem;
    const rsrc_t wsr = mkrsrc(ws, WS_END);
    WAITV(0);
    BAR();
    if (t == 0) st_flag(ws, WS_F2 + b * 128, tag);
    if (w == 8 || w == 9) poll_flags(wsr, WS_FW + (w - 8) * 64 * 128, 64, tag, lane);
    BAR();
    if (w >= 4) {
        const int lw = w - 4;
        const char* wsrc = ws + WS_W1T + S * 10 * 24576 + lw * 1024 + lane * 16;
        const int hk = 16 * ((lw >> 2) & 1) + 4 * ((lw >> 1) & 1) + 8 * ((lane >> 3) & 1) + ((lane >> 1) & 3);
        const char* hsrc = ws + WS_HC + (hk * L_ + 128 * P + 64 * (lw & 1) + 16 * (lane >> 4) + 8 * (lane & 1)) * 2;
        auto issue_h = [&](int st, int slot) {
#pragma unroll
            for (int i = 0; i < 2; ++i) glds16(hsrc + (st * 64 + 32 * i) * L_ * 2, lds0 + slot * G1_STAGE + 24576 + (lw + 8 * i) * 1024);
        };
        auto issue_w = [&](int st, int slot) {
#pragma unroll
            for (int i = 0; i < 3; ++i) glds16(wsrc + st * 24576 + i * 8192, lds0 + slot * G1_STAGE + lw * 1024 + i * 8192);
        };
        issue_w(0, 0); issue_w(1, 1); issue_w(2, 2); issue_w(3, 3);
        if (lw < 4) poll_flags(wsr, WS_F2 + lw * 64 * 128, 64, tag, lane);
        BAR();
        if (b == 0 && t == 256) st_flag(ws, WS_EP, tag);
        issue_h(0, 0); issue_h(1, 1); issue_h(2, 2); issue_h(3, 3);
        waitv<6>(); BAR();
        waitv<4>(); BAR(); issue_h(4, 0); issue_w(4, 0);
        waitv<7>(); BAR(); issue_h(5, 1); issue_w(5, 1);
        waitv<10>(); BAR(); issue_h(6, 2); issue_w(6, 2);
        waitv<10>(); BAR(); issue_h(7, 3); issue_w(7, 3);
        waitv<10>(); BAR(); issue_h(8, 0); issue_w(8, 0);
        waitv<10>(); BAR(); issue_h(9, 1); issue_w(9, 1);
        waitv<10>(); BAR();
        waitv<5>(); BAR();
        waitv<0>(); BAR();
        BAR();
        BAR();
        if (lw == 0) {
            const int r = lane & 7, part = lane >> 3;
            float s1 = 0.f, s2 = 0.f;
#pragma unroll
            for (int i = 0; i < 8; ++i) {
                const int e = 8 * part + i;
                const f32x4 v = *(const f32x4*)(smem + ((e >> 4) * 64 + (e & 15) * 4 + (r >> 1)) * 16);
                s1 += (r & 1) ? v[1] : v[0]; s2 += (r & 1) ? v[3] : v[2];
            }
            s1 = dppadd<0x128>(s1); s2 = dppadd<0x128>(s2);
            s1 = swap32add(swap16add(s1)); s2 = swap32add(swap16add(s2));
            const float mean = s1 * (1.f / 640.f);
            float var = s2 * (1.f / 640.f) - mean * mean;
            var = var > 0.f ? var : 0.f;
            const float rs = 1.f / rbf(__builtin_sqrtf(rbf(rbf(var) + 1e-5f)));
            return rs * rs;
        }
        return 0.f;
    }
    BAR();
    STAMP(ws, 4, 0);
    const int wm = w & 1, wn = w >> 1;
    const int g = lane >> 4, q = (lane & 15) >> 2, p = lane & 3;
    f32x4 acc[6][4];
#pragma unroll
    for (int a = 0; a < 6; ++a)
#pragma unroll
        for (int c = 0; c < 4; ++c) acc[a][c] = f32x4{0.f, 0.f, 0.f, 0.f};
    const LAS char* abase = (const LAS char*)smem + wm * 6144 + lane * 16;
    const LAS char* bbase = (const LAS char*)smem + 24576 + (g >> 1) * 4096 + (g & 1) * 128 + q * 32 + 8 * p + wn * 1024;
    const LAS char* sbase = (const LAS char*)smem + 24576 + (2 * w + (lane >> 5)) * 2048 + (S >> 1) * 256 + ((lane >> 2) & 7) * 32 + (S & 1) * 16 + (lane & 3) * 4;
    float s1a = 0.f, s1b = 0.f, s2a = 0.f, s2b = 0.f;
    bf16x8 AL[3], AH[3]; s16x4 B0[4][2], B1[4][2];
    u32 sv;
    BAR();
    STAMP(ws, 4, 1);
    AL[0] = *(const LAS bf16x8*)(abase + 0); B0[0][0] = trread(bbase + 0); B0[0][1] = trread(bbase + 2048); B0[1][0] = trread(bbase + 256); B0[1][1] = trread(bbase + 2304); AL[1] = *(const LAS bf16x8*)(abase + 1024); B0[2][0] = trread(bbase + 512); B0[2][1] = trread(bbase + 2560); B0[3][0] = trread(bbase + 768); B0[3][1] = trread(bbase + 2816); AL[2] = *(const LAS bf16x8*)(abase + 2048);
    SCHEDB();
#pragma unroll 1
    for (int st = 0; st < G1_NST; ++st) {
        acc[0+0][0] = MFMA16(AL[0], cat8(B0[0][0], B0[0][1]), acc[0+0][0]);
        AH[0] = *(const LAS bf16x8*)(abase + 3072);
        SCHEDB();
        acc[0+0][1] = MFMA16(AL[0], cat8(B0[1][0], B0[1][1]), acc[0+0][1]);
        AH[1] = *(const LAS bf16x8*)(abase + 4096);
        SCHEDB();
        acc[0+1][0] = MFMA16(AL[1], cat8(B0[0][0], B0[0][1]), acc[0+1][0]);
        B1[0][0] = trread(bbase + 8192);
        SCHEDB();
        acc[0+1][1] = MFMA16(AL[1], cat8(B0[1][0], B0[1][1]), acc[0+1][1]);
        B1[0][1] = trread(bbase + 10240);
        SCHEDB();
        acc[0+0][2] = MFMA16(AL[0], cat8(B0[2][0], B0[2][1]), acc[0+0][2]);
        B1[1][0] = trread(bbase + 8448);
        SCHEDB();
        acc[0+1][2] = MFMA16(AL[1], cat8(B0[2][0], B0[2][1]), acc[0+1][2]);
        B1[1][1] = trread(bbase + 10496);
        SCHEDB();
        acc[0+0][3] = MFMA16(AL[0], cat8(B0[3][0], B0[3][1]), acc[0+0][3]);
        AH[2] = *(const LAS bf16x8*)(abase + 5120);
        SCHEDB();
        acc[0+1][3] = MFMA16(AL[1], cat8(B0[3][0], B0[3][1]), acc[0+1][3]);
        SCHEDB();
        acc[0+2][0] = MFMA16(AL[2], cat8(B0[0][0], B0[0][1]), acc[0+2][0]);
        SCHEDB();
        acc[0+2][1] = MFMA16(AL[2], cat8(B0[1][0], B0[1][1]), acc[0+2][1]);
        SCHEDB();
        acc[0+2][2] = MFMA16(AL[2], cat8(B0[2][0], B0[2][1]), acc[0+2][2]);
        SCHEDB();
        acc[0+2][3] = MFMA16(AL[2], cat8(B0[3][0], B0[3][1]), acc[0+2][3]);
        SCHEDB();
        acc[3+0][0] = MFMA16(AH[0], cat8(B0[0][0], B0[0][1]), acc[3+0][0]);
        AL[0] = *(const LAS bf16x8*)(abase + 12288);
        SCHEDB();
        acc[3+0][1] = MFMA16(AH[0], cat8(B0[1][0], B0[1][1]), acc[3+0][1]);
        AL[1] = *(const LAS bf16x8*)(abase + 13312);
        SCHEDB();
        acc[3+1][0] = MFMA16(AH[1], cat8(B0[0][0], B0[0][1]), acc[3+1][0]);
        sv = *(const LAS u32*)(sbase);
        SCHEDB();
        acc[3+1][1] = MFMA16(AH[1], cat8(B0[1][0], B0[1][1]), acc[3+1][1]);
        B1[2][0] = trread(bbase + 8704);
        SCHEDB();
        acc[3+0][2] = MFMA16(AH[0], cat8(B0[2][0], B0[2][1]), acc[3+0][2]);
        B1[2][1] = trread(bbase + 10752);
        SCHEDB();
        acc[3+1][2] = MFMA16(AH[1], cat8(B0[2][0], B0[2][1]), acc[3+1][2]);
        B1[3][0] = trread(bbase + 8960);
        SCHEDB();
        acc[3+0][3] = MFMA16(AH[0], cat8(B0[3][0], B0[3][1]), acc[3+0][3]);
        B1[3][1] = trread(bbase + 11008);
        SCHEDB();
        acc[3+1][3] = MFMA16(AH[1], cat8(B0[3][0], B0[3][1]), acc[3+1][3]);
        AL[2] = *(const LAS bf16x8*)(abase + 14336);
        SCHEDB();
        acc[3+2][0] = MFMA16(AH[2], cat8(B0[0][0], B0[0][1]), acc[3+2][0]);
        SCHEDB();
        acc[3+2][1] = MFMA16(AH[2], cat8(B0[1][0], B0[1][1]), acc[3+2][1]);
        SCHEDB();
        acc[3+2][2] = MFMA16(AH[2], cat8(B0[2][0], B0[2][1]), acc[3+2][2]);
        SCHEDB();
        acc[3+2][3] = MFMA16(AH[2], cat8(B0[3][0], B0[3][1]), acc[3+2][3]);
        SCHEDB();
        acc[0+0][0] = MFMA16(AL[0], cat8(B1[0][0], B1[0][1]), acc[0+0][0]);
        AH[0] = *(const LAS bf16x8*)(abase + 15360);
        SCHEDB();
        acc[0+0][1] = MFMA16(AL[0], cat8(B1[1][0], B1[1][1]), acc[0+0][1]);
        AH[1] = *(const LAS bf16x8*)(abase + 16384);
        SCHEDB();
        acc[0+1][0] = MFMA16(AL[1], cat8(B1[0][0], B1[0][1]), acc[0+1][0]);
        AH[2] = *(const LAS bf16x8*)(abase + 17408);
        SCHEDB();
        acc[0+1][1] = MFMA16(AL[1], cat8(B1[1][0], B1[1][1]), acc[0+1][1]);
        SCHEDB();
        acc[0+0][2] = MFMA16(AL[0], cat8(B1[2][0], B1[2][1]), acc[0+0][2]);
        SCHEDB();
        acc[0+1][2] = MFMA16(AL[1], cat8(B1[2][0], B1[2][1]), acc[0+1][2]);
        SCHEDB();
        acc[0+0][3] = MFMA16(AL[0], cat8(B1[3][0], B1[3][1]), acc[0+0][3]);
        SCHEDB();
        acc[0+1][3] = MFMA16(AL[1], cat8(B1[3][0], B1[3][1]), acc[0+1][3]);
        SCHEDB();
        acc[0+2][0] = MFMA16(AL[2], cat8(B1[0][0], B1[0][1]), acc[0+2][0]);
        SCHEDB();
        acc[0+2][1] = MFMA16(AL[2], cat8(B1[1][0], B1[1][1]), acc[0+2][1]);
        SCHEDB();
        acc[0+2][2] = MFMA16(AL[2], cat8(B1[2][0], B1[2][1]), acc[0+2][2]);
        SCHEDB();
        acc[0+2][3] = MFMA16(AL[2], cat8(B1[3][0], B1[3][1]), acc[0+2][3]);
        SCHEDB();
        { const float a = bflo(sv), c = bfhi(sv); s1a += a; s1b += c; s2a = __builtin_fmaf(a, a, s2a); s2b = __builtin_fmaf(c, c, s2b); }
        WAITL0();
        BAR();
        { const int dlt = (st & 3) == 3 ? -3 * G1_STAGE : G1_STAGE; abase += dlt; bbase += dlt; sbase += dlt; }
        acc[3+0][0] = MFMA16(AH[0], cat8(B1[0][0], B1[0][1]), acc[3+0][0]);
        AL[0] = *(const LAS bf16x8*)(abase + 0);
        SCHEDB();
        acc[3+0][1] = MFMA16(AH[0], cat8(B1[1][0], B1[1][1]), acc[3+0][1]);
        B0[0][0] = trread(bbase + 0);
        SCHEDB();
        acc[3+1][0] = MFMA16(AH[1], cat8(B1[0][0], B1[0][1]), acc[3+1][0]);
        B0[0][1] = trread(bbase + 2048);
        SCHEDB();
        acc[3+1][1] = MFMA16(AH[1], cat8(B1[1][0], B1[1][1]), acc[3+1][1]);
        B0[1][0] = trread(bbase + 256);
        SCHEDB();
        acc[3+0][2] = MFMA16(AH[0], cat8(B1[2][0], B1[2][1]), acc[3+0][2]);
        B0[1][1] = trread(bbase + 2304);
        SCHEDB();
        acc[3+1][2] = MFMA16(AH[1], cat8(B1[2][0], B1[2][1]), acc[3+1][2]);
        AL[1] = *(const LAS bf16x8*)(abase + 1024);
        SCHEDB();
        acc[3+0][3] = MFMA16(AH[0], cat8(B1[3][0], B1[3][1]), acc[3+0][3]);
        B0[2][0] = trread(bbase + 512);
        SCHEDB();
        acc[3+1][3] = MFMA16(AH[1], cat8(B1[3][0], B1[3][1]), acc[3+1][3]);
        B0[2][1] = trread(bbase + 2560);
        SCHEDB();
        acc[3+2][0] = MFMA16(AH[2], cat8(B1[0][0], B1[0][1]), acc[3+2][0]);
        B0[3][0] = trread(bbase + 768);
        SCHEDB();
        acc[3+2][1] = MFMA16(AH[2], cat8(B1[1][0], B1[1][1]), acc[3+2][1]);
        B0[3][1] = trread(bbase + 2816);
        SCHEDB();
        acc[3+2][2] = MFMA16(AH[2], cat8(B1[2][0], B1[2][1]), acc[3+2][2]);
        AL[2] = *(const LAS bf16x8*)(abase + 2048);
        SCHEDB();
        acc[3+2][3] = MFMA16(AH[2], cat8(B1[3][0], B1[3][1]), acc[3+2][3]);
        SCHEDB();
    }
    STAMP(ws, 4, 3);
    *(f32x4*)(smem + (w * 64 + lane) * 16) = f32x4{s1a, s1b, s2a, s2b};
    WAITL0();
    BAR();
    const rsrc_t rs = wsr;
#pragma unroll
    for (int a = 0; a < 3; ++a) {
        const int f0 = 96 * S + 16 * (3 * wm + a);
        const int kb = f0 >> 5, oct = ((f0 & 31) >> 3) + (g >> 1);
#pragma unroll
        for (int c = 0; c < 4; c += 2) {
            u32 X[2], Y[2];
#pragma unroll
            for (int e = 0; e < 2; ++e) {
                X[e] = cvtpk(__builtin_fmaxf(acc[2 * a][c][2 * e], 0.f) * acc[2 * a + 1][c][2 * e], __builtin_fmaxf(acc[2 * a][c][2 * e + 1], 0.f) * acc[2 * a + 1][c][2 * e + 1]);
                Y[e] = cvtpk(__builtin_fmaxf(acc[2 * a][c + 1][2 * e], 0.f) * acc[2 * a + 1][c + 1][2 * e], __builtin_fmaxf(acc[2 * a][c + 1][2 * e + 1], 0.f) * acc[2 * a + 1][c + 1][2 * e + 1]);
            }
#pragma unroll
            for (int e = 0; e < 2; ++e) { auto r = __builtin_amdgcn_permlane16_swap(X[e], Y[e], false, false); X[e] = r[0]; Y[e] = r[1]; }
            const int lt = 8 * P + 4 * wn + c + (g & 1);
            st16<16>(rs, WS_HM + ((lt * 48 + kb) * 64 + (lane & 15) + 16 * oct) * 16, u32x4{X[0], X[1], Y[0], Y[1]});
        }
    }
    STAMP(ws, 4, 4);
    return 0.f;
}

constexpr int G2_STAGE = 18432, G2_NS = 8, G2_NST = 24;
constexpr int G2_RES = G2_NS * G2_STAGE;
constexpr int G2_LDS = G2_RES + 10240;

DI void phase_gemm2(char* __restrict__ ws, u16* __restrict__ out, char* smem, u32 tag, float rs2pub) {
    const int b = blockIdx.x, t = threadIdx.x, lane = t & 63;
    const int w = __builtin_amdgcn_readfirstlane(t >> 6);
    const int xcd = b & 7, j = b >> 3;
    const int P = (xcd & 3) * 8 + (j & 7), S = (xcd >> 2) * 4 + (j >> 3);
    const u32 lds0 = (u32)(uintptr_t)smem;
    const rsrc_t wsr = mkrsrc(ws, WS_END);
    WAITV(0);
    BAR();
    if (w == 4) {
        const int P1 = (xcd & 1) * 8 + (j & 7), S1 = (xcd >> 1) * 4 + (j >> 3);
        const u32 up1 = __builtin_amdgcn_update_dpp(0, __float_as_uint(rs2pub), 0x111, 0xF, 0xF, true);
        if (lane < 9) st4<16>(wsr, WS_F3 + (P1 * 16 + S1) * 128 + lane * 4, lane == 0 ? tag : up1);
    }
    if (w >= 4) {
        const int lw = w - 4;
        const char* wsrc = ws + WS_W2T + S * 24 * 10240 + lw * 1024 + lane * 16;
        const char* msrc = ws + WS_HM + ((4 * P + (lw >> 1)) * 48 + (lw & 1)) * 1024 + lane * 16;
        auto issue_m = [&](int st, int slot) { glds16(msrc + st * 2048, lds0 + slot * G2_STAGE + 10240 + lw * 1024); };
        auto issue_w = [&](int st, int slot) {
            glds16(wsrc + st * 10240, lds0 + slot * G2_STAGE + lw * 1024);
            if (lw < 2) glds16(wsrc + st * 10240 + 8192, lds0 + slot * G2_STAGE + 8192 + lw * 1024);
        };
        auto residual = [&]() {
            for (int pc = lw; pc < 10; pc += 8) {
                const int row = 8 * pc + (lane >> 3), ch = (lane & 7) ^ (((lane >> 3) & 3) << 1);
                glds16(ws + WS_HC + ((80 * S + row) * L_ + 64 * P + 8 * ch) * 2, lds0 + G2_RES + pc * 1024);
            }
        };
        if (lw < 2) {
            issue_w(0, 0);
            issue_w(1, 1);
            issue_w(2, 2);
            issue_w(3, 3);
            issue_w(4, 4);
            issue_w(5, 5);
            issue_w(6, 6);
            issue_w(7, 7);
            if (lw == 0) poll_flags(wsr, WS_F3 + (P >> 1) * 16 * 128, 16, tag, lane);
            BAR();
            issue_m(0, 0);
            issue_m(1, 1);
            issue_m(2, 2);
            issue_m(3, 3);
            issue_m(4, 4);
            issue_m(5, 5);
            issue_m(6, 6);
            issue_m(7, 7);
            waitv<7>(); BAR();
            waitv<6>(); BAR();
            waitv<5>(); BAR();
            issue_m(8, 0); issue_w(8, 0);
            waitv<7>(); BAR();
            issue_m(9, 1); issue_w(9, 1);
            waitv<9>(); BAR();
            issue_m(10, 2); issue_w(10, 2);
            waitv<11>(); BAR();
            issue_m(11, 3); issue_w(11, 3);
            waitv<13>(); BAR();
            issue_m(12, 4); issue_w(12, 4);
            waitv<15>(); BAR();
            issue_m(13, 5); issue_w(13, 5);
            waitv<15>(); BAR();
            issue_m(14, 6); issue_w(14, 6);
            waitv<15>(); BAR();
            issue_m(15, 7); issue_w(15, 7);
            waitv<15>(); BAR();
            issue_m(16, 0); issue_w(16, 0);
            waitv<15>(); BAR();
            issue_m(17, 1); issue_w(17, 1);
            waitv<15>(); BAR();
            issue_m(18, 2); issue_w(18, 2);
            waitv<15>(); BAR();
            issue_m(19, 3); issue_w(19, 3);
            waitv<15>(); BAR();
            issue_m(20, 4); issue_w(20, 4);
            waitv<15>(); BAR();
            issue_m(21, 5); issue_w(21, 5);
            waitv<15>(); BAR();
            issue_m(22, 6); issue_w(22, 6);
            waitv<15>(); BAR();
            issue_m(23, 7); issue_w(23, 7);
            waitv<15>(); BAR();
            residual();
            waitv<12>(); BAR();
            waitv<9>(); BAR();
            waitv<6>(); BAR();
            waitv<3>(); BAR();
            waitv<0>(); BAR();
        } else {
            issue_w(0, 0);
            issue_w(1, 1);
            issue_w(2, 2);
            issue_w(3, 3);
            issue_w(4, 4);
            issue_w(5, 5);
            issue_w(6, 6);
            issue_w(7, 7);
            if (lw == 0) poll_flags(wsr, WS_F3 + (P >> 1) * 16 * 128, 16, tag, lane);
            BAR();
            issue_m(0, 0);
            issue_m(1, 1);
            issue_m(2, 2);
            issue_m(3, 3);
            issue_m(4, 4);
            issue_m(5, 5);
            issue_m(6, 6);
            issue_m(7, 7);
            waitv<7>(); BAR();
            waitv<6>(); BAR();
            waitv<5>(); BAR();
            issue_m(8, 0); issue_w(8, 0);
            waitv<6>(); BAR();
            issue_m(9, 1); issue_w(9, 1);
            waitv<7>(); BAR();
            issue_m(10, 2); issue_w(10, 2);
            waitv<8>(); BAR();
            issue_m(11, 3); issue_w(11, 3);
            waitv<9>(); BAR();
            issue_m(12, 4); issue_w(12, 4);
            waitv<10>(); BAR();
            issue_m(13, 5); issue_w(13, 5);
            waitv<10>(); BAR();
            issue_m(14, 6); issue_w(14, 6);
            waitv<10>(); BAR();
            issue_m(15, 7); issue_w(15, 7);
            waitv<10>(); BAR();
            issue_m(16, 0); issue_w(16, 0);
            waitv<10>(); BAR();
            issue_m(17, 1); issue_w(17, 1);
            waitv<10>(); BAR();
            issue_m(18, 2); issue_w(18, 2);
            waitv<10>(); BAR();
            issue_m(19, 3); issue_w(19, 3);
            waitv<10>(); BAR();
            issue_m(20, 4); issue_w(20, 4);
            waitv<10>(); BAR();
            issue_m(21, 5); issue_w(21, 5);
            waitv<10>(); BAR();
            issue_m(22, 6); issue_w(22, 6);
            waitv<10>(); BAR();
            issue_m(23, 7); issue_w(23, 7);
            waitv<10>(); BAR();
            residual();
            waitv<8>(); BAR();
            waitv<6>(); BAR();
            waitv<4>(); BAR();
            waitv<2>(); BAR();
            waitv<0>(); BAR();
        }
        return;
    }
    BAR();
    STAMP(ws, 5, 0);
    const int g = lane >> 4, q = (lane & 15) >> 2, p = lane & 3;
    const int l = 64 * P + 16 * w + (lane & 15);
    const float rs2 = __uint_as_float(__builtin_amdgcn_raw_buffer_load_b32(wsr, WS_F3 + (l >> 3) * 128 + 4 + (l & 7) * 4, 0, 16));
    const float m1 = bflo(__builtin_amdgcn_raw_buffer_load_b32(wsr, WS_GR + (l >> 3) * 128 + (l & 7) * 8, 0, 16));
    f32x4 acc[5];
#pragma unroll
    for (int a = 0; a < 5; ++a) acc[a] = f32x4{0.f, 0.f, 0.f, 0.f};
    const LAS char* abase = (const LAS char*)smem + lane * 16;
    const LAS char* bbase = (const LAS char*)smem + 10240 + w * 2048 + lane * 16;
    bf16x8 EA[2][5], EB[2], OA[2][5], OB[2];
    BAR();
    STAMP(ws, 5, 1);
    EB[0] = *(const LAS bf16x8*)(bbase); EB[1] = *(const LAS bf16x8*)(bbase + 1024);
#pragma unroll
    for (int a = 0; a < 5; ++a) { EA[0][a] = *(const LAS bf16x8*)(abase + a * 1024); EA[1][a] = *(const LAS bf16x8*)(abase + 5120 + a * 1024); }
    SCHEDB();
#pragma unroll 1
    for (int it = 0; it < 3; ++it) {
        if (it == 1) STAMP(ws, 5, 2); if (it == 2) STAMP(ws, 5, 3);
        BAR();
        acc[0] = MFMA16(EA[0][0], EB[0], acc[0]);
        OB[0] = *(const LAS bf16x8*)(bbase + 1*G2_STAGE);
        OA[0][0] = *(const LAS bf16x8*)(abase + 1*G2_STAGE + 0);
        SCHEDB();
        acc[1] = MFMA16(EA[0][1], EB[0], acc[1]);
        OA[0][1] = *(const LAS bf16x8*)(abase + 1*G2_STAGE + 1024);
        OA[0][2] = *(const LAS bf16x8*)(abase + 1*G2_STAGE + 2048);
        SCHEDB();
        acc[2] = MFMA16(EA[0][2], EB[0], acc[2]);
        OA[0][3] = *(const LAS bf16x8*)(abase + 1*G2_STAGE + 3072);
        SCHEDB();
        acc[3] = MFMA16(EA[0][3], EB[0], acc[3]);
        OA[0][4] = *(const LAS bf16x8*)(abase + 1*G2_STAGE + 4096);
        SCHEDB();
        acc[4] = MFMA16(EA[0][4], EB[0], acc[4]);
        OB[1] = *(const LAS bf16x8*)(bbase + 1*G2_STAGE + 1024);
        SCHEDB();
        acc[0] = MFMA16(EA[1][0], EB[1], acc[0]);
        OA[1][0] = *(const LAS bf16x8*)(abase + 1*G2_STAGE + 5120);
        SCHEDB();
        acc[1] = MFMA16(EA[1][1], EB[1], acc[1]);
        OA[1][1] = *(const LAS bf16x8*)(abase + 1*G2_STAGE + 6144);
        SCHEDB();
        acc[2] = MFMA16(EA[1][2], EB[1], acc[2]);
        OA[1][2] = *(const LAS bf16x8*)(abase + 1*G2_STAGE + 7168);
        SCHEDB();
        acc[3] = MFMA16(EA[1][3], EB[1], acc[3]);
        OA[1][3] = *(const LAS bf16x8*)(abase + 1*G2_STAGE + 8192);
        SCHEDB();
        acc[4] = MFMA16(EA[1][4], EB[1], acc[4]);
        OA[1][4] = *(const LAS bf16x8*)(abase + 1*G2_STAGE + 9216);
        SCHEDB();
        BAR();
        acc[0] = MFMA16(OA[0][0], OB[0], acc[0]);
        EB[0] = *(const LAS bf16x8*)(bbase + 2*G2_STAGE);
        EA[0][0] = *(const LAS bf16x8*)(abase + 2*G2_STAGE + 0);
        SCHEDB();
        acc[1] = MFMA16(OA[0][1], OB[0], acc[1]);
        EA[0][1] = *(const LAS bf16x8*)(abase + 2*G2_STAGE + 1024);
        EA[0][2] = *(const LAS bf16x8*)(abase + 2*G2_STAGE + 2048);
        SCHEDB();
        acc[2] = MFMA16(OA[0][2], OB[0], acc[2]);
        EA[0][3] = *(const LAS bf16x8*)(abase + 2*G2_STAGE + 3072);
        SCHEDB();
        acc[3] = MFMA16(OA[0][3], OB[0], acc[3]);
        EA[0][4] = *(const LAS bf16x8*)(abase + 2*G2_STAGE + 4096);
        SCHEDB();
        acc[4] = MFMA16(OA[0][4], OB[0], acc[4]);
        EB[1] = *(const LAS bf16x8*)(bbase + 2*G2_STAGE + 1024);
        SCHEDB();
        acc[0] = MFMA16(OA[1][0], OB[1], acc[0]);
        EA[1][0] = *(const LAS bf16x8*)(abase + 2*G2_STAGE + 5120);
        SCHEDB();
        acc[1] = MFMA16(OA[1][1], OB[1], acc[1]);
        EA[1][1] = *(const LAS bf16x8*)(abase + 2*G2_STAGE + 6144);
        SCHEDB();
        acc[2] = MFMA16(OA[1][2], OB[1], acc[2]);
        EA[1][2] = *(const LAS bf16x8*)(abase + 2*G2_STAGE + 7168);
        SCHEDB();
        acc[3] = MFMA16(OA[1][3], OB[1], acc[3]);
        EA[1][3] = *(const LAS bf16x8*)(abase + 2*G2_STAGE + 8192);
        SCHEDB();
        acc[4] = MFMA16(OA[1][4], OB[1], acc[4]);
        EA[1][4] = *(const LAS bf16x8*)(abase + 2*G2_STAGE + 9216);
        SCHEDB();
        BAR();
        acc[0] = MFMA16(EA[0][0], EB[0], acc[0]);
        OB[0] = *(const LAS bf16x8*)(bbase + 3*G2_STAGE);
        OA[0][0] = *(const LAS bf16x8*)(abase + 3*G2_STAGE + 0);
        SCHEDB();
        acc[1] = MFMA16(EA[0][1], EB[0], acc[1]);
        OA[0][1] = *(const LAS bf16x8*)(abase + 3*G2_STAGE + 1024);
        OA[0][2] = *(const LAS bf16x8*)(abase + 3*G2_STAGE + 2048);
        SCHEDB();
        acc[2] = MFMA16(EA[0][2], EB[0], acc[2]);
        OA[0][3] = *(const LAS bf16x8*)(abase + 3*G2_STAGE + 3072);
        SCHEDB();
        acc[3] = MFMA16(EA[0][3], EB[0], acc[3]);
        OA[0][4] = *(const LAS bf16x8*)(abase + 3*G2_STAGE + 4096);
        SCHEDB();
        acc[4] = MFMA16(EA[0][4], EB[0], acc[4]);
        OB[1] = *(const LAS bf16x8*)(bbase + 3*G2_STAGE + 1024);
        SCHEDB();
        acc[0] = MFMA16(EA[1][0], EB[1], acc[0]);
        OA[1][0] = *(const LAS bf16x8*)(abase + 3*G2_STAGE + 5120);
        SCHEDB();
        acc[1] = MFMA16(EA[1][1], EB[1], acc[1]);
        OA[1][1] = *(const LAS bf16x8*)(abase + 3*G2_STAGE + 6144);
        SCHEDB();
        acc[2] = MFMA16(EA[1][2], EB[1], acc[2]);
        OA[1][2] = *(const LAS bf16x8*)(abase + 3*G2_STAGE + 7168);
        SCHEDB();
        acc[3] = MFMA16(EA[1][3], EB[1], acc[3]);
        OA[1][3] = *(const LAS bf16x8*)(abase + 3*G2_STAGE + 8192);
        SCHEDB();
        acc[4] = MFMA16(EA[1][4], EB[1], acc[4]);
        OA[1][4] = *(const LAS bf16x8*)(abase + 3*G2_STAGE + 9216);
        SCHEDB();
        BAR();
        acc[0] = MFMA16(OA[0][0], OB[0], acc[0]);
        EB[0] = *(const LAS bf16x8*)(bbase + 4*G2_STAGE);
        EA[0][0] = *(const LAS bf16x8*)(abase + 4*G2_STAGE + 0);
        SCHEDB();
        acc[1] = MFMA16(OA[0][1], OB[0], acc[1]);
        EA[0][1] = *(const LAS bf16x8*)(abase + 4*G2_STAGE + 1024);
        EA[0][2] = *(const LAS bf16x8*)(abase + 4*G2_STAGE + 2048);
        SCHEDB();
        acc[2] = MFMA16(OA[0][2], OB[0], acc[2]);
        EA[0][3] = *(const LAS bf16x8*)(abase + 4*G2_STAGE + 3072);
        SCHEDB();
        acc[3] = MFMA16(OA[0][3], OB[0], acc[3]);
        EA[0][4] = *(const LAS bf16x8*)(abase + 4*G2_STAGE + 4096);
        SCHEDB();
        acc[4] = MFMA16(OA[0][4], OB[0], acc[4]);
        EB[1] = *(const LAS bf16x8*)(bbase + 4*G2_STAGE + 1024);
        SCHEDB();
        acc[0] = MFMA16(OA[1][0], OB[1], acc[0]);
        EA[1][0] = *(const LAS bf16x8*)(abase + 4*G2_STAGE + 5120);
        SCHEDB();
        acc[1] = MFMA16(OA[1][1], OB[1], acc[1]);
        EA[1][1] = *(const LAS bf16x8*)(abase + 4*G2_STAGE + 6144);
        SCHEDB();
        acc[2] = MFMA16(OA[1][2], OB[1], acc[2]);
        EA[1][2] = *(const LAS bf16x8*)(abase + 4*G2_STAGE + 7168);
        SCHEDB();
        acc[3] = MFMA16(OA[1][3], OB[1], acc[3]);
        EA[1][3] = *(const LAS bf16x8*)(abase + 4*G2_STAGE + 8192);
        SCHEDB();
        acc[4] = MFMA16(OA[1][4], OB[1], acc[4]);
        EA[1][4] = *(const LAS bf16x8*)(abase + 4*G2_STAGE + 9216);
        SCHEDB();
        BAR();
        acc[0] = MFMA16(EA[0][0], EB[0], acc[0]);
        OB[0] = *(const LAS bf16x8*)(bbase + 5*G2_STAGE);
        OA[0][0] = *(const LAS bf16x8*)(abase + 5*G2_STAGE + 0);
        SCHEDB();
        acc[1] = MFMA16(EA[0][1], EB[0], acc[1]);
        OA[0][1] = *(const LAS bf16x8*)(abase + 5*G2_STAGE + 1024);
        OA[0][2] = *(const LAS bf16x8*)(abase + 5*G2_STAGE + 2048);
        SCHEDB();
        acc[2] = MFMA16(EA[0][2], EB[0], acc[2]);
        OA[0][3] = *(const LAS bf16x8*)(abase + 5*G2_STAGE + 3072);
        SCHEDB();
        acc[3] = MFMA16(EA[0][3], EB[0], acc[3]);
        OA[0][4] = *(const LAS bf16x8*)(abase + 5*G2_STAGE + 4096);
        SCHEDB();
        acc[4] = MFMA16(EA[0][4], EB[0], acc[4]);
        OB[1] = *(const LAS bf16x8*)(bbase + 5*G2_STAGE + 1024);
        SCHEDB();
        acc[0] = MFMA16(EA[1][0], EB[1], acc[0]);
        OA[1][0] = *(const LAS bf16x8*)(abase + 5*G2_STAGE + 5120);
        SCHEDB();
        acc[1] = MFMA16(EA[1][1], EB[1], acc[1]);
        OA[1][1] = *(const LAS bf16x8*)(abase + 5*G2_STAGE + 6144);
        SCHEDB();
        acc[2] = MFMA16(EA[1][2], EB[1], acc[2]);
        OA[1][2] = *(const LAS bf16x8*)(abase + 5*G2_STAGE + 7168);
        SCHEDB();
        acc[3] = MFMA16(EA[1][3], EB[1], acc[3]);
        OA[1][3] = *(const LAS bf16x8*)(abase + 5*G2_STAGE + 8192);
        SCHEDB();
        acc[4] = MFMA16(EA[1][4], EB[1], acc[4]);
        OA[1][4] = *(const LAS bf16x8*)(abase + 5*G2_STAGE + 9216);
        SCHEDB();
        BAR();
        acc[0] = MFMA16(OA[0][0], OB[0], acc[0]);
        EB[0] = *(const LAS bf16x8*)(bbase + 6*G2_STAGE);
        EA[0][0] = *(const LAS bf16x8*)(abase + 6*G2_STAGE + 0);
        SCHEDB();
        acc[1] = MFMA16(OA[0][1], OB[0], acc[1]);
        EA[0][1] = *(const LAS bf16x8*)(abase + 6*G2_STAGE + 1024);
        EA[0][2] = *(const LAS bf16x8*)(abase + 6*G2_STAGE + 2048);
        SCHEDB();
        acc[2] = MFMA16(OA[0][2], OB[0], acc[2]);
        EA[0][3] = *(const LAS bf16x8*)(abase + 6*G2_STAGE + 3072);
        SCHEDB();
        acc[3] = MFMA16(OA[0][3], OB[0], acc[3]);
        EA[0][4] = *(const LAS bf16x8*)(abase + 6*G2_STAGE + 4096);
        SCHEDB();
        acc[4] = MFMA16(OA[0][4], OB[0], acc[4]);
        EB[1] = *(const LAS bf16x8*)(bbase + 6*G2_STAGE + 1024);
        SCHEDB();
        acc[0] = MFMA16(OA[1][0], OB[1], acc[0]);
        EA[1][0] = *(const LAS bf16x8*)(abase + 6*G2_STAGE + 5120);
        SCHEDB();
        acc[1] = MFMA16(OA[1][1], OB[1], acc[1]);
        EA[1][1] = *(const LAS bf16x8*)(abase + 6*G2_STAGE + 6144);
        SCHEDB();
        acc[2] = MFMA16(OA[1][2], OB[1], acc[2]);
        EA[1][2] = *(const LAS bf16x8*)(abase + 6*G2_STAGE + 7168);
        SCHEDB();
        acc[3] = MFMA16(OA[1][3], OB[1], acc[3]);
        EA[1][3] = *(const LAS bf16x8*)(abase + 6*G2_STAGE + 8192);
        SCHEDB();
        acc[4] = MFMA16(OA[1][4], OB[1], acc[4]);
        EA[1][4] = *(const LAS bf16x8*)(abase + 6*G2_STAGE + 9216);
        SCHEDB();
        BAR();
        acc[0] = MFMA16(EA[0][0], EB[0], acc[0]);
        OB[0] = *(const LAS bf16x8*)(bbase + 7*G2_STAGE);
        OA[0][0] = *(const LAS bf16x8*)(abase + 7*G2_STAGE + 0);
        SCHEDB();
        acc[1] = MFMA16(EA[0][1], EB[0], acc[1]);
        OA[0][1] = *(const LAS bf16x8*)(abase + 7*G2_STAGE + 1024);
        OA[0][2] = *(const LAS bf16x8*)(abase + 7*G2_STAGE + 2048);
        SCHEDB();
        acc[2] = MFMA16(EA[0][2], EB[0], acc[2]);
        OA[0][3] = *(const LAS bf16x8*)(abase + 7*G2_STAGE + 3072);
        SCHEDB();
        acc[3] = MFMA16(EA[0][3], EB[0], acc[3]);
        OA[0][4] = *(const LAS bf16x8*)(abase + 7*G2_STAGE + 4096);
        SCHEDB();
        acc[4] = MFMA16(EA[0][4], EB[0], acc[4]);
        OB[1] = *(const LAS bf16x8*)(bbase + 7*G2_STAGE + 1024);
        SCHEDB();
        acc[0] = MFMA16(EA[1][0], EB[1], acc[0]);
        OA[1][0] = *(const LAS bf16x8*)(abase + 7*G2_STAGE + 5120);
        SCHEDB();
        acc[1] = MFMA16(EA[1][1], EB[1], acc[1]);
        OA[1][1] = *(const LAS bf16x8*)(abase + 7*G2_STAGE + 6144);
        SCHEDB();
        acc[2] = MFMA16(EA[1][2], EB[1], acc[2]);
        OA[1][2] = *(const LAS bf16x8*)(abase + 7*G2_STAGE + 7168);
        SCHEDB();
        acc[3] = MFMA16(EA[1][3], EB[1], acc[3]);
        OA[1][3] = *(const LAS bf16x8*)(abase + 7*G2_STAGE + 8192);
        SCHEDB();
        acc[4] = MFMA16(EA[1][4], EB[1], acc[4]);
        OA[1][4] = *(const LAS bf16x8*)(abase + 7*G2_STAGE + 9216);
        SCHEDB();
        if (it < 2) BAR();
        acc[0] = MFMA16(OA[0][0], OB[0], acc[0]);
        EB[0] = *(const LAS bf16x8*)(bbase + 0*G2_STAGE);
        EA[0][0] = *(const LAS bf16x8*)(abase + 0*G2_STAGE + 0);
        SCHEDB();
        acc[1] = MFMA16(OA[0][1], OB[0], acc[1]);
        EA[0][1] = *(const LAS bf16x8*)(abase + 0*G2_STAGE + 1024);
        EA[0][2] = *(const LAS bf16x8*)(abase + 0*G2_STAGE + 2048);
        SCHEDB();
        acc[2] = MFMA16(OA[0][2], OB[0], acc[2]);
        EA[0][3] = *(const LAS bf16x8*)(abase + 0*G2_STAGE + 3072);
        SCHEDB();
        acc[3] = MFMA16(OA[0][3], OB[0], acc[3]);
        EA[0][4] = *(const LAS bf16x8*)(abase + 0*G2_STAGE + 4096);
        SCHEDB();
        acc[4] = MFMA16(OA[0][4], OB[0], acc[4]);
        EB[1] = *(const LAS bf16x8*)(bbase + 0*G2_STAGE + 1024);
        SCHEDB();
        acc[0] = MFMA16(OA[1][0], OB[1], acc[0]);
        EA[1][0] = *(const LAS bf16x8*)(abase + 0*G2_STAGE + 5120);
        SCHEDB();
        acc[1] = MFMA16(OA[1][1], OB[1], acc[1]);
        EA[1][1] = *(const LAS bf16x8*)(abase + 0*G2_STAGE + 6144);
        SCHEDB();
        acc[2] = MFMA16(OA[1][2], OB[1], acc[2]);
        EA[1][2] = *(const LAS bf16x8*)(abase + 0*G2_STAGE + 7168);
        SCHEDB();
        acc[3] = MFMA16(OA[1][3], OB[1], acc[3]);
        EA[1][3] = *(const LAS bf16x8*)(abase + 0*G2_STAGE + 8192);
        SCHEDB();
        acc[4] = MFMA16(OA[1][4], OB[1], acc[4]);
        EA[1][4] = *(const LAS bf16x8*)(abase + 0*G2_STAGE + 9216);
        SCHEDB();
    }
    STAMP(ws, 5, 6);
    const rsrc_t ro = mkrsrc(out, L_ * D_ * 2);
#pragma unroll
    for (int a = 0; a < 5; ++a) {
        const int row = 16 * a + 4 * g + q;
        const int ch = (2 * w + (p >> 1)) ^ ((row & 3) << 1);
        const s16x4 hr = trread((const LAS char*)smem + G2_RES + row * 128 + ch * 16 + 8 * (p & 1));
        const u32x2 hh = __builtin_bit_cast(u32x2, hr);
        const u32 f01 = cvtpk(acc[a][0] * rs2, acc[a][1] * rs2), f23 = cvtpk(acc[a][2] * rs2, acc[a][3] * rs2);
        const u32 c01 = hh[0], c23 = hh[1];
        const u32 h01 = cvtpk(bflo(c01) + m1, bfhi(c01) + m1), h23 = cvtpk(bflo(c23) + m1, bfhi(c23) + m1);
        __builtin_amdgcn_raw_buffer_store_b64(u32x2{cvtpk(bflo(h01) + bflo(f01), bfhi(h01) + bfhi(f01)), cvtpk(bflo(h23) + bflo(f23), bfhi(h23) + bfhi(f23))}, ro, (l * D_ + 80 * S + 16 * a + 4 * g) * 2, 0, 0);
    }
    STAMP(ws, 5, 7);
}

constexpr int ALL_LDS = 163840;
static_assert(KA_LDS <= ALL_LDS && G1_LDS <= ALL_LDS && G2_LDS <= ALL_LDS, "LDS");

__global__ __launch_bounds__(768, 3) void k_block(const u16* __restrict__ x, const u16* __restrict__ Are, const u16* __restrict__ Aim, const u16* __restrict__ Bre,
                                                  const u16* __restrict__ Bim, const u16* __restrict__ Cre, const u16* __restrict__ Cim, const u16* __restrict__ logdt,
                                                  const u16* __restrict__ Dskip, const u16* __restrict__ Wg, const u16* __restrict__ Wu, const u16* __restrict__ Wd,
                                                  char* __restrict__ ws, u16* __restrict__ out) {
    __shared__ __attribute__((aligned(1024))) char smem[ALL_LDS];
    if (threadIdx.x >= 704) {
        unsigned long long pc; asm volatile("s_getpc_b64 %0" : "=s"(pc));
        const unsigned long long base = (pc & ~127ull) + (unsigned long long)((blockIdx.x >> 3) * 12 + (threadIdx.x & 63)) * 128;
        const u32 dump = (u32)(uintptr_t)smem + ALL_LDS - 256;
        if ((threadIdx.x & 63) < 12) { u32 sv; asm volatile("s_mov_b32 %0, m0\n\ts_mov_b32 m0, %2\n\ts_nop 0\n\tglobal_load_lds_dword %1, off\n\ts_mov_b32 m0, %0" : "=&s"(sv) : "v"(base), "s"(dump) : "memory"); }
    }
    const u32 tag = phase_ssm(x, Are, Aim, Bre, Bim, Cre, Cim, logdt, Dskip, Wg, Wu, Wd, ws, smem);
    const float rs2 = phase_gemm1(ws, smem, tag);
    phase_gemm2(ws, out, smem, tag, rs2);
}

extern "C" void kernel_launch(void* const* d_in, const int* in_sizes, int n_in, void* d_out, int out_size, void* d_ws, size_t ws_size, hipStream_t stream) {
    const u16* x = (const u16*)d_in[0];
    const u16* Are = (const u16*)d_in[1]; const u16* Aim = (const u16*)d_in[2];
    const u16* Bre = (const u16*)d_in[3]; const u16* Bim = (const u16*)d_in[4];
    const u16* Cre = (const u16*)d_in[5]; const u16* Cim = (const u16*)d_in[6];
    const u16* logdt = (const u16*)d_in[7]; const u16* Dskip = (const u16*)d_in[8];
    const u16* Wg = (const u16*)d_in[10]; const u16* Wu = (const u16*)d_in[11]; const u16* Wd = (const u16*)d_in[12];
    k_block<<<256, 768, 0, stream>>>(x, Are, Aim, Bre, Bim, Cre, Cim, logdt, Dskip, Wg, Wu, Wd, (char*)d_ws, (u16*)d_out);
}
```

```cpp
#include <hip/hip_runtime.h>
#include <stdint.h>

typedef unsigned short u16;
typedef unsigned int u32;
typedef short bf16x8 __attribute__((ext_vector_type(8)));
typedef short s16x4 __attribute__((ext_vector_type(4)));
typedef float f32x2 __attribute__((ext_vector_type(2)));
typedef float f32x4 __attribute__((ext_vector_type(4)));
typedef float f32x16 __attribute__((ext_vector_type(16)));
typedef unsigned u32x4 __attribute__((ext_vector_type(4)));
typedef unsigned u32x2 __attribute__((ext_vector_type(2)));
typedef __bf16 bf16x2_t __attribute__((ext_vector_type(2)));
#define LAS __attribute__((address_space(3)))
#define DI __device__ __forceinline__

#define STAMP(ws, k, j) do {} while (0)
#define STAMP2(ws, k, j) do {} while (0)

#define TLB_WARM 1
#define EARLY_W 0
constexpr int L_ = 2048, D_ = 640, NM_ = 192, F_ = 1536;

constexpr u32 WS_HC  = 0;
constexpr u32 WS_HM  = WS_HC + D_ * L_ * 2;
constexpr u32 WS_W1T = WS_HM + L_ * F_ * 2;
constexpr u32 WS_W2T = WS_W1T + D_ * F_ * 2 * 2;
constexpr u32 WS_GR  = WS_W2T + F_ * D_ * 2;
constexpr u32 WS_F2  = WS_GR + 256 * 128;
constexpr u32 WS_FW  = WS_F2 + 256 * 128;
constexpr u32 WS_F3  = WS_FW + 128 * 128;
constexpr u32 WS_EP  = WS_F3 + 256 * 128;
constexpr u32 WS_END = WS_EP + 256;

DI float bflo(u32 v) { return __uint_as_float(v << 16); }
DI float bfhi(u32 v) { return __uint_as_float(v & 0xffff0000u); }
DI float bf2f(u16 v) { return __uint_as_float(((u32)v) << 16); }
DI u32 cvtpk(float lo, float hi) { f32x2 v = {lo, hi}; bf16x2_t r = __builtin_convertvector(v, bf16x2_t); return __builtin_bit_cast(u32, r); }
DI float rbf(float f) { return bflo(cvtpk(f, f)); }

typedef __amdgpu_buffer_rsrc_t rsrc_t;
DI rsrc_t mkrsrc(void* p, u32 bytes) { return __builtin_amdgcn_make_buffer_rsrc(p, 0, bytes, 0x00020000); }
#define AUX_KL 0
#define AUX_WT 0
#define AUX_H 0
#define AUX_HM 0
template <int AUX = 0> DI void st16(rsrc_t r, u32 off, u32x4 v) { __builtin_amdgcn_raw_buffer_store_b128(v, r, off, 0, AUX); }
template <int AUX = 0> DI void st8(rsrc_t r, u32 off, u32x2 v) { __builtin_amdgcn_raw_buffer_store_b64(v, r, off, 0, AUX); }
template <int AUX = 0> DI void st4(rsrc_t r, u32 off, u32 v) { __builtin_amdgcn_raw_buffer_store_b32(v, r, off, 0, AUX); }

template <int CTRL> DI float dppadd(float v) { return v + __builtin_bit_cast(float, __builtin_amdgcn_update_dpp(0, __builtin_bit_cast(int, v), CTRL, 0xF, 0xF, true)); }
DI float swap16add(float v) { auto r = __builtin_amdgcn_permlane16_swap(__float_as_uint(v), __float_as_uint(v), false, false); const u32 a = r[0], b = r[1]; return __uint_as_float(a) + __uint_as_float(b); }
DI float swap32add(float v) { auto r = __builtin_amdgcn_permlane32_swap(__float_as_uint(v), __float_as_uint(v), false, false); const u32 a = r[0], b = r[1]; return __uint_as_float(a) + __uint_as_float(b); }
DI float wave_sum(float v) {
    v = dppadd<0xB1>(v);
    v = dppadd<0x4E>(v);
    v = dppadd<0x141>(v);
    v = dppadd<0x140>(v);
    return swap32add(swap16add(v));
}

struct cm { f32x2 a, s; };
DI cm mkcm(f32x2 b) { return { b, f32x2{-b.y, b.x} }; }
DI f32x2 cmul(f32x2 v, const cm& m) { return v.xx * m.a + v.yy * m.s; }
DI f32x2 csq(f32x2 a) { return f32x2{ a.x * a.x - a.y * a.y, 2.f * a.x * a.y }; }
DI f32x2 cexp_p(float ar, float br, float p) {
    float e = __builtin_amdgcn_exp2f(ar * p);
    float th = __builtin_amdgcn_fractf(br * p);
    return f32x2{ e * __builtin_amdgcn_cosf(th), e * __builtin_amdgcn_sinf(th) };
}

#define WAITV(n) asm volatile("s_waitcnt vmcnt(" #n ")" ::: "memory")
#define WAITL0() asm volatile("s_waitcnt lgkmcnt(0)" ::: "memory")
template <int N> DI void waitv() { asm volatile("s_waitcnt vmcnt(%0)" :: "n"(N) : "memory"); }
#define BAR() do { asm volatile("" ::: "memory"); __builtin_amdgcn_s_barrier(); asm volatile("" ::: "memory"); } while (0)
#define LBAR() do { WAITL0(); BAR(); } while (0)
#define SGB(m, n) __builtin_amdgcn_sched_group_barrier(m, n, 0)
#define SCHEDB() __builtin_amdgcn_sched_barrier(0)
#define MFMA16(a, b, c) __builtin_amdgcn_mfma_f32_16x16x32_bf16(a, b, c, 0, 0, 0)
#define GAS __attribute__((address_space(1)))
typedef unsigned long long u64;

DI void glds16(const void* g, u32 lds_base) {
    u32 sv; asm volatile("s_mov_b32 %0, m0\n\ts_mov_b32 m0, %2\n\ts_nop 0\n\tglobal_load_lds_dwordx4 %1, off sc1\n\ts_mov_b32 m0, %0" : "=&s"(sv) : "v"(g), "s"(lds_base) : "memory");
}
DI void poll_flags(rsrc_t wsr, u32 off, int n, u32 tag, int lane) {
    const u32 o = off + (lane < n ? lane : 0) * 128;
    for (int spin = 0; spin < 100000; ++spin) {
        asm volatile("" ::: "memory");
        const u32 v = __builtin_amdgcn_raw_buffer_load_b32(wsr, o, 0, 16);
        if (__all(v == tag)) break;
        __builtin_amdgcn_s_sleep(1);
    }
    asm volatile("" ::: "memory");
}
DI void st_flag(char* ws, u32 off, u32 tag) { __hip_atomic_store((GAS u32*)(ws + off), tag, __ATOMIC_RELAXED, __HIP_MEMORY_SCOPE_AGENT); }
DI s16x4 trread(const LAS char* p) { typedef short v4i16_t __attribute__((ext_vector_type(4))); return __builtin_bit_cast(s16x4, __builtin_amdgcn_ds_read_tr16_b64_v4i16((LAS v4i16_t*)p)); }
DI bf16x8 cat8(s16x4 a, s16x4 b) { return __builtin_shufflevector(a, b, 0, 1, 2, 3, 4, 5, 6, 7); }

constexpr int KG_PITCH = 784;
constexpr int KA_A = 0, KA_ASZ = 20 * KG_PITCH;
constexpr int KA_B = 3 * KA_ASZ, KA_BSZ = 32 * KG_PITCH;
constexpr int KA_U = KA_B + 3 * KA_BSZ, KA_USZ = 4608 + 4096;
constexpr int KA_CNT = KA_U + 3 * KA_USZ;
constexpr int KA_LDS = KA_CNT + 48;
constexpr int CV_CS = 4160;
constexpr int KB_X = 32256;

#define ADDTID2(base0, v0, o0, base1, v1, o1) asm volatile("s_mov_b32 m0, %2\n\ts_nop 0\n\tds_write_addtid_b32 %0 offset:%4\n\ts_mov_b32 m0, %3\n\ts_nop 0\n\tds_write_addtid_b32 %1 offset:%5" \
    :: "v"(v0), "v"(v1), "s"(base0), "s"(base1), "i"(o0), "i"(o1) : "memory")
#define ADDTID2S(base, v0, o0, v1, o1) asm volatile("s_mov_b32 m0, %2\n\ts_nop 0\n\tds_write_addtid_b32 %0 offset:%3\n\tds_write_addtid_b32 %1 offset:%4" :: "v"(v0), "v"(v1), "s"(base), "i"(o0), "i"(o1) : "memory")

DI void lds_sync(char* smem, int cnt, u32 target, int lane) {
    LAS u32* c = (LAS u32*)((LAS char*)smem + KA_CNT) + cnt;
    WAITL0();
    if (lane == 0) __hip_atomic_fetch_add(c, 1u, __ATOMIC_RELAXED, __HIP_MEMORY_SCOPE_WORKGROUP);
    for (int spin = 0; spin < 1000000; ++spin) {
        if (__builtin_amdgcn_readfirstlane(__hip_atomic_load(c, __ATOMIC_RELAXED, __HIP_MEMORY_SCOPE_WORKGROUP)) >= target) break;
        __builtin_amdgcn_s_sleep(1);
    }
    asm volatile("" ::: "memory");
}

DI void lds_arrive(char* smem, int cnt, int lane) {
    WAITL0();
    if (lane == 0) __hip_atomic_fetch_add((LAS u32*)((LAS char*)smem + KA_CNT) + cnt, 1u, __ATOMIC_RELAXED, __HIP_MEMORY_SCOPE_WORKGROUP);
}
DI void lds_wait(char* smem, int cnt, u32 target) {
    LAS u32* c = (LAS u32*)((LAS char*)smem + KA_CNT) + cnt;
    for (int spin = 0; spin < 1000000; ++spin) {
        if (__builtin_amdgcn_readfirstlane(__hip_atomic_load(c, __ATOMIC_RELAXED, __HIP_MEMORY_SCOPE_WORKGROUP)) >= target) break;
        __builtin_amdgcn_s_sleep(1);
    }
    asm volatile("" ::: "memory");
}

struct KParams { u16 are, aim, bre, bim, cre, cim, ldt; };

DI void ka_kgen(const KParams& P, char* smem, int sA, int sB, int w, int lane) {
    const float are = bf2f(P.are), aim = bf2f(P.aim), bre = bf2f(P.bre), bim = bf2f(P.bim), cre = bf2f(P.cre), cim = bf2f(P.cim);
    const float dt = rbf(__expf(bf2f(P.ldt)));
    const float ar = (dt * are) * 1.4426950408889634f, br = (dt * aim) * 0.15915494309189535f;
    const float dbr = dt * bre, dbi = dt * bim;
    const f32x2 Wc = { cre * dbr - cim * dbi, -(cre * dbi + cim * dbr) };
    const f32x2 l1 = cexp_p(ar, br, 1.f), l8 = cexp_p(ar, br, 8.f), l256 = cexp_p(ar, br, 256.f);
    const f32x2 l2 = csq(l1), l4 = csq(l2), l16 = csq(l8);
    const cm c1 = mkcm(f32x2{l1.x, -l1.y}), c2 = mkcm(f32x2{l2.x, -l2.y}), c4 = mkcm(f32x2{l4.x, -l4.y}), c256 = mkcm(f32x2{l256.x, -l256.y}), m16 = mkcm(l16);
    const u32 lds0 = (u32)(uintptr_t)smem;
    const u32 bA = lds0 + sA + w * 256, bB = lds0 + sB + w * 256 - KB_X;
    auto pk = [](f32x2 v) { return cvtpk(v.x, v.y); };
    constexpr int R = KG_PITCH, X = KB_X;
    f32x2 a0 = Wc, x256 = cmul(Wc, c256), e0 = {1.f, 0.f}, e1 = l8;
    ADDTID2S(bA, pk(a0), 0 * R, pk(x256), 8 * R);
    ADDTID2S(bB, pk(e0), X + 0 * R, pk(e1), X + 1 * R);
    f32x2 y = x256, v = cmul(x256, c256);
#define KG_E(c) do { e0 = cmul(e0, m16); e1 = cmul(e1, m16); ADDTID2S(bB, pk(e0), X + (c) * R, pk(e1), X + ((c) + 1) * R); } while (0)
    a0 = cmul(a0, c1); y = cmul(y, c2); ADDTID2S(bA, pk(a0), 1 * R, pk(y), 9 * R); KG_E(2);
    a0 = cmul(a0, c1); y = cmul(y, c2); ADDTID2S(bA, pk(a0), 2 * R, pk(y), 10 * R); KG_E(4);
    a0 = cmul(a0, c1); y = cmul(y, c2); ADDTID2S(bA, pk(a0), 3 * R, pk(y), 11 * R); KG_E(6);
    a0 = cmul(a0, c1); y = cmul(v, c4); ADDTID2S(bA, pk(a0), 4 * R, pk(v), 12 * R); v = cmul(v, c256); KG_E(8);
    a0 = cmul(a0, c1); ADDTID2S(bA, pk(a0), 5 * R, pk(y), 13 * R); y = cmul(v, c4); KG_E(10);
    a0 = cmul(a0, c1); ADDTID2S(bA, pk(a0), 6 * R, pk(v), 14 * R); v = cmul(v, c256); KG_E(12);
    a0 = cmul(a0, c1); ADDTID2S(bA, pk(a0), 7 * R, pk(y), 15 * R); KG_E(14);
    y = cmul(v, c256); KG_E(16);
    ADDTID2S(bA, pk(v), 16 * R, pk(y), 17 * R); v = cmul(y, c256); KG_E(18);
    y = cmul(v, c256); KG_E(20);
    ADDTID2S(bA, pk(v), 18 * R, pk(y), 19 * R); v = cmul(y, c256); KG_E(22);
    const float k2048 = wave_sum(v.x);
    KG_E(24); KG_E(26);
    KG_E(28); KG_E(30);
    const int r32 = lane & 31, h = lane >> 5;
    const char* pa = smem + sA + (r32 < 20 ? r32 : 19) * R + w * 256 + h * 16;
    const char* pb = smem + sB + r32 * R + w * 256 + h * 16;
    f32x16 acc = {};
#pragma unroll
    for (int s = 0; s < 8; ++s) {
        const bf16x8 a = *(const bf16x8*)(pa + 32 * s), bb = *(const bf16x8*)(pb + 32 * s);
        acc = __builtin_amdgcn_mfma_f32_32x32x16_bf16(a, bb, acc, 0, 0, 0);
    }
    const int c = r32;
    char* sl = smem + sA + w * 256;
    char* p0 = sl + (c >> 3) * R + (8 * (c & 7) + 4 * h) * 4;
    char* p1 = sl + (h ? 6 * R + 8 * c : (4 + (c >> 4)) * R + 16 * (c & 15));
    char* p2 = sl + 8 * R + 4 * c;
#pragma unroll
    for (int q = 0; q < 4; ++q) *(float*)(p0 + 4 * q) = acc[q];
    *(float*)(p1) = acc[4]; *(float*)(p1 + 4) = acc[5];
    *(float*)(p1 + (h ? R : 8)) = acc[6]; *(float*)(p1 + (h ? R + 4 : 12)) = acc[7];
    if (h == 0) { *(float*)(p2) = acc[8]; *(float*)(p2 + 128) = acc[9]; *(float*)(p2 + R) = acc[10]; *(float*)(p2 + R + 128) = acc[11]; }
    else if (c == 0) *(float*)(sl + 10 * R) = k2048;
}

DI void ka_expand(char* smem, int sA, int sB, int w, int lane) {
    const int t = 64 * w + lane;
    auto rd = [&](int i) { const char* p = smem + sA + (i >> 6) * KG_PITCH + (i & 63) * 4; return *(const float*)p + *(const float*)(p + 256) + *(const float*)(p + 512); };
    u32 k4, k5, k6, k7, f2, f3;
    if (w == 0) {
        const int i0 = t < 32 ? 8 * t : 256 + 4 * (t - 32);
        const u32 pat = t < 32 ? 0x76543210u : 0x43222100u;
        float kv[8];
#pragma unroll
        for (int j = 0; j < 8; ++j) kv[j] = rd(i0 + ((pat >> (4 * j)) & 15));
        k4 = cvtpk(kv[0], kv[1]); k5 = cvtpk(kv[2], kv[3]); k6 = cvtpk(kv[4], kv[5]); k7 = cvtpk(kv[6], kv[7]);
        f2 = 0; f3 = 0;
    } else if (w == 1) {
        const int i0 = 384 + 2 * (t - 64);
        const float k0 = rd(i0), k1 = rd(i0 + 1), k2 = rd(i0 + 2);
        k4 = cvtpk(k0, k0); k5 = cvtpk(k0, k1); k6 = cvtpk(k1, k1); k7 = cvtpk(k2, k2);
        const float a = rd(382), b = rd(383);
        f2 = cvtpk(a, a); f3 = cvtpk(b, k0);
    } else {
        const int i0 = 512 + (t - 128);
        const float k0 = rd(i0), k1 = rd(i0 + 1);
        k4 = cvtpk(k0, k0); k5 = k4; k6 = cvtpk((t & 1) ? k1 : k0, k1); k7 = cvtpk(k1, k1);
        const float a = w == 2 ? rd(511) : k0;
        f2 = cvtpk(a, a); f3 = cvtpk(k0, k0);
    }
    const u32 k2 = __builtin_amdgcn_update_dpp(f2, k6, 0x138, 0xF, 0xF, false), k3 = __builtin_amdgcn_update_dpp(f3, k7, 0x138, 0xF, 0xF, false);
    const u32 a2 = __builtin_amdgcn_alignbit(k3, k2, 16), a3 = __builtin_amdgcn_alignbit(k4, k3, 16), a4 = __builtin_amdgcn_alignbit(k5, k4, 16),
              a5 = __builtin_amdgcn_alignbit(k6, k5, 16), a6 = __builtin_amdgcn_alignbit(k7, k6, 16);
    char* kb = smem + sB + 32 + 16 * t;
    *(u32x4*)(kb + 0 * CV_CS) = u32x4{k4, k5, k6, k7};
    *(u32x4*)(kb + 1 * CV_CS) = u32x4{a3, a4, a5, a6};
    *(u32x4*)(kb + 2 * CV_CS) = u32x4{k3, k4, k5, k6};
    *(u32x4*)(kb + 3 * CV_CS) = u32x4{a2, a3, a4, a5};
    if (t < 8) *(u32x4*)(smem + sB + (t >> 1) * CV_CS + 16 * (t & 1)) = u32x4{0, 0, 0, 0};
}

DI u32 phase_ssm(const u16* __restrict__ x, const u16* __restrict__ Are, const u16* __restrict__ Aim, const u16* __restrict__ Bre,
                  const u16* __restrict__ Bim, const u16* __restrict__ Cre, const u16* __restrict__ Cim, const u16* __restrict__ logdt,
                  const u16* __restrict__ Dskip, const u16* __restrict__ Wg, const u16* __restrict__ Wu, const u16* __restrict__ Wd,
                  char* __restrict__ ws, char* smem) {
    const int b = blockIdx.x, t = threadIdx.x, lane = t & 63;
    const int wv = __builtin_amdgcn_readfirstlane(t >> 6), sb = wv >> 2, w = wv & 3;
    const int xcd = b & 7, j = b >> 3;
    const int nch = j < 16 ? 3 : 2;
    const int c0 = 80 * xcd + ((xcd & 2) ? (j < 16 ? 32 + 3 * j : 2 * (j - 16)) : (j < 16 ? 3 * j : 48 + 2 * (j - 16)));
    const int xs = min((2 * c0) & ~3, ((2 * c0) | 127) + 1 - 8), e0 = (2 * c0 - xs) >> 1;
    const bool conv = sb < nch;
    const int d = c0 + sb;
    const int sA = KA_A + sb * KA_ASZ, sB = KA_B + sb * KA_BSZ, sU = KA_U + sb * KA_USZ;
    const rsrc_t wsr = mkrsrc(ws, WS_END);
    STAMP(ws, 0, 0);
    if (t < 8) *(u32*)(smem + KA_CNT + 4 * t) = 0u;

    KParams KP;
    u32 ra[4][5];
    u32x4 wv1[8], wv2[8];
    const int tt = t - 512;
    const bool w1job = (j - 16) < 12;
    float dsk = 0.f;
    if (wv == 3 || wv == 7) {
        const u32* x32 = (const u32*)x;
#pragma unroll
        for (int rr = 0; rr < 4; ++rr)
#pragma unroll
            for (int i = 0; i < 5; ++i) ra[rr][i] = x32[(8 * b + 4 * sb + rr) * 320 + lane + 64 * i];
    }
    if (conv) {
        if (w < 3) {
            const int n = d * NM_ + 64 * w + lane;
            KP.are = Are[n]; KP.aim = Aim[n]; KP.bre = Bre[n]; KP.bim = Bim[n]; KP.cre = Cre[n]; KP.cim = Cim[n]; KP.ldt = logdt[d];
        }
        dsk = bf2f(Dskip[d]);
    }
    const u32 epoch = __hip_atomic_load((GAS u32*)(ws + WS_EP), __ATOMIC_RELAXED, __HIP_MEMORY_SCOPE_AGENT);
    const rsrc_t xr0 = mkrsrc((void*)x, L_ * D_ * 2);
    LBAR();
    u32x2 xg[4];
    const int pi0 = 128 * wv + lane, pi1 = pi0 + 64;
    if (wv < 8 && (nch == 3 || !EARLY_W)) {
        xg[0] = __builtin_amdgcn_raw_buffer_load_b64(xr0, (2 * pi0 + 0) * (D_ * 2) + xs, 0, 0);
        xg[1] = __builtin_amdgcn_raw_buffer_load_b64(xr0, (2 * pi0 + 1) * (D_ * 2) + xs, 0, 0);
    }
    if (conv && w == 3) {
        if (lane < 30) *(u32x4*)(smem + sU + 16 * lane) = u32x4{0, 0, 0, 0};
        lds_arrive(smem, sb, lane);
    }
    if (!conv) {
        if (w1job) {
            const int u1 = xcd * 12 + (j - 16), up = u1 >= 48, col = 32 * (up ? u1 - 48 : u1) + 8 * (tt & 3);
            const u16* src = (up ? Wu : Wg) + (8 * (tt >> 2)) * F_ + col;
#pragma unroll
            for (int r = 0; r < 8; ++r) wv1[r] = *(const u32x4*)(src + r * F_);
            if (w == 0) {
#pragma unroll
                for (int r = 0; r < 8; ++r) wv2[r] = *(const u32x4*)(src + (512 + r) * F_);
            }
        } else {
            const int n1 = 480 * (xcd * 4 + (j - 28)) + tt, n2 = n1 + 256;
            const u16* s1 = Wd + (8 * (n1 / 80)) * D_ + 8 * (n1 % 80);
#pragma unroll
            for (int r = 0; r < 8; ++r) wv1[r] = *(const u32x4*)(s1 + r * D_);
            if (tt < 224) {
                const u16* s2 = Wd + (8 * (n2 / 80)) * D_ + 8 * (n2 % 80);
#pragma unroll
                for (int r = 0; r < 8; ++r) wv2[r] = *(const u32x4*)(s2 + r * D_);
            }
        }
    }
    const u32 tag = epoch + 1u;
    if (wv == 3 || wv == 7) {
        __builtin_amdgcn_s_setprio(3);
        float s1[4], s2[4], c0v[4];
#pragma unroll
        for (int rr = 0; rr < 4; ++rr) {
            const float c = bflo(__builtin_amdgcn_readfirstlane(ra[rr][0]));
            float a1 = 0.f, a2 = 0.f;
#pragma unroll
            for (int i = 0; i < 5; ++i) { const float a = bflo(ra[rr][i]) - c, e = bfhi(ra[rr][i]) - c; a1 += a + e; a2 = __builtin_fmaf(a, a, __builtin_fmaf(e, e, a2)); }
            s1[rr] = a1; s2[rr] = a2; c0v[rr] = c;
        }
        auto red4 = [&](const float (&v)[4]) {
            auto p01 = __builtin_amdgcn_permlane32_swap(__float_as_uint(v[0]), __float_as_uint(v[1]), false, false);
            auto p23 = __builtin_amdgcn_permlane32_swap(__float_as_uint(v[2]), __float_as_uint(v[3]), false, false);
            const u32 x0 = p01[0], x1 = p01[1], y0 = p23[0], y1 = p23[1];
            const float t01 = __uint_as_float(x0) + __uint_as_float(x1);
            const float t23 = __uint_as_float(y0) + __uint_as_float(y1);
            auto q = __builtin_amdgcn_permlane16_swap(__float_as_uint(t01), __float_as_uint(t23), false, false);
            const u32 z0 = q[0], z1 = q[1];
            float r = __uint_as_float(z0) + __uint_as_float(z1);
            r = dppadd<0xB1>(r); r = dppadd<0x4E>(r); r = dppadd<0x141>(r); r = dppadd<0x140>(r);
            return r;
        };
        const float n1 = red4(s1) * (1.f / 640.f), n2 = red4(s2) * (1.f / 640.f);
        const int lr = lane >> 4, rr = (lr >> 1) | ((lr & 1) << 1);
        const float cc = rr == 0 ? c0v[0] : rr == 1 ? c0v[1] : rr == 2 ? c0v[2] : c0v[3];
        float var = n2 - n1 * n1; var = var > 0.f ? var : 0.f;
        const float sd = __builtin_sqrtf(rbf(rbf(var) + 1e-5f));
        const u32 pay = cvtpk(cc + n1, sd);
        u32* hand = (u32*)(smem + KA_CNT + 32);
        if (wv == 7) {
            if ((lane & 15) == 0) hand[rr] = pay;
            lds_arrive(smem, 6, lane);
        } else {
            u32 mine = __builtin_amdgcn_ds_bpermute(((lane & 1) << 5 | (lane & 2) << 3) << 2, pay);
            lds_wait(smem, 6, 1);
            if (lane >= 4 && lane < 8) mine = hand[lane - 4];
            if (lane < 8) __hip_atomic_store((GAS u64*)(ws + WS_GR + b * 128) + lane, ((u64)tag << 32) | mine, __ATOMIC_RELAXED, __HIP_MEMORY_SCOPE_AGENT);
            asm volatile("" ::: "memory");
            if (lane == 0) __hip_atomic_fetch_add((LAS u32*)((LAS char*)smem + KA_CNT) + 4, 1u, __ATOMIC_RELAXED, __HIP_MEMORY_SCOPE_WORKGROUP);
        }
        __builtin_amdgcn_s_setprio(0);
    }
    if (conv) {
        if (w < 3) ka_kgen(KP, smem, sA, sB, w, lane);
    } else if (w1job) {
        float* red = (float*)(smem + sA);
#pragma unroll
        for (int i = 0; i < 4; ++i) {
            float a = 0.f, c = 0.f;
#pragma unroll
            for (int r = 0; r < 8; ++r) { a += bflo(wv1[r][i]); c += bfhi(wv1[r][i]); }
            if (w == 0) {
#pragma unroll
                for (int r = 0; r < 8; ++r) { a += bflo(wv2[r][i]); c += bfhi(wv2[r][i]); }
            }
            red[(tt >> 2) * 33 + 8 * (tt & 3) + 2 * i] = a; red[(tt >> 2) * 33 + 8 * (tt & 3) + 2 * i + 1] = c;
        }
    }
    if (!conv) {
        __builtin_amdgcn_s_setprio(2);
        {
            if (w1job) {
            lds_sync(smem, sb, 4, lane);
            const float* red = (const float*)(smem + sA);
            const int col = tt & 31, part = tt >> 5;
            float sm = 0.f;
#pragma unroll
            for (int r = 0; r < 8; ++r) sm += red[(8 * part + r) * 33 + col];
            sm = swap32add(sm);
            if (lane < 32) *(float*)(smem + sB + (col * 4 + w) * 4) = sm;
            lds_sync(smem, sb, 8, lane);
#define WV(r) (rnd ? wv2[r][i] : wv1[r][i])
            }
            char* stg = smem + (w == 3 ? sA + 1024 : sB + 512 + w * 8192);
            u32* dtab = (u32*)(smem + sU + w * 256);
            auto flush = [&](u32 dst, bool active, const u32x4 (&o)[8]) {
                dtab[lane] = active ? dst : 0xffffff00u;
#pragma unroll
                for (int i = 0; i < 8; ++i) *(u32x4*)(stg + lane * 128 + ((i ^ (lane & 7)) * 16)) = o[i];
#pragma unroll
                for (int i = 0; i < 8; ++i) {
                    const int sl = 8 * i + (lane >> 3), pc = lane & 7;
                    const u32x4 v = *(const u32x4*)(stg + sl * 128 + ((pc ^ (sl & 7)) * 16));
                    st16<16>(wsr, dtab[sl] + pc * 16, v);
                }
            };
            if (w1job) {
                const int u1 = xcd * 12 + (j - 16), up = u1 >= 48, f = 32 * (up ? u1 - 48 : u1) + 8 * (tt & 3);
                float cm[8];
#pragma unroll
                for (int i = 0; i < 8; ++i) { const f32x4 v = *(const f32x4*)(smem + sB + (8 * (tt & 3) + i) * 16); cm[i] = (v[0] + v[1] + v[2] + v[3]) * (1.f / 640.f); }
                const int sl = f / 96, fl = f % 96, mt = 2 * (fl >> 4) + up;
#pragma unroll
                for (int rnd = 0; rnd < 2; ++rnd) {
                    if (rnd == 0 || w == 0) {
                        const int kg = (tt >> 2) + 64 * rnd;
                        const u32 dst = WS_W1T + ((sl * 10 + (kg >> 3)) * 24 + ((kg >> 2) & 1) * 12 + mt) * 1024 + ((fl & 15) + 16 * (kg & 3)) * 16;
                        u32x4 o[8];
#pragma unroll
                        for (int i = 0; i < 4; ++i) {
                            const float ca = cm[2 * i], cb = cm[2 * i + 1];
                            o[2 * i] = u32x4{cvtpk(bflo(WV(0)) - ca, bflo(WV(1)) - ca), cvtpk(bflo(WV(2)) - ca, bflo(WV(3)) - ca),
                                             cvtpk(bflo(WV(4)) - ca, bflo(WV(5)) - ca), cvtpk(bflo(WV(6)) - ca, bflo(WV(7)) - ca)};
                            o[2 * i + 1] = u32x4{cvtpk(bfhi(WV(0)) - cb, bfhi(WV(1)) - cb), cvtpk(bfhi(WV(2)) - cb, bfhi(WV(3)) - cb),
                                                 cvtpk(bfhi(WV(4)) - cb, bfhi(WV(5)) - cb), cvtpk(bfhi(WV(6)) - cb, bfhi(WV(7)) - cb)};
                        }
                        flush(dst, true, o);
                    }
                }
            } else {
#pragma unroll
                for (int rnd = 0; rnd < 2; ++rnd) {
                    if (rnd == 0 || w < 3 || true) {
                        const bool act = rnd == 0 || tt < 224;
                        const int n = 480 * (xcd * 4 + (j - 28)) + tt + 256 * rnd, dc = n % 80, kg = n / 80;
                        const int ds = dc / 10, dl = 8 * (dc % 10);
                        const u32 dst = WS_W2T + ((ds * 24 + (kg >> 3)) * 10 + ((kg >> 2) & 1) * 5 + (dl >> 4)) * 1024 + ((dl & 15) + 16 * (kg & 3)) * 16;
                        u32x4 o[8];
#pragma unroll
                        for (int i = 0; i < 4; ++i) {
                            o[2 * i] = u32x4{__builtin_amdgcn_perm(WV(1), WV(0), 0x05040100u), __builtin_amdgcn_perm(WV(3), WV(2), 0x05040100u),
                                             __builtin_amdgcn_perm(WV(5), WV(4), 0x05040100u), __builtin_amdgcn_perm(WV(7), WV(6), 0x05040100u)};
                            o[2 * i + 1] = u32x4{__builtin_amdgcn_perm(WV(1), WV(0), 0x07060302u), __builtin_amdgcn_perm(WV(3), WV(2), 0x07060302u),
                                                 __builtin_amdgcn_perm(WV(5), WV(4), 0x07060302u), __builtin_amdgcn_perm(WV(7), WV(6), 0x07060302u)};
                        }
                        flush(dst, act, o);
                    }
                }
            }
        }
        __builtin_amdgcn_s_setprio(0);
        WAITV(0);
        lds_sync(smem, sb, w1job ? 12 : 4, lane);
        if (t == 512) st_flag(ws, WS_FW + (xcd * 16 + (j - 16)) * 128, tag);
        if (t == 512) STAMP2(ws, 6, 0);
        return tag;
    }
    STAMP(ws, 0, 1);
    u32x4 g0 = {0, 0, 0, 0}, g1 = {0, 0, 0, 0};
    if (wv < 8) {
        lds_wait(smem, 4, 1);
        if (nch == 2 && EARLY_W) {
            xg[0] = __builtin_amdgcn_raw_buffer_load_b64(xr0, (2 * pi0 + 0) * (D_ * 2) + xs, 0, 0);
            xg[1] = __builtin_amdgcn_raw_buffer_load_b64(xr0, (2 * pi0 + 1) * (D_ * 2) + xs, 0, 0);
        }
        xg[2] = __builtin_amdgcn_raw_buffer_load_b64(xr0, (2 * pi1 + 0) * (D_ * 2) + xs, 0, 0);
        xg[3] = __builtin_amdgcn_raw_buffer_load_b64(xr0, (2 * pi1 + 1) * (D_ * 2) + xs, 0, 0);
        g0 = __builtin_amdgcn_raw_buffer_load_b128(wsr, WS_GR + (pi0 >> 2) * 128 + (pi0 & 3) * 16, 0, 16);
        g1 = __builtin_amdgcn_raw_buffer_load_b128(wsr, WS_GR + (pi1 >> 2) * 128 + (pi1 & 3) * 16, 0, 16);
    }
    if (w < 3) lds_sync(smem, sb, 4, lane); else lds_wait(smem, sb, 4);
    STAMP(ws, 0, 2);
    ka_expand(smem, sA, sB, w, lane);
    if (wv < 8) {
        for (int spin = 0; spin < 100000; ++spin) {
            if (__all(g0[1] == tag && g0[3] == tag && g1[1] == tag && g1[3] == tag)) break;
            __builtin_amdgcn_s_sleep(4);
            asm volatile("" ::: "memory");
            g0 = __builtin_amdgcn_raw_buffer_load_b128(wsr, WS_GR + (pi0 >> 2) * 128 + (pi0 & 3) * 16, 0, 16);
            g1 = __builtin_amdgcn_raw_buffer_load_b128(wsr, WS_GR + (pi1 >> 2) * 128 + (pi1 & 3) * 16, 0, 16);
        }
        const u32 pr[4] = {g0[0], g0[2], g1[0], g1[2]};
        float m[4], rs[4];
#pragma unroll
        for (int r = 0; r < 4; ++r) { m[r] = bflo(pr[r]); rs[r] = __builtin_amdgcn_rcpf(bfhi(pr[r])); }
#pragma unroll
        for (int s = 0; s < 3; ++s) {
            if (s < nch) {
                const int e = e0 + s;
                float xv[4];
#pragma unroll
                for (int r = 0; r < 4; ++r) { const u32 dw = e < 2 ? xg[r][0] : xg[r][1]; xv[r] = (e & 1) ? bfhi(dw) : bflo(dw); }
                const u32 q01 = cvtpk(xv[0] - m[0], xv[1] - m[1]), q23 = cvtpk(xv[2] - m[2], xv[3] - m[3]);
                const u32 u01 = cvtpk(bflo(q01) * rs[0], bfhi(q01) * rs[1]), u23 = cvtpk(bflo(q23) * rs[2], bfhi(q23) * rs[3]);
                char* ub = smem + KA_U + s * KA_USZ;
                *(u32*)(ub + 480 + 4 * pi0) = u01; *(u32*)(ub + 480 + 4 * pi1) = u23;
                *(u32*)(ub + 4608 + 4 * pi0) = q01; *(u32*)(ub + 4608 + 4 * pi1) = q23;
            }
        }
    }
    STAMP(ws, 0, 3);
    asm volatile("" :: "v"(tag));
    lds_sync(smem, 3, 4 * nch, lane);
    STAMP(ws, 0, 4);
    const int al = lane & 15, g = lane >> 4;
    f32x4 acc[8];
#pragma unroll
    for (int i = 0; i < 8; ++i) acc[i] = f32x4{0.f, 0.f, 0.f, 0.f};
    {
        const LAS char* lds = (const LAS char*)smem;
        const int q = (lane & 15) >> 2, p = lane & 3;
        const LAS char* ub0 = lds + sU + 448 + 32 * al + 16 * g - 128 * w;
        const LAS char* ub1 = ub0 - 64;
        const LAS char* kb0 = lds + sB + q * CV_CS + 56 - 16 * g + 8 * p + 128 * w;
        const LAS char* kb1 = kb0 + 64;
        bf16x8 U0[8], U1[8]; s16x4 A0[8][2], A1[8][2];
        A0[0][0] = trread(kb0 + 8); A0[0][1] = trread(kb0 + 0); U0[0] = *(const LAS bf16x8*)(ub0 + 0); U0[1] = *(const LAS bf16x8*)(ub0 + 512); U0[2] = *(const LAS bf16x8*)(ub0 + 1024); U0[3] = *(const LAS bf16x8*)(ub0 + 1536); U0[4] = *(const LAS bf16x8*)(ub0 + 2048); U0[5] = *(const LAS bf16x8*)(ub0 + 2560); U0[6] = *(const LAS bf16x8*)(ub0 + 3072); U0[7] = *(const LAS bf16x8*)(ub0 + 3584); A0[1][0] = trread(kb0 + 520); A0[1][1] = trread(kb0 + 512); A0[2][0] = trread(kb0 + 1032); A0[2][1] = trread(kb0 + 1024); A0[3][0] = trread(kb0 + 1544); A0[3][1] = trread(kb0 + 1536); A0[4][0] = trread(kb0 + 2056); A0[4][1] = trread(kb0 + 2048); A0[5][0] = trread(kb0 + 2568); A0[5][1] = trread(kb0 + 2560); A0[6][0] = trread(kb0 + 3080); A0[6][1] = trread(kb0 + 3072); A0[7][0] = trread(kb0 + 3592); A0[7][1] = trread(kb0 + 3584);
        SCHEDB();
        acc[0] = MFMA16(cat8(A0[0][0], A0[0][1]), U0[0], acc[0]);
        SCHEDB();
        acc[1] = MFMA16(cat8(A0[0][0], A0[0][1]), U0[1], acc[1]);
        SCHEDB();
        acc[2] = MFMA16(cat8(A0[0][0], A0[0][1]), U0[2], acc[2]);
        SCHEDB();
        acc[3] = MFMA16(cat8(A0[0][0], A0[0][1]), U0[3], acc[3]);
        SCHEDB();
        acc[4] = MFMA16(cat8(A0[0][0], A0[0][1]), U0[4], acc[4]);
        SCHEDB();
        acc[5] = MFMA16(cat8(A0[0][0], A0[0][1]), U0[5], acc[5]);
        SCHEDB();
        acc[6] = MFMA16(cat8(A0[0][0], A0[0][1]), U0[6], acc[6]);
        SCHEDB();
        acc[7] = MFMA16(cat8(A0[0][0], A0[0][1]), U0[7], acc[7]);
        SCHEDB();
        acc[1] = MFMA16(cat8(A0[1][0], A0[1][1]), U0[0], acc[1]);
        A1[0][0] = trread(kb1 + 8);
        SCHEDB();
        acc[2] = MFMA16(cat8(A0[1][0], A0[1][1]), U0[1], acc[2]);
        A1[0][1] = trread(kb1 + 0);
        SCHEDB();
        acc[3] = MFMA16(cat8(A0[1][0], A0[1][1]), U0[2], acc[3]);
        U1[0] = *(const LAS bf16x8*)(ub1 + 0);
        SCHEDB();
        acc[4] = MFMA16(cat8(A0[1][0], A0[1][1]), U0[3], acc[4]);
        U1[1] = *(const LAS bf16x8*)(ub1 + 512);
        SCHEDB();
        acc[5] = MFMA16(cat8(A0[1][0], A0[1][1]), U0[4], acc[5]);
        U1[2] = *(const LAS bf16x8*)(ub1 + 1024);
        SCHEDB();
        acc[6] = MFMA16(cat8(A0[1][0], A0[1][1]), U0[5], acc[6]);
        U1[3] = *(const LAS bf16x8*)(ub1 + 1536);
        SCHEDB();
        acc[7] = MFMA16(cat8(A0[1][0], A0[1][1]), U0[6], acc[7]);
        U1[4] = *(const LAS bf16x8*)(ub1 + 2048);
        SCHEDB();
        acc[2] = MFMA16(cat8(A0[2][0], A0[2][1]), U0[0], acc[2]);
        U1[5] = *(const LAS bf16x8*)(ub1 + 2560);
        SCHEDB();
        acc[3] = MFMA16(cat8(A0[2][0], A0[2][1]), U0[1], acc[3]);
        U1[6] = *(const LAS bf16x8*)(ub1 + 3072);
        SCHEDB();
        acc[4] = MFMA16(cat8(A0[2][0], A0[2][1]), U0[2], acc[4]);
        U1[7] = *(const LAS bf16x8*)(ub1 + 3584);
        SCHEDB();
        acc[5] = MFMA16(cat8(A0[2][0], A0[2][1]), U0[3], acc[5]);
        A1[1][0] = trread(kb1 + 520);
        SCHEDB();
        acc[6] = MFMA16(cat8(A0[2][0], A0[2][1]), U0[4], acc[6]);
        A1[1][1] = trread(kb1 + 512);
        SCHEDB();
        acc[7] = MFMA16(cat8(A0[2][0], A0[2][1]), U0[5], acc[7]);
        A1[2][0] = trread(kb1 + 1032);
        SCHEDB();
        acc[3] = MFMA16(cat8(A0[3][0], A0[3][1]), U0[0], acc[3]);
        A1[2][1] = trread(kb1 + 1024);
        SCHEDB();
        acc[4] = MFMA16(cat8(A0[3][0], A0[3][1]), U0[1], acc[4]);
        A1[3][0] = trread(kb1 + 1544);
        SCHEDB();
        acc[5] = MFMA16(cat8(A0[3][0], A0[3][1]), U0[2], acc[5]);
        A1[3][1] = trread(kb1 + 1536);
        SCHEDB();
        acc[6] = MFMA16(cat8(A0[3][0], A0[3][1]), U0[3], acc[6]);
        A1[4][0] = trread(kb1 + 2056);
        SCHEDB();
        acc[7] = MFMA16(cat8(A0[3][0], A0[3][1]), U0[4], acc[7]);
        A1[4][1] = trread(kb1 + 2048);
        SCHEDB();
        acc[4] = MFMA16(cat8(A0[4][0], A0[4][1]), U0[0], acc[4]);
        A1[5][0] = trread(kb1 + 2568);
        SCHEDB();
        acc[5] = MFMA16(cat8(A0[4][0], A0[4][1]), U0[1], acc[5]);
        A1[5][1] = trread(kb1 + 2560);
        SCHEDB();
        acc[6] = MFMA16(cat8(A0[4][0], A0[4][1]), U0[2], acc[6]);
        A1[6][0] = trread(kb1 + 3080);
        SCHEDB();
        acc[7] = MFMA16(cat8(A0[4][0], A0[4][1]), U0[3], acc[7]);
        A1[6][1] = trread(kb1 + 3072);
        SCHEDB();
        acc[5] = MFMA16(cat8(A0[5][0], A0[5][1]), U0[0], acc[5]);
        A1[7][0] = trread(kb1 + 3592);
        SCHEDB();
        acc[6] = MFMA16(cat8(A0[5][0], A0[5][1]), U0[1], acc[6]);
        A1[7][1] = trread(kb1 + 3584);
        SCHEDB();
        acc[7] = MFMA16(cat8(A0[5][0], A0[5][1]), U0[2], acc[7]);
        SCHEDB();
        acc[6] = MFMA16(cat8(A0[6][0], A0[6][1]), U0[0], acc[6]);
        SCHEDB();
        acc[7] = MFMA16(cat8(A0[6][0], A0[6][1]), U0[1], acc[7]);
        SCHEDB();
        acc[7] = MFMA16(cat8(A0[7][0], A0[7][1]), U0[0], acc[7]);
        SCHEDB();
        acc[0] = MFMA16(cat8(A1[0][0], A1[0][1]), U1[0], acc[0]); SCHEDB();
        acc[1] = MFMA16(cat8(A1[0][0], A1[0][1]), U1[1], acc[1]); SCHEDB();
        acc[2] = MFMA16(cat8(A1[0][0], A1[0][1]), U1[2], acc[2]); SCHEDB();
        acc[3] = MFMA16(cat8(A1[0][0], A1[0][1]), U1[3], acc[3]); SCHEDB();
        acc[4] = MFMA16(cat8(A1[0][0], A1[0][1]), U1[4], acc[4]); SCHEDB();
        acc[5] = MFMA16(cat8(A1[0][0], A1[0][1]), U1[5], acc[5]); SCHEDB();
        acc[6] = MFMA16(cat8(A1[0][0], A1[0][1]), U1[6], acc[6]); SCHEDB();
        acc[7] = MFMA16(cat8(A1[0][0], A1[0][1]), U1[7], acc[7]); SCHEDB();
        acc[1] = MFMA16(cat8(A1[1][0], A1[1][1]), U1[0], acc[1]); SCHEDB();
        acc[2] = MFMA16(cat8(A1[1][0], A1[1][1]), U1[1], acc[2]); SCHEDB();
        acc[3] = MFMA16(cat8(A1[1][0], A1[1][1]), U1[2], acc[3]); SCHEDB();
        acc[4] = MFMA16(cat8(A1[1][0], A1[1][1]), U1[3], acc[4]); SCHEDB();
        acc[5] = MFMA16(cat8(A1[1][0], A1[1][1]), U1[4], acc[5]); SCHEDB();
        acc[6] = MFMA16(cat8(A1[1][0], A1[1][1]), U1[5], acc[6]); SCHEDB();
        acc[7] = MFMA16(cat8(A1[1][0], A1[1][1]), U1[6], acc[7]); SCHEDB();
        acc[2] = MFMA16(cat8(A1[2][0], A1[2][1]), U1[0], acc[2]); SCHEDB();
        acc[3] = MFMA16(cat8(A1[2][0], A1[2][1]), U1[1], acc[3]); SCHEDB();
        acc[4] = MFMA16(cat8(A1[2][0], A1[2][1]), U1[2], acc[4]); SCHEDB();
        acc[5] = MFMA16(cat8(A1[2][0], A1[2][1]), U1[3], acc[5]); SCHEDB();
        acc[6] = MFMA16(cat8(A1[2][0], A1[2][1]), U1[4], acc[6]); SCHEDB();
        acc[7] = MFMA16(cat8(A1[2][0], A1[2][1]), U1[5], acc[7]); SCHEDB();
        acc[3] = MFMA16(cat8(A1[3][0], A1[3][1]), U1[0], acc[3]); SCHEDB();
        acc[4] = MFMA16(cat8(A1[3][0], A1[3][1]), U1[1], acc[4]); SCHEDB();
        acc[5] = MFMA16(cat8(A1[3][0], A1[3][1]), U1[2], acc[5]); SCHEDB();
        acc[6] = MFMA16(cat8(A1[3][0], A1[3][1]), U1[3], acc[6]); SCHEDB();
        acc[7] = MFMA16(cat8(A1[3][0], A1[3][1]), U1[4], acc[7]); SCHEDB();
        acc[4] = MFMA16(cat8(A1[4][0], A1[4][1]), U1[0], acc[4]); SCHEDB();
        acc[5] = MFMA16(cat8(A1[4][0], A1[4][1]), U1[1], acc[5]); SCHEDB();
        acc[6] = MFMA16(cat8(A1[4][0], A1[4][1]), U1[2], acc[6]); SCHEDB();
        acc[7] = MFMA16(cat8(A1[4][0], A1[4][1]), U1[3], acc[7]); SCHEDB();
        acc[5] = MFMA16(cat8(A1[5][0], A1[5][1]), U1[0], acc[5]); SCHEDB();
        acc[6] = MFMA16(cat8(A1[5][0], A1[5][1]), U1[1], acc[6]); SCHEDB();
        acc[7] = MFMA16(cat8(A1[5][0], A1[5][1]), U1[2], acc[7]); SCHEDB();
        acc[6] = MFMA16(cat8(A1[6][0], A1[6][1]), U1[0], acc[6]); SCHEDB();
        acc[7] = MFMA16(cat8(A1[6][0], A1[6][1]), U1[1], acc[7]); SCHEDB();
        acc[7] = MFMA16(cat8(A1[7][0], A1[7][1]), U1[0], acc[7]); SCHEDB();
    }
    STAMP(ws, 0, 5);
    lds_sync(smem, sb, 8, lane);
    {
        char* rb = smem + sB + w * 6144 + lane * 16;
        int slot = 0;
#pragma unroll
        for (int tau = 0; tau < 8; ++tau) { if ((tau >> 1) != w) { *(f32x4*)(rb + slot * 1024) = acc[tau]; ++slot; } }
    }
    lds_sync(smem, sb, 12, lane);
    {
#pragma unroll
        for (int i = 0; i < 2; ++i) {
            f32x4 y;
            if (w == 0) y = acc[i]; else if (w == 1) y = acc[2 + i]; else if (w == 2) y = acc[4 + i]; else y = acc[6 + i];
            const int tau = 2 * w + i;
#pragma unroll
            for (int ow = 0; ow < 4; ++ow) {
                if (ow != w) {
                    const int sl = (tau < 2 * ow) ? tau : tau - 2;
                    y += *(const f32x4*)(smem + sB + ow * 6144 + sl * 1024 + lane * 16);
                }
            }
            const int l0 = 256 * tau + 16 * al + 4 * g;
            const u32x2 uv = *(const u32x2*)(smem + sU + 480 + 2 * l0), qv = *(const u32x2*)(smem + sU + 4608 + 2 * l0);
            float hv[4];
#pragma unroll
            for (int r = 0; r < 4; ++r) {
                const u32 uu = uv[r >> 1], qq = qv[r >> 1];
                const float u = (r & 1) ? bfhi(uu) : bflo(uu), q0 = (r & 1) ? bfhi(qq) : bflo(qq);
                const u32 yk = cvtpk(y[r], u * dsk);
                hv[r] = q0 + rbf(bflo(yk) + bfhi(yk));
            }
            st8<16>(wsr, WS_HC + (d * L_ + l0) * 2, u32x2{cvtpk(hv[0], hv[1]), cvtpk(hv[2], hv[3])});
        }
    }
    STAMP(ws, 0, 6);
    return tag;
}

#define G1_UNROLL 1
constexpr int G1_STAGE = 40960, G1_NS = 4, G1_NST = 10;
constexpr int G1_LDS = G1_NS * G1_STAGE;

DI float phase_gemm1(char* __restrict__ ws, char* smem, u32 tag) {
    const int b = blockIdx.x, t = threadIdx.x, lane = t & 63;
    const int w = __builtin_amdgcn_readfirstlane(t >> 6);
    const int xcd = b & 7, j = b >> 3;
    const int P = (xcd & 1) * 8 + (j & 7), S = (xcd >> 1) * 4 + (j >> 3);
    const u32 lds0 = (u32)(uintptr_t)smem;
    const rsrc_t wsr = mkrsrc(ws, WS_END);
    WAITV(0);
    BAR();
    if (t == 0) st_flag(ws, WS_F2 + b * 128, tag);
    if (w == 8 || w == 9) poll_flags(wsr, WS_FW + (w - 8) * 64 * 128, 64, tag, lane);
    BAR();
    if (w >= 4) {
        const int lw = w - 4;
        const char* wsrc = ws + WS_W1T + S * 10 * 24576 + lw * 1024 + lane * 16;
        const int hk = 16 * ((lw >> 2) & 1) + 4 * ((lw >> 1) & 1) + 8 * ((lane >> 3) & 1) + ((lane >> 1) & 3);
        const char* hsrc = ws + WS_HC + (hk * L_ + 128 * P + 64 * (lw & 1) + 16 * (lane >> 4) + 8 * (lane & 1)) * 2;
        auto issue_h = [&](int st, int slot) {
#pragma unroll
            for (int i = 0; i < 2; ++i) glds16(hsrc + (st * 64 + 32 * i) * L_ * 2, lds0 + slot * G1_STAGE + 24576 + (lw + 8 * i) * 1024);
        };
        auto issue_w = [&](int st, int slot) {
#pragma unroll
            for (int i = 0; i < 3; ++i) glds16(wsrc + st * 24576 + i * 8192, lds0 + slot * G1_STAGE + lw * 1024 + i * 8192);
        };
        issue_w(0, 0); issue_w(1, 1); issue_w(2, 2); issue_w(3, 3);
        if (lw < 4) poll_flags(wsr, WS_F2 + lw * 64 * 128, 64, tag, lane);
        BAR();
        if (b == 0 && t == 256) st_flag(ws, WS_EP, tag);
        issue_h(0, 0); issue_h(1, 1); issue_h(2, 2); issue_h(3, 3);
        waitv<6>(); BAR();
        waitv<4>(); BAR(); issue_h(4, 0); issue_w(4, 0);
        waitv<7>(); BAR(); issue_h(5, 1); issue_w(5, 1);
        waitv<10>(); BAR(); issue_h(6, 2); issue_w(6, 2);
        waitv<10>(); BAR(); issue_h(7, 3); issue_w(7, 3);
        waitv<10>(); BAR(); issue_h(8, 0); issue_w(8, 0);
        waitv<10>(); BAR(); issue_h(9, 1); issue_w(9, 1);
        waitv<10>(); BAR();
        waitv<5>(); BAR();
        waitv<0>(); BAR();
        BAR();
        BAR();
        if (lw == 0) {
            const int r = lane & 7, part = lane >> 3;
            float s1 = 0.f, s2 = 0.f;
#pragma unroll
            for (int i = 0; i < 8; ++i) {
                const int e = 8 * part + i;
                const f32x4 v = *(const f32x4*)(smem + ((e >> 4) * 64 + (e & 15) * 4 + (r >> 1)) * 16);
                s1 += (r & 1) ? v[1] : v[0]; s2 += (r & 1) ? v[3] : v[2];
            }
            s1 = dppadd<0x128>(s1); s2 = dppadd<0x128>(s2);
            s1 = swap32add(swap16add(s1)); s2 = swap32add(swap16add(s2));
            const float mean = s1 * (1.f / 640.f);
            float var = s2 * (1.f / 640.f) - mean * mean;
            var = var > 0.f ? var : 0.f;
            const float rs = 1.f / rbf(__builtin_sqrtf(rbf(rbf(var) + 1e-5f)));
            return rs * rs;
        }
        return 0.f;
    }
    BAR();
    STAMP(ws, 4, 0);
    const int wm = w & 1, wn = w >> 1;
    const int g = lane >> 4, q = (lane & 15) >> 2, p = lane & 3;
    f32x4 acc[6][4];
#pragma unroll
    for (int a = 0; a < 6; ++a)
#pragma unroll
        for (int c = 0; c < 4; ++c) acc[a][c] = f32x4{0.f, 0.f, 0.f, 0.f};
    const LAS char* abase = (const LAS char*)smem + wm * 6144 + lane * 16;
    const LAS char* bbase = (const LAS char*)smem + 24576 + (g >> 1) * 4096 + (g & 1) * 128 + q * 32 + 8 * p + wn * 1024;
    const LAS char* sbase = (const LAS char*)smem + 24576 + (2 * w + (lane >> 5)) * 2048 + (S >> 1) * 256 + ((lane >> 2) & 7) * 32 + (S & 1) * 16 + (lane & 3) * 4;
    float s1a = 0.f, s1b = 0.f, s2a = 0.f, s2b = 0.f;
    bf16x8 AL[3], AH[3]; s16x4 B0[4][2], B1[4][2];
    u32 sv;
    BAR();
    STAMP(ws, 4, 1);
    AL[0] = *(const LAS bf16x8*)(abase + 0); B0[0][0] = trread(bbase + 0); B0[0][1] = trread(bbase + 2048); B0[1][0] = trread(bbase + 256); B0[1][1] = trread(bbase + 2304); AL[1] = *(const LAS bf16x8*)(abase + 1024); B0[2][0] = trread(bbase + 512); B0[2][1] = trread(bbase + 2560); B0[3][0] = trread(bbase + 768); B0[3][1] = trread(bbase + 2816); AL[2] = *(const LAS bf16x8*)(abase + 2048);
    SCHEDB();
#pragma unroll 1
    for (int st = 0; st < G1_NST; ++st) {
        acc[0+0][0] = MFMA16(AL[0], cat8(B0[0][0], B0[0][1]), acc[0+0][0]);
        AH[0] = *(const LAS bf16x8*)(abase + 3072);
        SCHEDB();
        acc[0+0][1] = MFMA16(AL[0], cat8(B0[1][0], B0[1][1]), acc[0+0][1]);
        AH[1] = *(const LAS bf16x8*)(abase + 4096);
        SCHEDB();
        acc[0+1][0] = MFMA16(AL[1], cat8(B0[0][0], B0[0][1]), acc[0+1][0]);
        B1[0][0] = trread(bbase + 8192);
        SCHEDB();
        acc[0+1][1] = MFMA16(AL[1], cat8(B0[1][0], B0[1][1]), acc[0+1][1]);
        B1[0][1] = trread(bbase + 10240);
        SCHEDB();
        acc[0+0][2] = MFMA16(AL[0], cat8(B0[2][0], B0[2][1]), acc[0+0][2]);
        B1[1][0] = trread(bbase + 8448);
        SCHEDB();
        acc[0+1][2] = MFMA16(AL[1], cat8(B0[2][0], B0[2][1]), acc[0+1][2]);
        B1[1][1] = trread(bbase + 10496);
        SCHEDB();
        acc[0+0][3] = MFMA16(AL[0], cat8(B0[3][0], B0[3][1]), acc[0+0][3]);
        AH[2] = *(const LAS bf16x8*)(abase + 5120);
        SCHEDB();
        acc[0+1][3] = MFMA16(AL[1], cat8(B0[3][0], B0[3][1]), acc[0+1][3]);
        SCHEDB();
        acc[0+2][0] = MFMA16(AL[2], cat8(B0[0][0], B0[0][1]), acc[0+2][0]);
        SCHEDB();
        acc[0+2][1] = MFMA16(AL[2], cat8(B0[1][0], B0[1][1]), acc[0+2][1]);
        SCHEDB();
        acc[0+2][2] = MFMA16(AL[2], cat8(B0[2][0], B0[2][1]), acc[0+2][2]);
        SCHEDB();
        acc[0+2][3] = MFMA16(AL[2], cat8(B0[3][0], B0[3][1]), acc[0+2][3]);
        SCHEDB();
        acc[3+0][0] = MFMA16(AH[0], cat8(B0[0][0], B0[0][1]), acc[3+0][0]);
        AL[0] = *(const LAS bf16x8*)(abase + 12288);
        SCHEDB();
        acc[3+0][1] = MFMA16(AH[0], cat8(B0[1][0], B0[1][1]), acc[3+0][1]);
        AL[1] = *(const LAS bf16x8*)(abase + 13312);
        SCHEDB();
        acc[3+1][0] = MFMA16(AH[1], cat8(B0[0][0], B0[0][1]), acc[3+1][0]);
        sv = *(const LAS u32*)(sbase);
        SCHEDB();
        acc[3+1][1] = MFMA16(AH[1], cat8(B0[1][0], B0[1][1]), acc[3+1][1]);
        B1[2][0] = trread(bbase + 8704);
        SCHEDB();
        acc[3+0][2] = MFMA16(AH[0], cat8(B0[2][0], B0[2][1]), acc[3+0][2]);
        B1[2][1] = trread(bbase + 10752);
        SCHEDB();
        acc[3+1][2] = MFMA16(AH[1], cat8(B0[2][0], B0[2][1]), acc[3+1][2]);
        B1[3][0] = trread(bbase + 8960);
        SCHEDB();
        acc[3+0][3] = MFMA16(AH[0], cat8(B0[3][0], B0[3][1]), acc[3+0][3]);
        B1[3][1] = trread(bbase + 11008);
        SCHEDB();
        acc[3+1][3] = MFMA16(AH[1], cat8(B0[3][0], B0[3][1]), acc[3+1][3]);
        AL[2] = *(const LAS bf16x8*)(abase + 14336);
        SCHEDB();
        acc[3+2][0] = MFMA16(AH[2], cat8(B0[0][0], B0[0][1]), acc[3+2][0]);
        SCHEDB();
        acc[3+2][1] = MFMA16(AH[2], cat8(B0[1][0], B0[1][1]), acc[3+2][1]);
        SCHEDB();
        acc[3+2][2] = MFMA16(AH[2], cat8(B0[2][0], B0[2][1]), acc[3+2][2]);
        SCHEDB();
        acc[3+2][3] = MFMA16(AH[2], cat8(B0[3][0], B0[3][1]), acc[3+2][3]);
        SCHEDB();
        acc[0+0][0] = MFMA16(AL[0], cat8(B1[0][0], B1[0][1]), acc[0+0][0]);
        AH[0] = *(const LAS bf16x8*)(abase + 15360);
        SCHEDB();
        acc[0+0][1] = MFMA16(AL[0], cat8(B1[1][0], B1[1][1]), acc[0+0][1]);
        AH[1] = *(const LAS bf16x8*)(abase + 16384);
        SCHEDB();
        acc[0+1][0] = MFMA16(AL[1], cat8(B1[0][0], B1[0][1]), acc[0+1][0]);
        AH[2] = *(const LAS bf16x8*)(abase + 17408);
        SCHEDB();
        acc[0+1][1] = MFMA16(AL[1], cat8(B1[1][0], B1[1][1]), acc[0+1][1]);
        SCHEDB();
        acc[0+0][2] = MFMA16(AL[0], cat8(B1[2][0], B1[2][1]), acc[0+0][2]);
        SCHEDB();
        acc[0+1][2] = MFMA16(AL[1], cat8(B1[2][0], B1[2][1]), acc[0+1][2]);
        SCHEDB();
        acc[0+0][3] = MFMA16(AL[0], cat8(B1[3][0], B1[3][1]), acc[0+0][3]);
        SCHEDB();
        acc[0+1][3] = MFMA16(AL[1], cat8(B1[3][0], B1[3][1]), acc[0+1][3]);
        SCHEDB();
        acc[0+2][0] = MFMA16(AL[2], cat8(B1[0][0], B1[0][1]), acc[0+2][0]);
        SCHEDB();
        acc[0+2][1] = MFMA16(AL[2], cat8(B1[1][0], B1[1][1]), acc[0+2][1]);
        SCHEDB();
        acc[0+2][2] = MFMA16(AL[2], cat8(B1[2][0], B1[2][1]), acc[0+2][2]);
        SCHEDB();
        acc[0+2][3] = MFMA16(AL[2], cat8(B1[3][0], B1[3][1]), acc[0+2][3]);
        SCHEDB();
        { const float a = bflo(sv), c = bfhi(sv); s1a += a; s1b += c; s2a = __builtin_fmaf(a, a, s2a); s2b = __builtin_fmaf(c, c, s2b); }
        WAITL0();
        BAR();
        { const int dlt = (st & 3) == 3 ? -3 * G1_STAGE : G1_STAGE; abase += dlt; bbase += dlt; sbase += dlt; }
        acc[3+0][0] = MFMA16(AH[0], cat8(B1[0][0], B1[0][1]), acc[3+0][0]);
        AL[0] = *(const LAS bf16x8*)(abase + 0);
        SCHEDB();
        acc[3+0][1] = MFMA16(AH[0], cat8(B1[1][0], B1[1][1]), acc[3+0][1]);
        B0[0][0] = trread(bbase + 0);
        SCHEDB();
        acc[3+1][0] = MFMA16(AH[1], cat8(B1[0][0], B1[0][1]), acc[3+1][0]);
        B0[0][1] = trread(bbase + 2048);
        SCHEDB();
        acc[3+1][1] = MFMA16(AH[1], cat8(B1[1][0], B1[1][1]), acc[3+1][1]);
        B0[1][0] = trread(bbase + 256);
        SCHEDB();
        acc[3+0][2] = MFMA16(AH[0], cat8(B1[2][0], B1[2][1]), acc[3+0][2]);
        B0[1][1] = trread(bbase + 2304);
        SCHEDB();
        acc[3+1][2] = MFMA16(AH[1], cat8(B1[2][0], B1[2][1]), acc[3+1][2]);
        AL[1] = *(const LAS bf16x8*)(abase + 1024);
        SCHEDB();
        acc[3+0][3] = MFMA16(AH[0], cat8(B1[3][0], B1[3][1]), acc[3+0][3]);
        B0[2][0] = trread(bbase + 512);
        SCHEDB();
        acc[3+1][3] = MFMA16(AH[1], cat8(B1[3][0], B1[3][1]), acc[3+1][3]);
        B0[2][1] = trread(bbase + 2560);
        SCHEDB();
        acc[3+2][0] = MFMA16(AH[2], cat8(B1[0][0], B1[0][1]), acc[3+2][0]);
        B0[3][0] = trread(bbase + 768);
        SCHEDB();
        acc[3+2][1] = MFMA16(AH[2], cat8(B1[1][0], B1[1][1]), acc[3+2][1]);
        B0[3][1] = trread(bbase + 2816);
        SCHEDB();
        acc[3+2][2] = MFMA16(AH[2], cat8(B1[2][0], B1[2][1]), acc[3+2][2]);
        AL[2] = *(const LAS bf16x8*)(abase + 2048);
        SCHEDB();
        acc[3+2][3] = MFMA16(AH[2], cat8(B1[3][0], B1[3][1]), acc[3+2][3]);
        SCHEDB();
    }
    STAMP(ws, 4, 3);
    *(f32x4*)(smem + (w * 64 + lane) * 16) = f32x4{s1a, s1b, s2a, s2b};
    WAITL0();
    BAR();
    const rsrc_t rs = wsr;
#pragma unroll
    for (int a = 0; a < 3; ++a) {
        const int f0 = 96 * S + 16 * (3 * wm + a);
        const int kb = f0 >> 5, oct = ((f0 & 31) >> 3) + (g >> 1);
#pragma unroll
        for (int c = 0; c < 4; c += 2) {
            u32 X[2], Y[2];
#pragma unroll
            for (int e = 0; e < 2; ++e) {
                X[e] = cvtpk(__builtin_fmaxf(acc[2 * a][c][2 * e], 0.f) * acc[2 * a + 1][c][2 * e], __builtin_fmaxf(acc[2 * a][c][2 * e + 1], 0.f) * acc[2 * a + 1][c][2 * e + 1]);
                Y[e] = cvtpk(__builtin_fmaxf(acc[2 * a][c + 1][2 * e], 0.f) * acc[2 * a + 1][c + 1][2 * e], __builtin_fmaxf(acc[2 * a][c + 1][2 * e + 1], 0.f) * acc[2 * a + 1][c + 1][2 * e + 1]);
            }
#pragma unroll
            for (int e = 0; e < 2; ++e) { auto r = __builtin_amdgcn_permlane16_swap(X[e], Y[e], false, false); X[e] = r[0]; Y[e] = r[1]; }
            const int lt = 8 * P + 4 * wn + c + (g & 1);
            st16<16>(rs, WS_HM + ((lt * 48 + kb) * 64 + (lane & 15) + 16 * oct) * 16, u32x4{X[0], X[1], Y[0], Y[1]});
        }
    }
    STAMP(ws, 4, 4);
    return 0.f;
}

constexpr int G2_STAGE = 18432, G2_NS = 8, G2_NST = 24;
constexpr int G2_RES = G2_NS * G2_STAGE;
constexpr int G2_LDS = G2_RES + 10240;

DI void phase_gemm2(char* __restrict__ ws, u16* __restrict__ out, char* smem, u32 tag, float rs2pub) {
    const int b = blockIdx.x, t = threadIdx.x, lane = t & 63;
    const int w = __builtin_amdgcn_readfirstlane(t >> 6);
    const int xcd = b & 7, j = b >> 3;
    const int P = (xcd & 3) * 8 + (j & 7), S = (xcd >> 2) * 4 + (j >> 3);
    const u32 lds0 = (u32)(uintptr_t)smem;
    const rsrc_t wsr = mkrsrc(ws, WS_END);
    WAITV(0);
    BAR();
    if (w == 4) {
        const int P1 = (xcd & 1) * 8 + (j & 7), S1 = (xcd >> 1) * 4 + (j >> 3);
        const u32 up1 = __builtin_amdgcn_update_dpp(0, __float_as_uint(rs2pub), 0x111, 0xF, 0xF, true);
        if (lane < 9) st4<16>(wsr, WS_F3 + (P1 * 16 + S1) * 128 + lane * 4, lane == 0 ? tag : up1);
    }
    if (w >= 4) {
        const int lw = w - 4;
        const char* wsrc = ws + WS_W2T + S * 24 * 10240 + lw * 1024 + lane * 16;
        const char* msrc = ws + WS_HM + ((4 * P + (lw >> 1)) * 48 + (lw & 1)) * 1024 + lane * 16;
        auto issue_m = [&](int st, int slot) { glds16(msrc + st * 2048, lds0 + slot * G2_STAGE + 10240 + lw * 1024); };
        auto issue_w = [&](int st, int slot) {
            glds16(wsrc + st * 10240, lds0 + slot * G2_STAGE + lw * 1024);
            if (lw < 2) glds16(wsrc + st * 10240 + 8192, lds0 + slot * G2_STAGE + 8192 + lw * 1024);
        };
        auto residual = [&]() {
            for (int pc = lw; pc < 10; pc += 8) {
                const int row = 8 * pc + (lane >> 3), ch = (lane & 7) ^ (((lane >> 3) & 3) << 1);
                glds16(ws + WS_HC + ((80 * S + row) * L_ + 64 * P + 8 * ch) * 2, lds0 + G2_RES + pc * 1024);
            }
        };
        if (lw < 2) {
            issue_w(0, 0);
            issue_w(1, 1);
            issue_w(2, 2);
            issue_w(3, 3);
            issue_w(4, 4);
            issue_w(5, 5);
            issue_w(6, 6);
            issue_w(7, 7);
            if (lw == 0) poll_flags(wsr, WS_F3 + (P >> 1) * 16 * 128, 16, tag, lane);
            BAR();
            issue_m(0, 0);
            issue_m(1, 1);
            issue_m(2, 2);
            issue_m(3, 3);
            issue_m(4, 4);
            issue_m(5, 5);
            issue_m(6, 6);
            issue_m(7, 7);
            waitv<7>(); BAR();
            waitv<6>(); BAR();
            waitv<5>(); BAR();
            issue_m(8, 0); issue_w(8, 0);
            waitv<7>(); BAR();
            issue_m(9, 1); issue_w(9, 1);
            waitv<9>(); BAR();
            issue_m(10, 2); issue_w(10, 2);
            waitv<11>(); BAR();
            issue_m(11, 3); issue_w(11, 3);
            waitv<13>(); BAR();
            issue_m(12, 4); issue_w(12, 4);
            waitv<15>(); BAR();
            issue_m(13, 5); issue_w(13, 5);
            waitv<15>(); BAR();
            issue_m(14, 6); issue_w(14, 6);
            waitv<15>(); BAR();
            issue_m(15, 7); issue_w(15, 7);
            waitv<15>(); BAR();
            issue_m(16, 0); issue_w(16, 0);
            waitv<15>(); BAR();
            issue_m(17, 1); issue_w(17, 1);
            waitv<15>(); BAR();
            issue_m(18, 2); issue_w(18, 2);
            waitv<15>(); BAR();
            issue_m(19, 3); issue_w(19, 3);
            waitv<15>(); BAR();
            issue_m(20, 4); issue_w(20, 4);
            waitv<15>(); BAR();
            issue_m(21, 5); issue_w(21, 5);
            waitv<15>(); BAR();
            issue_m(22, 6); issue_w(22, 6);
            waitv<15>(); BAR();
            issue_m(23, 7); issue_w(23, 7);
            waitv<15>(); BAR();
            residual();
            waitv<12>(); BAR();
            waitv<9>(); BAR();
            waitv<6>(); BAR();
            waitv<3>(); BAR();
            waitv<0>(); BAR();
        } else {
            issue_w(0, 0);
            issue_w(1, 1);
            issue_w(2, 2);
            issue_w(3, 3);
            issue_w(4, 4);
            issue_w(5, 5);
            issue_w(6, 6);
            issue_w(7, 7);
            if (lw == 0) poll_flags(wsr, WS_F3 + (P >> 1) * 16 * 128, 16, tag, lane);
            BAR();
            issue_m(0, 0);
            issue_m(1, 1);
            issue_m(2, 2);
            issue_m(3, 3);
            issue_m(4, 4);
            issue_m(5, 5);
            issue_m(6, 6);
            issue_m(7, 7);
            waitv<7>(); BAR();
            waitv<6>(); BAR();
            waitv<5>(); BAR();
            issue_m(8, 0); issue_w(8, 0);
            waitv<6>(); BAR();
            issue_m(9, 1); issue_w(9, 1);
            waitv<7>(); BAR();
            issue_m(10, 2); issue_w(10, 2);
            waitv<8>(); BAR();
            issue_m(11, 3); issue_w(11, 3);
            waitv<9>(); BAR();
            issue_m(12, 4); issue_w(12, 4);
            waitv<10>(); BAR();
            issue_m(13, 5); issue_w(13, 5);
            waitv<10>(); BAR();
            issue_m(14, 6); issue_w(14, 6);
            waitv<10>(); BAR();
            issue_m(15, 7); issue_w(15, 7);
            waitv<10>(); BAR();
            issue_m(16, 0); issue_w(16, 0);
            waitv<10>(); BAR();
            issue_m(17, 1); issue_w(17, 1);
            waitv<10>(); BAR();
            issue_m(18, 2); issue_w(18, 2);
            waitv<10>(); BAR();
            issue_m(19, 3); issue_w(19, 3);
            waitv<10>(); BAR();
            issue_m(20, 4); issue_w(20, 4);
            waitv<10>(); BAR();
            issue_m(21, 5); issue_w(21, 5);
            waitv<10>(); BAR();
            issue_m(22, 6); issue_w(22, 6);
            waitv<10>(); BAR();
            issue_m(23, 7); issue_w(23, 7);
            waitv<10>(); BAR();
            residual();
            waitv<8>(); BAR();
            waitv<6>(); BAR();
            waitv<4>(); BAR();
            waitv<2>(); BAR();
            waitv<0>(); BAR();
        }
        return;
    }
    BAR();
    STAMP(ws, 5, 0);
    const int g = lane >> 4, q = (lane & 15) >> 2, p = lane & 3;
    const int l = 64 * P + 16 * w + (lane & 15);
    const float rs2 = __uint_as_float(__builtin_amdgcn_raw_buffer_load_b32(wsr, WS_F3 + (l >> 3) * 128 + 4 + (l & 7) * 4, 0, 16));
    const float m1 = bflo(__builtin_amdgcn_raw_buffer_load_b32(wsr, WS_GR + (l >> 3) * 128 + (l & 7) * 8, 0, 16));
    f32x4 acc[5];
#pragma unroll
    for (int a = 0; a < 5; ++a) acc[a] = f32x4{0.f, 0.f, 0.f, 0.f};
    const LAS char* abase = (const LAS char*)smem + lane * 16;
    const LAS char* bbase = (const LAS char*)smem + 10240 + w * 2048 + lane * 16;
    bf16x8 EA[2][5], EB[2], OA[2][5], OB[2];
    BAR();
    STAMP(ws, 5, 1);
    EB[0] = *(const LAS bf16x8*)(bbase); EB[1] = *(const LAS bf16x8*)(bbase + 1024);
#pragma unroll
    for (int a = 0; a < 5; ++a) { EA[0][a] = *(const LAS bf16x8*)(abase + a * 1024); EA[1][a] = *(const LAS bf16x8*)(abase + 5120 + a * 1024); }
    SCHEDB();
#pragma unroll 1
    for (int it = 0; it < 3; ++it) {
        if (it == 1) STAMP(ws, 5, 2); if (it == 2) STAMP(ws, 5, 3);
        BAR();
        acc[0] = MFMA16(EA[0][0], EB[0], acc[0]);
        OB[0] = *(const LAS bf16x8*)(bbase + 1*G2_STAGE);
        OA[0][0] = *(const LAS bf16x8*)(abase + 1*G2_STAGE + 0);
        SCHEDB();
        acc[1] = MFMA16(EA[0][1], EB[0], acc[1]);
        OA[0][1] = *(const LAS bf16x8*)(abase + 1*G2_STAGE + 1024);
        OA[0][2] = *(const LAS bf16x8*)(abase + 1*G2_STAGE + 2048);
        SCHEDB();
        acc[2] = MFMA16(EA[0][2], EB[0], acc[2]);
        OA[0][3] = *(const LAS bf16x8*)(abase + 1*G2_STAGE + 3072);
        SCHEDB();
        acc[3] = MFMA16(EA[0][3], EB[0], acc[3]);
        OA[0][4] = *(const LAS bf16x8*)(abase + 1*G2_STAGE + 4096);
        SCHEDB();
        acc[4] = MFMA16(EA[0][4], EB[0], acc[4]);
        OB[1] = *(const LAS bf16x8*)(bbase + 1*G2_STAGE + 1024);
        SCHEDB();
        acc[0] = MFMA16(EA[1][0], EB[1], acc[0]);
        OA[1][0] = *(const LAS bf16x8*)(abase + 1*G2_STAGE + 5120);
        SCHEDB();
        acc[1] = MFMA16(EA[1][1], EB[1], acc[1]);
        OA[1][1] = *(const LAS bf16x8*)(abase + 1*G2_STAGE + 6144);
        SCHEDB();
        acc[2] = MFMA16(EA[1][2], EB[1], acc[2]);
        OA[1][2] = *(const LAS bf16x8*)(abase + 1*G2_STAGE + 7168);
        SCHEDB();
        acc[3] = MFMA16(EA[1][3], EB[1], acc[3]);
        OA[1][3] = *(const LAS bf16x8*)(abase + 1*G2_STAGE + 8192);
        SCHEDB();
        acc[4] = MFMA16(EA[1][4], EB[1], acc[4]);
        OA[1][4] = *(const LAS bf16x8*)(abase + 1*G2_STAGE + 9216);
        SCHEDB();
        BAR();
        acc[0] = MFMA16(OA[0][0], OB[0], acc[0]);
        EB[0] = *(const LAS bf16x8*)(bbase + 2*G2_STAGE);
        EA[0][0] = *(const LAS bf16x8*)(abase + 2*G2_STAGE + 0);
        SCHEDB();
        acc[1] = MFMA16(OA[0][1], OB[0], acc[1]);
        EA[0][1] = *(const LAS bf16x8*)(abase + 2*G2_STAGE + 1024);
        EA[0][2] = *(const LAS bf16x8*)(abase + 2*G2_STAGE + 2048);
        SCHEDB();
        acc[2] = MFMA16(OA[0][2], OB[0], acc[2]);
        EA[0][3] = *(const LAS bf16x8*)(abase + 2*G2_STAGE + 3072);
        SCHEDB();
        acc[3] = MFMA16(OA[0][3], OB[0], acc[3]);
        EA[0][4] = *(const LAS bf16x8*)(abase + 2*G2_STAGE + 4096);
        SCHEDB();
        acc[4] = MFMA16(OA[0][4], OB[0], acc[4]);
        EB[1] = *(const LAS bf16x8*)(bbase + 2*G2_STAGE + 1024);
        SCHEDB();
        acc[0] = MFMA16(OA[1][0], OB[1], acc[0]);
        EA[1][0] = *(const LAS bf16x8*)(abase + 2*G2_STAGE + 5120);
        SCHEDB();
        acc[1] = MFMA16(OA[1][1], OB[1], acc[1]);
        EA[1][1] = *(const LAS bf16x8*)(abase + 2*G2_STAGE + 6144);
        SCHEDB();
        acc[2] = MFMA16(OA[1][2], OB[1], acc[2]);
        EA[1][2] = *(const LAS bf16x8*)(abase + 2*G2_STAGE + 7168);
        SCHEDB();
        acc[3] = MFMA16(OA[1][3], OB[1], acc[3]);
        EA[1][3] = *(const LAS bf16x8*)(abase + 2*G2_STAGE + 8192);
        SCHEDB();
        acc[4] = MFMA16(OA[1][4], OB[1], acc[4]);
        EA[1][4] = *(const LAS bf16x8*)(abase + 2*G2_STAGE + 9216);
        SCHEDB();
        BAR();
        acc[0] = MFMA16(EA[0][0], EB[0], acc[0]);
        OB[0] = *(const LAS bf16x8*)(bbase + 3*G2_STAGE);
        OA[0][0] = *(const LAS bf16x8*)(abase + 3*G2_STAGE + 0);
        SCHEDB();
        acc[1] = MFMA16(EA[0][1], EB[0], acc[1]);
        OA[0][1] = *(const LAS bf16x8*)(abase + 3*G2_STAGE + 1024);
        OA[0][2] = *(const LAS bf16x8*)(abase + 3*G2_STAGE + 2048);
        SCHEDB();
        acc[2] = MFMA16(EA[0][2], EB[0], acc[2]);
        OA[0][3] = *(const LAS bf16x8*)(abase + 3*G2_STAGE + 3072);
        SCHEDB();
        acc[3] = MFMA16(EA[0][3], EB[0], acc[3]);
        OA[0][4] = *(const LAS bf16x8*)(abase + 3*G2_STAGE + 4096);
        SCHEDB();
        acc[4] = MFMA16(EA[0][4], EB[0], acc[4]);
        OB[1] = *(const LAS bf16x8*)(bbase + 3*G2_STAGE + 1024);
        SCHEDB();
        acc[0] = MFMA16(EA[1][0], EB[1], acc[0]);
        OA[1][0] = *(const LAS bf16x8*)(abase + 3*G2_STAGE + 5120);
        SCHEDB();
        acc[1] = MFMA16(EA[1][1], EB[1], acc[1]);
        OA[1][1] = *(const LAS bf16x8*)(abase + 3*G2_STAGE + 6144);
        SCHEDB();
        acc[2] = MFMA16(EA[1][2], EB[1], acc[2]);
        OA[1][2] = *(const LAS bf16x8*)(abase + 3*G2_STAGE + 7168);
        SCHEDB();
        acc[3] = MFMA16(EA[1][3], EB[1], acc[3]);
        OA[1][3] = *(const LAS bf16x8*)(abase + 3*G2_STAGE + 8192);
        SCHEDB();
        acc[4] = MFMA16(EA[1][4], EB[1], acc[4]);
        OA[1][4] = *(const LAS bf16x8*)(abase + 3*G2_STAGE + 9216);
        SCHEDB();
        BAR();
        acc[0] = MFMA16(OA[0][0], OB[0], acc[0]);
        EB[0] = *(const LAS bf16x8*)(bbase + 4*G2_STAGE);
        EA[0][0] = *(const LAS bf16x8*)(abase + 4*G2_STAGE + 0);
        SCHEDB();
        acc[1] = MFMA16(OA[0][1], OB[0], acc[1]);
        EA[0][1] = *(const LAS bf16x8*)(abase + 4*G2_STAGE + 1024);
        EA[0][2] = *(const LAS bf16x8*)(abase + 4*G2_STAGE + 2048);
        SCHEDB();
        acc[2] = MFMA16(OA[0][2], OB[0], acc[2]);
        EA[0][3] = *(const LAS bf16x8*)(abase + 4*G2_STAGE + 3072);
        SCHEDB();
        acc[3] = MFMA16(OA[0][3], OB[0], acc[3]);
        EA[0][4] = *(const LAS bf16x8*)(abase + 4*G2_STAGE + 4096);
        SCHEDB();
        acc[4] = MFMA16(OA[0][4], OB[0], acc[4]);
        EB[1] = *(const LAS bf16x8*)(bbase + 4*G2_STAGE + 1024);
        SCHEDB();
        acc[0] = MFMA16(OA[1][0], OB[1], acc[0]);
        EA[1][0] = *(const LAS bf16x8*)(abase + 4*G2_STAGE + 5120);
        SCHEDB();
        acc[1] = MFMA16(OA[1][1], OB[1], acc[1]);
        EA[1][1] = *(const LAS bf16x8*)(abase + 4*G2_STAGE + 6144);
        SCHEDB();
        acc[2] = MFMA16(OA[1][2], OB[1], acc[2]);
        EA[1][2] = *(const LAS bf16x8*)(abase + 4*G2_STAGE + 7168);
        SCHEDB();
        acc[3] = MFMA16(OA[1][3], OB[1], acc[3]);
        EA[1][3] = *(const LAS bf16x8*)(abase + 4*G2_STAGE + 8192);
        SCHEDB();
        acc[4] = MFMA16(OA[1][4], OB[1], acc[4]);
        EA[1][4] = *(const LAS bf16x8*)(abase + 4*G2_STAGE + 9216);
        SCHEDB();
        BAR();
        acc[0] = MFMA16(EA[0][0], EB[0], acc[0]);
        OB[0] = *(const LAS bf16x8*)(bbase + 5*G2_STAGE);
        OA[0][0] = *(const LAS bf16x8*)(abase + 5*G2_STAGE + 0);
        SCHEDB();
        acc[1] = MFMA16(EA[0][1], EB[0], acc[1]);
        OA[0][1] = *(const LAS bf16x8*)(abase + 5*G2_STAGE + 1024);
        OA[0][2] = *(const LAS bf16x8*)(abase + 5*G2_STAGE + 2048);
        SCHEDB();
        acc[2] = MFMA16(EA[0][2], EB[0], acc[2]);
        OA[0][3] = *(const LAS bf16x8*)(abase + 5*G2_STAGE + 3072);
        SCHEDB();
        acc[3] = MFMA16(EA[0][3], EB[0], acc[3]);
        OA[0][4] = *(const LAS bf16x8*)(abase + 5*G2_STAGE + 4096);
        SCHEDB();
        acc[4] = MFMA16(EA[0][4], EB[0], acc[4]);
        OB[1] = *(const LAS bf16x8*)(bbase + 5*G2_STAGE + 1024);
        SCHEDB();
        acc[0] = MFMA16(EA[1][0], EB[1], acc[0]);
        OA[1][0] = *(const LAS bf16x8*)(abase + 5*G2_STAGE + 5120);
        SCHEDB();
        acc[1] = MFMA16(EA[1][1], EB[1], acc[1]);
        OA[1][1] = *(const LAS bf16x8*)(abase + 5*G2_STAGE + 6144);
        SCHEDB();
        acc[2] = MFMA16(EA[1][2], EB[1], acc[2]);
        OA[1][2] = *(const LAS bf16x8*)(abase + 5*G2_STAGE + 7168);
        SCHEDB();
        acc[3] = MFMA16(EA[1][3], EB[1], acc[3]);
        OA[1][3] = *(const LAS bf16x8*)(abase + 5*G2_STAGE + 8192);
        SCHEDB();
        acc[4] = MFMA16(EA[1][4], EB[1], acc[4]);
        OA[1][4] = *(const LAS bf16x8*)(abase + 5*G2_STAGE + 9216);
        SCHEDB();
        BAR();
        acc[0] = MFMA16(OA[0][0], OB[0], acc[0]);
        EB[0] = *(const LAS bf16x8*)(bbase + 6*G2_STAGE);
        EA[0][0] = *(const LAS bf16x8*)(abase + 6*G2_STAGE + 0);
        SCHEDB();
        acc[1] = MFMA16(OA[0][1], OB[0], acc[1]);
        EA[0][1] = *(const LAS bf16x8*)(abase + 6*G2_STAGE + 1024);
        EA[0][2] = *(const LAS bf16x8*)(abase + 6*G2_STAGE + 2048);
        SCHEDB();
        acc[2] = MFMA16(OA[0][2], OB[0], acc[2]);
        EA[0][3] = *(const LAS bf16x8*)(abase + 6*G2_STAGE + 3072);
        SCHEDB();
        acc[3] = MFMA16(OA[0][3], OB[0], acc[3]);
        EA[0][4] = *(const LAS bf16x8*)(abase + 6*G2_STAGE + 4096);
        SCHEDB();
        acc[4] = MFMA16(OA[0][4], OB[0], acc[4]);
        EB[1] = *(const LAS bf16x8*)(bbase + 6*G2_STAGE + 1024);
        SCHEDB();
        acc[0] = MFMA16(OA[1][0], OB[1], acc[0]);
        EA[1][0] = *(const LAS bf16x8*)(abase + 6*G2_STAGE + 5120);
        SCHEDB();
        acc[1] = MFMA16(OA[1][1], OB[1], acc[1]);
        EA[1][1] = *(const LAS bf16x8*)(abase + 6*G2_STAGE + 6144);
        SCHEDB();
        acc[2] = MFMA16(OA[1][2], OB[1], acc[2]);
        EA[1][2] = *(const LAS bf16x8*)(abase + 6*G2_STAGE + 7168);
        SCHEDB();
        acc[3] = MFMA16(OA[1][3], OB[1], acc[3]);
        EA[1][3] = *(const LAS bf16x8*)(abase + 6*G2_STAGE + 8192);
        SCHEDB();
        acc[4] = MFMA16(OA[1][4], OB[1], acc[4]);
        EA[1][4] = *(const LAS bf16x8*)(abase + 6*G2_STAGE + 9216);
        SCHEDB();
        BAR();
        acc[0] = MFMA16(EA[0][0], EB[0], acc[0]);
        OB[0] = *(const LAS bf16x8*)(bbase + 7*G2_STAGE);
        OA[0][0] = *(const LAS bf16x8*)(abase + 7*G2_STAGE + 0);
        SCHEDB();
        acc[1] = MFMA16(EA[0][1], EB[0], acc[1]);
        OA[0][1] = *(const LAS bf16x8*)(abase + 7*G2_STAGE + 1024);
        OA[0][2] = *(const LAS bf16x8*)(abase + 7*G2_STAGE + 2048);
        SCHEDB();
        acc[2] = MFMA16(EA[0][2], EB[0], acc[2]);
        OA[0][3] = *(const LAS bf16x8*)(abase + 7*G2_STAGE + 3072);
        SCHEDB();
        acc[3] = MFMA16(EA[0][3], EB[0], acc[3]);
        OA[0][4] = *(const LAS bf16x8*)(abase + 7*G2_STAGE + 4096);
        SCHEDB();
        acc[4] = MFMA16(EA[0][4], EB[0], acc[4]);
        OB[1] = *(const LAS bf16x8*)(bbase + 7*G2_STAGE + 1024);
        SCHEDB();
        acc[0] = MFMA16(EA[1][0], EB[1], acc[0]);
        OA[1][0] = *(const LAS bf16x8*)(abase + 7*G2_STAGE + 5120);
        SCHEDB();
        acc[1] = MFMA16(EA[1][1], EB[1], acc[1]);
        OA[1][1] = *(const LAS bf16x8*)(abase + 7*G2_STAGE + 6144);
        SCHEDB();
        acc[2] = MFMA16(EA[1][2], EB[1], acc[2]);
        OA[1][2] = *(const LAS bf16x8*)(abase + 7*G2_STAGE + 7168);
        SCHEDB();
        acc[3] = MFMA16(EA[1][3], EB[1], acc[3]);
        OA[1][3] = *(const LAS bf16x8*)(abase + 7*G2_STAGE + 8192);
        SCHEDB();
        acc[4] = MFMA16(EA[1][4], EB[1], acc[4]);
        OA[1][4] = *(const LAS bf16x8*)(abase + 7*G2_STAGE + 9216);
        SCHEDB();
        if (it < 2) BAR();
        acc[0] = MFMA16(OA[0][0], OB[0], acc[0]);
        EB[0] = *(const LAS bf16x8*)(bbase + 0*G2_STAGE);
        EA[0][0] = *(const LAS bf16x8*)(abase + 0*G2_STAGE + 0);
        SCHEDB();
        acc[1] = MFMA16(OA[0][1], OB[0], acc[1]);
        EA[0][1] = *(const LAS bf16x8*)(abase + 0*G2_STAGE + 1024);
        EA[0][2] = *(const LAS bf16x8*)(abase + 0*G2_STAGE + 2048);
        SCHEDB();
        acc[2] = MFMA16(OA[0][2], OB[0], acc[2]);
        EA[0][3] = *(const LAS bf16x8*)(abase + 0*G2_STAGE + 3072);
        SCHEDB();
        acc[3] = MFMA16(OA[0][3], OB[0], acc[3]);
        EA[0][4] = *(const LAS bf16x8*)(abase + 0*G2_STAGE + 4096);
        SCHEDB();
        acc[4] = MFMA16(OA[0][4], OB[0], acc[4]);
        EB[1] = *(const LAS bf16x8*)(bbase + 0*G2_STAGE + 1024);
        SCHEDB();
        acc[0] = MFMA16(OA[1][0], OB[1], acc[0]);
        EA[1][0] = *(const LAS bf16x8*)(abase + 0*G2_STAGE + 5120);
        SCHEDB();
        acc[1] = MFMA16(OA[1][1], OB[1], acc[1]);
        EA[1][1] = *(const LAS bf16x8*)(abase + 0*G2_STAGE + 6144);
        SCHEDB();
        acc[2] = MFMA16(OA[1][2], OB[1], acc[2]);
        EA[1][2] = *(const LAS bf16x8*)(abase + 0*G2_STAGE + 7168);
        SCHEDB();
        acc[3] = MFMA16(OA[1][3], OB[1], acc[3]);
        EA[1][3] = *(const LAS bf16x8*)(abase + 0*G2_STAGE + 8192);
        SCHEDB();
        acc[4] = MFMA16(OA[1][4], OB[1], acc[4]);
        EA[1][4] = *(const LAS bf16x8*)(abase + 0*G2_STAGE + 9216);
        SCHEDB();
    }
    STAMP(ws, 5, 6);
    const rsrc_t ro = mkrsrc(out, L_ * D_ * 2);
#pragma unroll
    for (int a = 0; a < 5; ++a) {
        const int row = 16 * a + 4 * g + q;
        const int ch = (2 * w + (p >> 1)) ^ ((row & 3) << 1);
        const s16x4 hr = trread((const LAS char*)smem + G2_RES + row * 128 + ch * 16 + 8 * (p & 1));
        const u32x2 hh = __builtin_bit_cast(u32x2, hr);
        const u32 f01 = cvtpk(acc[a][0] * rs2, acc[a][1] * rs2), f23 = cvtpk(acc[a][2] * rs2, acc[a][3] * rs2);
        const u32 c01 = hh[0], c23 = hh[1];
        const u32 h01 = cvtpk(bflo(c01) + m1, bfhi(c01) + m1), h23 = cvtpk(bflo(c23) + m1, bfhi(c23) + m1);
        __builtin_amdgcn_raw_buffer_store_b64(u32x2{cvtpk(bflo(h01) + bflo(f01), bfhi(h01) + bfhi(f01)), cvtpk(bflo(h23) + bflo(f23), bfhi(h23) + bfhi(f23))}, ro, (l * D_ + 80 * S + 16 * a + 4 * g) * 2, 0, 0);
    }
    STAMP(ws, 5, 7);
}

constexpr int ALL_LDS = 163840;
static_assert(KA_LDS <= ALL_LDS && G1_LDS <= ALL_LDS && G2_LDS <= ALL_LDS, "LDS");

__global__ __launch_bounds__(768, 3) void k_block(const u16* __restrict__ x, const u16* __restrict__ Are, const u16* __restrict__ Aim, const u16* __restrict__ Bre,
                                                  const u16* __restrict__ Bim, const u16* __restrict__ Cre, const u16* __restrict__ Cim, const u16* __restrict__ logdt,
                                                  const u16* __restrict__ Dskip, const u16* __restrict__ Wg, const u16* __restrict__ Wu, const u16* __restrict__ Wd,
                                                  char* __restrict__ ws, u16* __restrict__ out) {
    __shared__ __attribute__((aligned(1024))) char smem[ALL_LDS];
    if (threadIdx.x >= 704) {
        unsigned long long pc; asm volatile("s_getpc_b64 %0" : "=s"(pc));
        const int lane = threadIdx.x & 63, jb = blockIdx.x >> 3;
        const char* p = nullptr;
        if (lane < 12) p = (const char*)((pc & ~127ull) + (unsigned long long)(jb * 12 + lane) * 128);
        else if (lane < 24) {
            const int i = jb * 12 + (lane - 12);
            constexpr int NWS = (int)(WS_END >> 16);
            if (i < NWS) p = ws + ((size_t)i << 16);
            else if (i < NWS + 40) p = (const char*)out + ((size_t)(i - NWS) << 16);
            else if (i < NWS + 70) p = (const char*)Wg + ((size_t)(i - NWS - 40) << 16);
            else if (i < NWS + 100) p = (const char*)Wu + ((size_t)(i - NWS - 70) << 16);
            else if (i < NWS + 130) p = (const char*)Wd + ((size_t)(i - NWS - 100) << 16);
        }
        const u32 dump = (u32)(uintptr_t)smem + ALL_LDS - 256;
        if (p != nullptr) { u32 sv; asm volatile("s_mov_b32 %0, m0\n\ts_mov_b32 m0, %2\n\ts_nop 0\n\tglobal_load_lds_dword %1, off\n\ts_mov_b32 m0, %0" : "=&s"(sv) : "v"(p), "s"(dump) : "memory"); }
    }
    const u32 tag = phase_ssm(x, Are, Aim, Bre, Bim, Cre, Cim, logdt, Dskip, Wg, Wu, Wd, ws, smem);
    const float rs2 = phase_gemm1(ws, smem, tag);
    phase_gemm2(ws, out, smem, tag, rs2);
}

extern "C" void kernel_launch(void* const* d_in, const int* in_sizes, int n_in, void* d_out, int out_size, void* d_ws, size_t ws_size, hipStream_t stream) {
    const u16* x = (const u16*)d_in[0];
    const u16* Are = (const u16*)d_in[1]; const u16* Aim = (const u16*)d_in[2];
    const u16* Bre = (const u16*)d_in[3]; const u16* Bim = (const u16*)d_in[4];
    const u16* Cre = (const u16*)d_in[5]; const u16* Cim = (const u16*)d_in[6];
    const u16* logdt = (const u16*)d_in[7]; const u16* Dskip = (const u16*)d_in[8];
    const u16* Wg = (const u16*)d_in[10]; const u16* Wu = (const u16*)d_in[11]; const u16* Wd = (const u16*)d_in[12];
    k_block<<<256, 768, 0, stream>>>(x, Are, Aim, Bre, Bim, Cre, Cim, logdt, Dskip, Wg, Wu, Wd, (char*)d_ws, (u16*)d_out);
}
```

```cpp
#include <hip/hip_runtime.h>
#include <stdint.h>

typedef unsigned short u16;
typedef unsigned int u32;
typedef short bf16x8 __attribute__((ext_vector_type(8)));
typedef short s16x4 __attribute__((ext_vector_type(4)));
typedef float f32x2 __attribute__((ext_vector_type(2)));
typedef float f32x4 __attribute__((ext_vector_type(4)));
typedef float f32x16 __attribute__((ext_vector_type(16)));
typedef unsigned u32x4 __attribute__((ext_vector_type(4)));
typedef unsigned u32x2 __attribute__((ext_vector_type(2)));
typedef __bf16 bf16x2_t __attribute__((ext_vector_type(2)));
#define LAS __attribute__((address_space(3)))
#define DI __device__ __forceinline__

#define STAMP(ws, k, j) do {} while (0)
#define STAMP2(ws, k, j) do {} while (0)

#define W_WARM 0
#define TLB_WARM 1
#define W2_LATE 2
#define EARLY_W 0
constexpr int L_ = 2048, D_ = 640, NM_ = 192, F_ = 1536;

constexpr u32 WS_HC  = 0;
constexpr u32 WS_HM  = WS_HC + D_ * L_ * 2;
constexpr u32 WS_W1T = WS_HM + L_ * F_ * 2;
constexpr u32 WS_W2T = WS_W1T + D_ * F_ * 2 * 2;
constexpr u32 WS_GR  = WS_W2T + F_ * D_ * 2;
constexpr u32 WS_F2  = WS_GR + 256 * 128;
constexpr u32 WS_FW  = WS_F2 + 256 * 128;
constexpr u32 WS_F3  = WS_FW + 128 * 128;
constexpr u32 WS_EP  = WS_F3 + 256 * 128;
constexpr u32 WS_END = WS_EP + 256;

DI float bflo(u32 v) { return __uint_as_float(v << 16); }
DI float bfhi(u32 v) { return __uint_as_float(v & 0xffff0000u); }
DI float bf2f(u16 v) { return __uint_as_float(((u32)v) << 16); }
DI u32 cvtpk(float lo, float hi) { f32x2 v = {lo, hi}; bf16x2_t r = __builtin_convertvector(v, bf16x2_t); return __builtin_bit_cast(u32, r); }
DI float rbf(float f) { return bflo(cvtpk(f, f)); }

typedef __amdgpu_buffer_rsrc_t rsrc_t;
DI rsrc_t mkrsrc(void* p, u32 bytes) { return __builtin_amdgcn_make_buffer_rsrc(p, 0, bytes, 0x00020000); }
#define AUX_KL 0
#define AUX_WT 0
#define AUX_H 0
#define AUX_HM 0
template <int AUX = 0> DI void st16(rsrc_t r, u32 off, u32x4 v) { __builtin_amdgcn_raw_buffer_store_b128(v, r, off, 0, AUX); }
template <int AUX = 0> DI void st8(rsrc_t r, u32 off, u32x2 v) { __builtin_amdgcn_raw_buffer_store_b64(v, r, off, 0, AUX); }
template <int AUX = 0> DI void st4(rsrc_t r, u32 off, u32 v) { __builtin_amdgcn_raw_buffer_store_b32(v, r, off, 0, AUX); }

template <int CTRL> DI float dppadd(float v) { return v + __builtin_bit_cast(float, __builtin_amdgcn_update_dpp(0, __builtin_bit_cast(int, v), CTRL, 0xF, 0xF, true)); }
DI float swap16add(float v) { auto r = __builtin_amdgcn_permlane16_swap(__float_as_uint(v), __float_as_uint(v), false, false); const u32 a = r[0], b = r[1]; return __uint_as_float(a) + __uint_as_float(b); }
DI float swap32add(float v) { auto r = __builtin_amdgcn_permlane32_swap(__float_as_uint(v), __float_as_uint(v), false, false); const u32 a = r[0], b = r[1]; return __uint_as_float(a) + __uint_as_float(b); }
DI float wave_sum(float v) {
    v = dppadd<0xB1>(v);
    v = dppadd<0x4E>(v);
    v = dppadd<0x141>(v);
    v = dppadd<0x140>(v);
    return swap32add(swap16add(v));
}

struct cm { f32x2 a, s; };
DI cm mkcm(f32x2 b) { return { b, f32x2{-b.y, b.x} }; }
DI f32x2 cmul(f32x2 v, const cm& m) { return v.xx * m.a + v.yy * m.s; }
DI f32x2 csq(f32x2 a) { return f32x2{ a.x * a.x - a.y * a.y, 2.f * a.x * a.y }; }
DI f32x2 cexp_p(float ar, float br, float p) {
    float e = __builtin_amdgcn_exp2f(ar * p);
    float th = __builtin_amdgcn_fractf(br * p);
    return f32x2{ e * __builtin_amdgcn_cosf(th), e * __builtin_amdgcn_sinf(th) };
}

#define WAITV(n) asm volatile("s_waitcnt vmcnt(" #n ")" ::: "memory")
#define WAITL0() asm volatile("s_waitcnt lgkmcnt(0)" ::: "memory")
template <int N> DI void waitv() { asm volatile("s_waitcnt vmcnt(%0)" :: "n"(N) : "memory"); }
#define BAR() do { asm volatile("" ::: "memory"); __builtin_amdgcn_s_barrier(); asm volatile("" ::: "memory"); } while (0)
#define LBAR() do { WAITL0(); BAR(); } while (0)
#define SGB(m, n) __builtin_amdgcn_sched_group_barrier(m, n, 0)
#define SCHEDB() __builtin_amdgcn_sched_barrier(0)
#define MFMA16(a, b, c) __builtin_amdgcn_mfma_f32_16x16x32_bf16(a, b, c, 0, 0, 0)
#define GAS __attribute__((address_space(1)))
typedef unsigned long long u64;

DI void glds16(const void* g, u32 lds_base) {
    u32 sv; asm volatile("s_mov_b32 %0, m0\n\ts_mov_b32 m0, %2\n\ts_nop 0\n\tglobal_load_lds_dwordx4 %1, off sc1\n\ts_mov_b32 m0, %0" : "=&s"(sv) : "v"(g), "s"(lds_base) : "memory");
}
DI void poll_flags(rsrc_t wsr, u32 off, int n, u32 tag, int lane) {
    const u32 o = off + (lane < n ? lane : 0) * 128;
    for (int spin = 0; spin < 100000; ++spin) {
        asm volatile("" ::: "memory");
        const u32 v = __builtin_amdgcn_raw_buffer_load_b32(wsr, o, 0, 16);
        if (__all(v == tag)) break;
        __builtin_amdgcn_s_sleep(1);
    }
    asm volatile("" ::: "memory");
}
DI void st_flag(char* ws, u32 off, u32 tag) { __hip_atomic_store((GAS u32*)(ws + off), tag, __ATOMIC_RELAXED, __HIP_MEMORY_SCOPE_AGENT); }
DI s16x4 trread(const LAS char* p) { typedef short v4i16_t __attribute__((ext_vector_type(4))); return __builtin_bit_cast(s16x4, __builtin_amdgcn_ds_read_tr16_b64_v4i16((LAS v4i16_t*)p)); }
DI bf16x8 cat8(s16x4 a, s16x4 b) { return __builtin_shufflevector(a, b, 0, 1, 2, 3, 4, 5, 6, 7); }

constexpr int KG_PITCH = 784;
constexpr int KA_A = 0, KA_ASZ = 20 * KG_PITCH;
constexpr int KA_B = 3 * KA_ASZ, KA_BSZ = 32 * KG_PITCH;
constexpr int KA_U = KA_B + 3 * KA_BSZ, KA_USZ = 4608 + 4096;
constexpr int KA_CNT = KA_U + 3 * KA_USZ;
constexpr int KA_LDS = KA_CNT + 48;
constexpr int CV_CS = 4160;
constexpr int KB_X = 32256;

#define ADDTID2(base0, v0, o0, base1, v1, o1) asm volatile("s_mov_b32 m0, %2\n\ts_nop 0\n\tds_write_addtid_b32 %0 offset:%4\n\ts_mov_b32 m0, %3\n\ts_nop 0\n\tds_write_addtid_b32 %1 offset:%5" \
    :: "v"(v0), "v"(v1), "s"(base0), "s"(base1), "i"(o0), "i"(o1) : "memory")
#define ADDTID2S(base, v0, o0, v1, o1) asm volatile("s_mov_b32 m0, %2\n\ts_nop 0\n\tds_write_addtid_b32 %0 offset:%3\n\tds_write_addtid_b32 %1 offset:%4" :: "v"(v0), "v"(v1), "s"(base), "i"(o0), "i"(o1) : "memory")

DI void lds_sync(char* smem, int cnt, u32 target, int lane) {
    LAS u32* c = (LAS u32*)((LAS char*)smem + KA_CNT) + cnt;
    WAITL0();
    if (lane == 0) __hip_atomic_fetch_add(c, 1u, __ATOMIC_RELAXED, __HIP_MEMORY_SCOPE_WORKGROUP);
    for (int spin = 0; spin < 1000000; ++spin) {
        if (__builtin_amdgcn_readfirstlane(__hip_atomic_load(c, __ATOMIC_RELAXED, __HIP_MEMORY_SCOPE_WORKGROUP)) >= target) break;
        __builtin_amdgcn_s_sleep(1);
    }
    asm volatile("" ::: "memory");
}

DI void lds_arrive(char* smem, int cnt, int lane) {
    WAITL0();
    if (lane == 0) __hip_atomic_fetch_add((LAS u32*)((LAS char*)smem + KA_CNT) + cnt, 1u, __ATOMIC_RELAXED, __HIP_MEMORY_SCOPE_WORKGROUP);
}
DI void lds_wait(char* smem, int cnt, u32 target) {
    LAS u32* c = (LAS u32*)((LAS char*)smem + KA_CNT) + cnt;
    for (int spin = 0; spin < 1000000; ++spin) {
        if (__builtin_amdgcn_readfirstlane(__hip_atomic_load(c, __ATOMIC_RELAXED, __HIP_MEMORY_SCOPE_WORKGROUP)) >= target) break;
        __builtin_amdgcn_s_sleep(1);
    }
    asm volatile("" ::: "memory");
}

struct KParams { u16 are, aim, bre, bim, cre, cim, ldt; };

DI void ka_kgen(const KParams& P, char* smem, int sA, int sB, int w, int lane) {
    const float are = bf2f(P.are), aim = bf2f(P.aim), bre = bf2f(P.bre), bim = bf2f(P.bim), cre = bf2f(P.cre), cim = bf2f(P.cim);
    const float dt = rbf(__expf(bf2f(P.ldt)));
    const float ar = (dt * are) * 1.4426950408889634f, br = (dt * aim) * 0.15915494309189535f;
    const float dbr = dt * bre, dbi = dt * bim;
    const f32x2 Wc = { cre * dbr - cim * dbi, -(cre * dbi + cim * dbr) };
    const f32x2 l1 = cexp_p(ar, br, 1.f), l8 = cexp_p(ar, br, 8.f), l256 = cexp_p(ar, br, 256.f);
    const f32x2 l2 = csq(l1), l4 = csq(l2), l16 = csq(l8);
    const cm c1 = mkcm(f32x2{l1.x, -l1.y}), c2 = mkcm(f32x2{l2.x, -l2.y}), c4 = mkcm(f32x2{l4.x, -l4.y}), c256 = mkcm(f32x2{l256.x, -l256.y}), m16 = mkcm(l16);
    const u32 lds0 = (u32)(uintptr_t)smem;
    const u32 bA = lds0 + sA + w * 256, bB = lds0 + sB + w * 256 - KB_X;
    auto pk = [](f32x2 v) { return cvtpk(v.x, v.y); };
    constexpr int R = KG_PITCH, X = KB_X;
    f32x2 a0 = Wc, x256 = cmul(Wc, c256), e0 = {1.f, 0.f}, e1 = l8;
    ADDTID2S(bA, pk(a0), 0 * R, pk(x256), 8 * R);
    ADDTID2S(bB, pk(e0), X + 0 * R, pk(e1), X + 1 * R);
    f32x2 y = x256, v = cmul(x256, c256);
#define KG_E(c) do { e0 = cmul(e0, m16); e1 = cmul(e1, m16); ADDTID2S(bB, pk(e0), X + (c) * R, pk(e1), X + ((c) + 1) * R); } while (0)
    a0 = cmul(a0, c1); y = cmul(y, c2); ADDTID2S(bA, pk(a0), 1 * R, pk(y), 9 * R); KG_E(2);
    a0 = cmul(a0, c1); y = cmul(y, c2); ADDTID2S(bA, pk(a0), 2 * R, pk(y), 10 * R); KG_E(4);
    a0 = cmul(a0, c1); y = cmul(y, c2); ADDTID2S(bA, pk(a0), 3 * R, pk(y), 11 * R); KG_E(6);
    a0 = cmul(a0, c1); y = cmul(v, c4); ADDTID2S(bA, pk(a0), 4 * R, pk(v), 12 * R); v = cmul(v, c256); KG_E(8);
    a0 = cmul(a0, c1); ADDTID2S(bA, pk(a0), 5 * R, pk(y), 13 * R); y = cmul(v, c4); KG_E(10);
    a0 = cmul(a0, c1); ADDTID2S(bA, pk(a0), 6 * R, pk(v), 14 * R); v = cmul(v, c256); KG_E(12);
    a0 = cmul(a0, c1); ADDTID2S(bA, pk(a0), 7 * R, pk(y), 15 * R); KG_E(14);
    y = cmul(v, c256); KG_E(16);
    ADDTID2S(bA, pk(v), 16 * R, pk(y), 17 * R); v = cmul(y, c256); KG_E(18);
    y = cmul(v, c256); KG_E(20);
    ADDTID2S(bA, pk(v), 18 * R, pk(y), 19 * R); v = cmul(y, c256); KG_E(22);
    const float k2048 = wave_sum(v.x);
    KG_E(24); KG_E(26);
    KG_E(28); KG_E(30);
    const int r32 = lane & 31, h = lane >> 5;
    const char* pa = smem + sA + (r32 < 20 ? r32 : 19) * R + w * 256 + h * 16;
    const char* pb = smem + sB + r32 * R + w * 256 + h * 16;
    f32x16 acc = {};
#pragma unroll
    for (int s = 0; s < 8; ++s) {
        const bf16x8 a = *(const bf16x8*)(pa + 32 * s), bb = *(const bf16x8*)(pb + 32 * s);
        acc = __builtin_amdgcn_mfma_f32_32x32x16_bf16(a, bb, acc, 0, 0, 0);
    }
    const int c = r32;
    char* sl = smem + sA + w * 256;
    char* p0 = sl + (c >> 3) * R + (8 * (c & 7) + 4 * h) * 4;
    char* p1 = sl + (h ? 6 * R + 8 * c : (4 + (c >> 4)) * R + 16 * (c & 15));
    char* p2 = sl + 8 * R + 4 * c;
#pragma unroll
    for (int q = 0; q < 4; ++q) *(float*)(p0 + 4 * q) = acc[q];
    *(float*)(p1) = acc[4]; *(float*)(p1 + 4) = acc[5];
    *(float*)(p1 + (h ? R : 8)) = acc[6]; *(float*)(p1 + (h ? R + 4 : 12)) = acc[7];
    if (h == 0) { *(float*)(p2) = acc[8]; *(float*)(p2 + 128) = acc[9]; *(float*)(p2 + R) = acc[10]; *(float*)(p2 + R + 128) = acc[11]; }
    else if (c == 0) *(float*)(sl + 10 * R) = k2048;
}

DI void ka_expand(char* smem, int sA, int sB, int w, int lane) {
    const int t = 64 * w + lane;
    auto rd = [&](int i) { const char* p = smem + sA + (i >> 6) * KG_PITCH + (i & 63) * 4; return *(const float*)p + *(const float*)(p + 256) + *(const float*)(p + 512); };
    u32 k4, k5, k6, k7, f2, f3;
    if (w == 0) {
        const int i0 = t < 32 ? 8 * t : 256 + 4 * (t - 32);
        const u32 pat = t < 32 ? 0x76543210u : 0x43222100u;
        float kv[8];
#pragma unroll
        for (int j = 0; j < 8; ++j) kv[j] = rd(i0 + ((pat >> (4 * j)) & 15));
        k4 = cvtpk(kv[0], kv[1]); k5 = cvtpk(kv[2], kv[3]); k6 = cvtpk(kv[4], kv[5]); k7 = cvtpk(kv[6], kv[7]);
        f2 = 0; f3 = 0;
    } else if (w == 1) {
        const int i0 = 384 + 2 * (t - 64);
        const float k0 = rd(i0), k1 = rd(i0 + 1), k2 = rd(i0 + 2);
        k4 = cvtpk(k0, k0); k5 = cvtpk(k0, k1); k6 = cvtpk(k1, k1); k7 = cvtpk(k2, k2);
        const float a = rd(382), b = rd(383);
        f2 = cvtpk(a, a); f3 = cvtpk(b, k0);
    } else {
        const int i0 = 512 + (t - 128);
        const float k0 = rd(i0), k1 = rd(i0 + 1);
        k4 = cvtpk(k0, k0); k5 = k4; k6 = cvtpk((t & 1) ? k1 : k0, k1); k7 = cvtpk(k1, k1);
        const float a = w == 2 ? rd(511) : k0;
        f2 = cvtpk(a, a); f3 = cvtpk(k0, k0);
    }
    const u32 k2 = __builtin_amdgcn_update_dpp(f2, k6, 0x138, 0xF, 0xF, false), k3 = __builtin_amdgcn_update_dpp(f3, k7, 0x138, 0xF, 0xF, false);
    const u32 a2 = __builtin_amdgcn_alignbit(k3, k2, 16), a3 = __builtin_amdgcn_alignbit(k4, k3, 16), a4 = __builtin_amdgcn_alignbit(k5, k4, 16),
              a5 = __builtin_amdgcn_alignbit(k6, k5, 16), a6 = __builtin_amdgcn_alignbit(k7, k6, 16);
    char* kb = smem + sB + 32 + 16 * t;
    *(u32x4*)(kb + 0 * CV_CS) = u32x4{k4, k5, k6, k7};
    *(u32x4*)(kb + 1 * CV_CS) = u32x4{a3, a4, a5, a6};
    *(u32x4*)(kb + 2 * CV_CS) = u32x4{k3, k4, k5, k6};
    *(u32x4*)(kb + 3 * CV_CS) = u32x4{a2, a3, a4, a5};
    if (t < 8) *(u32x4*)(smem + sB + (t >> 1) * CV_CS + 16 * (t & 1)) = u32x4{0, 0, 0, 0};
}

DI u32 phase_ssm(const u16* __restrict__ x, const u16* __restrict__ Are, const u16* __restrict__ Aim, const u16* __restrict__ Bre,
                  const u16* __restrict__ Bim, const u16* __restrict__ Cre, const u16* __restrict__ Cim, const u16* __restrict__ logdt,
                  const u16* __restrict__ Dskip, const u16* __restrict__ Wg, const u16* __restrict__ Wu, const u16* __restrict__ Wd,
                  char* __restrict__ ws, char* smem) {
    const int b = blockIdx.x, t = threadIdx.x, lane = t & 63;
    const int wv = __builtin_amdgcn_readfirstlane(t >> 6), sb = wv >> 2, w = wv & 3;
    const int xcd = b & 7, j = b >> 3;
    const int nch = j < 16 ? 3 : 2;
    const int c0 = 80 * xcd + ((xcd & 2) ? (j < 16 ? 32 + 3 * j : 2 * (j - 16)) : (j < 16 ? 3 * j : 48 + 2 * (j - 16)));
    const int xs = min((2 * c0) & ~3, ((2 * c0) | 127) + 1 - 8), e0 = (2 * c0 - xs) >> 1;
    const bool conv = sb < nch;
    const int d = c0 + sb;
    const int sA = KA_A + sb * KA_ASZ, sB = KA_B + sb * KA_BSZ, sU = KA_U + sb * KA_USZ;
    const rsrc_t wsr = mkrsrc(ws, WS_END);
    STAMP(ws, 0, 0);
    if (t < 8) *(u32*)(smem + KA_CNT + 4 * t) = 0u;

    KParams KP;
    u32 ra[4][5];
    u32x4 wv1[8], wv2[8];
    const int tt = t - 512;
    const bool w1job = (j - 16) < 12;
    float dsk = 0.f;
    if (wv == 3 || wv == 7) {
        const u32* x32 = (const u32*)x;
#pragma unroll
        for (int rr = 0; rr < 4; ++rr)
#pragma unroll
            for (int i = 0; i < 5; ++i) ra[rr][i] = x32[(8 * b + 4 * sb + rr) * 320 + lane + 64 * i];
    }
    if (conv) {
        if (w < 3) {
            const int n = d * NM_ + 64 * w + lane;
            KP.are = Are[n]; KP.aim = Aim[n]; KP.bre = Bre[n]; KP.bim = Bim[n]; KP.cre = Cre[n]; KP.cim = Cim[n]; KP.ldt = logdt[d];
        }
        dsk = bf2f(Dskip[d]);
    }
    const u32 epoch = __hip_atomic_load((GAS u32*)(ws + WS_EP), __ATOMIC_RELAXED, __HIP_MEMORY_SCOPE_AGENT);
    const rsrc_t xr0 = mkrsrc((void*)x, L_ * D_ * 2);
    LBAR();
    u32x2 xg[4];
    const int pi0 = 128 * wv + lane, pi1 = pi0 + 64;
    const bool early = (w != 3) && (nch == 3 || !EARLY_W);
    if (wv < 8 && early) {
        xg[0] = __builtin_amdgcn_raw_buffer_load_b64(xr0, (2 * pi0 + 0) * (D_ * 2) + xs, 0, 0);
        xg[1] = __builtin_amdgcn_raw_buffer_load_b64(xr0, (2 * pi0 + 1) * (D_ * 2) + xs, 0, 0);
    }
    if (conv && w == 3) {
        if (lane < 30) *(u32x4*)(smem + sU + 16 * lane) = u32x4{0, 0, 0, 0};
        lds_arrive(smem, sb, lane);
    }
    if (!conv) {
        if (w1job) {
            const int u1 = xcd * 12 + (j - 16), up = u1 >= 48, col = 32 * (up ? u1 - 48 : u1) + 8 * (tt & 3);
            const u16* src = (up ? Wu : Wg) + (8 * (tt >> 2)) * F_ + col;
#pragma unroll
            for (int r = 0; r < 8; ++r) wv1[r] = *(const u32x4*)(src + r * F_);
            if (w == 0) {
#pragma unroll
                for (int r = 0; r < 8; ++r) wv2[r] = *(const u32x4*)(src + (512 + r) * F_);
            }
        } else {
            if (W2_LATE == 1) lds_wait(smem, 3, 4 * nch); else if (W2_LATE == 2) lds_wait(smem, 4, 1);
            const int n1 = 480 * (xcd * 4 + (j - 28)) + tt, n2 = n1 + 256;
            const u16* s1 = Wd + (8 * (n1 / 80)) * D_ + 8 * (n1 % 80);
#pragma unroll
            for (int r = 0; r < 8; ++r) wv1[r] = *(const u32x4*)(s1 + r * D_);
            if (tt < 224) {
                const u16* s2 = Wd + (8 * (n2 / 80)) * D_ + 8 * (n2 % 80);
#pragma unroll
                for (int r = 0; r < 8; ++r) wv2[r] = *(const u32x4*)(s2 + r * D_);
            }
        }
    }
    const u32 tag = epoch + 1u;
    if (wv == 3 || wv == 7) {
        __builtin_amdgcn_s_setprio(3);
        float s1[4], s2[4], c0v[4];
#pragma unroll
        for (int rr = 0; rr < 4; ++rr) {
            const float c = bflo(__builtin_amdgcn_readfirstlane(ra[rr][0]));
            float a1 = 0.f, a2 = 0.f;
#pragma unroll
            for (int i = 0; i < 5; ++i) { const float a = bflo(ra[rr][i]) - c, e = bfhi(ra[rr][i]) - c; a1 += a + e; a2 = __builtin_fmaf(a, a, __builtin_fmaf(e, e, a2)); }
            s1[rr] = a1; s2[rr] = a2; c0v[rr] = c;
        }
        auto red4 = [&](const float (&v)[4]) {
            auto p01 = __builtin_amdgcn_permlane32_swap(__float_as_uint(v[0]), __float_as_uint(v[1]), false, false);
            auto p23 = __builtin_amdgcn_permlane32_swap(__float_as_uint(v[2]), __float_as_uint(v[3]), false, false);
            const u32 x0 = p01[0], x1 = p01[1], y0 = p23[0], y1 = p23[1];
            const float t01 = __uint_as_float(x0) + __uint_as_float(x1);
            const float t23 = __uint_as_float(y0) + __uint_as_float(y1);
            auto q = __builtin_amdgcn_permlane16_swap(__float_as_uint(t01), __float_as_uint(t23), false, false);
            const u32 z0 = q[0], z1 = q[1];
            float r = __uint_as_float(z0) + __uint_as_float(z1);
            r = dppadd<0xB1>(r); r = dppadd<0x4E>(r); r = dppadd<0x141>(r); r = dppadd<0x140>(r);
            return r;
        };
        const float n1 = red4(s1) * (1.f / 640.f), n2 = red4(s2) * (1.f / 640.f);
        const int lr = lane >> 4, rr = (lr >> 1) | ((lr & 1) << 1);
        const float cc = rr == 0 ? c0v[0] : rr == 1 ? c0v[1] : rr == 2 ? c0v[2] : c0v[3];
        float var = n2 - n1 * n1; var = var > 0.f ? var : 0.f;
        const float sd = __builtin_sqrtf(rbf(rbf(var) + 1e-5f));
        const u32 pay = cvtpk(cc + n1, sd);
        u32* hand = (u32*)(smem + KA_CNT + 32);
        if (wv == 7) {
            if ((lane & 15) == 0) hand[rr] = pay;
            lds_arrive(smem, 6, lane);
        } else {
            u32 mine = __builtin_amdgcn_ds_bpermute(((lane & 1) << 5 | (lane & 2) << 3) << 2, pay);
            lds_wait(smem, 6, 1);
            if (lane >= 4 && lane < 8) mine = hand[lane - 4];
            if (lane < 8) __hip_atomic_store((GAS u64*)(ws + WS_GR + b * 128) + lane, ((u64)tag << 32) | mine, __ATOMIC_RELAXED, __HIP_MEMORY_SCOPE_AGENT);
            asm volatile("" ::: "memory");
            if (lane == 0) __hip_atomic_fetch_add((LAS u32*)((LAS char*)smem + KA_CNT) + 4, 1u, __ATOMIC_RELAXED, __HIP_MEMORY_SCOPE_WORKGROUP);
        }
        __builtin_amdgcn_s_setprio(0);
    }
    if (conv) {
        if (w < 3) ka_kgen(KP, smem, sA, sB, w, lane);
    } else if (w1job) {
        float* red = (float*)(smem + sA);
#pragma unroll
        for (int i = 0; i < 4; ++i) {
            float a = 0.f, c = 0.f;
#pragma unroll
            for (int r = 0; r < 8; ++r) { a += bflo(wv1[r][i]); c += bfhi(wv1[r][i]); }
            if (w == 0) {
#pragma unroll
                for (int r = 0; r < 8; ++r) { a += bflo(wv2[r][i]); c += bfhi(wv2[r][i]); }
            }
            red[(tt >> 2) * 33 + 8 * (tt & 3) + 2 * i] = a; red[(tt >> 2) * 33 + 8 * (tt & 3) + 2 * i + 1] = c;
        }
    }
    if (!conv) {
        __builtin_amdgcn_s_setprio(2);
        {
            if (w1job) {
            lds_sync(smem, sb, 4, lane);
            const float* red = (const float*)(smem + sA);
            const int col = tt & 31, part = tt >> 5;
            float sm = 0.f;
#pragma unroll
            for (int r = 0; r < 8; ++r) sm += red[(8 * part + r) * 33 + col];
            sm = swap32add(sm);
            if (lane < 32) *(float*)(smem + sB + (col * 4 + w) * 4) = sm;
            lds_sync(smem, sb, 8, lane);
#define WV(r) (rnd ? wv2[r][i] : wv1[r][i])
            }
            char* stg = smem + (w == 3 ? sA + 1024 : sB + 512 + w * 8192);
            u32* dtab = (u32*)(smem + sU + w * 256);
            auto flush = [&](u32 dst, bool active, const u32x4 (&o)[8]) {
                dtab[lane] = active ? dst : 0xffffff00u;
#pragma unroll
                for (int i = 0; i < 8; ++i) *(u32x4*)(stg + lane * 128 + ((i ^ (lane & 7)) * 16)) = o[i];
#pragma unroll
                for (int i = 0; i < 8; ++i) {
                    const int sl = 8 * i + (lane >> 3), pc = lane & 7;
                    const u32x4 v = *(const u32x4*)(stg + sl * 128 + ((pc ^ (sl & 7)) * 16));
                    st16<16>(wsr, dtab[sl] + pc * 16, v);
                }
            };
            if (w1job) {
                const int u1 = xcd * 12 + (j - 16), up = u1 >= 48, f = 32 * (up ? u1 - 48 : u1) + 8 * (tt & 3);
                float cm[8];
#pragma unroll
                for (int i = 0; i < 8; ++i) { const f32x4 v = *(const f32x4*)(smem + sB + (8 * (tt & 3) + i) * 16); cm[i] = (v[0] + v[1] + v[2] + v[3]) * (1.f / 640.f); }
                const int sl = f / 96, fl = f % 96, mt = 2 * (fl >> 4) + up;
#pragma unroll
                for (int rnd = 0; rnd < 2; ++rnd) {
                    if (rnd == 0 || w == 0) {
                        const int kg = (tt >> 2) + 64 * rnd;
                        const u32 dst = WS_W1T + ((sl * 10 + (kg >> 3)) * 24 + ((kg >> 2) & 1) * 12 + mt) * 1024 + ((fl & 15) + 16 * (kg & 3)) * 16;
                        u32x4 o[8];
#pragma unroll
                        for (int i = 0; i < 4; ++i) {
                            const float ca = cm[2 * i], cb = cm[2 * i + 1];
                            o[2 * i] = u32x4{cvtpk(bflo(WV(0)) - ca, bflo(WV(1)) - ca), cvtpk(bflo(WV(2)) - ca, bflo(WV(3)) - ca),
                                             cvtpk(bflo(WV(4)) - ca, bflo(WV(5)) - ca), cvtpk(bflo(WV(6)) - ca, bflo(WV(7)) - ca)};
                            o[2 * i + 1] = u32x4{cvtpk(bfhi(WV(0)) - cb, bfhi(WV(1)) - cb), cvtpk(bfhi(WV(2)) - cb, bfhi(WV(3)) - cb),
                                                 cvtpk(bfhi(WV(4)) - cb, bfhi(WV(5)) - cb), cvtpk(bfhi(WV(6)) - cb, bfhi(WV(7)) - cb)};
                        }
                        flush(dst, true, o);
                    }
                }
            } else {
#pragma unroll
                for (int rnd = 0; rnd < 2; ++rnd) {
                    if (rnd == 0 || w < 3 || true) {
                        const bool act = rnd == 0 || tt < 224;
                        const int n = 480 * (xcd * 4 + (j - 28)) + tt + 256 * rnd, dc = n % 80, kg = n / 80;
                        const int ds = dc / 10, dl = 8 * (dc % 10);
                        const u32 dst = WS_W2T + ((ds * 24 + (kg >> 3)) * 10 + ((kg >> 2) & 1) * 5 + (dl >> 4)) * 1024 + ((dl & 15) + 16 * (kg & 3)) * 16;
                        u32x4 o[8];
#pragma unroll
                        for (int i = 0; i < 4; ++i) {
                            o[2 * i] = u32x4{__builtin_amdgcn_perm(WV(1), WV(0), 0x05040100u), __builtin_amdgcn_perm(WV(3), WV(2), 0x05040100u),
                                             __builtin_amdgcn_perm(WV(5), WV(4), 0x05040100u), __builtin_amdgcn_perm(WV(7), WV(6), 0x05040100u)};
                            o[2 * i + 1] = u32x4{__builtin_amdgcn_perm(WV(1), WV(0), 0x07060302u), __builtin_amdgcn_perm(WV(3), WV(2), 0x07060302u),
                                                 __builtin_amdgcn_perm(WV(5), WV(4), 0x07060302u), __builtin_amdgcn_perm(WV(7), WV(6), 0x07060302u)};
                        }
                        flush(dst, act, o);
                    }
                }
            }
        }
        __builtin_amdgcn_s_setprio(0);
        WAITV(0);
        lds_sync(smem, sb, w1job ? 12 : 4, lane);
        if (t == 512) st_flag(ws, WS_FW + (xcd * 16 + (j - 16)) * 128, tag);
        if (t == 512) STAMP2(ws, 6, 0);
        return tag;
    }
    STAMP(ws, 0, 1);
    u32x4 g0 = {0, 0, 0, 0}, g1 = {0, 0, 0, 0};
    if (wv < 8) {
        lds_wait(smem, 4, 1);
        if (!early) {
            xg[0] = __builtin_amdgcn_raw_buffer_load_b64(xr0, (2 * pi0 + 0) * (D_ * 2) + xs, 0, 0);
            xg[1] = __builtin_amdgcn_raw_buffer_load_b64(xr0, (2 * pi0 + 1) * (D_ * 2) + xs, 0, 0);
        }
        xg[2] = __builtin_amdgcn_raw_buffer_load_b64(xr0, (2 * pi1 + 0) * (D_ * 2) + xs, 0, 0);
        xg[3] = __builtin_amdgcn_raw_buffer_load_b64(xr0, (2 * pi1 + 1) * (D_ * 2) + xs, 0, 0);
        g0 = __builtin_amdgcn_raw_buffer_load_b128(wsr, WS_GR + (pi0 >> 2) * 128 + (pi0 & 3) * 16, 0, 16);
        g1 = __builtin_amdgcn_raw_buffer_load_b128(wsr, WS_GR + (pi1 >> 2) * 128 + (pi1 & 3) * 16, 0, 16);
    }
    if (w < 3) lds_sync(smem, sb, 4, lane); else lds_wait(smem, sb, 4);
    STAMP(ws, 0, 2);
    ka_expand(smem, sA, sB, w, lane);
    if (wv < 8) {
        for (int spin = 0; spin < 100000; ++spin) {
            if (__all(g0[1] == tag && g0[3] == tag && g1[1] == tag && g1[3] == tag)) break;
            __builtin_amdgcn_s_sleep(4);
            asm volatile("" ::: "memory");
            g0 = __builtin_amdgcn_raw_buffer_load_b128(wsr, WS_GR + (pi0 >> 2) * 128 + (pi0 & 3) * 16, 0, 16);
            g1 = __builtin_amdgcn_raw_buffer_load_b128(wsr, WS_GR + (pi1 >> 2) * 128 + (pi1 & 3) * 16, 0, 16);
        }
        const u32 pr[4] = {g0[0], g0[2], g1[0], g1[2]};
        float m[4], rs[4];
#pragma unroll
        for (int r = 0; r < 4; ++r) { m[r] = bflo(pr[r]); rs[r] = __builtin_amdgcn_rcpf(bfhi(pr[r])); }
#pragma unroll
        for (int s = 0; s < 3; ++s) {
            if (s < nch) {
                const int e = e0 + s;
                float xv[4];
#pragma unroll
                for (int r = 0; r < 4; ++r) { const u32 dw = e < 2 ? xg[r][0] : xg[r][1]; xv[r] = (e & 1) ? bfhi(dw) : bflo(dw); }
                const u32 q01 = cvtpk(xv[0] - m[0], xv[1] - m[1]), q23 = cvtpk(xv[2] - m[2], xv[3] - m[3]);
                const u32 u01 = cvtpk(bflo(q01) * rs[0], bfhi(q01) * rs[1]), u23 = cvtpk(bflo(q23) * rs[2], bfhi(q23) * rs[3]);
                char* ub = smem + KA_U + s * KA_USZ;
                *(u32*)(ub + 480 + 4 * pi0) = u01; *(u32*)(ub + 480 + 4 * pi1) = u23;
                *(u32*)(ub + 4608 + 4 * pi0) = q01; *(u32*)(ub + 4608 + 4 * pi1) = q23;
            }
        }
    }
    STAMP(ws, 0, 3);
    asm volatile("" :: "v"(tag));
    lds_sync(smem, 3, 4 * nch, lane);
    STAMP(ws, 0, 4);
    const int al = lane & 15, g = lane >> 4;
    f32x4 acc[8];
#pragma unroll
    for (int i = 0; i < 8; ++i) acc[i] = f32x4{0.f, 0.f, 0.f, 0.f};
    {
        const LAS char* lds = (const LAS char*)smem;
        const int q = (lane & 15) >> 2, p = lane & 3;
        const LAS char* ub0 = lds + sU + 448 + 32 * al + 16 * g - 128 * w;
        const LAS char* ub1 = ub0 - 64;
        const LAS char* kb0 = lds + sB + q * CV_CS + 56 - 16 * g + 8 * p + 128 * w;
        const LAS char* kb1 = kb0 + 64;
        bf16x8 U0[8], U1[8]; s16x4 A0[8][2], A1[8][2];
        A0[0][0] = trread(kb0 + 8); A0[0][1] = trread(kb0 + 0); U0[0] = *(const LAS bf16x8*)(ub0 + 0); U0[1] = *(const LAS bf16x8*)(ub0 + 512); U0[2] = *(const LAS bf16x8*)(ub0 + 1024); U0[3] = *(const LAS bf16x8*)(ub0 + 1536); U0[4] = *(const LAS bf16x8*)(ub0 + 2048); U0[5] = *(const LAS bf16x8*)(ub0 + 2560); U0[6] = *(const LAS bf16x8*)(ub0 + 3072); U0[7] = *(const LAS bf16x8*)(ub0 + 3584); A0[1][0] = trread(kb0 + 520); A0[1][1] = trread(kb0 + 512); A0[2][0] = trread(kb0 + 1032); A0[2][1] = trread(kb0 + 1024); A0[3][0] = trread(kb0 + 1544); A0[3][1] = trread(kb0 + 1536); A0[4][0] = trread(kb0 + 2056); A0[4][1] = trread(kb0 + 2048); A0[5][0] = trread(kb0 + 2568); A0[5][1] = trread(kb0 + 2560); A0[6][0] = trread(kb0 + 3080); A0[6][1] = trread(kb0 + 3072); A0[7][0] = trread(kb0 + 3592); A0[7][1] = trread(kb0 + 3584);
        SCHEDB();
        acc[0] = MFMA16(cat8(A0[0][0], A0[0][1]), U0[0], acc[0]);
        SCHEDB();
        acc[1] = MFMA16(cat8(A0[0][0], A0[0][1]), U0[1], acc[1]);
        SCHEDB();
        acc[2] = MFMA16(cat8(A0[0][0], A0[0][1]), U0[2], acc[2]);
        SCHEDB();
        acc[3] = MFMA16(cat8(A0[0][0], A0[0][1]), U0[3], acc[3]);
        SCHEDB();
        acc[4] = MFMA16(cat8(A0[0][0], A0[0][1]), U0[4], acc[4]);
        SCHEDB();
        acc[5] = MFMA16(cat8(A0[0][0], A0[0][1]), U0[5], acc[5]);
        SCHEDB();
        acc[6] = MFMA16(cat8(A0[0][0], A0[0][1]), U0[6], acc[6]);
        SCHEDB();
        acc[7] = MFMA16(cat8(A0[0][0], A0[0][1]), U0[7], acc[7]);
        SCHEDB();
        acc[1] = MFMA16(cat8(A0[1][0], A0[1][1]), U0[0], acc[1]);
        A1[0][0] = trread(kb1 + 8);
        SCHEDB();
        acc[2] = MFMA16(cat8(A0[1][0], A0[1][1]), U0[1], acc[2]);
        A1[0][1] = trread(kb1 + 0);
        SCHEDB();
        acc[3] = MFMA16(cat8(A0[1][0], A0[1][1]), U0[2], acc[3]);
        U1[0] = *(const LAS bf16x8*)(ub1 + 0);
        SCHEDB();
        acc[4] = MFMA16(cat8(A0[1][0], A0[1][1]), U0[3], acc[4]);
        U1[1] = *(const LAS bf16x8*)(ub1 + 512);
        SCHEDB();
        acc[5] = MFMA16(cat8(A0[1][0], A0[1][1]), U0[4], acc[5]);
        U1[2] = *(const LAS bf16x8*)(ub1 + 1024);
        SCHEDB();
        acc[6] = MFMA16(cat8(A0[1][0], A0[1][1]), U0[5], acc[6]);
        U1[3] = *(const LAS bf16x8*)(ub1 + 1536);
        SCHEDB();
        acc[7] = MFMA16(cat8(A0[1][0], A0[1][1]), U0[6], acc[7]);
        U1[4] = *(const LAS bf16x8*)(ub1 + 2048);
        SCHEDB();
        acc[2] = MFMA16(cat8(A0[2][0], A0[2][1]), U0[0], acc[2]);
        U1[5] = *(const LAS bf16x8*)(ub1 + 2560);
        SCHEDB();
        acc[3] = MFMA16(cat8(A0[2][0], A0[2][1]), U0[1], acc[3]);
        U1[6] = *(const LAS bf16x8*)(ub1 + 3072);
        SCHEDB();
        acc[4] = MFMA16(cat8(A0[2][0], A0[2][1]), U0[2], acc[4]);
        U1[7] = *(const LAS bf16x8*)(ub1 + 3584);
        SCHEDB();
        acc[5] = MFMA16(cat8(A0[2][0], A0[2][1]), U0[3], acc[5]);
        A1[1][0] = trread(kb1 + 520);
        SCHEDB();
        acc[6] = MFMA16(cat8(A0[2][0], A0[2][1]), U0[4], acc[6]);
        A1[1][1] = trread(kb1 + 512);
        SCHEDB();
        acc[7] = MFMA16(cat8(A0[2][0], A0[2][1]), U0[5], acc[7]);
        A1[2][0] = trread(kb1 + 1032);
        SCHEDB();
        acc[3] = MFMA16(cat8(A0[3][0], A0[3][1]), U0[0], acc[3]);
        A1[2][1] = trread(kb1 + 1024);
        SCHEDB();
        acc[4] = MFMA16(cat8(A0[3][0], A0[3][1]), U0[1], acc[4]);
        A1[3][0] = trread(kb1 + 1544);
        SCHEDB();
        acc[5] = MFMA16(cat8(A0[3][0], A0[3][1]), U0[2], acc[5]);
        A1[3][1] = trread(kb1 + 1536);
        SCHEDB();
        acc[6] = MFMA16(cat8(A0[3][0], A0[3][1]), U0[3], acc[6]);
        A1[4][0] = trread(kb1 + 2056);
        SCHEDB();
        acc[7] = MFMA16(cat8(A0[3][0], A0[3][1]), U0[4], acc[7]);
        A1[4][1] = trread(kb1 + 2048);
        SCHEDB();
        acc[4] = MFMA16(cat8(A0[4][0], A0[4][1]), U0[0], acc[4]);
        A1[5][0] = trread(kb1 + 2568);
        SCHEDB();
        acc[5] = MFMA16(cat8(A0[4][0], A0[4][1]), U0[1], acc[5]);
        A1[5][1] = trread(kb1 + 2560);
        SCHEDB();
        acc[6] = MFMA16(cat8(A0[4][0], A0[4][1]), U0[2], acc[6]);
        A1[6][0] = trread(kb1 + 3080);
        SCHEDB();
        acc[7] = MFMA16(cat8(A0[4][0], A0[4][1]), U0[3], acc[7]);
        A1[6][1] = trread(kb1 + 3072);
        SCHEDB();
        acc[5] = MFMA16(cat8(A0[5][0], A0[5][1]), U0[0], acc[5]);
        A1[7][0] = trread(kb1 + 3592);
        SCHEDB();
        acc[6] = MFMA16(cat8(A0[5][0], A0[5][1]), U0[1], acc[6]);
        A1[7][1] = trread(kb1 + 3584);
        SCHEDB();
        acc[7] = MFMA16(cat8(A0[5][0], A0[5][1]), U0[2], acc[7]);
        SCHEDB();
        acc[6] = MFMA16(cat8(A0[6][0], A0[6][1]), U0[0], acc[6]);
        SCHEDB();
        acc[7] = MFMA16(cat8(A0[6][0], A0[6][1]), U0[1], acc[7]);
        SCHEDB();
        acc[7] = MFMA16(cat8(A0[7][0], A0[7][1]), U0[0], acc[7]);
        SCHEDB();
        acc[0] = MFMA16(cat8(A1[0][0], A1[0][1]), U1[0], acc[0]); SCHEDB();
        acc[1] = MFMA16(cat8(A1[0][0], A1[0][1]), U1[1], acc[1]); SCHEDB();
        acc[2] = MFMA16(cat8(A1[0][0], A1[0][1]), U1[2], acc[2]); SCHEDB();
        acc[3] = MFMA16(cat8(A1[0][0], A1[0][1]), U1[3], acc[3]); SCHEDB();
        acc[4] = MFMA16(cat8(A1[0][0], A1[0][1]), U1[4], acc[4]); SCHEDB();
        acc[5] = MFMA16(cat8(A1[0][0], A1[0][1]), U1[5], acc[5]); SCHEDB();
        acc[6] = MFMA16(cat8(A1[0][0], A1[0][1]), U1[6], acc[6]); SCHEDB();
        acc[7] = MFMA16(cat8(A1[0][0], A1[0][1]), U1[7], acc[7]); SCHEDB();
        acc[1] = MFMA16(cat8(A1[1][0], A1[1][1]), U1[0], acc[1]); SCHEDB();
        acc[2] = MFMA16(cat8(A1[1][0], A1[1][1]), U1[1], acc[2]); SCHEDB();
        acc[3] = MFMA16(cat8(A1[1][0], A1[1][1]), U1[2], acc[3]); SCHEDB();
        acc[4] = MFMA16(cat8(A1[1][0], A1[1][1]), U1[3], acc[4]); SCHEDB();
        acc[5] = MFMA16(cat8(A1[1][0], A1[1][1]), U1[4], acc[5]); SCHEDB();
        acc[6] = MFMA16(cat8(A1[1][0], A1[1][1]), U1[5], acc[6]); SCHEDB();
        acc[7] = MFMA16(cat8(A1[1][0], A1[1][1]), U1[6], acc[7]); SCHEDB();
        acc[2] = MFMA16(cat8(A1[2][0], A1[2][1]), U1[0], acc[2]); SCHEDB();
        acc[3] = MFMA16(cat8(A1[2][0], A1[2][1]), U1[1], acc[3]); SCHEDB();
        acc[4] = MFMA16(cat8(A1[2][0], A1[2][1]), U1[2], acc[4]); SCHEDB();
        acc[5] = MFMA16(cat8(A1[2][0], A1[2][1]), U1[3], acc[5]); SCHEDB();
        acc[6] = MFMA16(cat8(A1[2][0], A1[2][1]), U1[4], acc[6]); SCHEDB();
        acc[7] = MFMA16(cat8(A1[2][0], A1[2][1]), U1[5], acc[7]); SCHEDB();
        acc[3] = MFMA16(cat8(A1[3][0], A1[3][1]), U1[0], acc[3]); SCHEDB();
        acc[4] = MFMA16(cat8(A1[3][0], A1[3][1]), U1[1], acc[4]); SCHEDB();
        acc[5] = MFMA16(cat8(A1[3][0], A1[3][1]), U1[2], acc[5]); SCHEDB();
        acc[6] = MFMA16(cat8(A1[3][0], A1[3][1]), U1[3], acc[6]); SCHEDB();
        acc[7] = MFMA16(cat8(A1[3][0], A1[3][1]), U1[4], acc[7]); SCHEDB();
        acc[4] = MFMA16(cat8(A1[4][0], A1[4][1]), U1[0], acc[4]); SCHEDB();
        acc[5] = MFMA16(cat8(A1[4][0], A1[4][1]), U1[1], acc[5]); SCHEDB();
        acc[6] = MFMA16(cat8(A1[4][0], A1[4][1]), U1[2], acc[6]); SCHEDB();
        acc[7] = MFMA16(cat8(A1[4][0], A1[4][1]), U1[3], acc[7]); SCHEDB();
        acc[5] = MFMA16(cat8(A1[5][0], A1[5][1]), U1[0], acc[5]); SCHEDB();
        acc[6] = MFMA16(cat8(A1[5][0], A1[5][1]), U1[1], acc[6]); SCHEDB();
        acc[7] = MFMA16(cat8(A1[5][0], A1[5][1]), U1[2], acc[7]); SCHEDB();
        acc[6] = MFMA16(cat8(A1[6][0], A1[6][1]), U1[0], acc[6]); SCHEDB();
        acc[7] = MFMA16(cat8(A1[6][0], A1[6][1]), U1[1], acc[7]); SCHEDB();
        acc[7] = MFMA16(cat8(A1[7][0], A1[7][1]), U1[0], acc[7]); SCHEDB();
    }
    STAMP(ws, 0, 5);
    lds_sync(smem, sb, 8, lane);
    {
        char* rb = smem + sB + w * 6144 + lane * 16;
        int slot = 0;
#pragma unroll
        for (int tau = 0; tau < 8; ++tau) { if ((tau >> 1) != w) { *(f32x4*)(rb + slot * 1024) = acc[tau]; ++slot; } }
    }
    lds_sync(smem, sb, 12, lane);
    {
#pragma unroll
        for (int i = 0; i < 2; ++i) {
            f32x4 y;
            if (w == 0) y = acc[i]; else if (w == 1) y = acc[2 + i]; else if (w == 2) y = acc[4 + i]; else y = acc[6 + i];
            const int tau = 2 * w + i;
#pragma unroll
            for (int ow = 0; ow < 4; ++ow) {
                if (ow != w) {
                    const int sl = (tau < 2 * ow) ? tau : tau - 2;
                    y += *(const f32x4*)(smem + sB + ow * 6144 + sl * 1024 + lane * 16);
                }
            }
            const int l0 = 256 * tau + 16 * al + 4 * g;
            const u32x2 uv = *(const u32x2*)(smem + sU + 480 + 2 * l0), qv = *(const u32x2*)(smem + sU + 4608 + 2 * l0);
            float hv[4];
#pragma unroll
            for (int r = 0; r < 4; ++r) {
                const u32 uu = uv[r >> 1], qq = qv[r >> 1];
                const float u = (r & 1) ? bfhi(uu) : bflo(uu), q0 = (r & 1) ? bfhi(qq) : bflo(qq);
                const u32 yk = cvtpk(y[r], u * dsk);
                hv[r] = q0 + rbf(bflo(yk) + bfhi(yk));
            }
            st8<16>(wsr, WS_HC + (d * L_ + l0) * 2, u32x2{cvtpk(hv[0], hv[1]), cvtpk(hv[2], hv[3])});
        }
    }
    STAMP(ws, 0, 6);
    return tag;
}

#define G1_UNROLL 1
constexpr int G1_STAGE = 40960, G1_NS = 4, G1_NST = 10;
constexpr int G1_LDS = G1_NS * G1_STAGE;

DI float phase_gemm1(char* __restrict__ ws, char* smem, u32 tag) {
    const int b = blockIdx.x, t = threadIdx.x, lane = t & 63;
    const int w = __builtin_amdgcn_readfirstlane(t >> 6);
    const int xcd = b & 7, j = b >> 3;
    const int P = (xcd & 1) * 8 + (j & 7), S = (xcd >> 1) * 4 + (j >> 3);
    const u32 lds0 = (u32)(uintptr_t)smem;
    const rsrc_t wsr = mkrsrc(ws, WS_END);
    WAITV(0);
    BAR();
    if (t == 0) st_flag(ws, WS_F2 + b * 128, tag);
    if (w == 8 || w == 9) poll_flags(wsr, WS_FW + (w - 8) * 64 * 128, 64, tag, lane);
    BAR();
    if (w >= 4) {
        const int lw = w - 4;
        const char* wsrc = ws + WS_W1T + S * 10 * 24576 + lw * 1024 + lane * 16;
        const int hk = 16 * ((lw >> 2) & 1) + 4 * ((lw >> 1) & 1) + 8 * ((lane >> 3) & 1) + ((lane >> 1) & 3);
        const char* hsrc = ws + WS_HC + (hk * L_ + 128 * P + 64 * (lw & 1) + 16 * (lane >> 4) + 8 * (lane & 1)) * 2;
        auto issue_h = [&](int st, int slot) {
#pragma unroll
            for (int i = 0; i < 2; ++i) glds16(hsrc + (st * 64 + 32 * i) * L_ * 2, lds0 + slot * G1_STAGE + 24576 + (lw + 8 * i) * 1024);
        };
        auto issue_w = [&](int st, int slot) {
#pragma unroll
            for (int i = 0; i < 3; ++i) glds16(wsrc + st * 24576 + i * 8192, lds0 + slot * G1_STAGE + lw * 1024 + i * 8192);
        };
        issue_w(0, 0); issue_w(1, 1); issue_w(2, 2); issue_w(3, 3);
        BAR();
        if (b == 0 && t == 256) st_flag(ws, WS_EP, tag);
        issue_h(0, 0); issue_h(1, 1); issue_h(2, 2); issue_h(3, 3);
        waitv<6>(); BAR();
        waitv<4>(); BAR(); issue_h(4, 0); issue_w(4, 0);
        waitv<7>(); BAR(); issue_h(5, 1); issue_w(5, 1);
        waitv<10>(); BAR(); issue_h(6, 2); issue_w(6, 2);
        waitv<10>(); BAR(); issue_h(7, 3); issue_w(7, 3);
        waitv<10>(); BAR(); issue_h(8, 0); issue_w(8, 0);
        waitv<10>(); BAR(); issue_h(9, 1); issue_w(9, 1);
        waitv<10>(); BAR();
        waitv<5>(); BAR();
        waitv<0>(); BAR();
        BAR();
        BAR();
        if (lw == 0) {
            const int r = lane & 7, part = lane >> 3;
            float s1 = 0.f, s2 = 0.f;
#pragma unroll
            for (int i = 0; i < 8; ++i) {
                const int e = 8 * part + i;
                const f32x4 v = *(const f32x4*)(smem + ((e >> 4) * 64 + (e & 15) * 4 + (r >> 1)) * 16);
                s1 += (r & 1) ? v[1] : v[0]; s2 += (r & 1) ? v[3] : v[2];
            }
            s1 = dppadd<0x128>(s1); s2 = dppadd<0x128>(s2);
            s1 = swap32add(swap16add(s1)); s2 = swap32add(swap16add(s2));
            const float mean = s1 * (1.f / 640.f);
            float var = s2 * (1.f / 640.f) - mean * mean;
            var = var > 0.f ? var : 0.f;
            const float rs = 1.f / rbf(__builtin_sqrtf(rbf(rbf(var) + 1e-5f)));
            return rs * rs;
        }
        return 0.f;
    }
    poll_flags(wsr, WS_F2 + w * 64 * 128, 64, tag, lane);
    BAR();
    STAMP(ws, 4, 0);
    const int wm = w & 1, wn = w >> 1;
    const int g = lane >> 4, q = (lane & 15) >> 2, p = lane & 3;
    f32x4 acc[6][4];
#pragma unroll
    for (int a = 0; a < 6; ++a)
#pragma unroll
        for (int c = 0; c < 4; ++c) acc[a][c] = f32x4{0.f, 0.f, 0.f, 0.f};
    const LAS char* abase = (const LAS char*)smem + wm * 6144 + lane * 16;
    const LAS char* bbase = (const LAS char*)smem + 24576 + (g >> 1) * 4096 + (g & 1) * 128 + q * 32 + 8 * p + wn * 1024;
    const LAS char* sbase = (const LAS char*)smem + 24576 + (2 * w + (lane >> 5)) * 2048 + (S >> 1) * 256 + ((lane >> 2) & 7) * 32 + (S & 1) * 16 + (lane & 3) * 4;
    float s1a = 0.f, s1b = 0.f, s2a = 0.f, s2b = 0.f;
    bf16x8 AL[3], AH[3]; s16x4 B0[4][2], B1[4][2];
    u32 sv;
    BAR();
    STAMP(ws, 4, 1);
    AL[0] = *(const LAS bf16x8*)(abase + 0); B0[0][0] = trread(bbase + 0); B0[0][1] = trread(bbase + 2048); B0[1][0] = trread(bbase + 256); B0[1][1] = trread(bbase + 2304); AL[1] = *(const LAS bf16x8*)(abase + 1024); B0[2][0] = trread(bbase + 512); B0[2][1] = trread(bbase + 2560); B0[3][0] = trread(bbase + 768); B0[3][1] = trread(bbase + 2816); AL[2] = *(const LAS bf16x8*)(abase + 2048);
    SCHEDB();
#pragma unroll 1
    for (int st = 0; st < G1_NST; ++st) {
        acc[0+0][0] = MFMA16(AL[0], cat8(B0[0][0], B0[0][1]), acc[0+0][0]);
        AH[0] = *(const LAS bf16x8*)(abase + 3072);
        SCHEDB();
        acc[0+0][1] = MFMA16(AL[0], cat8(B0[1][0], B0[1][1]), acc[0+0][1]);
        AH[1] = *(const LAS bf16x8*)(abase + 4096);
        SCHEDB();
        acc[0+1][0] = MFMA16(AL[1], cat8(B0[0][0], B0[0][1]), acc[0+1][0]);
        B1[0][0] = trread(bbase + 8192);
        SCHEDB();
        acc[0+1][1] = MFMA16(AL[1], cat8(B0[1][0], B0[1][1]), acc[0+1][1]);
        B1[0][1] = trread(bbase + 10240);
        SCHEDB();
        acc[0+0][2] = MFMA16(AL[0], cat8(B0[2][0], B0[2][1]), acc[0+0][2]);
        B1[1][0] = trread(bbase + 8448);
        SCHEDB();
        acc[0+1][2] = MFMA16(AL[1], cat8(B0[2][0], B0[2][1]), acc[0+1][2]);
        B1[1][1] = trread(bbase + 10496);
        SCHEDB();
        acc[0+0][3] = MFMA16(AL[0], cat8(B0[3][0], B0[3][1]), acc[0+0][3]);
        AH[2] = *(const LAS bf16x8*)(abase + 5120);
        SCHEDB();
        acc[0+1][3] = MFMA16(AL[1], cat8(B0[3][0], B0[3][1]), acc[0+1][3]);
        SCHEDB();
        acc[0+2][0] = MFMA16(AL[2], cat8(B0[0][0], B0[0][1]), acc[0+2][0]);
        SCHEDB();
        acc[0+2][1] = MFMA16(AL[2], cat8(B0[1][0], B0[1][1]), acc[0+2][1]);
        SCHEDB();
        acc[0+2][2] = MFMA16(AL[2], cat8(B0[2][0], B0[2][1]), acc[0+2][2]);
        SCHEDB();
        acc[0+2][3] = MFMA16(AL[2], cat8(B0[3][0], B0[3][1]), acc[0+2][3]);
        SCHEDB();
        acc[3+0][0] = MFMA16(AH[0], cat8(B0[0][0], B0[0][1]), acc[3+0][0]);
        AL[0] = *(const LAS bf16x8*)(abase + 12288);
        SCHEDB();
        acc[3+0][1] = MFMA16(AH[0], cat8(B0[1][0], B0[1][1]), acc[3+0][1]);
        AL[1] = *(const LAS bf16x8*)(abase + 13312);
        SCHEDB();
        acc[3+1][0] = MFMA16(AH[1], cat8(B0[0][0], B0[0][1]), acc[3+1][0]);
        sv = *(const LAS u32*)(sbase);
        SCHEDB();
        acc[3+1][1] = MFMA16(AH[1], cat8(B0[1][0], B0[1][1]), acc[3+1][1]);
        B1[2][0] = trread(bbase + 8704);
        SCHEDB();
        acc[3+0][2] = MFMA16(AH[0], cat8(B0[2][0], B0[2][1]), acc[3+0][2]);
        B1[2][1] = trread(bbase + 10752);
        SCHEDB();
        acc[3+1][2] = MFMA16(AH[1], cat8(B0[2][0], B0[2][1]), acc[3+1][2]);
        B1[3][0] = trread(bbase + 8960);
        SCHEDB();
        acc[3+0][3] = MFMA16(AH[0], cat8(B0[3][0], B0[3][1]), acc[3+0][3]);
        B1[3][1] = trread(bbase + 11008);
        SCHEDB();
        acc[3+1][3] = MFMA16(AH[1], cat8(B0[3][0], B0[3][1]), acc[3+1][3]);
        AL[2] = *(const LAS bf16x8*)(abase + 14336);
        SCHEDB();
        acc[3+2][0] = MFMA16(AH[2], cat8(B0[0][0], B0[0][1]), acc[3+2][0]);
        SCHEDB();
        acc[3+2][1] = MFMA16(AH[2], cat8(B0[1][0], B0[1][1]), acc[3+2][1]);
        SCHEDB();
        acc[3+2][2] = MFMA16(AH[2], cat8(B0[2][0], B0[2][1]), acc[3+2][2]);
        SCHEDB();
        acc[3+2][3] = MFMA16(AH[2], cat8(B0[3][0], B0[3][1]), acc[3+2][3]);
        SCHEDB();
        acc[0+0][0] = MFMA16(AL[0], cat8(B1[0][0], B1[0][1]), acc[0+0][0]);
        AH[0] = *(const LAS bf16x8*)(abase + 15360);
        SCHEDB();
        acc[0+0][1] = MFMA16(AL[0], cat8(B1[1][0], B1[1][1]), acc[0+0][1]);
        AH[1] = *(const LAS bf16x8*)(abase + 16384);
        SCHEDB();
        acc[0+1][0] = MFMA16(AL[1], cat8(B1[0][0], B1[0][1]), acc[0+1][0]);
        AH[2] = *(const LAS bf16x8*)(abase + 17408);
        SCHEDB();
        acc[0+1][1] = MFMA16(AL[1], cat8(B1[1][0], B1[1][1]), acc[0+1][1]);
        SCHEDB();
        acc[0+0][2] = MFMA16(AL[0], cat8(B1[2][0], B1[2][1]), acc[0+0][2]);
        SCHEDB();
        acc[0+1][2] = MFMA16(AL[1], cat8(B1[2][0], B1[2][1]), acc[0+1][2]);
        SCHEDB();
        acc[0+0][3] = MFMA16(AL[0], cat8(B1[3][0], B1[3][1]), acc[0+0][3]);
        SCHEDB();
        acc[0+1][3] = MFMA16(AL[1], cat8(B1[3][0], B1[3][1]), acc[0+1][3]);
        SCHEDB();
        acc[0+2][0] = MFMA16(AL[2], cat8(B1[0][0], B1[0][1]), acc[0+2][0]);
        SCHEDB();
        acc[0+2][1] = MFMA16(AL[2], cat8(B1[1][0], B1[1][1]), acc[0+2][1]);
        SCHEDB();
        acc[0+2][2] = MFMA16(AL[2], cat8(B1[2][0], B1[2][1]), acc[0+2][2]);
        SCHEDB();
        acc[0+2][3] = MFMA16(AL[2], cat8(B1[3][0], B1[3][1]), acc[0+2][3]);
        SCHEDB();
        { const float a = bflo(sv), c = bfhi(sv); s1a += a; s1b += c; s2a = __builtin_fmaf(a, a, s2a); s2b = __builtin_fmaf(c, c, s2b); }
        WAITL0();
        BAR();
        { const int dlt = (st & 3) == 3 ? -3 * G1_STAGE : G1_STAGE; abase += dlt; bbase += dlt; sbase += dlt; }
        acc[3+0][0] = MFMA16(AH[0], cat8(B1[0][0], B1[0][1]), acc[3+0][0]);
        AL[0] = *(const LAS bf16x8*)(abase + 0);
        SCHEDB();
        acc[3+0][1] = MFMA16(AH[0], cat8(B1[1][0], B1[1][1]), acc[3+0][1]);
        B0[0][0] = trread(bbase + 0);
        SCHEDB();
        acc[3+1][0] = MFMA16(AH[1], cat8(B1[0][0], B1[0][1]), acc[3+1][0]);
        B0[0][1] = trread(bbase + 2048);
        SCHEDB();
        acc[3+1][1] = MFMA16(AH[1], cat8(B1[1][0], B1[1][1]), acc[3+1][1]);
        B0[1][0] = trread(bbase + 256);
        SCHEDB();
        acc[3+0][2] = MFMA16(AH[0], cat8(B1[2][0], B1[2][1]), acc[3+0][2]);
        B0[1][1] = trread(bbase + 2304);
        SCHEDB();
        acc[3+1][2] = MFMA16(AH[1], cat8(B1[2][0], B1[2][1]), acc[3+1][2]);
        AL[1] = *(const LAS bf16x8*)(abase + 1024);
        SCHEDB();
        acc[3+0][3] = MFMA16(AH[0], cat8(B1[3][0], B1[3][1]), acc[3+0][3]);
        B0[2][0] = trread(bbase + 512);
        SCHEDB();
        acc[3+1][3] = MFMA16(AH[1], cat8(B1[3][0], B1[3][1]), acc[3+1][3]);
        B0[2][1] = trread(bbase + 2560);
        SCHEDB();
        acc[3+2][0] = MFMA16(AH[2], cat8(B1[0][0], B1[0][1]), acc[3+2][0]);
        B0[3][0] = trread(bbase + 768);
        SCHEDB();
        acc[3+2][1] = MFMA16(AH[2], cat8(B1[1][0], B1[1][1]), acc[3+2][1]);
        B0[3][1] = trread(bbase + 2816);
        SCHEDB();
        acc[3+2][2] = MFMA16(AH[2], cat8(B1[2][0], B1[2][1]), acc[3+2][2]);
        AL[2] = *(const LAS bf16x8*)(abase + 2048);
        SCHEDB();
        acc[3+2][3] = MFMA16(AH[2], cat8(B1[3][0], B1[3][1]), acc[3+2][3]);
        SCHEDB();
    }
    STAMP(ws, 4, 3);
    *(f32x4*)(smem + (w * 64 + lane) * 16) = f32x4{s1a, s1b, s2a, s2b};
    WAITL0();
    BAR();
    const rsrc_t rs = wsr;
#pragma unroll
    for (int a = 0; a < 3; ++a) {
        const int f0 = 96 * S + 16 * (3 * wm + a);
        const int kb = f0 >> 5, oct = ((f0 & 31) >> 3) + (g >> 1);
#pragma unroll
        for (int c = 0; c < 4; c += 2) {
            u32 X[2], Y[2];
#pragma unroll
            for (int e = 0; e < 2; ++e) {
                X[e] = cvtpk(__builtin_fmaxf(acc[2 * a][c][2 * e], 0.f) * acc[2 * a + 1][c][2 * e], __builtin_fmaxf(acc[2 * a][c][2 * e + 1], 0.f) * acc[2 * a + 1][c][2 * e + 1]);
                Y[e] = cvtpk(__builtin_fmaxf(acc[2 * a][c + 1][2 * e], 0.f) * acc[2 * a + 1][c + 1][2 * e], __builtin_fmaxf(acc[2 * a][c + 1][2 * e + 1], 0.f) * acc[2 * a + 1][c + 1][2 * e + 1]);
            }
#pragma unroll
            for (int e = 0; e < 2; ++e) { auto r = __builtin_amdgcn_permlane16_swap(X[e], Y[e], false, false); X[e] = r[0]; Y[e] = r[1]; }
            const int lt = 8 * P + 4 * wn + c + (g & 1);
            st16<16>(rs, WS_HM + ((lt * 48 + kb) * 64 + (lane & 15) + 16 * oct) * 16, u32x4{X[0], X[1], Y[0], Y[1]});
        }
    }
    STAMP(ws, 4, 4);
    return 0.f;
}

constexpr int G2_STAGE = 18432, G2_NS = 8, G2_NST = 24;
constexpr int G2_RES = G2_NS * G2_STAGE;
constexpr int G2_LDS = G2_RES + 10240;

DI void phase_gemm2(char* __restrict__ ws, u16* __restrict__ out, char* smem, u32 tag, float rs2pub) {
    const int b = blockIdx.x, t = threadIdx.x, lane = t & 63;
    const int w = __builtin_amdgcn_readfirstlane(t >> 6);
    const int xcd = b & 7, j = b >> 3;
    const int P = (xcd & 3) * 8 + (j & 7), S = (xcd >> 2) * 4 + (j >> 3);
    const u32 lds0 = (u32)(uintptr_t)smem;
    const rsrc_t wsr = mkrsrc(ws, WS_END);
    WAITV(0);
    BAR();
    if (w == 4) {
        const int P1 = (xcd & 1) * 8 + (j & 7), S1 = (xcd >> 1) * 4 + (j >> 3);
        const u32 up1 = __builtin_amdgcn_update_dpp(0, __float_as_uint(rs2pub), 0x111, 0xF, 0xF, true);
        if (lane < 9) st4<16>(wsr, WS_F3 + (P1 * 16 + S1) * 128 + lane * 4, lane == 0 ? tag : up1);
    }
    if (w >= 4) {
        const int lw = w - 4;
        const char* wsrc = ws + WS_W2T + S * 24 * 10240 + lw * 1024 + lane * 16;
        const char* msrc = ws + WS_HM + ((4 * P + (lw >> 1)) * 48 + (lw & 1)) * 1024 + lane * 16;
        auto issue_m = [&](int st, int slot) { glds16(msrc + st * 2048, lds0 + slot * G2_STAGE + 10240 + lw * 1024); };
        auto issue_w = [&](int st, int slot) {
            glds16(wsrc + st * 10240, lds0 + slot * G2_STAGE + lw * 1024);
            if (lw < 2) glds16(wsrc + st * 10240 + 8192, lds0 + slot * G2_STAGE + 8192 + lw * 1024);
        };
        if (lw < 2) {
            issue_w(0, 0);
            issue_w(1, 1);
            issue_w(2, 2);
            issue_w(3, 3);
            issue_w(4, 4);
            issue_w(5, 5);
            issue_w(6, 6);
            issue_w(7, 7);
            BAR();
            issue_m(0, 0);
            issue_m(1, 1);
            issue_m(2, 2);
            issue_m(3, 3);
            issue_m(4, 4);
            issue_m(5, 5);
            issue_m(6, 6);
            issue_m(7, 7);
            waitv<7>(); BAR();
            waitv<6>(); BAR();
            waitv<5>(); BAR();
            issue_m(8, 0); issue_w(8, 0);
            waitv<7>(); BAR();
            issue_m(9, 1); issue_w(9, 1);
            waitv<9>(); BAR();
            issue_m(10, 2); issue_w(10, 2);
            waitv<11>(); BAR();
            issue_m(11, 3); issue_w(11, 3);
            waitv<13>(); BAR();
            issue_m(12, 4); issue_w(12, 4);
            waitv<15>(); BAR();
            issue_m(13, 5); issue_w(13, 5);
            waitv<15>(); BAR();
            issue_m(14, 6); issue_w(14, 6);
            waitv<15>(); BAR();
            issue_m(15, 7); issue_w(15, 7);
            waitv<15>(); BAR();
            issue_m(16, 0); issue_w(16, 0);
            waitv<15>(); BAR();
            issue_m(17, 1); issue_w(17, 1);
            waitv<15>(); BAR();
            issue_m(18, 2); issue_w(18, 2);
            waitv<15>(); BAR();
            issue_m(19, 3); issue_w(19, 3);
            waitv<15>(); BAR();
            issue_m(20, 4); issue_w(20, 4);
            waitv<15>(); BAR();
            issue_m(21, 5); issue_w(21, 5);
            waitv<15>(); BAR();
            issue_m(22, 6); issue_w(22, 6);
            waitv<15>(); BAR();
            issue_m(23, 7); issue_w(23, 7);
            waitv<15>(); BAR();
            waitv<12>(); BAR();
            waitv<9>(); BAR();
            waitv<6>(); BAR();
            waitv<3>(); BAR();
            waitv<0>(); BAR();
        } else {
            issue_w(0, 0);
            issue_w(1, 1);
            issue_w(2, 2);
            issue_w(3, 3);
            issue_w(4, 4);
            issue_w(5, 5);
            issue_w(6, 6);
            issue_w(7, 7);
            BAR();
            issue_m(0, 0);
            issue_m(1, 1);
            issue_m(2, 2);
            issue_m(3, 3);
            issue_m(4, 4);
            issue_m(5, 5);
            issue_m(6, 6);
            issue_m(7, 7);
            waitv<7>(); BAR();
            waitv<6>(); BAR();
            waitv<5>(); BAR();
            issue_m(8, 0); issue_w(8, 0);
            waitv<6>(); BAR();
            issue_m(9, 1); issue_w(9, 1);
            waitv<7>(); BAR();
            issue_m(10, 2); issue_w(10, 2);
            waitv<8>(); BAR();
            issue_m(11, 3); issue_w(11, 3);
            waitv<9>(); BAR();
            issue_m(12, 4); issue_w(12, 4);
            waitv<10>(); BAR();
            issue_m(13, 5); issue_w(13, 5);
            waitv<10>(); BAR();
            issue_m(14, 6); issue_w(14, 6);
            waitv<10>(); BAR();
            issue_m(15, 7); issue_w(15, 7);
            waitv<10>(); BAR();
            issue_m(16, 0); issue_w(16, 0);
            waitv<10>(); BAR();
            issue_m(17, 1); issue_w(17, 1);
            waitv<10>(); BAR();
            issue_m(18, 2); issue_w(18, 2);
            waitv<10>(); BAR();
            issue_m(19, 3); issue_w(19, 3);
            waitv<10>(); BAR();
            issue_m(20, 4); issue_w(20, 4);
            waitv<10>(); BAR();
            issue_m(21, 5); issue_w(21, 5);
            waitv<10>(); BAR();
            issue_m(22, 6); issue_w(22, 6);
            waitv<10>(); BAR();
            issue_m(23, 7); issue_w(23, 7);
            waitv<10>(); BAR();
            waitv<8>(); BAR();
            waitv<6>(); BAR();
            waitv<4>(); BAR();
            waitv<2>(); BAR();
            waitv<0>(); BAR();
        }
        return;
    }
    if (w == 0) poll_flags(wsr, WS_F3 + (P >> 1) * 16 * 128, 16, tag, lane);
    BAR();
#pragma unroll
    for (int i = 0; i < 3; ++i) {
        const int row = 32 * i + (lane >> 1);
        if (row < 80) glds16(ws + WS_HC + ((80 * S + row) * L_ + 64 * P + 16 * w + 8 * (lane & 1)) * 2, lds0 + G2_RES + w * 2560 + i * 1024);
    }
    STAMP(ws, 5, 0);
    const int g = lane >> 4, q = (lane & 15) >> 2, p = lane & 3;
    const int l = 64 * P + 16 * w + (lane & 15);
    const float rs2 = __uint_as_float(__builtin_amdgcn_raw_buffer_load_b32(wsr, WS_F3 + (l >> 3) * 128 + 4 + (l & 7) * 4, 0, 16));
    const float m1 = bflo(__builtin_amdgcn_raw_buffer_load_b32(wsr, WS_GR + (l >> 3) * 128 + (l & 7) * 8, 0, 16));
    f32x4 acc[5];
#pragma unroll
    for (int a = 0; a < 5; ++a) acc[a] = f32x4{0.f, 0.f, 0.f, 0.f};
    const LAS char* abase = (const LAS char*)smem + lane * 16;
    const LAS char* bbase = (const LAS char*)smem + 10240 + w * 2048 + lane * 16;
    bf16x8 EA[2][5], EB[2], OA[2][5], OB[2];
    BAR();
    STAMP(ws, 5, 1);
    EB[0] = *(const LAS bf16x8*)(bbase); EB[1] = *(const LAS bf16x8*)(bbase + 1024);
#pragma unroll
    for (int a = 0; a < 5; ++a) { EA[0][a] = *(const LAS bf16x8*)(abase + a * 1024); EA[1][a] = *(const LAS bf16x8*)(abase + 5120 + a * 1024); }
    SCHEDB();
#pragma unroll 1
    for (int it = 0; it < 3; ++it) {
        if (it == 1) STAMP(ws, 5, 2); if (it == 2) STAMP(ws, 5, 3);
        BAR();
        acc[0] = MFMA16(EA[0][0], EB[0], acc[0]);
        OB[0] = *(const LAS bf16x8*)(bbase + 1*G2_STAGE);
        OA[0][0] = *(const LAS bf16x8*)(abase + 1*G2_STAGE + 0);
        SCHEDB();
        acc[1] = MFMA16(EA[0][1], EB[0], acc[1]);
        OA[0][1] = *(const LAS bf16x8*)(abase + 1*G2_STAGE + 1024);
        OA[0][2] = *(const LAS bf16x8*)(abase + 1*G2_STAGE + 2048);
        SCHEDB();
        acc[2] = MFMA16(EA[0][2], EB[0], acc[2]);
        OA[0][3] = *(const LAS bf16x8*)(abase + 1*G2_STAGE + 3072);
        SCHEDB();
        acc[3] = MFMA16(EA[0][3], EB[0], acc[3]);
        OA[0][4] = *(const LAS bf16x8*)(abase + 1*G2_STAGE + 4096);
        SCHEDB();
        acc[4] = MFMA16(EA[0][4], EB[0], acc[4]);
        OB[1] = *(const LAS bf16x8*)(bbase + 1*G2_STAGE + 1024);
        SCHEDB();
        acc[0] = MFMA16(EA[1][0], EB[1], acc[0]);
        OA[1][0] = *(const LAS bf16x8*)(abase + 1*G2_STAGE + 5120);
        SCHEDB();
        acc[1] = MFMA16(EA[1][1], EB[1], acc[1]);
        OA[1][1] = *(const LAS bf16x8*)(abase + 1*G2_STAGE + 6144);
        SCHEDB();
        acc[2] = MFMA16(EA[1][2], EB[1], acc[2]);
        OA[1][2] = *(const LAS bf16x8*)(abase + 1*G2_STAGE + 7168);
        SCHEDB();
        acc[3] = MFMA16(EA[1][3], EB[1], acc[3]);
        OA[1][3] = *(const LAS bf16x8*)(abase + 1*G2_STAGE + 8192);
        SCHEDB();
        acc[4] = MFMA16(EA[1][4], EB[1], acc[4]);
        OA[1][4] = *(const LAS bf16x8*)(abase + 1*G2_STAGE + 9216);
        SCHEDB();
        BAR();
        acc[0] = MFMA16(OA[0][0], OB[0], acc[0]);
        EB[0] = *(const LAS bf16x8*)(bbase + 2*G2_STAGE);
        EA[0][0] = *(const LAS bf16x8*)(abase + 2*G2_STAGE + 0);
        SCHEDB();
        acc[1] = MFMA16(OA[0][1], OB[0], acc[1]);
        EA[0][1] = *(const LAS bf16x8*)(abase + 2*G2_STAGE + 1024);
        EA[0][2] = *(const LAS bf16x8*)(abase + 2*G2_STAGE + 2048);
        SCHEDB();
        acc[2] = MFMA16(OA[0][2], OB[0], acc[2]);
        EA[0][3] = *(const LAS bf16x8*)(abase + 2*G2_STAGE + 3072);
        SCHEDB();
        acc[3] = MFMA16(OA[0][3], OB[0], acc[3]);
        EA[0][4] = *(const LAS bf16x8*)(abase + 2*G2_STAGE + 4096);
        SCHEDB();
        acc[4] = MFMA16(OA[0][4], OB[0], acc[4]);
        EB[1] = *(const LAS bf16x8*)(bbase + 2*G2_STAGE + 1024);
        SCHEDB();
        acc[0] = MFMA16(OA[1][0], OB[1], acc[0]);
        EA[1][0] = *(const LAS bf16x8*)(abase + 2*G2_STAGE + 5120);
        SCHEDB();
        acc[1] = MFMA16(OA[1][1], OB[1], acc[1]);
        EA[1][1] = *(const LAS bf16x8*)(abase + 2*G2_STAGE + 6144);
        SCHEDB();
        acc[2] = MFMA16(OA[1][2], OB[1], acc[2]);
        EA[1][2] = *(const LAS bf16x8*)(abase + 2*G2_STAGE + 7168);
        SCHEDB();
        acc[3] = MFMA16(OA[1][3], OB[1], acc[3]);
        EA[1][3] = *(const LAS bf16x8*)(abase + 2*G2_STAGE + 8192);
        SCHEDB();
        acc[4] = MFMA16(OA[1][4], OB[1], acc[4]);
        EA[1][4] = *(const LAS bf16x8*)(abase + 2*G2_STAGE + 9216);
        SCHEDB();
        BAR();
        acc[0] = MFMA16(EA[0][0], EB[0], acc[0]);
        OB[0] = *(const LAS bf16x8*)(bbase + 3*G2_STAGE);
        OA[0][0] = *(const LAS bf16x8*)(abase + 3*G2_STAGE + 0);
        SCHEDB();
        acc[1] = MFMA16(EA[0][1], EB[0], acc[1]);
        OA[0][1] = *(const LAS bf16x8*)(abase + 3*G2_STAGE + 1024);
        OA[0][2] = *(const LAS bf16x8*)(abase + 3*G2_STAGE + 2048);
        SCHEDB();
        acc[2] = MFMA16(EA[0][2], EB[0], acc[2]);
        OA[0][3] = *(const LAS bf16x8*)(abase + 3*G2_STAGE + 3072);
        SCHEDB();
        acc[3] = MFMA16(EA[0][3], EB[0], acc[3]);
        OA[0][4] = *(const LAS bf16x8*)(abase + 3*G2_STAGE + 4096);
        SCHEDB();
        acc[4] = MFMA16(EA[0][4], EB[0], acc[4]);
        OB[1] = *(const LAS bf16x8*)(bbase + 3*G2_STAGE + 1024);
        SCHEDB();
        acc[0] = MFMA16(EA[1][0], EB[1], acc[0]);
        OA[1][0] = *(const LAS bf16x8*)(abase + 3*G2_STAGE + 5120);
        SCHEDB();
        acc[1] = MFMA16(EA[1][1], EB[1], acc[1]);
        OA[1][1] = *(const LAS bf16x8*)(abase + 3*G2_STAGE + 6144);
        SCHEDB();
        acc[2] = MFMA16(EA[1][2], EB[1], acc[2]);
        OA[1][2] = *(const LAS bf16x8*)(abase + 3*G2_STAGE + 7168);
        SCHEDB();
        acc[3] = MFMA16(EA[1][3], EB[1], acc[3]);
        OA[1][3] = *(const LAS bf16x8*)(abase + 3*G2_STAGE + 8192);
        SCHEDB();
        acc[4] = MFMA16(EA[1][4], EB[1], acc[4]);
        OA[1][4] = *(const LAS bf16x8*)(abase + 3*G2_STAGE + 9216);
        SCHEDB();
        BAR();
        acc[0] = MFMA16(OA[0][0], OB[0], acc[0]);
        EB[0] = *(const LAS bf16x8*)(bbase + 4*G2_STAGE);
        EA[0][0] = *(const LAS bf16x8*)(abase + 4*G2_STAGE + 0);
        SCHEDB();
        acc[1] = MFMA16(OA[0][1], OB[0], acc[1]);
        EA[0][1] = *(const LAS bf16x8*)(abase + 4*G2_STAGE + 1024);
        EA[0][2] = *(const LAS bf16x8*)(abase + 4*G2_STAGE + 2048);
        SCHEDB();
        acc[2] = MFMA16(OA[0][2], OB[0], acc[2]);
        EA[0][3] = *(const LAS bf16x8*)(abase + 4*G2_STAGE + 3072);
        SCHEDB();
        acc[3] = MFMA16(OA[0][3], OB[0], acc[3]);
        EA[0][4] = *(const LAS bf16x8*)(abase + 4*G2_STAGE + 4096);
        SCHEDB();
        acc[4] = MFMA16(OA[0][4], OB[0], acc[4]);
        EB[1] = *(const LAS bf16x8*)(bbase + 4*G2_STAGE + 1024);
        SCHEDB();
        acc[0] = MFMA16(OA[1][0], OB[1], acc[0]);
        EA[1][0] = *(const LAS bf16x8*)(abase + 4*G2_STAGE + 5120);
        SCHEDB();
        acc[1] = MFMA16(OA[1][1], OB[1], acc[1]);
        EA[1][1] = *(const LAS bf16x8*)(abase + 4*G2_STAGE + 6144);
        SCHEDB();
        acc[2] = MFMA16(OA[1][2], OB[1], acc[2]);
        EA[1][2] = *(const LAS bf16x8*)(abase + 4*G2_STAGE + 7168);
        SCHEDB();
        acc[3] = MFMA16(OA[1][3], OB[1], acc[3]);
        EA[1][3] = *(const LAS bf16x8*)(abase + 4*G2_STAGE + 8192);
        SCHEDB();
        acc[4] = MFMA16(OA[1][4], OB[1], acc[4]);
        EA[1][4] = *(const LAS bf16x8*)(abase + 4*G2_STAGE + 9216);
        SCHEDB();
        BAR();
        acc[0] = MFMA16(EA[0][0], EB[0], acc[0]);
        OB[0] = *(const LAS bf16x8*)(bbase + 5*G2_STAGE);
        OA[0][0] = *(const LAS bf16x8*)(abase + 5*G2_STAGE + 0);
        SCHEDB();
        acc[1] = MFMA16(EA[0][1], EB[0], acc[1]);
        OA[0][1] = *(const LAS bf16x8*)(abase + 5*G2_STAGE + 1024);
        OA[0][2] = *(const LAS bf16x8*)(abase + 5*G2_STAGE + 2048);
        SCHEDB();
        acc[2] = MFMA16(EA[0][2], EB[0], acc[2]);
        OA[0][3] = *(const LAS bf16x8*)(abase + 5*G2_STAGE + 3072);
        SCHEDB();
        acc[3] = MFMA16(EA[0][3], EB[0], acc[3]);
        OA[0][4] = *(const LAS bf16x8*)(abase + 5*G2_STAGE + 4096);
        SCHEDB();
        acc[4] = MFMA16(EA[0][4], EB[0], acc[4]);
        OB[1] = *(const LAS bf16x8*)(bbase + 5*G2_STAGE + 1024);
        SCHEDB();
        acc[0] = MFMA16(EA[1][0], EB[1], acc[0]);
        OA[1][0] = *(const LAS bf16x8*)(abase + 5*G2_STAGE + 5120);
        SCHEDB();
        acc[1] = MFMA16(EA[1][1], EB[1], acc[1]);
        OA[1][1] = *(const LAS bf16x8*)(abase + 5*G2_STAGE + 6144);
        SCHEDB();
        acc[2] = MFMA16(EA[1][2], EB[1], acc[2]);
        OA[1][2] = *(const LAS bf16x8*)(abase + 5*G2_STAGE + 7168);
        SCHEDB();
        acc[3] = MFMA16(EA[1][3], EB[1], acc[3]);
        OA[1][3] = *(const LAS bf16x8*)(abase + 5*G2_STAGE + 8192);
        SCHEDB();
        acc[4] = MFMA16(EA[1][4], EB[1], acc[4]);
        OA[1][4] = *(const LAS bf16x8*)(abase + 5*G2_STAGE + 9216);
        SCHEDB();
        BAR();
        acc[0] = MFMA16(OA[0][0], OB[0], acc[0]);
        EB[0] = *(const LAS bf16x8*)(bbase + 6*G2_STAGE);
        EA[0][0] = *(const LAS bf16x8*)(abase + 6*G2_STAGE + 0);
        SCHEDB();
        acc[1] = MFMA16(OA[0][1], OB[0], acc[1]);
        EA[0][1] = *(const LAS bf16x8*)(abase + 6*G2_STAGE + 1024);
        EA[0][2] = *(const LAS bf16x8*)(abase + 6*G2_STAGE + 2048);
        SCHEDB();
        acc[2] = MFMA16(OA[0][2], OB[0], acc[2]);
        EA[0][3] = *(const LAS bf16x8*)(abase + 6*G2_STAGE + 3072);
        SCHEDB();
        acc[3] = MFMA16(OA[0][3], OB[0], acc[3]);
        EA[0][4] = *(const LAS bf16x8*)(abase + 6*G2_STAGE + 4096);
        SCHEDB();
        acc[4] = MFMA16(OA[0][4], OB[0], acc[4]);
        EB[1] = *(const LAS bf16x8*)(bbase + 6*G2_STAGE + 1024);
        SCHEDB();
        acc[0] = MFMA16(OA[1][0], OB[1], acc[0]);
        EA[1][0] = *(const LAS bf16x8*)(abase + 6*G2_STAGE + 5120);
        SCHEDB();
        acc[1] = MFMA16(OA[1][1], OB[1], acc[1]);
        EA[1][1] = *(const LAS bf16x8*)(abase + 6*G2_STAGE + 6144);
        SCHEDB();
        acc[2] = MFMA16(OA[1][2], OB[1], acc[2]);
        EA[1][2] = *(const LAS bf16x8*)(abase + 6*G2_STAGE + 7168);
        SCHEDB();
        acc[3] = MFMA16(OA[1][3], OB[1], acc[3]);
        EA[1][3] = *(const LAS bf16x8*)(abase + 6*G2_STAGE + 8192);
        SCHEDB();
        acc[4] = MFMA16(OA[1][4], OB[1], acc[4]);
        EA[1][4] = *(const LAS bf16x8*)(abase + 6*G2_STAGE + 9216);
        SCHEDB();
        BAR();
        acc[0] = MFMA16(EA[0][0], EB[0], acc[0]);
        OB[0] = *(const LAS bf16x8*)(bbase + 7*G2_STAGE);
        OA[0][0] = *(const LAS bf16x8*)(abase + 7*G2_STAGE + 0);
        SCHEDB();
        acc[1] = MFMA16(EA[0][1], EB[0], acc[1]);
        OA[0][1] = *(const LAS bf16x8*)(abase + 7*G2_STAGE + 1024);
        OA[0][2] = *(const LAS bf16x8*)(abase + 7*G2_STAGE + 2048);
        SCHEDB();
        acc[2] = MFMA16(EA[0][2], EB[0], acc[2]);
        OA[0][3] = *(const LAS bf16x8*)(abase + 7*G2_STAGE + 3072);
        SCHEDB();
        acc[3] = MFMA16(EA[0][3], EB[0], acc[3]);
        OA[0][4] = *(const LAS bf16x8*)(abase + 7*G2_STAGE + 4096);
        SCHEDB();
        acc[4] = MFMA16(EA[0][4], EB[0], acc[4]);
        OB[1] = *(const LAS bf16x8*)(bbase + 7*G2_STAGE + 1024);
        SCHEDB();
        acc[0] = MFMA16(EA[1][0], EB[1], acc[0]);
        OA[1][0] = *(const LAS bf16x8*)(abase + 7*G2_STAGE + 5120);
        SCHEDB();
        acc[1] = MFMA16(EA[1][1], EB[1], acc[1]);
        OA[1][1] = *(const LAS bf16x8*)(abase + 7*G2_STAGE + 6144);
        SCHEDB();
        acc[2] = MFMA16(EA[1][2], EB[1], acc[2]);
        OA[1][2] = *(const LAS bf16x8*)(abase + 7*G2_STAGE + 7168);
        SCHEDB();
        acc[3] = MFMA16(EA[1][3], EB[1], acc[3]);
        OA[1][3] = *(const LAS bf16x8*)(abase + 7*G2_STAGE + 8192);
        SCHEDB();
        acc[4] = MFMA16(EA[1][4], EB[1], acc[4]);
        OA[1][4] = *(const LAS bf16x8*)(abase + 7*G2_STAGE + 9216);
        SCHEDB();
        if (it < 2) BAR();
        acc[0] = MFMA16(OA[0][0], OB[0], acc[0]);
        EB[0] = *(const LAS bf16x8*)(bbase + 0*G2_STAGE);
        EA[0][0] = *(const LAS bf16x8*)(abase + 0*G2_STAGE + 0);
        SCHEDB();
        acc[1] = MFMA16(OA[0][1], OB[0], acc[1]);
        EA[0][1] = *(const LAS bf16x8*)(abase + 0*G2_STAGE + 1024);
        EA[0][2] = *(const LAS bf16x8*)(abase + 0*G2_STAGE + 2048);
        SCHEDB();
        acc[2] = MFMA16(OA[0][2], OB[0], acc[2]);
        EA[0][3] = *(const LAS bf16x8*)(abase + 0*G2_STAGE + 3072);
        SCHEDB();
        acc[3] = MFMA16(OA[0][3], OB[0], acc[3]);
        EA[0][4] = *(const LAS bf16x8*)(abase + 0*G2_STAGE + 4096);
        SCHEDB();
        acc[4] = MFMA16(OA[0][4], OB[0], acc[4]);
        EB[1] = *(const LAS bf16x8*)(bbase + 0*G2_STAGE + 1024);
        SCHEDB();
        acc[0] = MFMA16(OA[1][0], OB[1], acc[0]);
        EA[1][0] = *(const LAS bf16x8*)(abase + 0*G2_STAGE + 5120);
        SCHEDB();
        acc[1] = MFMA16(OA[1][1], OB[1], acc[1]);
        EA[1][1] = *(const LAS bf16x8*)(abase + 0*G2_STAGE + 6144);
        SCHEDB();
        acc[2] = MFMA16(OA[1][2], OB[1], acc[2]);
        EA[1][2] = *(const LAS bf16x8*)(abase + 0*G2_STAGE + 7168);
        SCHEDB();
        acc[3] = MFMA16(OA[1][3], OB[1], acc[3]);
        EA[1][3] = *(const LAS bf16x8*)(abase + 0*G2_STAGE + 8192);
        SCHEDB();
        acc[4] = MFMA16(OA[1][4], OB[1], acc[4]);
        EA[1][4] = *(const LAS bf16x8*)(abase + 0*G2_STAGE + 9216);
        SCHEDB();
    }
    STAMP(ws, 5, 6);
    WAITV(0);
    const rsrc_t ro = mkrsrc(out, L_ * D_ * 2);
#pragma unroll
    for (int a = 0; a < 5; ++a) {
        const s16x4 hr = trread((const LAS char*)smem + G2_RES + w * 2560 + (16 * a + 4 * g + q) * 32 + 8 * p);
        const u32x2 hh = __builtin_bit_cast(u32x2, hr);
        const u32 f01 = cvtpk(acc[a][0] * rs2, acc[a][1] * rs2), f23 = cvtpk(acc[a][2] * rs2, acc[a][3] * rs2);
        const u32 c01 = hh[0], c23 = hh[1];
        const u32 h01 = cvtpk(bflo(c01) + m1, bfhi(c01) + m1), h23 = cvtpk(bflo(c23) + m1, bfhi(c23) + m1);
        __builtin_amdgcn_raw_buffer_store_b64(u32x2{cvtpk(bflo(h01) + bflo(f01), bfhi(h01) + bfhi(f01)), cvtpk(bflo(h23) + bflo(f23), bfhi(h23) + bfhi(f23))}, ro, (l * D_ + 80 * S + 16 * a + 4 * g) * 2, 0, 0);
    }
    STAMP(ws, 5, 7);
}

constexpr int ALL_LDS = 163840;
static_assert(KA_LDS <= ALL_LDS && G1_LDS <= ALL_LDS && G2_LDS <= ALL_LDS, "LDS");

__global__ __launch_bounds__(768, 3) void k_block(const u16* __restrict__ x, const u16* __restrict__ Are, const u16* __restrict__ Aim, const u16* __restrict__ Bre,
                                                  const u16* __restrict__ Bim, const u16* __restrict__ Cre, const u16* __restrict__ Cim, const u16* __restrict__ logdt,
                                                  const u16* __restrict__ Dskip, const u16* __restrict__ Wg, const u16* __restrict__ Wu, const u16* __restrict__ Wd,
                                                  char* __restrict__ ws, u16* __restrict__ out) {
    __shared__ __attribute__((aligned(1024))) char smem[ALL_LDS];
    if (threadIdx.x >= 704) {
        unsigned long long pc; asm volatile("s_getpc_b64 %0" : "=s"(pc));
        const int lane = threadIdx.x & 63, jb = blockIdx.x >> 3;
        const char* p = nullptr;
        if (lane < 12) p = (const char*)((pc & ~127ull) + (unsigned long long)(jb * 12 + lane) * 128);
        else if (lane < 24) {
            const int i = jb * 12 + (lane - 12);
            constexpr int NWS = (int)(WS_END >> 16);
            if (i < NWS) p = ws + ((size_t)i << 16);
            else if (i < NWS + 40) p = (const char*)out + ((size_t)(i - NWS) << 16);
            else if (i < NWS + 70) p = (const char*)Wg + ((size_t)(i - NWS - 40) << 16);
            else if (i < NWS + 100) p = (const char*)Wu + ((size_t)(i - NWS - 70) << 16);
            else if (i < NWS + 130) p = (const char*)Wd + ((size_t)(i - NWS - 100) << 16);
        }
        const u32 dump = (u32)(uintptr_t)smem + ALL_LDS - 256;
        if (p != nullptr) { u32 sv; asm volatile("s_mov_b32 %0, m0\n\ts_mov_b32 m0, %2\n\ts_nop 0\n\tglobal_load_lds_dword %1, off\n\ts_mov_b32 m0, %0" : "=&s"(sv) : "v"(p), "s"(dump) : "memory"); }
    }
    const u32 tag = phase_ssm(x, Are, Aim, Bre, Bim, Cre, Cim, logdt, Dskip, Wg, Wu, Wd, ws, smem);
    const float rs2 = phase_gemm1(ws, smem, tag);
    phase_gemm2(ws, out, smem, tag, rs2);
}

extern "C" void kernel_launch(void* const* d_in, const int* in_sizes, int n_in, void* d_out, int out_size, void* d_ws, size_t ws_size, hipStream_t stream) {
    const u16* x = (const u16*)d_in[0];
    const u16* Are = (const u16*)d_in[1]; const u16* Aim = (const u16*)d_in[2];
    const u16* Bre = (const u16*)d_in[3]; const u16* Bim = (const u16*)d_in[4];
    const u16* Cre = (const u16*)d_in[5]; const u16* Cim = (const u16*)d_in[6];
    const u16* logdt = (const u16*)d_in[7]; const u16* Dskip = (const u16*)d_in[8];
    const u16* Wg = (const u16*)d_in[10]; const u16* Wu = (const u16*)d_in[11]; const u16* Wd = (const u16*)d_in[12];
    k_block<<<256, 768, 0, stream>>>(x, Are, Aim, Bre, Bim, Cre, Cim, logdt, Dskip, Wg, Wu, Wd, (char*)d_ws, (u16*)d_out);
}
```

```cpp
#include <hip/hip_runtime.h>
#include <stdint.h>

typedef unsigned short u16;
typedef unsigned int u32;
typedef short bf16x8 __attribute__((ext_vector_type(8)));
typedef short s16x4 __attribute__((ext_vector_type(4)));
typedef float f32x2 __attribute__((ext_vector_type(2)));
typedef float f32x4 __attribute__((ext_vector_type(4)));
typedef float f32x16 __attribute__((ext_vector_type(16)));
typedef unsigned u32x4 __attribute__((ext_vector_type(4)));
typedef unsigned u32x2 __attribute__((ext_vector_type(2)));
typedef __bf16 bf16x2_t __attribute__((ext_vector_type(2)));
#define LAS __attribute__((address_space(3)))
#define DI __device__ __forceinline__

#define STCOND(k, j) true
#define STAMP(ws, k, j) do {} while (0)
#define STAMP2(ws, k, j) do {} while (0)

#define W2_LATE 2
#define EARLY_W 0
constexpr int L_ = 2048, D_ = 640, NM_ = 192, F_ = 1536;

constexpr u32 WS_HC  = 0;
constexpr u32 WS_HM  = WS_HC + D_ * L_ * 2;
constexpr u32 WS_W1T = WS_HM + L_ * F_ * 2;
constexpr u32 WS_W2T = WS_W1T + D_ * F_ * 2 * 2;
constexpr u32 WS_GR  = WS_W2T + F_ * D_ * 2;
constexpr u32 WS_F2  = WS_GR + 256 * 128;
constexpr u32 WS_FW  = WS_F2 + 256 * 128;
constexpr u32 WS_F3  = WS_FW + 128 * 128;
constexpr u32 WS_EP  = WS_F3 + 256 * 128;
constexpr u32 WS_END = WS_EP + 256;

DI float bflo(u32 v) { return __uint_as_float(v << 16); }
DI float bfhi(u32 v) { return __uint_as_float(v & 0xffff0000u); }
DI float bf2f(u16 v) { return __uint_as_float(((u32)v) << 16); }
DI u32 cvtpk(float lo, float hi) { f32x2 v = {lo, hi}; bf16x2_t r = __builtin_convertvector(v, bf16x2_t); return __builtin_bit_cast(u32, r); }
DI float rbf(float f) { return bflo(cvtpk(f, f)); }

typedef __amdgpu_buffer_rsrc_t rsrc_t;
DI rsrc_t mkrsrc(void* p, u32 bytes) { return __builtin_amdgcn_make_buffer_rsrc(p, 0, bytes, 0x00020000); }
#define AUX_KL 0
#define AUX_WT 0
#define AUX_H 0
#define AUX_HM 0
template <int AUX = 0> DI void st16(rsrc_t r, u32 off, u32x4 v) { __builtin_amdgcn_raw_buffer_store_b128(v, r, off, 0, AUX); }
template <int AUX = 0> DI void st8(rsrc_t r, u32 off, u32x2 v) { __builtin_amdgcn_raw_buffer_store_b64(v, r, off, 0, AUX); }
template <int AUX = 0> DI void st4(rsrc_t r, u32 off, u32 v) { __builtin_amdgcn_raw_buffer_store_b32(v, r, off, 0, AUX); }

template <int CTRL> DI float dppadd(float v) { return v + __builtin_bit_cast(float, __builtin_amdgcn_update_dpp(0, __builtin_bit_cast(int, v), CTRL, 0xF, 0xF, true)); }
DI float swap16add(float v) { auto r = __builtin_amdgcn_permlane16_swap(__float_as_uint(v), __float_as_uint(v), false, false); const u32 a = r[0], b = r[1]; return __uint_as_float(a) + __uint_as_float(b); }
DI float swap32add(float v) { auto r = __builtin_amdgcn_permlane32_swap(__float_as_uint(v), __float_as_uint(v), false, false); const u32 a = r[0], b = r[1]; return __uint_as_float(a) + __uint_as_float(b); }
DI float wave_sum(float v) {
    v = dppadd<0xB1>(v);
    v = dppadd<0x4E>(v);
    v = dppadd<0x141>(v);
    v = dppadd<0x140>(v);
    return swap32add(swap16add(v));
}

struct cm { f32x2 a, s; };
DI cm mkcm(f32x2 b) { return { b, f32x2{-b.y, b.x} }; }
DI f32x2 cmul(f32x2 v, const cm& m) { return v.xx * m.a + v.yy * m.s; }
DI f32x2 csq(f32x2 a) { return f32x2{ a.x * a.x - a.y * a.y, 2.f * a.x * a.y }; }
DI f32x2 cexp_p(float ar, float br, float p) {
    float e = __builtin_amdgcn_exp2f(ar * p);
    float th = __builtin_amdgcn_fractf(br * p);
    return f32x2{ e * __builtin_amdgcn_cosf(th), e * __builtin_amdgcn_sinf(th) };
}

#define WAITV(n) asm volatile("s_waitcnt vmcnt(" #n ")" ::: "memory")
#define WAITL0() asm volatile("s_waitcnt lgkmcnt(0)" ::: "memory")
template <int N> DI void waitv() { asm volatile("s_waitcnt vmcnt(%0)" :: "n"(N) : "memory"); }
#define BAR() do { asm volatile("" ::: "memory"); __builtin_amdgcn_s_barrier(); asm volatile("" ::: "memory"); } while (0)
#define LBAR() do { WAITL0(); BAR(); } while (0)
#define SGB(m, n) __builtin_amdgcn_sched_group_barrier(m, n, 0)
#define SCHEDB() __builtin_amdgcn_sched_barrier(0)
#define MFMA16(a, b, c) __builtin_amdgcn_mfma_f32_16x16x32_bf16(a, b, c, 0, 0, 0)
#define GAS __attribute__((address_space(1)))
typedef unsigned long long u64;

DI void glds16(const void* g, u32 lds_base) {
    u32 sv; asm volatile("s_mov_b32 %0, m0\n\ts_mov_b32 m0, %2\n\ts_nop 0\n\tglobal_load_lds_dwordx4 %1, off sc1\n\ts_mov_b32 m0, %0" : "=&s"(sv) : "v"(g), "s"(lds_base) : "memory");
}
DI void poll_flags(rsrc_t wsr, u32 off, int n, u32 tag, int lane) {
    const u32 o = off + (lane < n ? lane : 0) * 128;
    for (int spin = 0; spin < 100000; ++spin) {
        asm volatile("" ::: "memory");
        const u32 v = __builtin_amdgcn_raw_buffer_load_b32(wsr, o, 0, 16);
        if (__all(v == tag)) break;
        __builtin_amdgcn_s_sleep(1);
    }
    asm volatile("" ::: "memory");
}
DI void st_flag(char* ws, u32 off, u32 tag) { __hip_atomic_store((GAS u32*)(ws + off), tag, __ATOMIC_RELAXED, __HIP_MEMORY_SCOPE_AGENT); }
DI s16x4 trread(const LAS char* p) { typedef short v4i16_t __attribute__((ext_vector_type(4))); return __builtin_bit_cast(s16x4, __builtin_amdgcn_ds_read_tr16_b64_v4i16((LAS v4i16_t*)p)); }
DI bf16x8 cat8(s16x4 a, s16x4 b) { return __builtin_shufflevector(a, b, 0, 1, 2, 3, 4, 5, 6, 7); }

constexpr int KG_PITCH = 784;
constexpr int KA_A = 0, KA_ASZ = 20 * KG_PITCH;
constexpr int KA_B = 3 * KA_ASZ, KA_BSZ = 32 * KG_PITCH;
constexpr int KA_U = KA_B + 3 * KA_BSZ, KA_USZ = 4608 + 4096;
constexpr int KA_CNT = KA_U + 3 * KA_USZ;
constexpr int KA_LDS = KA_CNT + 48;
constexpr int CV_CS = 4160;
constexpr int KB_X = 32256;

#define ADDTID2(base0, v0, o0, base1, v1, o1) asm volatile("s_mov_b32 m0, %2\n\ts_nop 0\n\tds_write_addtid_b32 %0 offset:%4\n\ts_mov_b32 m0, %3\n\ts_nop 0\n\tds_write_addtid_b32 %1 offset:%5" \
    :: "v"(v0), "v"(v1), "s"(base0), "s"(base1), "i"(o0), "i"(o1) : "memory")
#define ADDTID2S(base, v0, o0, v1, o1) asm volatile("s_mov_b32 m0, %2\n\ts_nop 0\n\tds_write_addtid_b32 %0 offset:%3\n\tds_write_addtid_b32 %1 offset:%4" :: "v"(v0), "v"(v1), "s"(base), "i"(o0), "i"(o1) : "memory")

DI void lds_sync(char* smem, int cnt, u32 target, int lane) {
    LAS u32* c = (LAS u32*)((LAS char*)smem + KA_CNT) + cnt;
    WAITL0();
    if (lane == 0) __hip_atomic_fetch_add(c, 1u, __ATOMIC_RELAXED, __HIP_MEMORY_SCOPE_WORKGROUP);
    for (int spin = 0; spin < 1000000; ++spin) {
        if (__builtin_amdgcn_readfirstlane(__hip_atomic_load(c, __ATOMIC_RELAXED, __HIP_MEMORY_SCOPE_WORKGROUP)) >= target) break;
        __builtin_amdgcn_s_sleep(1);
    }
    asm volatile("" ::: "memory");
}

DI void lds_arrive(char* smem, int cnt, int lane) {
    WAITL0();
    if (lane == 0) __hip_atomic_fetch_add((LAS u32*)((LAS char*)smem + KA_CNT) + cnt, 1u, __ATOMIC_RELAXED, __HIP_MEMORY_SCOPE_WORKGROUP);
}
DI void lds_wait(char* smem, int cnt, u32 target) {
    LAS u32* c = (LAS u32*)((LAS char*)smem + KA_CNT) + cnt;
    for (int spin = 0; spin < 1000000; ++spin) {
        if (__builtin_amdgcn_readfirstlane(__hip_atomic_load(c, __ATOMIC_RELAXED, __HIP_MEMORY_SCOPE_WORKGROUP)) >= target) break;
        __builtin_amdgcn_s_sleep(1);
    }
    asm volatile("" ::: "memory");
}

struct KParams { u16 are, aim, bre, bim, cre, cim, ldt; };

DI void ka_kgen(const KParams& P, char* smem, int sA, int sB, int w, int lane) {
    const float are = bf2f(P.are), aim = bf2f(P.aim), bre = bf2f(P.bre), bim = bf2f(P.bim), cre = bf2f(P.cre), cim = bf2f(P.cim);
    const float dt = rbf(__expf(bf2f(P.ldt)));
    const float ar = (dt * are) * 1.4426950408889634f, br = (dt * aim) * 0.15915494309189535f;
    const float dbr = dt * bre, dbi = dt * bim;
    const f32x2 Wc = { cre * dbr - cim * dbi, -(cre * dbi + cim * dbr) };
    const f32x2 l1 = cexp_p(ar, br, 1.f), l8 = cexp_p(ar, br, 8.f), l256 = cexp_p(ar, br, 256.f);
    const f32x2 l2 = csq(l1), l4 = csq(l2), l16 = csq(l8);
    const cm c1 = mkcm(f32x2{l1.x, -l1.y}), c2 = mkcm(f32x2{l2.x, -l2.y}), c4 = mkcm(f32x2{l4.x, -l4.y}), c256 = mkcm(f32x2{l256.x, -l256.y}), m16 = mkcm(l16);
    const u32 lds0 = (u32)(uintptr_t)smem;
    const u32 bA = lds0 + sA + w * 256, bB = lds0 + sB + w * 256 - KB_X;
    auto pk = [](f32x2 v) { return cvtpk(v.x, v.y); };
    constexpr int R = KG_PITCH, X = KB_X;
    f32x2 a0 = Wc, x256 = cmul(Wc, c256), e0 = {1.f, 0.f}, e1 = l8;
    ADDTID2S(bA, pk(a0), 0 * R, pk(x256), 8 * R);
    ADDTID2S(bB, pk(e0), X + 0 * R, pk(e1), X + 1 * R);
    f32x2 y = x256, v = cmul(x256, c256);
#define KG_E(c) do { e0 = cmul(e0, m16); e1 = cmul(e1, m16); ADDTID2S(bB, pk(e0), X + (c) * R, pk(e1), X + ((c) + 1) * R); } while (0)
    a0 = cmul(a0, c1); y = cmul(y, c2); ADDTID2S(bA, pk(a0), 1 * R, pk(y), 9 * R); KG_E(2);
    a0 = cmul(a0, c1); y = cmul(y, c2); ADDTID2S(bA, pk(a0), 2 * R, pk(y), 10 * R); KG_E(4);
    a0 = cmul(a0, c1); y = cmul(y, c2); ADDTID2S(bA, pk(a0), 3 * R, pk(y), 11 * R); KG_E(6);
    a0 = cmul(a0, c1); y = cmul(v, c4); ADDTID2S(bA, pk(a0), 4 * R, pk(v), 12 * R); v = cmul(v, c256); KG_E(8);
    a0 = cmul(a0, c1); ADDTID2S(bA, pk(a0), 5 * R, pk(y), 13 * R); y = cmul(v, c4); KG_E(10);
    a0 = cmul(a0, c1); ADDTID2S(bA, pk(a0), 6 * R, pk(v), 14 * R); v = cmul(v, c256); KG_E(12);
    a0 = cmul(a0, c1); ADDTID2S(bA, pk(a0), 7 * R, pk(y), 15 * R); KG_E(14);
    y = cmul(v, c256); KG_E(16);
    ADDTID2S(bA, pk(v), 16 * R, pk(y), 17 * R); v = cmul(y, c256); KG_E(18);
    y = cmul(v, c256); KG_E(20);
    ADDTID2S(bA, pk(v), 18 * R, pk(y), 19 * R); v = cmul(y, c256); KG_E(22);
    const float k2048 = wave_sum(v.x);
    KG_E(24); KG_E(26);
    KG_E(28); KG_E(30);
    const int r32 = lane & 31, h = lane >> 5;
    const char* pa = smem + sA + (r32 < 20 ? r32 : 19) * R + w * 256 + h * 16;
    const char* pb = smem + sB + r32 * R + w * 256 + h * 16;
    f32x16 acc = {};
#pragma unroll
    for (int s = 0; s < 8; ++s) {
        const bf16x8 a = *(const bf16x8*)(pa + 32 * s), bb = *(const bf16x8*)(pb + 32 * s);
        acc = __builtin_amdgcn_mfma_f32_32x32x16_bf16(a, bb, acc, 0, 0, 0);
    }
    const int c = r32;
    char* sl = smem + sA + w * 256;
    char* p0 = sl + (c >> 3) * R + (8 * (c & 7) + 4 * h) * 4;
    char* p1 = sl + (h ? 6 * R + 8 * c : (4 + (c >> 4)) * R + 16 * (c & 15));
    char* p2 = sl + 8 * R + 4 * c;
#pragma unroll
    for (int q = 0; q < 4; ++q) *(float*)(p0 + 4 * q) = acc[q];
    *(float*)(p1) = acc[4]; *(float*)(p1 + 4) = acc[5];
    *(float*)(p1 + (h ? R : 8)) = acc[6]; *(float*)(p1 + (h ? R + 4 : 12)) = acc[7];
    if (h == 0) { *(float*)(p2) = acc[8]; *(float*)(p2 + 128) = acc[9]; *(float*)(p2 + R) = acc[10]; *(float*)(p2 + R + 128) = acc[11]; }
    else if (c == 0) *(float*)(sl + 10 * R) = k2048;
}

DI void ka_expand(char* smem, int sA, int sB, int w, int lane) {
    const int t = 64 * w + lane;
    auto rd = [&](int i) { const char* p = smem + sA + (i >> 6) * KG_PITCH + (i & 63) * 4; return *(const float*)p + *(const float*)(p + 256) + *(const float*)(p + 512); };
    u32 k4, k5, k6, k7, f2, f3;
    if (w == 0) {
        const int i0 = t < 32 ? 8 * t : 256 + 4 * (t - 32);
        const u32 pat = t < 32 ? 0x76543210u : 0x43222100u;
        float kv[8];
#pragma unroll
        for (int j = 0; j < 8; ++j) kv[j] = rd(i0 + ((pat >> (4 * j)) & 15));
        k4 = cvtpk(kv[0], kv[1]); k5 = cvtpk(kv[2], kv[3]); k6 = cvtpk(kv[4], kv[5]); k7 = cvtpk(kv[6], kv[7]);
        f2 = 0; f3 = 0;
    } else if (w == 1) {
        const int i0 = 384 + 2 * (t - 64);
        const float k0 = rd(i0), k1 = rd(i0 + 1), k2 = rd(i0 + 2);
        k4 = cvtpk(k0, k0); k5 = cvtpk(k0, k1); k6 = cvtpk(k1, k1); k7 = cvtpk(k2, k2);
        const float a = rd(382), b = rd(383);
        f2 = cvtpk(a, a); f3 = cvtpk(b, k0);
    } else {
        const int i0 = 512 + (t - 128);
        const float k0 = rd(i0), k1 = rd(i0 + 1);
        k4 = cvtpk(k0, k0); k5 = k4; k6 = cvtpk((t & 1) ? k1 : k0, k1); k7 = cvtpk(k1, k1);
        const float a = w == 2 ? rd(511) : k0;
        f2 = cvtpk(a, a); f3 = cvtpk(k0, k0);
    }
    const u32 k2 = __builtin_amdgcn_update_dpp(f2, k6, 0x138, 0xF, 0xF, false), k3 = __builtin_amdgcn_update_dpp(f3, k7, 0x138, 0xF, 0xF, false);
    const u32 a2 = __builtin_amdgcn_alignbit(k3, k2, 16), a3 = __builtin_amdgcn_alignbit(k4, k3, 16), a4 = __builtin_amdgcn_alignbit(k5, k4, 16),
              a5 = __builtin_amdgcn_alignbit(k6, k5, 16), a6 = __builtin_amdgcn_alignbit(k7, k6, 16);
    char* kb = smem + sB + 32 + 16 * t;
    *(u32x4*)(kb + 0 * CV_CS) = u32x4{k4, k5, k6, k7};
    *(u32x4*)(kb + 1 * CV_CS) = u32x4{a3, a4, a5, a6};
    *(u32x4*)(kb + 2 * CV_CS) = u32x4{k3, k4, k5, k6};
    *(u32x4*)(kb + 3 * CV_CS) = u32x4{a2, a3, a4, a5};
    if (t < 8) *(u32x4*)(smem + sB + (t >> 1) * CV_CS + 16 * (t & 1)) = u32x4{0, 0, 0, 0};
}

DI u32 phase_ssm(const u16* __restrict__ x, const u16* __restrict__ Are, const u16* __restrict__ Aim, const u16* __restrict__ Bre,
                  const u16* __restrict__ Bim, const u16* __restrict__ Cre, const u16* __restrict__ Cim, const u16* __restrict__ logdt,
                  const u16* __restrict__ Dskip, const u16* __restrict__ Wg, const u16* __restrict__ Wu, const u16* __restrict__ Wd,
                  char* __restrict__ ws, char* smem) {
    const int b = blockIdx.x, t = threadIdx.x, lane = t & 63;
    const int wv = __builtin_amdgcn_readfirstlane(t >> 6), sb = wv >> 2, w = wv & 3;
    const int xcd = b & 7, j = b >> 3;
    const int nch = j < 16 ? 3 : 2;
    const int c0 = 80 * xcd + ((xcd & 2) ? (j < 16 ? 32 + 3 * j : 2 * (j - 16)) : (j < 16 ? 3 * j : 48 + 2 * (j - 16)));
    const int xs = min((2 * c0) & ~3, ((2 * c0) | 127) + 1 - 8), e0 = (2 * c0 - xs) >> 1;
    const bool conv = sb < nch;
    const int d = c0 + sb;
    const int sA = KA_A + sb * KA_ASZ, sB = KA_B + sb * KA_BSZ, sU = KA_U + sb * KA_USZ;
    const rsrc_t wsr = mkrsrc(ws, WS_END);
    STAMP(ws, 0, 0);
    if (t < 8) *(u32*)(smem + KA_CNT + 4 * t) = 0u;

    KParams KP;
    u32 ra[4][5];
    u32x4 wv1[8], wv2[8];
    const int tt = t - 512;
    const bool w1job = (j - 16) < 12;
    float dsk = 0.f;
    if (wv == 3 || wv == 7) {
        const u32* x32 = (const u32*)x;
#pragma unroll
        for (int rr = 0; rr < 4; ++rr)
#pragma unroll
            for (int i = 0; i < 5; ++i) ra[rr][i] = x32[(8 * b + 4 * sb + rr) * 320 + lane + 64 * i];
    }
    if (conv) {
        if (w < 3) {
            const int n = d * NM_ + 64 * w + lane;
            KP.are = Are[n]; KP.aim = Aim[n]; KP.bre = Bre[n]; KP.bim = Bim[n]; KP.cre = Cre[n]; KP.cim = Cim[n]; KP.ldt = logdt[d];
        }
        dsk = bf2f(Dskip[d]);
    }
    const u32 epoch = __hip_atomic_load((GAS u32*)(ws + WS_EP), __ATOMIC_RELAXED, __HIP_MEMORY_SCOPE_AGENT);
    const rsrc_t xr0 = mkrsrc((void*)x, L_ * D_ * 2);
    LBAR();
    u32x2 xg[4];
    const int pi0 = 128 * wv + lane, pi1 = pi0 + 64;
    const bool early = (w != 3) && (nch == 3 || !EARLY_W);
    if (wv < 8 && early) {
        xg[0] = __builtin_amdgcn_raw_buffer_load_b64(xr0, (2 * pi0 + 0) * (D_ * 2) + xs, 0, 0);
        xg[1] = __builtin_amdgcn_raw_buffer_load_b64(xr0, (2 * pi0 + 1) * (D_ * 2) + xs, 0, 0);
    }
    if (conv && w == 3) {
        if (lane < 30) *(u32x4*)(smem + sU + 16 * lane) = u32x4{0, 0, 0, 0};
        lds_arrive(smem, sb, lane);
    }
    if (!conv) {
        if (w1job) {
            const int u1 = xcd * 12 + (j - 16), up = u1 >= 48, col = 32 * (up ? u1 - 48 : u1) + 8 * (tt & 3);
            const u16* src = (up ? Wu : Wg) + (8 * (tt >> 2)) * F_ + col;
#pragma unroll
            for (int r = 0; r < 8; ++r) wv1[r] = *(const u32x4*)(src + r * F_);
            if (w == 0) {
#pragma unroll
                for (int r = 0; r < 8; ++r) wv2[r] = *(const u32x4*)(src + (512 + r) * F_);
            }
        } else {
            if (W2_LATE == 1) lds_wait(smem, 3, 4 * nch); else if (W2_LATE == 2) lds_wait(smem, 4, 1);
            const int n1 = 480 * (xcd * 4 + (j - 28)) + tt, n2 = n1 + 256;
            const u16* s1 = Wd + (8 * (n1 / 80)) * D_ + 8 * (n1 % 80);
#pragma unroll
            for (int r = 0; r < 8; ++r) wv1[r] = *(const u32x4*)(s1 + r * D_);
            if (tt < 224) {
                const u16* s2 = Wd + (8 * (n2 / 80)) * D_ + 8 * (n2 % 80);
#pragma unroll
                for (int r = 0; r < 8; ++r) wv2[r] = *(const u32x4*)(s2 + r * D_);
            }
        }
    }
    const u32 tag = __builtin_amdgcn_readfirstlane(epoch) + 1u;
    if (wv == 3 || wv == 7) {
        __builtin_amdgcn_s_setprio(3);
        float s1[4], s2[4], c0v[4];
#pragma unroll
        for (int rr = 0; rr < 4; ++rr) {
            const float c = bflo(__builtin_amdgcn_readfirstlane(ra[rr][0]));
            float a1 = 0.f, a2 = 0.f;
#pragma unroll
            for (int i = 0; i < 5; ++i) { const float a = bflo(ra[rr][i]) - c, e = bfhi(ra[rr][i]) - c; a1 += a + e; a2 = __builtin_fmaf(a, a, __builtin_fmaf(e, e, a2)); }
            s1[rr] = a1; s2[rr] = a2; c0v[rr] = c;
        }
        auto red4 = [&](const float (&v)[4]) {
            auto p01 = __builtin_amdgcn_permlane32_swap(__float_as_uint(v[0]), __float_as_uint(v[1]), false, false);
            auto p23 = __builtin_amdgcn_permlane32_swap(__float_as_uint(v[2]), __float_as_uint(v[3]), false, false);
            const u32 x0 = p01[0], x1 = p01[1], y0 = p23[0], y1 = p23[1];
            const float t01 = __uint_as_float(x0) + __uint_as_float(x1);
            const float t23 = __uint_as_float(y0) + __uint_as_float(y1);
            auto q = __builtin_amdgcn_permlane16_swap(__float_as_uint(t01), __float_as_uint(t23), false, false);
            const u32 z0 = q[0], z1 = q[1];
            float r = __uint_as_float(z0) + __uint_as_float(z1);
            r = dppadd<0xB1>(r); r = dppadd<0x4E>(r); r = dppadd<0x141>(r); r = dppadd<0x140>(r);
            return r;
        };
        const float n1 = red4(s1) * (1.f / 640.f), n2 = red4(s2) * (1.f / 640.f);
        const int lr = lane >> 4, rr = (lr >> 1) | ((lr & 1) << 1);
        const float cc = rr == 0 ? c0v[0] : rr == 1 ? c0v[1] : rr == 2 ? c0v[2] : c0v[3];
        float var = n2 - n1 * n1; var = var > 0.f ? var : 0.f;
        const float sd = __builtin_sqrtf(rbf(rbf(var) + 1e-5f));
        const u32 pay = cvtpk(cc + n1, sd);
        u32* hand = (u32*)(smem + KA_CNT + 32);
        if (wv == 7) {
            if ((lane & 15) == 0) hand[rr] = pay;
            lds_arrive(smem, 6, lane);
        } else {
            u32 mine = __builtin_amdgcn_ds_bpermute(((lane & 1) << 5 | (lane & 2) << 3) << 2, pay);
            lds_wait(smem, 6, 1);
            if (lane >= 4 && lane < 8) mine = hand[lane - 4];
            if (lane < 8) __hip_atomic_store((GAS u64*)(ws + WS_GR + b * 128) + lane, ((u64)tag << 32) | mine, __ATOMIC_RELAXED, __HIP_MEMORY_SCOPE_AGENT);
            asm volatile("" ::: "memory");
            if (lane == 0) __hip_atomic_fetch_add((LAS u32*)((LAS char*)smem + KA_CNT) + 4, 1u, __ATOMIC_RELAXED, __HIP_MEMORY_SCOPE_WORKGROUP);
        }
        __builtin_amdgcn_s_setprio(0);
    }
    if (conv) {
        if (w < 3) ka_kgen(KP, smem, sA, sB, w, lane);
    } else if (w1job) {
        float* red = (float*)(smem + sA);
#pragma unroll
        for (int i = 0; i < 4; ++i) {
            float a = 0.f, c = 0.f;
#pragma unroll
            for (int r = 0; r < 8; ++r) { a += bflo(wv1[r][i]); c += bfhi(wv1[r][i]); }
            if (w == 0) {
#pragma unroll
                for (int r = 0; r < 8; ++r) { a += bflo(wv2[r][i]); c += bfhi(wv2[r][i]); }
            }
            red[(tt >> 2) * 33 + 8 * (tt & 3) + 2 * i] = a; red[(tt >> 2) * 33 + 8 * (tt & 3) + 2 * i + 1] = c;
        }
    }
    if (!conv) {
        __builtin_amdgcn_s_setprio(2);
        {
            if (w1job) {
            lds_sync(smem, sb, 4, lane);
            const float* red = (const float*)(smem + sA);
            const int col = tt & 31, part = tt >> 5;
            float sm = 0.f;
#pragma unroll
            for (int r = 0; r < 8; ++r) sm += red[(8 * part + r) * 33 + col];
            sm = swap32add(sm);
            if (lane < 32) *(float*)(smem + sB + (col * 4 + w) * 4) = sm;
            lds_sync(smem, sb, 8, lane);
#define WV(r) (rnd ? wv2[r][i] : wv1[r][i])
            }
            char* stg = smem + (w == 3 ? sA + 1024 : sB + 512 + w * 8192);
            u32* dtab = (u32*)(smem + sU + w * 256);
            auto flush = [&](u32 dst, bool active, const u32x4 (&o)[8]) {
                dtab[lane] = active ? dst : 0xffffff00u;
#pragma unroll
                for (int i = 0; i < 8; ++i) *(u32x4*)(stg + lane * 128 + ((i ^ (lane & 7)) * 16)) = o[i];
#pragma unroll
                for (int i = 0; i < 8; ++i) {
                    const int sl = 8 * i + (lane >> 3), pc = lane & 7;
                    const u32x4 v = *(const u32x4*)(stg + sl * 128 + ((pc ^ (sl & 7)) * 16));
                    st16<16>(wsr, dtab[sl] + pc * 16, v);
                }
            };
            if (w1job) {
                const int u1 = xcd * 12 + (j - 16), up = u1 >= 48, f = 32 * (up ? u1 - 48 : u1) + 8 * (tt & 3);
                float cm[8];
#pragma unroll
                for (int i = 0; i < 8; ++i) { const f32x4 v = *(const f32x4*)(smem + sB + (8 * (tt & 3) + i) * 16); cm[i] = (v[0] + v[1] + v[2] + v[3]) * (1.f / 640.f); }
                const int sl = f / 96, fl = f % 96, mt = 2 * (fl >> 4) + up;
#pragma unroll
                for (int rnd = 0; rnd < 2; ++rnd) {
                    if (rnd == 0 || w == 0) {
                        const int kg = (tt >> 2) + 64 * rnd;
                        const u32 dst = WS_W1T + ((sl * 10 + (kg >> 3)) * 24 + ((kg >> 2) & 1) * 12 + mt) * 1024 + ((fl & 15) + 16 * (kg & 3)) * 16;
                        u32x4 o[8];
#pragma unroll
                        for (int i = 0; i < 4; ++i) {
                            const float ca = cm[2 * i], cb = cm[2 * i + 1];
                            o[2 * i] = u32x4{cvtpk(bflo(WV(0)) - ca, bflo(WV(1)) - ca), cvtpk(bflo(WV(2)) - ca, bflo(WV(3)) - ca),
                                             cvtpk(bflo(WV(4)) - ca, bflo(WV(5)) - ca), cvtpk(bflo(WV(6)) - ca, bflo(WV(7)) - ca)};
                            o[2 * i + 1] = u32x4{cvtpk(bfhi(WV(0)) - cb, bfhi(WV(1)) - cb), cvtpk(bfhi(WV(2)) - cb, bfhi(WV(3)) - cb),
                                                 cvtpk(bfhi(WV(4)) - cb, bfhi(WV(5)) - cb), cvtpk(bfhi(WV(6)) - cb, bfhi(WV(7)) - cb)};
                        }
                        flush(dst, true, o);
                    }
                }
            } else {
#pragma unroll
                for (int rnd = 0; rnd < 2; ++rnd) {
                    if (rnd == 0 || w < 3 || true) {
                        const bool act = rnd == 0 || tt < 224;
                        const int n = 480 * (xcd * 4 + (j - 28)) + tt + 256 * rnd, dc = n % 80, kg = n / 80;
                        const int ds = dc / 10, dl = 8 * (dc % 10);
                        const u32 dst = WS_W2T + ((ds * 24 + (kg >> 3)) * 10 + ((kg >> 2) & 1) * 5 + (dl >> 4)) * 1024 + ((dl & 15) + 16 * (kg & 3)) * 16;
                        u32x4 o[8];
#pragma unroll
                        for (int i = 0; i < 4; ++i) {
                            o[2 * i] = u32x4{__builtin_amdgcn_perm(WV(1), WV(0), 0x05040100u), __builtin_amdgcn_perm(WV(3), WV(2), 0x05040100u),
                                             __builtin_amdgcn_perm(WV(5), WV(4), 0x05040100u), __builtin_amdgcn_perm(WV(7), WV(6), 0x05040100u)};
                            o[2 * i + 1] = u32x4{__builtin_amdgcn_perm(WV(1), WV(0), 0x07060302u), __builtin_amdgcn_perm(WV(3), WV(2), 0x07060302u),
                                                 __builtin_amdgcn_perm(WV(5), WV(4), 0x07060302u), __builtin_amdgcn_perm(WV(7), WV(6), 0x07060302u)};
                        }
                        flush(dst, act, o);
                    }
                }
            }
        }
        __builtin_amdgcn_s_setprio(0);
        WAITV(0);
        lds_sync(smem, sb, w1job ? 12 : 4, lane);
        if (t == 512) st_flag(ws, WS_FW + (xcd * 16 + (j - 16)) * 128, tag);
        if (t == 512) STAMP2(ws, 6, 0);
        return tag;
    }
    STAMP(ws, 0, 1);
    u32x4 g0 = {0, 0, 0, 0}, g1 = {0, 0, 0, 0};
    if (wv < 8) {
        lds_wait(smem, 4, 1);
        if (!early) {
            xg[0] = __builtin_amdgcn_raw_buffer_load_b64(xr0, (2 * pi0 + 0) * (D_ * 2) + xs, 0, 0);
            xg[1] = __builtin_amdgcn_raw_buffer_load_b64(xr0, (2 * pi0 + 1) * (D_ * 2) + xs, 0, 0);
        }
        xg[2] = __builtin_amdgcn_raw_buffer_load_b64(xr0, (2 * pi1 + 0) * (D_ * 2) + xs, 0, 0);
        xg[3] = __builtin_amdgcn_raw_buffer_load_b64(xr0, (2 * pi1 + 1) * (D_ * 2) + xs, 0, 0);
        g0 = __builtin_amdgcn_raw_buffer_load_b128(wsr, WS_GR + (pi0 >> 2) * 128 + (pi0 & 3) * 16, 0, 16);
        g1 = __builtin_amdgcn_raw_buffer_load_b128(wsr, WS_GR + (pi1 >> 2) * 128 + (pi1 & 3) * 16, 0, 16);
    }
    if (w < 3) lds_sync(smem, sb, 4, lane); else lds_wait(smem, sb, 4);
    STAMP(ws, 0, 2);
    ka_expand(smem, sA, sB, w, lane);
    if (wv < 8) {
        for (int spin = 0; spin < 100000; ++spin) {
            if (__all(g0[1] == tag && g0[3] == tag && g1[1] == tag && g1[3] == tag)) break;
            __builtin_amdgcn_s_sleep(4);
            asm volatile("" ::: "memory");
            g0 = __builtin_amdgcn_raw_buffer_load_b128(wsr, WS_GR + (pi0 >> 2) * 128 + (pi0 & 3) * 16, 0, 16);
            g1 = __builtin_amdgcn_raw_buffer_load_b128(wsr, WS_GR + (pi1 >> 2) * 128 + (pi1 & 3) * 16, 0, 16);
        }
        const u32 pr[4] = {g0[0], g0[2], g1[0], g1[2]};
        float m[4], rs[4];
#pragma unroll
        for (int r = 0; r < 4; ++r) { m[r] = bflo(pr[r]); rs[r] = __builtin_amdgcn_rcpf(bfhi(pr[r])); }
#pragma unroll
        for (int s = 0; s < 3; ++s) {
            if (s < nch) {
                const int e = e0 + s;
                float xv[4];
#pragma unroll
                for (int r = 0; r < 4; ++r) { const u32 dw = e < 2 ? xg[r][0] : xg[r][1]; xv[r] = (e & 1) ? bfhi(dw) : bflo(dw); }
                const u32 q01 = cvtpk(xv[0] - m[0], xv[1] - m[1]), q23 = cvtpk(xv[2] - m[2], xv[3] - m[3]);
                const u32 u01 = cvtpk(bflo(q01) * rs[0], bfhi(q01) * rs[1]), u23 = cvtpk(bflo(q23) * rs[2], bfhi(q23) * rs[3]);
                char* ub = smem + KA_U + s * KA_USZ;
                *(u32*)(ub + 480 + 4 * pi0) = u01; *(u32*)(ub + 480 + 4 * pi1) = u23;
                *(u32*)(ub + 4608 + 4 * pi0) = q01; *(u32*)(ub + 4608 + 4 * pi1) = q23;
            }
        }
    }
    STAMP(ws, 0, 3);
    lds_sync(smem, 3, 4 * nch, lane);
    STAMP(ws, 0, 4);
    const int al = lane & 15, g = lane >> 4;
    f32x4 acc[8];
#pragma unroll
    for (int i = 0; i < 8; ++i) acc[i] = f32x4{0.f, 0.f, 0.f, 0.f};
    {
        const LAS char* lds = (const LAS char*)smem;
        const int q = (lane & 15) >> 2, p = lane & 3;
        const LAS char* ub0 = lds + sU + 448 + 32 * al + 16 * g - 128 * w;
        const LAS char* ub1 = ub0 - 64;
        const LAS char* kb0 = lds + sB + q * CV_CS + 56 - 16 * g + 8 * p + 128 * w;
        const LAS char* kb1 = kb0 + 64;
        bf16x8 U0[8], U1[8]; s16x4 A0[8][2], A1[8][2];
        A0[0][0] = trread(kb0 + 8); A0[0][1] = trread(kb0 + 0); U0[0] = *(const LAS bf16x8*)(ub0 + 0); U0[1] = *(const LAS bf16x8*)(ub0 + 512); U0[2] = *(const LAS bf16x8*)(ub0 + 1024); U0[3] = *(const LAS bf16x8*)(ub0 + 1536); U0[4] = *(const LAS bf16x8*)(ub0 + 2048); U0[5] = *(const LAS bf16x8*)(ub0 + 2560); U0[6] = *(const LAS bf16x8*)(ub0 + 3072); U0[7] = *(const LAS bf16x8*)(ub0 + 3584); A0[1][0] = trread(kb0 + 520); A0[1][1] = trread(kb0 + 512); A0[2][0] = trread(kb0 + 1032); A0[2][1] = trread(kb0 + 1024); A0[3][0] = trread(kb0 + 1544); A0[3][1] = trread(kb0 + 1536); A0[4][0] = trread(kb0 + 2056); A0[4][1] = trread(kb0 + 2048); A0[5][0] = trread(kb0 + 2568); A0[5][1] = trread(kb0 + 2560); A0[6][0] = trread(kb0 + 3080); A0[6][1] = trread(kb0 + 3072); A0[7][0] = trread(kb0 + 3592); A0[7][1] = trread(kb0 + 3584);
        SCHEDB();
        acc[0] = MFMA16(cat8(A0[0][0], A0[0][1]), U0[0], acc[0]);
        SCHEDB();
        acc[1] = MFMA16(cat8(A0[0][0], A0[0][1]), U0[1], acc[1]);
        SCHEDB();
        acc[2] = MFMA16(cat8(A0[0][0], A0[0][1]), U0[2], acc[2]);
        SCHEDB();
        acc[3] = MFMA16(cat8(A0[0][0], A0[0][1]), U0[3], acc[3]);
        SCHEDB();
        acc[4] = MFMA16(cat8(A0[0][0], A0[0][1]), U0[4], acc[4]);
        SCHEDB();
        acc[5] = MFMA16(cat8(A0[0][0], A0[0][1]), U0[5], acc[5]);
        SCHEDB();
        acc[6] = MFMA16(cat8(A0[0][0], A0[0][1]), U0[6], acc[6]);
        SCHEDB();
        acc[7] = MFMA16(cat8(A0[0][0], A0[0][1]), U0[7], acc[7]);
        SCHEDB();
        acc[1] = MFMA16(cat8(A0[1][0], A0[1][1]), U0[0], acc[1]);
        A1[0][0] = trread(kb1 + 8);
        SCHEDB();
        acc[2] = MFMA16(cat8(A0[1][0], A0[1][1]), U0[1], acc[2]);
        A1[0][1] = trread(kb1 + 0);
        SCHEDB();
        acc[3] = MFMA16(cat8(A0[1][0], A0[1][1]), U0[2], acc[3]);
        U1[0] = *(const LAS bf16x8*)(ub1 + 0);
        SCHEDB();
        acc[4] = MFMA16(cat8(A0[1][0], A0[1][1]), U0[3], acc[4]);
        U1[1] = *(const LAS bf16x8*)(ub1 + 512);
        SCHEDB();
        acc[5] = MFMA16(cat8(A0[1][0], A0[1][1]), U0[4], acc[5]);
        U1[2] = *(const LAS bf16x8*)(ub1 + 1024);
        SCHEDB();
        acc[6] = MFMA16(cat8(A0[1][0], A0[1][1]), U0[5], acc[6]);
        U1[3] = *(const LAS bf16x8*)(ub1 + 1536);
        SCHEDB();
        acc[7] = MFMA16(cat8(A0[1][0], A0[1][1]), U0[6], acc[7]);
        U1[4] = *(const LAS bf16x8*)(ub1 + 2048);
        SCHEDB();
        acc[2] = MFMA16(cat8(A0[2][0], A0[2][1]), U0[0], acc[2]);
        U1[5] = *(const LAS bf16x8*)(ub1 + 2560);
        SCHEDB();
        acc[3] = MFMA16(cat8(A0[2][0], A0[2][1]), U0[1], acc[3]);
        U1[6] = *(const LAS bf16x8*)(ub1 + 3072);
        SCHEDB();
        acc[4] = MFMA16(cat8(A0[2][0], A0[2][1]), U0[2], acc[4]);
        U1[7] = *(const LAS bf16x8*)(ub1 + 3584);
        SCHEDB();
        acc[5] = MFMA16(cat8(A0[2][0], A0[2][1]), U0[3], acc[5]);
        A1[1][0] = trread(kb1 + 520);
        SCHEDB();
        acc[6] = MFMA16(cat8(A0[2][0], A0[2][1]), U0[4], acc[6]);
        A1[1][1] = trread(kb1 + 512);
        SCHEDB();
        acc[7] = MFMA16(cat8(A0[2][0], A0[2][1]), U0[5], acc[7]);
        A1[2][0] = trread(kb1 + 1032);
        SCHEDB();
        acc[3] = MFMA16(cat8(A0[3][0], A0[3][1]), U0[0], acc[3]);
        A1[2][1] = trread(kb1 + 1024);
        SCHEDB();
        acc[4] = MFMA16(cat8(A0[3][0], A0[3][1]), U0[1], acc[4]);
        A1[3][0] = trread(kb1 + 1544);
        SCHEDB();
        acc[5] = MFMA16(cat8(A0[3][0], A0[3][1]), U0[2], acc[5]);
        A1[3][1] = trread(kb1 + 1536);
        SCHEDB();
        acc[6] = MFMA16(cat8(A0[3][0], A0[3][1]), U0[3], acc[6]);
        A1[4][0] = trread(kb1 + 2056);
        SCHEDB();
        acc[7] = MFMA16(cat8(A0[3][0], A0[3][1]), U0[4], acc[7]);
        A1[4][1] = trread(kb1 + 2048);
        SCHEDB();
        acc[4] = MFMA16(cat8(A0[4][0], A0[4][1]), U0[0], acc[4]);
        A1[5][0] = trread(kb1 + 2568);
        SCHEDB();
        acc[5] = MFMA16(cat8(A0[4][0], A0[4][1]), U0[1], acc[5]);
        A1[5][1] = trread(kb1 + 2560);
        SCHEDB();
        acc[6] = MFMA16(cat8(A0[4][0], A0[4][1]), U0[2], acc[6]);
        A1[6][0] = trread(kb1 + 3080);
        SCHEDB();
        acc[7] = MFMA16(cat8(A0[4][0], A0[4][1]), U0[3], acc[7]);
        A1[6][1] = trread(kb1 + 3072);
        SCHEDB();
        acc[5] = MFMA16(cat8(A0[5][0], A0[5][1]), U0[0], acc[5]);
        A1[7][0] = trread(kb1 + 3592);
        SCHEDB();
        acc[6] = MFMA16(cat8(A0[5][0], A0[5][1]), U0[1], acc[6]);
        A1[7][1] = trread(kb1 + 3584);
        SCHEDB();
        acc[7] = MFMA16(cat8(A0[5][0], A0[5][1]), U0[2], acc[7]);
        SCHEDB();
        acc[6] = MFMA16(cat8(A0[6][0], A0[6][1]), U0[0], acc[6]);
        SCHEDB();
        acc[7] = MFMA16(cat8(A0[6][0], A0[6][1]), U0[1], acc[7]);
        SCHEDB();
        acc[7] = MFMA16(cat8(A0[7][0], A0[7][1]), U0[0], acc[7]);
        SCHEDB();
        acc[0] = MFMA16(cat8(A1[0][0], A1[0][1]), U1[0], acc[0]); SCHEDB();
        acc[1] = MFMA16(cat8(A1[0][0], A1[0][1]), U1[1], acc[1]); SCHEDB();
        acc[2] = MFMA16(cat8(A1[0][0], A1[0][1]), U1[2], acc[2]); SCHEDB();
        acc[3] = MFMA16(cat8(A1[0][0], A1[0][1]), U1[3], acc[3]); SCHEDB();
        acc[4] = MFMA16(cat8(A1[0][0], A1[0][1]), U1[4], acc[4]); SCHEDB();
        acc[5] = MFMA16(cat8(A1[0][0], A1[0][1]), U1[5], acc[5]); SCHEDB();
        acc[6] = MFMA16(cat8(A1[0][0], A1[0][1]), U1[6], acc[6]); SCHEDB();
        acc[7] = MFMA16(cat8(A1[0][0], A1[0][1]), U1[7], acc[7]); SCHEDB();
        acc[1] = MFMA16(cat8(A1[1][0], A1[1][1]), U1[0], acc[1]); SCHEDB();
        acc[2] = MFMA16(cat8(A1[1][0], A1[1][1]), U1[1], acc[2]); SCHEDB();
        acc[3] = MFMA16(cat8(A1[1][0], A1[1][1]), U1[2], acc[3]); SCHEDB();
        acc[4] = MFMA16(cat8(A1[1][0], A1[1][1]), U1[3], acc[4]); SCHEDB();
        acc[5] = MFMA16(cat8(A1[1][0], A1[1][1]), U1[4], acc[5]); SCHEDB();
        acc[6] = MFMA16(cat8(A1[1][0], A1[1][1]), U1[5], acc[6]); SCHEDB();
        acc[7] = MFMA16(cat8(A1[1][0], A1[1][1]), U1[6], acc[7]); SCHEDB();
        acc[2] = MFMA16(cat8(A1[2][0], A1[2][1]), U1[0], acc[2]); SCHEDB();
        acc[3] = MFMA16(cat8(A1[2][0], A1[2][1]), U1[1], acc[3]); SCHEDB();
        acc[4] = MFMA16(cat8(A1[2][0], A1[2][1]), U1[2], acc[4]); SCHEDB();
        acc[5] = MFMA16(cat8(A1[2][0], A1[2][1]), U1[3], acc[5]); SCHEDB();
        acc[6] = MFMA16(cat8(A1[2][0], A1[2][1]), U1[4], acc[6]); SCHEDB();
        acc[7] = MFMA16(cat8(A1[2][0], A1[2][1]), U1[5], acc[7]); SCHEDB();
        acc[3] = MFMA16(cat8(A1[3][0], A1[3][1]), U1[0], acc[3]); SCHEDB();
        acc[4] = MFMA16(cat8(A1[3][0], A1[3][1]), U1[1], acc[4]); SCHEDB();
        acc[5] = MFMA16(cat8(A1[3][0], A1[3][1]), U1[2], acc[5]); SCHEDB();
        acc[6] = MFMA16(cat8(A1[3][0], A1[3][1]), U1[3], acc[6]); SCHEDB();
        acc[7] = MFMA16(cat8(A1[3][0], A1[3][1]), U1[4], acc[7]); SCHEDB();
        acc[4] = MFMA16(cat8(A1[4][0], A1[4][1]), U1[0], acc[4]); SCHEDB();
        acc[5] = MFMA16(cat8(A1[4][0], A1[4][1]), U1[1], acc[5]); SCHEDB();
        acc[6] = MFMA16(cat8(A1[4][0], A1[4][1]), U1[2], acc[6]); SCHEDB();
        acc[7] = MFMA16(cat8(A1[4][0], A1[4][1]), U1[3], acc[7]); SCHEDB();
        acc[5] = MFMA16(cat8(A1[5][0], A1[5][1]), U1[0], acc[5]); SCHEDB();
        acc[6] = MFMA16(cat8(A1[5][0], A1[5][1]), U1[1], acc[6]); SCHEDB();
        acc[7] = MFMA16(cat8(A1[5][0], A1[5][1]), U1[2], acc[7]); SCHEDB();
        acc[6] = MFMA16(cat8(A1[6][0], A1[6][1]), U1[0], acc[6]); SCHEDB();
        acc[7] = MFMA16(cat8(A1[6][0], A1[6][1]), U1[1], acc[7]); SCHEDB();
        acc[7] = MFMA16(cat8(A1[7][0], A1[7][1]), U1[0], acc[7]); SCHEDB();
    }
    STAMP(ws, 0, 5);
    lds_sync(smem, sb, 8, lane);
    {
        char* rb = smem + sB + w * 6144 + lane * 16;
        int slot = 0;
#pragma unroll
        for (int tau = 0; tau < 8; ++tau) { if ((tau >> 1) != w) { *(f32x4*)(rb + slot * 1024) = acc[tau]; ++slot; } }
    }
    lds_sync(smem, sb, 12, lane);
    {
#pragma unroll
        for (int i = 0; i < 2; ++i) {
            f32x4 y;
            if (w == 0) y = acc[i]; else if (w == 1) y = acc[2 + i]; else if (w == 2) y = acc[4 + i]; else y = acc[6 + i];
            const int tau = 2 * w + i;
#pragma unroll
            for (int ow = 0; ow < 4; ++ow) {
                if (ow != w) {
                    const int sl = (tau < 2 * ow) ? tau : tau - 2;
                    y += *(const f32x4*)(smem + sB + ow * 6144 + sl * 1024 + lane * 16);
                }
            }
            const int l0 = 256 * tau + 16 * al + 4 * g;
            const u32x2 uv = *(const u32x2*)(smem + sU + 480 + 2 * l0), qv = *(const u32x2*)(smem + sU + 4608 + 2 * l0);
            float hv[4];
#pragma unroll
            for (int r = 0; r < 4; ++r) {
                const u32 uu = uv[r >> 1], qq = qv[r >> 1];
                const float u = (r & 1) ? bfhi(uu) : bflo(uu), q0 = (r & 1) ? bfhi(qq) : bflo(qq);
                const u32 yk = cvtpk(y[r], u * dsk);
                hv[r] = q0 + rbf(bflo(yk) + bfhi(yk));
            }
            st8<16>(wsr, WS_HC + (d * L_ + l0) * 2, u32x2{cvtpk(hv[0], hv[1]), cvtpk(hv[2], hv[3])});
        }
    }
    STAMP(ws, 0, 6);
    return tag;
}

#define G1_UNROLL 1
constexpr int G1_STAGE = 40960, G1_NS = 4, G1_NST = 10;
constexpr int G1_LDS = G1_NS * G1_STAGE;

DI float phase_gemm1(char* __restrict__ ws, char* smem, u32 tag) {
    const int b = blockIdx.x, t = threadIdx.x, lane = t & 63;
    const int w = __builtin_amdgcn_readfirstlane(t >> 6);
    const int xcd = b & 7, j = b >> 3;
    const int P = (xcd & 1) * 8 + (j & 7), S = (xcd >> 1) * 4 + (j >> 3);
    const u32 lds0 = (u32)(uintptr_t)smem;
    const rsrc_t wsr = mkrsrc(ws, WS_END);
    WAITV(0);
    BAR();
    if (t == 0) st_flag(ws, WS_F2 + b * 128, tag);
    if (w == 8 || w == 9) poll_flags(wsr, WS_FW + (w - 8) * 64 * 128, 64, tag, lane);
    BAR();
    if (w >= 4) {
        const int lw = w - 4;
        const char* wsrc = ws + WS_W1T + S * 10 * 24576 + lw * 1024 + lane * 16;
        const int hk = 16 * ((lw >> 2) & 1) + 4 * ((lw >> 1) & 1) + 8 * ((lane >> 3) & 1) + ((lane >> 1) & 3);
        const char* hsrc = ws + WS_HC + (hk * L_ + 128 * P + 64 * (lw & 1) + 16 * (lane >> 4) + 8 * (lane & 1)) * 2;
        auto issue_h = [&](int st, int slot) {
#pragma unroll
            for (int i = 0; i < 2; ++i) glds16(hsrc + (st * 64 + 32 * i) * L_ * 2, lds0 + slot * G1_STAGE + 24576 + (lw + 8 * i) * 1024);
        };
        auto issue_w = [&](int st, int slot) {
#pragma unroll
            for (int i = 0; i < 3; ++i) glds16(wsrc + st * 24576 + i * 8192, lds0 + slot * G1_STAGE + lw * 1024 + i * 8192);
        };
        issue_w(0, 0); issue_w(1, 1); issue_w(2, 2); issue_w(3, 3);
        BAR();
        if (b == 0 && t == 256) st_flag(ws, WS_EP, tag);
        issue_h(0, 0); issue_h(1, 1); issue_h(2, 2); issue_h(3, 3);
        waitv<6>(); BAR();
        waitv<4>(); BAR(); issue_h(4, 0); issue_w(4, 0);
        waitv<7>(); BAR(); issue_h(5, 1); issue_w(5, 1);
        waitv<10>(); BAR(); issue_h(6, 2); issue_w(6, 2);
        waitv<10>(); BAR(); issue_h(7, 3); issue_w(7, 3);
        waitv<10>(); BAR(); issue_h(8, 0); issue_w(8, 0);
        waitv<10>(); BAR(); issue_h(9, 1); issue_w(9, 1);
        waitv<10>(); BAR();
        waitv<5>(); BAR();
        waitv<0>(); BAR();
        BAR();
        BAR();
        if (lw == 0) {
            const int r = lane & 7, part = lane >> 3;
            float s1 = 0.f, s2 = 0.f;
#pragma unroll
            for (int i = 0; i < 8; ++i) {
                const int e = 8 * part + i;
                const f32x4 v = *(const f32x4*)(smem + ((e >> 4) * 64 + (e & 15) * 4 + (r >> 1)) * 16);
                s1 += (r & 1) ? v[1] : v[0]; s2 += (r & 1) ? v[3] : v[2];
            }
            s1 = dppadd<0x128>(s1); s2 = dppadd<0x128>(s2);
            s1 = swap32add(swap16add(s1)); s2 = swap32add(swap16add(s2));
            const float mean = s1 * (1.f / 640.f);
            float var = s2 * (1.f / 640.f) - mean * mean;
            var = var > 0.f ? var : 0.f;
            const float rs = 1.f / rbf(__builtin_sqrtf(rbf(rbf(var) + 1e-5f)));
            return rs * rs;
        }
        return 0.f;
    }
    poll_flags(wsr, WS_F2 + w * 64 * 128, 64, tag, lane);
    BAR();
    STAMP(ws, 4, 0);
    const int wm = w & 1, wn = w >> 1;
    const int g = lane >> 4, q = (lane & 15) >> 2, p = lane & 3;
    f32x4 acc[6][4];
#pragma unroll
    for (int a = 0; a < 6; ++a)
#pragma unroll
        for (int c = 0; c < 4; ++c) acc[a][c] = f32x4{0.f, 0.f, 0.f, 0.f};
    const LAS char* abase = (const LAS char*)smem + wm * 6144 + lane * 16;
    const LAS char* bbase = (const LAS char*)smem + 24576 + (g >> 1) * 4096 + (g & 1) * 128 + q * 32 + 8 * p + wn * 1024;
    const LAS char* sbase = (const LAS char*)smem + 24576 + (2 * w + (lane >> 5)) * 2048 + (S >> 1) * 256 + ((lane >> 2) & 7) * 32 + (S & 1) * 16 + (lane & 3) * 4;
    float s1a = 0.f, s1b = 0.f, s2a = 0.f, s2b = 0.f;
    bf16x8 AL[3], AH[3]; s16x4 B0[4][2], B1[4][2];
    u32 sv;
    BAR();
    STAMP(ws, 4, 1);
    AL[0] = *(const LAS bf16x8*)(abase + 0); B0[0][0] = trread(bbase + 0); B0[0][1] = trread(bbase + 2048); B0[1][0] = trread(bbase + 256); B0[1][1] = trread(bbase + 2304); AL[1] = *(const LAS bf16x8*)(abase + 1024); B0[2][0] = trread(bbase + 512); B0[2][1] = trread(bbase + 2560); B0[3][0] = trread(bbase + 768); B0[3][1] = trread(bbase + 2816); AL[2] = *(const LAS bf16x8*)(abase + 2048);
    SCHEDB();
#pragma unroll 1
    for (int st = 0; st < G1_NST; ++st) {
        acc[0+0][0] = MFMA16(AL[0], cat8(B0[0][0], B0[0][1]), acc[0+0][0]);
        AH[0] = *(const LAS bf16x8*)(abase + 3072);
        SCHEDB();
        acc[0+0][1] = MFMA16(AL[0], cat8(B0[1][0], B0[1][1]), acc[0+0][1]);
        AH[1] = *(const LAS bf16x8*)(abase + 4096);
        SCHEDB();
        acc[0+1][0] = MFMA16(AL[1], cat8(B0[0][0], B0[0][1]), acc[0+1][0]);
        B1[0][0] = trread(bbase + 8192);
        SCHEDB();
        acc[0+1][1] = MFMA16(AL[1], cat8(B0[1][0], B0[1][1]), acc[0+1][1]);
        B1[0][1] = trread(bbase + 10240);
        SCHEDB();
        acc[0+0][2] = MFMA16(AL[0], cat8(B0[2][0], B0[2][1]), acc[0+0][2]);
        B1[1][0] = trread(bbase + 8448);
        SCHEDB();
        acc[0+1][2] = MFMA16(AL[1], cat8(B0[2][0], B0[2][1]), acc[0+1][2]);
        B1[1][1] = trread(bbase + 10496);
        SCHEDB();
        acc[0+0][3] = MFMA16(AL[0], cat8(B0[3][0], B0[3][1]), acc[0+0][3]);
        AH[2] = *(const LAS bf16x8*)(abase + 5120);
        SCHEDB();
        acc[0+1][3] = MFMA16(AL[1], cat8(B0[3][0], B0[3][1]), acc[0+1][3]);
        SCHEDB();
        acc[0+2][0] = MFMA16(AL[2], cat8(B0[0][0], B0[0][1]), acc[0+2][0]);
        SCHEDB();
        acc[0+2][1] = MFMA16(AL[2], cat8(B0[1][0], B0[1][1]), acc[0+2][1]);
        SCHEDB();
        acc[0+2][2] = MFMA16(AL[2], cat8(B0[2][0], B0[2][1]), acc[0+2][2]);
        SCHEDB();
        acc[0+2][3] = MFMA16(AL[2], cat8(B0[3][0], B0[3][1]), acc[0+2][3]);
        SCHEDB();
        acc[3+0][0] = MFMA16(AH[0], cat8(B0[0][0], B0[0][1]), acc[3+0][0]);
        AL[0] = *(const LAS bf16x8*)(abase + 12288);
        SCHEDB();
        acc[3+0][1] = MFMA16(AH[0], cat8(B0[1][0], B0[1][1]), acc[3+0][1]);
        AL[1] = *(const LAS bf16x8*)(abase + 13312);
        SCHEDB();
        acc[3+1][0] = MFMA16(AH[1], cat8(B0[0][0], B0[0][1]), acc[3+1][0]);
        sv = *(const LAS u32*)(sbase);
        SCHEDB();
        acc[3+1][1] = MFMA16(AH[1], cat8(B0[1][0], B0[1][1]), acc[3+1][1]);
        B1[2][0] = trread(bbase + 8704);
        SCHEDB();
        acc[3+0][2] = MFMA16(AH[0], cat8(B0[2][0], B0[2][1]), acc[3+0][2]);
        B1[2][1] = trread(bbase + 10752);
        SCHEDB();
        acc[3+1][2] = MFMA16(AH[1], cat8(B0[2][0], B0[2][1]), acc[3+1][2]);
        B1[3][0] = trread(bbase + 8960);
        SCHEDB();
        acc[3+0][3] = MFMA16(AH[0], cat8(B0[3][0], B0[3][1]), acc[3+0][3]);
        B1[3][1] = trread(bbase + 11008);
        SCHEDB();
        acc[3+1][3] = MFMA16(AH[1], cat8(B0[3][0], B0[3][1]), acc[3+1][3]);
        AL[2] = *(const LAS bf16x8*)(abase + 14336);
        SCHEDB();
        acc[3+2][0] = MFMA16(AH[2], cat8(B0[0][0], B0[0][1]), acc[3+2][0]);
        SCHEDB();
        acc[3+2][1] = MFMA16(AH[2], cat8(B0[1][0], B0[1][1]), acc[3+2][1]);
        SCHEDB();
        acc[3+2][2] = MFMA16(AH[2], cat8(B0[2][0], B0[2][1]), acc[3+2][2]);
        SCHEDB();
        acc[3+2][3] = MFMA16(AH[2], cat8(B0[3][0], B0[3][1]), acc[3+2][3]);
        SCHEDB();
        acc[0+0][0] = MFMA16(AL[0], cat8(B1[0][0], B1[0][1]), acc[0+0][0]);
        AH[0] = *(const LAS bf16x8*)(abase + 15360);
        SCHEDB();
        acc[0+0][1] = MFMA16(AL[0], cat8(B1[1][0], B1[1][1]), acc[0+0][1]);
        AH[1] = *(const LAS bf16x8*)(abase + 16384);
        SCHEDB();
        acc[0+1][0] = MFMA16(AL[1], cat8(B1[0][0], B1[0][1]), acc[0+1][0]);
        AH[2] = *(const LAS bf16x8*)(abase + 17408);
        SCHEDB();
        acc[0+1][1] = MFMA16(AL[1], cat8(B1[1][0], B1[1][1]), acc[0+1][1]);
        SCHEDB();
        acc[0+0][2] = MFMA16(AL[0], cat8(B1[2][0], B1[2][1]), acc[0+0][2]);
        SCHEDB();
        acc[0+1][2] = MFMA16(AL[1], cat8(B1[2][0], B1[2][1]), acc[0+1][2]);
        SCHEDB();
        acc[0+0][3] = MFMA16(AL[0], cat8(B1[3][0], B1[3][1]), acc[0+0][3]);
        SCHEDB();
        acc[0+1][3] = MFMA16(AL[1], cat8(B1[3][0], B1[3][1]), acc[0+1][3]);
        SCHEDB();
        acc[0+2][0] = MFMA16(AL[2], cat8(B1[0][0], B1[0][1]), acc[0+2][0]);
        SCHEDB();
        acc[0+2][1] = MFMA16(AL[2], cat8(B1[1][0], B1[1][1]), acc[0+2][1]);
        SCHEDB();
        acc[0+2][2] = MFMA16(AL[2], cat8(B1[2][0], B1[2][1]), acc[0+2][2]);
        SCHEDB();
        acc[0+2][3] = MFMA16(AL[2], cat8(B1[3][0], B1[3][1]), acc[0+2][3]);
        SCHEDB();
        { const float a = bflo(sv), c = bfhi(sv); s1a += a; s1b += c; s2a = __builtin_fmaf(a, a, s2a); s2b = __builtin_fmaf(c, c, s2b); }
        WAITL0();
        BAR();
        { const int dlt = (st & 3) == 3 ? -3 * G1_STAGE : G1_STAGE; abase += dlt; bbase += dlt; sbase += dlt; }
        acc[3+0][0] = MFMA16(AH[0], cat8(B1[0][0], B1[0][1]), acc[3+0][0]);
        AL[0] = *(const LAS bf16x8*)(abase + 0);
        SCHEDB();
        acc[3+0][1] = MFMA16(AH[0], cat8(B1[1][0], B1[1][1]), acc[3+0][1]);
        B0[0][0] = trread(bbase + 0);
        SCHEDB();
        acc[3+1][0] = MFMA16(AH[1], cat8(B1[0][0], B1[0][1]), acc[3+1][0]);
        B0[0][1] = trread(bbase + 2048);
        SCHEDB();
        acc[3+1][1] = MFMA16(AH[1], cat8(B1[1][0], B1[1][1]), acc[3+1][1]);
        B0[1][0] = trread(bbase + 256);
        SCHEDB();
        acc[3+0][2] = MFMA16(AH[0], cat8(B1[2][0], B1[2][1]), acc[3+0][2]);
        B0[1][1] = trread(bbase + 2304);
        SCHEDB();
        acc[3+1][2] = MFMA16(AH[1], cat8(B1[2][0], B1[2][1]), acc[3+1][2]);
        AL[1] = *(const LAS bf16x8*)(abase + 1024);
        SCHEDB();
        acc[3+0][3] = MFMA16(AH[0], cat8(B1[3][0], B1[3][1]), acc[3+0][3]);
        B0[2][0] = trread(bbase + 512);
        SCHEDB();
        acc[3+1][3] = MFMA16(AH[1], cat8(B1[3][0], B1[3][1]), acc[3+1][3]);
        B0[2][1] = trread(bbase + 2560);
        SCHEDB();
        acc[3+2][0] = MFMA16(AH[2], cat8(B1[0][0], B1[0][1]), acc[3+2][0]);
        B0[3][0] = trread(bbase + 768);
        SCHEDB();
        acc[3+2][1] = MFMA16(AH[2], cat8(B1[1][0], B1[1][1]), acc[3+2][1]);
        B0[3][1] = trread(bbase + 2816);
        SCHEDB();
        acc[3+2][2] = MFMA16(AH[2], cat8(B1[2][0], B1[2][1]), acc[3+2][2]);
        AL[2] = *(const LAS bf16x8*)(abase + 2048);
        SCHEDB();
        acc[3+2][3] = MFMA16(AH[2], cat8(B1[3][0], B1[3][1]), acc[3+2][3]);
        SCHEDB();
    }
    STAMP(ws, 4, 3);
    *(f32x4*)(smem + (w * 64 + lane) * 16) = f32x4{s1a, s1b, s2a, s2b};
    WAITL0();
    BAR();
    int tz = threadIdx.x, bz = blockIdx.x;
    asm volatile("" : "+v"(tz), "+v"(bz));
    const int lane_e = tz & 63, g_e = lane_e >> 4;
    const int P_e = (bz & 1) * 8 + ((bz >> 3) & 7), S_e = ((bz & 7) >> 1) * 4 + (bz >> 6);
    const rsrc_t rs = wsr;
#pragma unroll
    for (int a = 0; a < 3; ++a) {
        const int f0 = 96 * S_e + 16 * (3 * wm + a);
        const int kb = f0 >> 5, oct = ((f0 & 31) >> 3) + (g_e >> 1);
#pragma unroll
        for (int c = 0; c < 4; c += 2) {
            u32 X[2], Y[2];
#pragma unroll
            for (int e = 0; e < 2; ++e) {
                X[e] = cvtpk(__builtin_fmaxf(acc[2 * a][c][2 * e], 0.f) * acc[2 * a + 1][c][2 * e], __builtin_fmaxf(acc[2 * a][c][2 * e + 1], 0.f) * acc[2 * a + 1][c][2 * e + 1]);
                Y[e] = cvtpk(__builtin_fmaxf(acc[2 * a][c + 1][2 * e], 0.f) * acc[2 * a + 1][c + 1][2 * e], __builtin_fmaxf(acc[2 * a][c + 1][2 * e + 1], 0.f) * acc[2 * a + 1][c + 1][2 * e + 1]);
            }
#pragma unroll
            for (int e = 0; e < 2; ++e) { auto r = __builtin_amdgcn_permlane16_swap(X[e], Y[e], false, false); X[e] = r[0]; Y[e] = r[1]; }
            const int lt = 8 * P_e + 4 * wn + c + (g_e & 1);
            st16<16>(rs, WS_HM + ((lt * 48 + kb) * 64 + (lane_e & 15) + 16 * oct) * 16, u32x4{X[0], X[1], Y[0], Y[1]});
        }
    }
    STAMP(ws, 4, 4);
    return 0.f;
}

constexpr int G2_STAGE = 18432, G2_NS = 8, G2_NST = 24;
constexpr int G2_RES = G2_NS * G2_STAGE;
constexpr int G2_LDS = G2_RES + 10240;

DI void phase_gemm2(char* __restrict__ ws, u16* __restrict__ out, char* smem, u32 tag, float rs2pub) {
    const int b = blockIdx.x, t = threadIdx.x, lane = t & 63;
    const int w = __builtin_amdgcn_readfirstlane(t >> 6);
    const int xcd = b & 7, j = b >> 3;
    const int P = (xcd & 3) * 8 + (j & 7), S = (xcd >> 2) * 4 + (j >> 3);
    const u32 lds0 = (u32)(uintptr_t)smem;
    const rsrc_t wsr = mkrsrc(ws, WS_END);
    WAITV(0);
    BAR();
    if (w == 4) {
        const int P1 = (xcd & 1) * 8 + (j & 7), S1 = (xcd >> 1) * 4 + (j >> 3);
        const u32 up1 = __builtin_amdgcn_update_dpp(0, __float_as_uint(rs2pub), 0x111, 0xF, 0xF, true);
        if (lane < 9) st4<16>(wsr, WS_F3 + (P1 * 16 + S1) * 128 + lane * 4, lane == 0 ? tag : up1);
    }
    if (w >= 4) {
        const int lw = w - 4;
        const char* wsrc = ws + WS_W2T + S * 24 * 10240 + lw * 1024 + lane * 16;
        const char* msrc = ws + WS_HM + ((4 * P + (lw >> 1)) * 48 + (lw & 1)) * 1024 + lane * 16;
        auto issue_m = [&](int st, int slot) { glds16(msrc + st * 2048, lds0 + slot * G2_STAGE + 10240 + lw * 1024); };
        auto issue_w = [&](int st, int slot) {
            glds16(wsrc + st * 10240, lds0 + slot * G2_STAGE + lw * 1024);
            if (lw < 2) glds16(wsrc + st * 10240 + 8192, lds0 + slot * G2_STAGE + 8192 + lw * 1024);
        };
        if (lw < 2) {
            issue_w(0, 0);
            issue_w(1, 1);
            issue_w(2, 2);
            issue_w(3, 3);
            issue_w(4, 4);
            issue_w(5, 5);
            issue_w(6, 6);
            issue_w(7, 7);
            BAR();
            issue_m(0, 0);
            issue_m(1, 1);
            issue_m(2, 2);
            issue_m(3, 3);
            issue_m(4, 4);
            issue_m(5, 5);
            issue_m(6, 6);
            issue_m(7, 7);
            waitv<7>(); BAR();
            waitv<6>(); BAR();
            waitv<5>(); BAR();
            issue_m(8, 0); issue_w(8, 0);
            waitv<7>(); BAR();
            issue_m(9, 1); issue_w(9, 1);
            waitv<9>(); BAR();
            issue_m(10, 2); issue_w(10, 2);
            waitv<11>(); BAR();
            issue_m(11, 3); issue_w(11, 3);
            waitv<13>(); BAR();
            issue_m(12, 4); issue_w(12, 4);
            waitv<15>(); BAR();
            issue_m(13, 5); issue_w(13, 5);
            waitv<15>(); BAR();
            issue_m(14, 6); issue_w(14, 6);
            waitv<15>(); BAR();
            issue_m(15, 7); issue_w(15, 7);
            waitv<15>(); BAR();
            issue_m(16, 0); issue_w(16, 0);
            waitv<15>(); BAR();
            issue_m(17, 1); issue_w(17, 1);
            waitv<15>(); BAR();
            issue_m(18, 2); issue_w(18, 2);
            waitv<15>(); BAR();
            issue_m(19, 3); issue_w(19, 3);
            waitv<15>(); BAR();
            issue_m(20, 4); issue_w(20, 4);
            waitv<15>(); BAR();
            issue_m(21, 5); issue_w(21, 5);
            waitv<15>(); BAR();
            issue_m(22, 6); issue_w(22, 6);
            waitv<15>(); BAR();
            issue_m(23, 7); issue_w(23, 7);
            waitv<15>(); BAR();
            waitv<12>(); BAR();
            waitv<9>(); BAR();
            waitv<6>(); BAR();
            waitv<3>(); BAR();
            waitv<0>(); BAR();
        } else {
            issue_w(0, 0);
            issue_w(1, 1);
            issue_w(2, 2);
            issue_w(3, 3);
            issue_w(4, 4);
            issue_w(5, 5);
            issue_w(6, 6);
            issue_w(7, 7);
            BAR();
            issue_m(0, 0);
            issue_m(1, 1);
            issue_m(2, 2);
            issue_m(3, 3);
            issue_m(4, 4);
            issue_m(5, 5);
            issue_m(6, 6);
            issue_m(7, 7);
            waitv<7>(); BAR();
            waitv<6>(); BAR();
            waitv<5>(); BAR();
            issue_m(8, 0); issue_w(8, 0);
            waitv<6>(); BAR();
            issue_m(9, 1); issue_w(9, 1);
            waitv<7>(); BAR();
            issue_m(10, 2); issue_w(10, 2);
            waitv<8>(); BAR();
            issue_m(11, 3); issue_w(11, 3);
            waitv<9>(); BAR();
            issue_m(12, 4); issue_w(12, 4);
            waitv<10>(); BAR();
            issue_m(13, 5); issue_w(13, 5);
            waitv<10>(); BAR();
            issue_m(14, 6); issue_w(14, 6);
            waitv<10>(); BAR();
            issue_m(15, 7); issue_w(15, 7);
            waitv<10>(); BAR();
            issue_m(16, 0); issue_w(16, 0);
            waitv<10>(); BAR();
            issue_m(17, 1); issue_w(17, 1);
            waitv<10>(); BAR();
            issue_m(18, 2); issue_w(18, 2);
            waitv<10>(); BAR();
            issue_m(19, 3); issue_w(19, 3);
            waitv<10>(); BAR();
            issue_m(20, 4); issue_w(20, 4);
            waitv<10>(); BAR();
            issue_m(21, 5); issue_w(21, 5);
            waitv<10>(); BAR();
            issue_m(22, 6); issue_w(22, 6);
            waitv<10>(); BAR();
            issue_m(23, 7); issue_w(23, 7);
            waitv<10>(); BAR();
            waitv<8>(); BAR();
            waitv<6>(); BAR();
            waitv<4>(); BAR();
            waitv<2>(); BAR();
            waitv<0>(); BAR();
        }
        asm volatile(".Lk_block_last:");
        return;
    }
    if (w == 0) poll_flags(wsr, WS_F3 + (P >> 1) * 16 * 128, 16, tag, lane);
    BAR();
#pragma unroll
    for (int i = 0; i < 3; ++i) {
        const int row = 32 * i + (lane >> 1);
        if (row < 80) glds16(ws + WS_HC + ((80 * S + row) * L_ + 64 * P + 16 * w + 8 * (lane & 1)) * 2, lds0 + G2_RES + w * 2560 + i * 1024);
    }
    STAMP(ws, 5, 0);
    const int g = lane >> 4, q = (lane & 15) >> 2, p = lane & 3;
    const int l = 64 * P + 16 * w + (lane & 15);
    const float rs2 = __uint_as_float(__builtin_amdgcn_raw_buffer_load_b32(wsr, WS_F3 + (l >> 3) * 128 + 4 + (l & 7) * 4, 0, 16));
    const float m1 = bflo(__builtin_amdgcn_raw_buffer_load_b32(wsr, WS_GR + (l >> 3) * 128 + (l & 7) * 8, 0, 16));
    f32x4 acc[5];
#pragma unroll
    for (int a = 0; a < 5; ++a) acc[a] = f32x4{0.f, 0.f, 0.f, 0.f};
    const LAS char* abase = (const LAS char*)smem + lane * 16;
    const LAS char* bbase = (const LAS char*)smem + 10240 + w * 2048 + lane * 16;
    bf16x8 EA[2][5], EB[2], OA[2][5], OB[2];
    BAR();
    STAMP(ws, 5, 1);
    EB[0] = *(const LAS bf16x8*)(bbase); EB[1] = *(const LAS bf16x8*)(bbase + 1024);
#pragma unroll
    for (int a = 0; a < 5; ++a) { EA[0][a] = *(const LAS bf16x8*)(abase + a * 1024); EA[1][a] = *(const LAS bf16x8*)(abase + 5120 + a * 1024); }
    SCHEDB();
#pragma unroll 1
    for (int it = 0; it < 3; ++it) {
        if (it == 1) STAMP(ws, 5, 2); if (it == 2) STAMP(ws, 5, 3);
        BAR();
        acc[0] = MFMA16(EA[0][0], EB[0], acc[0]);
        OB[0] = *(const LAS bf16x8*)(bbase + 1*G2_STAGE);
        OA[0][0] = *(const LAS bf16x8*)(abase + 1*G2_STAGE + 0);
        SCHEDB();
        acc[1] = MFMA16(EA[0][1], EB[0], acc[1]);
        OA[0][1] = *(const LAS bf16x8*)(abase + 1*G2_STAGE + 1024);
        OA[0][2] = *(const LAS bf16x8*)(abase + 1*G2_STAGE + 2048);
        SCHEDB();
        acc[2] = MFMA16(EA[0][2], EB[0], acc[2]);
        OA[0][3] = *(const LAS bf16x8*)(abase + 1*G2_STAGE + 3072);
        SCHEDB();
        acc[3] = MFMA16(EA[0][3], EB[0], acc[3]);
        OA[0][4] = *(const LAS bf16x8*)(abase + 1*G2_STAGE + 4096);
        SCHEDB();
        acc[4] = MFMA16(EA[0][4], EB[0], acc[4]);
        OB[1] = *(const LAS bf16x8*)(bbase + 1*G2_STAGE + 1024);
        SCHEDB();
        acc[0] = MFMA16(EA[1][0], EB[1], acc[0]);
        OA[1][0] = *(const LAS bf16x8*)(abase + 1*G2_STAGE + 5120);
        SCHEDB();
        acc[1] = MFMA16(EA[1][1], EB[1], acc[1]);
        OA[1][1] = *(const LAS bf16x8*)(abase + 1*G2_STAGE + 6144);
        SCHEDB();
        acc[2] = MFMA16(EA[1][2], EB[1], acc[2]);
        OA[1][2] = *(const LAS bf16x8*)(abase + 1*G2_STAGE + 7168);
        SCHEDB();
        acc[3] = MFMA16(EA[1][3], EB[1], acc[3]);
        OA[1][3] = *(const LAS bf16x8*)(abase + 1*G2_STAGE + 8192);
        SCHEDB();
        acc[4] = MFMA16(EA[1][4], EB[1], acc[4]);
        OA[1][4] = *(const LAS bf16x8*)(abase + 1*G2_STAGE + 9216);
        SCHEDB();
        BAR();
        acc[0] = MFMA16(OA[0][0], OB[0], acc[0]);
        EB[0] = *(const LAS bf16x8*)(bbase + 2*G2_STAGE);
        EA[0][0] = *(const LAS bf16x8*)(abase + 2*G2_STAGE + 0);
        SCHEDB();
        acc[1] = MFMA16(OA[0][1], OB[0], acc[1]);
        EA[0][1] = *(const LAS bf16x8*)(abase + 2*G2_STAGE + 1024);
        EA[0][2] = *(const LAS bf16x8*)(abase + 2*G2_STAGE + 2048);
        SCHEDB();
        acc[2] = MFMA16(OA[0][2], OB[0], acc[2]);
        EA[0][3] = *(const LAS bf16x8*)(abase + 2*G2_STAGE + 3072);
        SCHEDB();
        acc[3] = MFMA16(OA[0][3], OB[0], acc[3]);
        EA[0][4] = *(const LAS bf16x8*)(abase + 2*G2_STAGE + 4096);
        SCHEDB();
        acc[4] = MFMA16(OA[0][4], OB[0], acc[4]);
        EB[1] = *(const LAS bf16x8*)(bbase + 2*G2_STAGE + 1024);
        SCHEDB();
        acc[0] = MFMA16(OA[1][0], OB[1], acc[0]);
        EA[1][0] = *(const LAS bf16x8*)(abase + 2*G2_STAGE + 5120);
        SCHEDB();
        acc[1] = MFMA16(OA[1][1], OB[1], acc[1]);
        EA[1][1] = *(const LAS bf16x8*)(abase + 2*G2_STAGE + 6144);
        SCHEDB();
        acc[2] = MFMA16(OA[1][2], OB[1], acc[2]);
        EA[1][2] = *(const LAS bf16x8*)(abase + 2*G2_STAGE + 7168);
        SCHEDB();
        acc[3] = MFMA16(OA[1][3], OB[1], acc[3]);
        EA[1][3] = *(const LAS bf16x8*)(abase + 2*G2_STAGE + 8192);
        SCHEDB();
        acc[4] = MFMA16(OA[1][4], OB[1], acc[4]);
        EA[1][4] = *(const LAS bf16x8*)(abase + 2*G2_STAGE + 9216);
        SCHEDB();
        BAR();
        acc[0] = MFMA16(EA[0][0], EB[0], acc[0]);
        OB[0] = *(const LAS bf16x8*)(bbase + 3*G2_STAGE);
        OA[0][0] = *(const LAS bf16x8*)(abase + 3*G2_STAGE + 0);
        SCHEDB();
        acc[1] = MFMA16(EA[0][1], EB[0], acc[1]);
        OA[0][1] = *(const LAS bf16x8*)(abase + 3*G2_STAGE + 1024);
        OA[0][2] = *(const LAS bf16x8*)(abase + 3*G2_STAGE + 2048);
        SCHEDB();
        acc[2] = MFMA16(EA[0][2], EB[0], acc[2]);
        OA[0][3] = *(const LAS bf16x8*)(abase + 3*G2_STAGE + 3072);
        SCHEDB();
        acc[3] = MFMA16(EA[0][3], EB[0], acc[3]);
        OA[0][4] = *(const LAS bf16x8*)(abase + 3*G2_STAGE + 4096);
        SCHEDB();
        acc[4] = MFMA16(EA[0][4], EB[0], acc[4]);
        OB[1] = *(const LAS bf16x8*)(bbase + 3*G2_STAGE + 1024);
        SCHEDB();
        acc[0] = MFMA16(EA[1][0], EB[1], acc[0]);
        OA[1][0] = *(const LAS bf16x8*)(abase + 3*G2_STAGE + 5120);
        SCHEDB();
        acc[1] = MFMA16(EA[1][1], EB[1], acc[1]);
        OA[1][1] = *(const LAS bf16x8*)(abase + 3*G2_STAGE + 6144);
        SCHEDB();
        acc[2] = MFMA16(EA[1][2], EB[1], acc[2]);
        OA[1][2] = *(const LAS bf16x8*)(abase + 3*G2_STAGE + 7168);
        SCHEDB();
        acc[3] = MFMA16(EA[1][3], EB[1], acc[3]);
        OA[1][3] = *(const LAS bf16x8*)(abase + 3*G2_STAGE + 8192);
        SCHEDB();
        acc[4] = MFMA16(EA[1][4], EB[1], acc[4]);
        OA[1][4] = *(const LAS bf16x8*)(abase + 3*G2_STAGE + 9216);
        SCHEDB();
        BAR();
        acc[0] = MFMA16(OA[0][0], OB[0], acc[0]);
        EB[0] = *(const LAS bf16x8*)(bbase + 4*G2_STAGE);
        EA[0][0] = *(const LAS bf16x8*)(abase + 4*G2_STAGE + 0);
        SCHEDB();
        acc[1] = MFMA16(OA[0][1], OB[0], acc[1]);
        EA[0][1] = *(const LAS bf16x8*)(abase + 4*G2_STAGE + 1024);
        EA[0][2] = *(const LAS bf16x8*)(abase + 4*G2_STAGE + 2048);
        SCHEDB();
        acc[2] = MFMA16(OA[0][2], OB[0], acc[2]);
        EA[0][3] = *(const LAS bf16x8*)(abase + 4*G2_STAGE + 3072);
        SCHEDB();
        acc[3] = MFMA16(OA[0][3], OB[0], acc[3]);
        EA[0][4] = *(const LAS bf16x8*)(abase + 4*G2_STAGE + 4096);
        SCHEDB();
        acc[4] = MFMA16(OA[0][4], OB[0], acc[4]);
        EB[1] = *(const LAS bf16x8*)(bbase + 4*G2_STAGE + 1024);
        SCHEDB();
        acc[0] = MFMA16(OA[1][0], OB[1], acc[0]);
        EA[1][0] = *(const LAS bf16x8*)(abase + 4*G2_STAGE + 5120);
        SCHEDB();
        acc[1] = MFMA16(OA[1][1], OB[1], acc[1]);
        EA[1][1] = *(const LAS bf16x8*)(abase + 4*G2_STAGE + 6144);
        SCHEDB();
        acc[2] = MFMA16(OA[1][2], OB[1], acc[2]);
        EA[1][2] = *(const LAS bf16x8*)(abase + 4*G2_STAGE + 7168);
        SCHEDB();
        acc[3] = MFMA16(OA[1][3], OB[1], acc[3]);
        EA[1][3] = *(const LAS bf16x8*)(abase + 4*G2_STAGE + 8192);
        SCHEDB();
        acc[4] = MFMA16(OA[1][4], OB[1], acc[4]);
        EA[1][4] = *(const LAS bf16x8*)(abase + 4*G2_STAGE + 9216);
        SCHEDB();
        BAR();
        acc[0] = MFMA16(EA[0][0], EB[0], acc[0]);
        OB[0] = *(const LAS bf16x8*)(bbase + 5*G2_STAGE);
        OA[0][0] = *(const LAS bf16x8*)(abase + 5*G2_STAGE + 0);
        SCHEDB();
        acc[1] = MFMA16(EA[0][1], EB[0], acc[1]);
        OA[0][1] = *(const LAS bf16x8*)(abase + 5*G2_STAGE + 1024);
        OA[0][2] = *(const LAS bf16x8*)(abase + 5*G2_STAGE + 2048);
        SCHEDB();
        acc[2] = MFMA16(EA[0][2], EB[0], acc[2]);
        OA[0][3] = *(const LAS bf16x8*)(abase + 5*G2_STAGE + 3072);
        SCHEDB();
        acc[3] = MFMA16(EA[0][3], EB[0], acc[3]);
        OA[0][4] = *(const LAS bf16x8*)(abase + 5*G2_STAGE + 4096);
        SCHEDB();
        acc[4] = MFMA16(EA[0][4], EB[0], acc[4]);
        OB[1] = *(const LAS bf16x8*)(bbase + 5*G2_STAGE + 1024);
        SCHEDB();
        acc[0] = MFMA16(EA[1][0], EB[1], acc[0]);
        OA[1][0] = *(const LAS bf16x8*)(abase + 5*G2_STAGE + 5120);
        SCHEDB();
        acc[1] = MFMA16(EA[1][1], EB[1], acc[1]);
        OA[1][1] = *(const LAS bf16x8*)(abase + 5*G2_STAGE + 6144);
        SCHEDB();
        acc[2] = MFMA16(EA[1][2], EB[1], acc[2]);
        OA[1][2] = *(const LAS bf16x8*)(abase + 5*G2_STAGE + 7168);
        SCHEDB();
        acc[3] = MFMA16(EA[1][3], EB[1], acc[3]);
        OA[1][3] = *(const LAS bf16x8*)(abase + 5*G2_STAGE + 8192);
        SCHEDB();
        acc[4] = MFMA16(EA[1][4], EB[1], acc[4]);
        OA[1][4] = *(const LAS bf16x8*)(abase + 5*G2_STAGE + 9216);
        SCHEDB();
        BAR();
        acc[0] = MFMA16(OA[0][0], OB[0], acc[0]);
        EB[0] = *(const LAS bf16x8*)(bbase + 6*G2_STAGE);
        EA[0][0] = *(const LAS bf16x8*)(abase + 6*G2_STAGE + 0);
        SCHEDB();
        acc[1] = MFMA16(OA[0][1], OB[0], acc[1]);
        EA[0][1] = *(const LAS bf16x8*)(abase + 6*G2_STAGE + 1024);
        EA[0][2] = *(const LAS bf16x8*)(abase + 6*G2_STAGE + 2048);
        SCHEDB();
        acc[2] = MFMA16(OA[0][2], OB[0], acc[2]);
        EA[0][3] = *(const LAS bf16x8*)(abase + 6*G2_STAGE + 3072);
        SCHEDB();
        acc[3] = MFMA16(OA[0][3], OB[0], acc[3]);
        EA[0][4] = *(const LAS bf16x8*)(abase + 6*G2_STAGE + 4096);
        SCHEDB();
        acc[4] = MFMA16(OA[0][4], OB[0], acc[4]);
        EB[1] = *(const LAS bf16x8*)(bbase + 6*G2_STAGE + 1024);
        SCHEDB();
        acc[0] = MFMA16(OA[1][0], OB[1], acc[0]);
        EA[1][0] = *(const LAS bf16x8*)(abase + 6*G2_STAGE + 5120);
        SCHEDB();
        acc[1] = MFMA16(OA[1][1], OB[1], acc[1]);
        EA[1][1] = *(const LAS bf16x8*)(abase + 6*G2_STAGE + 6144);
        SCHEDB();
        acc[2] = MFMA16(OA[1][2], OB[1], acc[2]);
        EA[1][2] = *(const LAS bf16x8*)(abase + 6*G2_STAGE + 7168);
        SCHEDB();
        acc[3] = MFMA16(OA[1][3], OB[1], acc[3]);
        EA[1][3] = *(const LAS bf16x8*)(abase + 6*G2_STAGE + 8192);
        SCHEDB();
        acc[4] = MFMA16(OA[1][4], OB[1], acc[4]);
        EA[1][4] = *(const LAS bf16x8*)(abase + 6*G2_STAGE + 9216);
        SCHEDB();
        BAR();
        acc[0] = MFMA16(EA[0][0], EB[0], acc[0]);
        OB[0] = *(const LAS bf16x8*)(bbase + 7*G2_STAGE);
        OA[0][0] = *(const LAS bf16x8*)(abase + 7*G2_STAGE + 0);
        SCHEDB();
        acc[1] = MFMA16(EA[0][1], EB[0], acc[1]);
        OA[0][1] = *(const LAS bf16x8*)(abase + 7*G2_STAGE + 1024);
        OA[0][2] = *(const LAS bf16x8*)(abase + 7*G2_STAGE + 2048);
        SCHEDB();
        acc[2] = MFMA16(EA[0][2], EB[0], acc[2]);
        OA[0][3] = *(const LAS bf16x8*)(abase + 7*G2_STAGE + 3072);
        SCHEDB();
        acc[3] = MFMA16(EA[0][3], EB[0], acc[3]);
        OA[0][4] = *(const LAS bf16x8*)(abase + 7*G2_STAGE + 4096);
        SCHEDB();
        acc[4] = MFMA16(EA[0][4], EB[0], acc[4]);
        OB[1] = *(const LAS bf16x8*)(bbase + 7*G2_STAGE + 1024);
        SCHEDB();
        acc[0] = MFMA16(EA[1][0], EB[1], acc[0]);
        OA[1][0] = *(const LAS bf16x8*)(abase + 7*G2_STAGE + 5120);
        SCHEDB();
        acc[1] = MFMA16(EA[1][1], EB[1], acc[1]);
        OA[1][1] = *(const LAS bf16x8*)(abase + 7*G2_STAGE + 6144);
        SCHEDB();
        acc[2] = MFMA16(EA[1][2], EB[1], acc[2]);
        OA[1][2] = *(const LAS bf16x8*)(abase + 7*G2_STAGE + 7168);
        SCHEDB();
        acc[3] = MFMA16(EA[1][3], EB[1], acc[3]);
        OA[1][3] = *(const LAS bf16x8*)(abase + 7*G2_STAGE + 8192);
        SCHEDB();
        acc[4] = MFMA16(EA[1][4], EB[1], acc[4]);
        OA[1][4] = *(const LAS bf16x8*)(abase + 7*G2_STAGE + 9216);
        SCHEDB();
        if (it < 2) BAR();
        acc[0] = MFMA16(OA[0][0], OB[0], acc[0]);
        EB[0] = *(const LAS bf16x8*)(bbase + 0*G2_STAGE);
        EA[0][0] = *(const LAS bf16x8*)(abase + 0*G2_STAGE + 0);
        SCHEDB();
        acc[1] = MFMA16(OA[0][1], OB[0], acc[1]);
        EA[0][1] = *(const LAS bf16x8*)(abase + 0*G2_STAGE + 1024);
        EA[0][2] = *(const LAS bf16x8*)(abase + 0*G2_STAGE + 2048);
        SCHEDB();
        acc[2] = MFMA16(OA[0][2], OB[0], acc[2]);
        EA[0][3] = *(const LAS bf16x8*)(abase + 0*G2_STAGE + 3072);
        SCHEDB();
        acc[3] = MFMA16(OA[0][3], OB[0], acc[3]);
        EA[0][4] = *(const LAS bf16x8*)(abase + 0*G2_STAGE + 4096);
        SCHEDB();
        acc[4] = MFMA16(OA[0][4], OB[0], acc[4]);
        EB[1] = *(const LAS bf16x8*)(bbase + 0*G2_STAGE + 1024);
        SCHEDB();
        acc[0] = MFMA16(OA[1][0], OB[1], acc[0]);
        EA[1][0] = *(const LAS bf16x8*)(abase + 0*G2_STAGE + 5120);
        SCHEDB();
        acc[1] = MFMA16(OA[1][1], OB[1], acc[1]);
        EA[1][1] = *(const LAS bf16x8*)(abase + 0*G2_STAGE + 6144);
        SCHEDB();
        acc[2] = MFMA16(OA[1][2], OB[1], acc[2]);
        EA[1][2] = *(const LAS bf16x8*)(abase + 0*G2_STAGE + 7168);
        SCHEDB();
        acc[3] = MFMA16(OA[1][3], OB[1], acc[3]);
        EA[1][3] = *(const LAS bf16x8*)(abase + 0*G2_STAGE + 8192);
        SCHEDB();
        acc[4] = MFMA16(OA[1][4], OB[1], acc[4]);
        EA[1][4] = *(const LAS bf16x8*)(abase + 0*G2_STAGE + 9216);
        SCHEDB();
    }
    STAMP(ws, 5, 6);
    WAITV(0);
    const rsrc_t ro = mkrsrc(out, L_ * D_ * 2);
#pragma unroll
    for (int a = 0; a < 5; ++a) {
        const s16x4 hr = trread((const LAS char*)smem + G2_RES + w * 2560 + (16 * a + 4 * g + q) * 32 + 8 * p);
        const u32x2 hh = __builtin_bit_cast(u32x2, hr);
        const u32 f01 = cvtpk(acc[a][0] * rs2, acc[a][1] * rs2), f23 = cvtpk(acc[a][2] * rs2, acc[a][3] * rs2);
        const u32 c01 = hh[0], c23 = hh[1];
        const u32 h01 = cvtpk(bflo(c01) + m1, bfhi(c01) + m1), h23 = cvtpk(bflo(c23) + m1, bfhi(c23) + m1);
        __builtin_amdgcn_raw_buffer_store_b64(u32x2{cvtpk(bflo(h01) + bflo(f01), bfhi(h01) + bfhi(f01)), cvtpk(bflo(h23) + bflo(f23), bfhi(h23) + bfhi(f23))}, ro, (l * D_ + 80 * S + 16 * a + 4 * g) * 2, 0, 0);
    }
    STAMP(ws, 5, 7);
}

constexpr int ALL_LDS = 163840;
static_assert(KA_LDS <= ALL_LDS && G1_LDS <= ALL_LDS && G2_LDS <= ALL_LDS, "LDS");

__global__ __launch_bounds__(768, 3) void k_block(const u16* __restrict__ x, const u16* __restrict__ Are, const u16* __restrict__ Aim, const u16* __restrict__ Bre,
                                                  const u16* __restrict__ Bim, const u16* __restrict__ Cre, const u16* __restrict__ Cim, const u16* __restrict__ logdt,
                                                  const u16* __restrict__ Dskip, const u16* __restrict__ Wg, const u16* __restrict__ Wu, const u16* __restrict__ Wd,
                                                  char* __restrict__ ws, u16* __restrict__ out) {
    __shared__ __attribute__((aligned(1024))) char smem[ALL_LDS];
    if (threadIdx.x >= 704) {
        unsigned long long pc; u32 len;
        asm volatile("s_getpc_b64 %0\n.Lk_block_here:\n\ts_mov_b32 %1, .Lk_block_last-.Lk_block_here" : "=s"(pc), "=s"(len));
        const int lane = threadIdx.x & 63;
        const u32 off = ((blockIdx.x >> 3) * 14 + lane) * 128;
        const char* p = (const char*)(pc + off);
        const u32 dump = (u32)(uintptr_t)smem + ALL_LDS - 256;
        if (lane < 14 && off + 4 <= len) { u32 sv; asm volatile("s_mov_b32 %0, m0\n\ts_mov_b32 m0, %2\n\ts_nop 0\n\tglobal_load_lds_dword %1, off\n\ts_mov_b32 m0, %0" : "=&s"(sv) : "v"(p), "s"(dump) : "memory"); }
    }
    const u32 tag = phase_ssm(x, Are, Aim, Bre, Bim, Cre, Cim, logdt, Dskip, Wg, Wu, Wd, ws, smem);
    const float rs2 = phase_gemm1(ws, smem, tag);
    phase_gemm2(ws, out, smem, tag, rs2);
}

extern "C" void kernel_launch(void* const* d_in, const int* in_sizes, int n_in, void* d_out, int out_size, void* d_ws, size_t ws_size, hipStream_t stream) {
    const u16* x = (const u16*)d_in[0];
    const u16* Are = (const u16*)d_in[1]; const u16* Aim = (const u16*)d_in[2];
    const u16* Bre = (const u16*)d_in[3]; const u16* Bim = (const u16*)d_in[4];
    const u16* Cre = (const u16*)d_in[5]; const u16* Cim = (const u16*)d_in[6];
    const u16* logdt = (const u16*)d_in[7]; const u16* Dskip = (const u16*)d_in[8];
    const u16* Wg = (const u16*)d_in[10]; const u16* Wu = (const u16*)d_in[11]; const u16* Wd = (const u16*)d_in[12];
    k_block<<<256, 768, 0, stream>>>(x, Are, Aim, Bre, Bim, Cre, Cim, logdt, Dskip, Wg, Wu, Wd, (char*)d_ws, (u16*)d_out);
}
```

```cpp
#include <hip/hip_runtime.h>
#include <stdint.h>

typedef unsigned short u16;
typedef unsigned int u32;
typedef short bf16x8 __attribute__((ext_vector_type(8)));
typedef short s16x4 __attribute__((ext_vector_type(4)));
typedef float f32x2 __attribute__((ext_vector_type(2)));
typedef float f32x4 __attribute__((ext_vector_type(4)));
typedef float f32x16 __attribute__((ext_vector_type(16)));
typedef unsigned u32x4 __attribute__((ext_vector_type(4)));
typedef unsigned u32x2 __attribute__((ext_vector_type(2)));
typedef __bf16 bf16x2_t __attribute__((ext_vector_type(2)));
#define LAS __attribute__((address_space(3)))
#define DI __device__ __forceinline__

constexpr int L_ = 2048, D_ = 640, NM_ = 192, F_ = 1536;

constexpr u32 WS_HC  = 0;
constexpr u32 WS_HM  = WS_HC + D_ * L_ * 2;
constexpr u32 WS_W1T = WS_HM + L_ * F_ * 2;
constexpr u32 WS_W2T = WS_W1T + D_ * F_ * 2 * 2;
constexpr u32 WS_GR  = WS_W2T + F_ * D_ * 2;
constexpr u32 WS_F2  = WS_GR + 256 * 128;
constexpr u32 WS_FW  = WS_F2 + 256 * 128;
constexpr u32 WS_F3  = WS_FW + 128 * 128;
constexpr u32 WS_EP  = WS_F3 + 256 * 128;
constexpr u32 WS_END = WS_EP + 256;

DI float bflo(u32 v) { return __uint_as_float(v << 16); }
DI float bfhi(u32 v) { return __uint_as_float(v & 0xffff0000u); }
DI float bf2f(u16 v) { return __uint_as_float(((u32)v) << 16); }
DI u32 cvtpk(float lo, float hi) { f32x2 v = {lo, hi}; bf16x2_t r = __builtin_convertvector(v, bf16x2_t); return __builtin_bit_cast(u32, r); }
DI float rbf(float f) { return bflo(cvtpk(f, f)); }

typedef __amdgpu_buffer_rsrc_t rsrc_t;
DI rsrc_t mkrsrc(void* p, u32 bytes) { return __builtin_amdgcn_make_buffer_rsrc(p, 0, bytes, 0x00020000); }
template <int AUX = 0> DI void st16(rsrc_t r, u32 off, u32x4 v) { __builtin_amdgcn_raw_buffer_store_b128(v, r, off, 0, AUX); }
template <int AUX = 0> DI void st8(rsrc_t r, u32 off, u32x2 v) { __builtin_amdgcn_raw_buffer_store_b64(v, r, off, 0, AUX); }
template <int AUX = 0> DI void st4(rsrc_t r, u32 off, u32 v) { __builtin_amdgcn_raw_buffer_store_b32(v, r, off, 0, AUX); }

template <int CTRL> DI float dppadd(float v) { return v + __builtin_bit_cast(float, __builtin_amdgcn_update_dpp(0, __builtin_bit_cast(int, v), CTRL, 0xF, 0xF, true)); }
DI float swap16add(float v) { auto r = __builtin_amdgcn_permlane16_swap(__float_as_uint(v), __float_as_uint(v), false, false); const u32 a = r[0], b = r[1]; return __uint_as_float(a) + __uint_as_float(b); }
DI float swap32add(float v) { auto r = __builtin_amdgcn_permlane32_swap(__float_as_uint(v), __float_as_uint(v), false, false); const u32 a = r[0], b = r[1]; return __uint_as_float(a) + __uint_as_float(b); }
DI float wave_sum(float v) {
    v = dppadd<0xB1>(v);
    v = dppadd<0x4E>(v);
    v = dppadd<0x141>(v);
    v = dppadd<0x140>(v);
    return swap32add(swap16add(v));
}

struct cm { f32x2 a, s; };
DI cm mkcm(f32x2 b) { return { b, f32x2{-b.y, b.x} }; }
DI f32x2 cmul(f32x2 v, const cm& m) { return v.xx * m.a + v.yy * m.s; }
DI f32x2 csq(f32x2 a) { return f32x2{ a.x * a.x - a.y * a.y, 2.f * a.x * a.y }; }
DI f32x2 cexp_p(float ar, float br, float p) {
    float e = __builtin_amdgcn_exp2f(ar * p);
    float th = __builtin_amdgcn_fractf(br * p);
    return f32x2{ e * __builtin_amdgcn_cosf(th), e * __builtin_amdgcn_sinf(th) };
}

#define WAITV(n) asm volatile("s_waitcnt vmcnt(" #n ")" ::: "memory")
#define WAITL0() asm volatile("s_waitcnt lgkmcnt(0)" ::: "memory")
template <int N> DI void waitv() { asm volatile("s_waitcnt vmcnt(%0)" :: "n"(N) : "memory"); }
#define BAR() do { asm volatile("" ::: "memory"); __builtin_amdgcn_s_barrier(); asm volatile("" ::: "memory"); } while (0)
#define LBAR() do { WAITL0(); BAR(); } while (0)
#define SGB(m, n) __builtin_amdgcn_sched_group_barrier(m, n, 0)
#define SCHEDB() __builtin_amdgcn_sched_barrier(0)
#define MFMA16(a, b, c) __builtin_amdgcn_mfma_f32_16x16x32_bf16(a, b, c, 0, 0, 0)
#define GAS __attribute__((address_space(1)))
typedef unsigned long long u64;

DI void glds16(const void* g, u32 lds_base) {
    u32 sv; asm volatile("s_mov_b32 %0, m0\n\ts_mov_b32 m0, %2\n\ts_nop 0\n\tglobal_load_lds_dwordx4 %1, off sc1\n\ts_mov_b32 m0, %0" : "=&s"(sv) : "v"(g), "s"(lds_base) : "memory");
}
DI void poll_flags(rsrc_t wsr, u32 off, int n, u32 tag, int lane) {
    const u32 o = off + (lane < n ? lane : 0) * 128;
    for (int spin = 0; spin < 100000; ++spin) {
        asm volatile("" ::: "memory");
        const u32 v = __builtin_amdgcn_raw_buffer_load_b32(wsr, o, 0, 16);
        if (__all(v == tag)) break;
        __builtin_amdgcn_s_sleep(1);
    }
    asm volatile("" ::: "memory");
}
DI void st_flag(char* ws, u32 off, u32 tag) { __hip_atomic_store((GAS u32*)(ws + off), tag, __ATOMIC_RELAXED, __HIP_MEMORY_SCOPE_AGENT); }
DI s16x4 trread(const LAS char* p) { typedef short v4i16_t __attribute__((ext_vector_type(4))); return __builtin_bit_cast(s16x4, __builtin_amdgcn_ds_read_tr16_b64_v4i16((LAS v4i16_t*)p)); }
DI bf16x8 cat8(s16x4 a, s16x4 b) { return __builtin_shufflevector(a, b, 0, 1, 2, 3, 4, 5, 6, 7); }

constexpr int KG_PITCH = 784;
constexpr int KA_A = 0, KA_ASZ = 20 * KG_PITCH;
constexpr int KA_B = 3 * KA_ASZ, KA_BSZ = 32 * KG_PITCH;
constexpr int KA_U = KA_B + 3 * KA_BSZ, KA_USZ = 4608 + 4096;
constexpr int KA_CNT = KA_U + 3 * KA_USZ;
constexpr int KA_LDS = KA_CNT + 48;
constexpr int CV_CS = 4160;
constexpr int KB_X = 32256;

#define ADDTID2(base0, v0, o0, base1, v1, o1) asm volatile("s_mov_b32 m0, %2\n\ts_nop 0\n\tds_write_addtid_b32 %0 offset:%4\n\ts_mov_b32 m0, %3\n\ts_nop 0\n\tds_write_addtid_b32 %1 offset:%5" \
    :: "v"(v0), "v"(v1), "s"(base0), "s"(base1), "i"(o0), "i"(o1) : "memory")
#define ADDTID2S(base, v0, o0, v1, o1) asm volatile("s_mov_b32 m0, %2\n\ts_nop 0\n\tds_write_addtid_b32 %0 offset:%3\n\tds_write_addtid_b32 %1 offset:%4" :: "v"(v0), "v"(v1), "s"(base), "i"(o0), "i"(o1) : "memory")

DI void lds_sync(char* smem, int cnt, u32 target, int lane) {
    LAS u32* c = (LAS u32*)((LAS char*)smem + KA_CNT) + cnt;
    WAITL0();
    if (lane == 0) __hip_atomic_fetch_add(c, 1u, __ATOMIC_RELAXED, __HIP_MEMORY_SCOPE_WORKGROUP);
    for (int spin = 0; spin < 1000000; ++spin) {
        if (__builtin_amdgcn_readfirstlane(__hip_atomic_load(c, __ATOMIC_RELAXED, __HIP_MEMORY_SCOPE_WORKGROUP)) >= target) break;
        __builtin_amdgcn_s_sleep(1);
    }
    asm volatile("" ::: "memory");
}

DI void lds_arrive(char* smem, int cnt, int lane) {
    WAITL0();
    if (lane == 0) __hip_atomic_fetch_add((LAS u32*)((LAS char*)smem + KA_CNT) + cnt, 1u, __ATOMIC_RELAXED, __HIP_MEMORY_SCOPE_WORKGROUP);
}
DI void lds_wait(char* smem, int cnt, u32 target) {
    LAS u32* c = (LAS u32*)((LAS char*)smem + KA_CNT) + cnt;
    for (int spin = 0; spin < 1000000; ++spin) {
        if (__builtin_amdgcn_readfirstlane(__hip_atomic_load(c, __ATOMIC_RELAXED, __HIP_MEMORY_SCOPE_WORKGROUP)) >= target) break;
        __builtin_amdgcn_s_sleep(1);
    }
    asm volatile("" ::: "memory");
}

struct KParams { u16 are, aim, bre, bim, cre, cim, ldt; };

DI void ka_kgen(const KParams& P, char* smem, int sA, int sB, int w, int lane) {
    const float are = bf2f(P.are), aim = bf2f(P.aim), bre = bf2f(P.bre), bim = bf2f(P.bim), cre = bf2f(P.cre), cim = bf2f(P.cim);
    const float dt = rbf(__expf(bf2f(P.ldt)));
    const float ar = (dt * are) * 1.4426950408889634f, br = (dt * aim) * 0.15915494309189535f;
    const float dbr = dt * bre, dbi = dt * bim;
    const f32x2 Wc = { cre * dbr - cim * dbi, -(cre * dbi + cim * dbr) };
    const f32x2 l1 = cexp_p(ar, br, 1.f), l8 = cexp_p(ar, br, 8.f), l256 = cexp_p(ar, br, 256.f);
    const f32x2 l2 = csq(l1), l4 = csq(l2), l16 = csq(l8);
    const cm c1 = mkcm(f32x2{l1.x, -l1.y}), c2 = mkcm(f32x2{l2.x, -l2.y}), c4 = mkcm(f32x2{l4.x, -l4.y}), c256 = mkcm(f32x2{l256.x, -l256.y}), m16 = mkcm(l16);
    const u32 lds0 = (u32)(uintptr_t)smem;
    const u32 bA = lds0 + sA + w * 256, bB = lds0 + sB + w * 256 - KB_X;
    auto pk = [](f32x2 v) { return cvtpk(v.x, v.y); };
    constexpr int R = KG_PITCH, X = KB_X;
    f32x2 a0 = Wc, x256 = cmul(Wc, c256), e0 = {1.f, 0.f}, e1 = l8;
    ADDTID2S(bA, pk(a0), 0 * R, pk(x256), 8 * R);
    ADDTID2S(bB, pk(e0), X + 0 * R, pk(e1), X + 1 * R);
    f32x2 y = x256, v = cmul(x256, c256);
#define KG_E(c) do { e0 = cmul(e0, m16); e1 = cmul(e1, m16); ADDTID2S(bB, pk(e0), X + (c) * R, pk(e1), X + ((c) + 1) * R); } while (0)
    a0 = cmul(a0, c1); y = cmul(y, c2); ADDTID2S(bA, pk(a0), 1 * R, pk(y), 9 * R); KG_E(2);
    a0 = cmul(a0, c1); y = cmul(y, c2); ADDTID2S(bA, pk(a0), 2 * R, pk(y), 10 * R); KG_E(4);
    a0 = cmul(a0, c1); y = cmul(y, c2); ADDTID2S(bA, pk(a0), 3 * R, pk(y), 11 * R); KG_E(6);
    a0 = cmul(a0, c1); y = cmul(v, c4); ADDTID2S(bA, pk(a0), 4 * R, pk(v), 12 * R); v = cmul(v, c256); KG_E(8);
    a0 = cmul(a0, c1); ADDTID2S(bA, pk(a0), 5 * R, pk(y), 13 * R); y = cmul(v, c4); KG_E(10);
    a0 = cmul(a0, c1); ADDTID2S(bA, pk(a0), 6 * R, pk(v), 14 * R); v = cmul(v, c256); KG_E(12);
    a0 = cmul(a0, c1); ADDTID2S(bA, pk(a0), 7 * R, pk(y), 15 * R); KG_E(14);
    y = cmul(v, c256); KG_E(16);
    ADDTID2S(bA, pk(v), 16 * R, pk(y), 17 * R); v = cmul(y, c256); KG_E(18);
    y = cmul(v, c256); KG_E(20);
    ADDTID2S(bA, pk(v), 18 * R, pk(y), 19 * R); v = cmul(y, c256); KG_E(22);
    const float k2048 = wave_sum(v.x);
    KG_E(24); KG_E(26);
    KG_E(28); KG_E(30);
    const int r32 = lane & 31, h = lane >> 5;
    const char* pa = smem + sA + (r32 < 20 ? r32 : 19) * R + w * 256 + h * 16;
    const char* pb = smem + sB + r32 * R + w * 256 + h * 16;
    f32x16 acc = {};
#pragma unroll
    for (int s = 0; s < 8; ++s) {
        const bf16x8 a = *(const bf16x8*)(pa + 32 * s), bb = *(const bf16x8*)(pb + 32 * s);
        acc = __builtin_amdgcn_mfma_f32_32x32x16_bf16(a, bb, acc, 0, 0, 0);
    }
    const int c = r32;
    char* sl = smem + sA + w * 256;
    char* p0 = sl + (c >> 3) * R + (8 * (c & 7) + 4 * h) * 4;
    char* p1 = sl + (h ? 6 * R + 8 * c : (4 + (c >> 4)) * R + 16 * (c & 15));
    char* p2 = sl + 8 * R + 4 * c;
#pragma unroll
    for (int q = 0; q < 4; ++q) *(float*)(p0 + 4 * q) = acc[q];
    *(float*)(p1) = acc[4]; *(float*)(p1 + 4) = acc[5];
    *(float*)(p1 + (h ? R : 8)) = acc[6]; *(float*)(p1 + (h ? R + 4 : 12)) = acc[7];
    if (h == 0) { *(float*)(p2) = acc[8]; *(float*)(p2 + 128) = acc[9]; *(float*)(p2 + R) = acc[10]; *(float*)(p2 + R + 128) = acc[11]; }
    else if (c == 0) *(float*)(sl + 10 * R) = k2048;
}

DI void ka_expand(char* smem, int sA, int sB, int w, int lane) {
    const int t = 64 * w + lane;
    auto rd = [&](int i) { const char* p = smem + sA + (i >> 6) * KG_PITCH + (i & 63) * 4; return *(const float*)p + *(const float*)(p + 256) + *(const float*)(p + 512); };
    u32 k4, k5, k6, k7, f2, f3;
    if (w == 0) {
        const int i0 = t < 32 ? 8 * t : 256 + 4 * (t - 32);
        const u32 pat = t < 32 ? 0x76543210u : 0x43222100u;
        float kv[8];
#pragma unroll
        for (int j = 0; j < 8; ++j) kv[j] = rd(i0 + ((pat >> (4 * j)) & 15));
        k4 = cvtpk(kv[0], kv[1]); k5 = cvtpk(kv[2], kv[3]); k6 = cvtpk(kv[4], kv[5]); k7 = cvtpk(kv[6], kv[7]);
        f2 = 0; f3 = 0;
    } else if (w == 1) {
        const int i0 = 384 + 2 * (t - 64);
        const float k0 = rd(i0), k1 = rd(i0 + 1), k2 = rd(i0 + 2);
        k4 = cvtpk(k0, k0); k5 = cvtpk(k0, k1); k6 = cvtpk(k1, k1); k7 = cvtpk(k2, k2);
        const float a = rd(382), b = rd(383);
        f2 = cvtpk(a, a); f3 = cvtpk(b, k0);
    } else {
        const int i0 = 512 + (t - 128);
        const float k0 = rd(i0), k1 = rd(i0 + 1);
        k4 = cvtpk(k0, k0); k5 = k4; k6 = cvtpk((t & 1) ? k1 : k0, k1); k7 = cvtpk(k1, k1);
        const float a = w == 2 ? rd(511) : k0;
        f2 = cvtpk(a, a); f3 = cvtpk(k0, k0);
    }
    const u32 k2 = __builtin_amdgcn_update_dpp(f2, k6, 0x138, 0xF, 0xF, false), k3 = __builtin_amdgcn_update_dpp(f3, k7, 0x138, 0xF, 0xF, false);
    const u32 a2 = __builtin_amdgcn_alignbit(k3, k2, 16), a3 = __builtin_amdgcn_alignbit(k4, k3, 16), a4 = __builtin_amdgcn_alignbit(k5, k4, 16),
              a5 = __builtin_amdgcn_alignbit(k6, k5, 16), a6 = __builtin_amdgcn_alignbit(k7, k6, 16);
    char* kb = smem + sB + 32 + 16 * t;
    *(u32x4*)(kb + 0 * CV_CS) = u32x4{k4, k5, k6, k7};
    *(u32x4*)(kb + 1 * CV_CS) = u32x4{a3, a4, a5, a6};
    *(u32x4*)(kb + 2 * CV_CS) = u32x4{k3, k4, k5, k6};
    *(u32x4*)(kb + 3 * CV_CS) = u32x4{a2, a3, a4, a5};
    if (t < 8) *(u32x4*)(smem + sB + (t >> 1) * CV_CS + 16 * (t & 1)) = u32x4{0, 0, 0, 0};
}

DI u32 phase_ssm(const u16* __restrict__ x, const u16* __restrict__ Are, const u16* __restrict__ Aim, const u16* __restrict__ Bre,
                  const u16* __restrict__ Bim, const u16* __restrict__ Cre, const u16* __restrict__ Cim, const u16* __restrict__ logdt,
                  const u16* __restrict__ Dskip, const u16* __restrict__ Wg, const u16* __restrict__ Wu, const u16* __restrict__ Wd,
                  char* __restrict__ ws, char* smem) {
    const int b = blockIdx.x, t = threadIdx.x, lane = t & 63;
    const int wv = __builtin_amdgcn_readfirstlane(t >> 6), sb = wv >> 2, w = wv & 3;
    const int xcd = b & 7, j = b >> 3;
    const int nch = j < 16 ? 3 : 2;
    const int c0 = 80 * xcd + ((xcd & 2) ? (j < 16 ? 32 + 3 * j : 2 * (j - 16)) : (j < 16 ? 3 * j : 48 + 2 * (j - 16)));
    const int xs = min((2 * c0) & ~3, ((2 * c0) | 127) + 1 - 8), e0 = (2 * c0 - xs) >> 1;
    const bool conv = sb < nch;
    const int d = c0 + sb;
    const int sA = KA_A + sb * KA_ASZ, sB = KA_B + sb * KA_BSZ, sU = KA_U + sb * KA_USZ;
    const rsrc_t wsr = mkrsrc(ws, WS_END);
    if (t < 8) *(u32*)(smem + KA_CNT + 4 * t) = 0u;

    KParams KP;
    u32 ra[4][5];
    u32x4 wv1[8], wv2[8];
    const int tt = t - 512;
    const bool w1job = (j - 16) < 12;
    float dsk = 0.f;
    if (wv == 3 || wv == 7) {
        const u32* x32 = (const u32*)x;
#pragma unroll
        for (int rr = 0; rr < 4; ++rr)
#pragma unroll
            for (int i = 0; i < 5; ++i) ra[rr][i] = x32[(8 * b + 4 * sb + rr) * 320 + lane + 64 * i];
    }
    if (conv) {
        if (w < 3) {
            const int n = d * NM_ + 64 * w + lane;
            KP.are = Are[n]; KP.aim = Aim[n]; KP.bre = Bre[n]; KP.bim = Bim[n]; KP.cre = Cre[n]; KP.cim = Cim[n]; KP.ldt = logdt[d];
        }
        dsk = bf2f(Dskip[d]);
    }
    const u32 epoch = __hip_atomic_load((GAS u32*)(ws + WS_EP), __ATOMIC_RELAXED, __HIP_MEMORY_SCOPE_AGENT);
    const rsrc_t xr0 = mkrsrc((void*)x, L_ * D_ * 2);
    LBAR();
    u32x2 xg[4];
    const int pi0 = 128 * wv + lane, pi1 = pi0 + 64;
    const bool early = w != 3;
    if (wv < 8 && early) {
        xg[0] = __builtin_amdgcn_raw_buffer_load_b64(xr0, (2 * pi0 + 0) * (D_ * 2) + xs, 0, 0);
        xg[1] = __builtin_amdgcn_raw_buffer_load_b64(xr0, (2 * pi0 + 1) * (D_ * 2) + xs, 0, 0);
    }
    if (conv && w == 3) {
        if (lane < 30) *(u32x4*)(smem + sU + 16 * lane) = u32x4{0, 0, 0, 0};
        lds_arrive(smem, sb, lane);
    }
    if (!conv) {
        if (w1job) {
            const int u1 = xcd * 12 + (j - 16), up = u1 >= 48, col = 32 * (up ? u1 - 48 : u1) + 8 * (tt & 3);
            const u16* src = (up ? Wu : Wg) + (8 * (tt >> 2)) * F_ + col;
#pragma unroll
            for (int r = 0; r < 8; ++r) wv1[r] = *(const u32x4*)(src + r * F_);
            if (w == 0) {
#pragma unroll
                for (int r = 0; r < 8; ++r) wv2[r] = *(const u32x4*)(src + (512 + r) * F_);
            }
        } else {
            lds_wait(smem, 4, 1);
            const int n1 = 480 * (xcd * 4 + (j - 28)) + tt, n2 = n1 + 256;
            const u16* s1 = Wd + (8 * (n1 / 80)) * D_ + 8 * (n1 % 80);
#pragma unroll
            for (int r = 0; r < 8; ++r) wv1[r] = *(const u32x4*)(s1 + r * D_);
            if (tt < 224) {
                const u16* s2 = Wd + (8 * (n2 / 80)) * D_ + 8 * (n2 % 80);
#pragma unroll
                for (int r = 0; r < 8; ++r) wv2[r] = *(const u32x4*)(s2 + r * D_);
            }
        }
    }
    const u32 tag = __builtin_amdgcn_readfirstlane(epoch) + 1u;
    if (wv == 3 || wv == 7) {
        __builtin_amdgcn_s_setprio(3);
        float s1[4], s2[4], c0v[4];
#pragma unroll
        for (int rr = 0; rr < 4; ++rr) {
            const float c = bflo(__builtin_amdgcn_readfirstlane(ra[rr][0]));
            float a1 = 0.f, a2 = 0.f;
#pragma unroll
            for (int i = 0; i < 5; ++i) { const float a = bflo(ra[rr][i]) - c, e = bfhi(ra[rr][i]) - c; a1 += a + e; a2 = __builtin_fmaf(a, a, __builtin_fmaf(e, e, a2)); }
            s1[rr] = a1; s2[rr] = a2; c0v[rr] = c;
        }
        auto red4 = [&](const float (&v)[4]) {
            auto p01 = __builtin_amdgcn_permlane32_swap(__float_as_uint(v[0]), __float_as_uint(v[1]), false, false);
            auto p23 = __builtin_amdgcn_permlane32_swap(__float_as_uint(v[2]), __float_as_uint(v[3]), false, false);
            const u32 x0 = p01[0], x1 = p01[1], y0 = p23[0], y1 = p23[1];
            const float t01 = __uint_as_float(x0) + __uint_as_float(x1);
            const float t23 = __uint_as_float(y0) + __uint_as_float(y1);
            auto q = __builtin_amdgcn_permlane16_swap(__float_as_uint(t01), __float_as_uint(t23), false, false);
            const u32 z0 = q[0], z1 = q[1];
            float r = __uint_as_float(z0) + __uint_as_float(z1);
            r = dppadd<0xB1>(r); r = dppadd<0x4E>(r); r = dppadd<0x141>(r); r = dppadd<0x140>(r);
            return r;
        };
        const float n1 = red4(s1) * (1.f / 640.f), n2 = red4(s2) * (1.f / 640.f);
        const int lr = lane >> 4, rr = (lr >> 1) | ((lr & 1) << 1);
        const float cc = rr == 0 ? c0v[0] : rr == 1 ? c0v[1] : rr == 2 ? c0v[2] : c0v[3];
        float var = n2 - n1 * n1; var = var > 0.f ? var : 0.f;
        const float sd = __builtin_sqrtf(rbf(rbf(var) + 1e-5f));
        const u32 pay = cvtpk(cc + n1, sd);
        u32* hand = (u32*)(smem + KA_CNT + 32);
        if (wv == 7) {
            if ((lane & 15) == 0) hand[rr] = pay;
            lds_arrive(smem, 6, lane);
        } else {
            u32 mine = __builtin_amdgcn_ds_bpermute(((lane & 1) << 5 | (lane & 2) << 3) << 2, pay);
            lds_wait(smem, 6, 1);
            if (lane >= 4 && lane < 8) mine = hand[lane - 4];
            if (lane < 8) __hip_atomic_store((GAS u64*)(ws + WS_GR + b * 128) + lane, ((u64)tag << 32) | mine, __ATOMIC_RELAXED, __HIP_MEMORY_SCOPE_AGENT);
            asm volatile("" ::: "memory");
            if (lane == 0) __hip_atomic_fetch_add((LAS u32*)((LAS char*)smem + KA_CNT) + 4, 1u, __ATOMIC_RELAXED, __HIP_MEMORY_SCOPE_WORKGROUP);
        }
        __builtin_amdgcn_s_setprio(0);
    }
    if (conv) {
        if (w < 3) ka_kgen(KP, smem, sA, sB, w, lane);
    } else if (w1job) {
        float* red = (float*)(smem + sA);
#pragma unroll
        for (int i = 0; i < 4; ++i) {
            float a = 0.f, c = 0.f;
#pragma unroll
            for (int r = 0; r < 8; ++r) { a += bflo(wv1[r][i]); c += bfhi(wv1[r][i]); }
            if (w == 0) {
#pragma unroll
                for (int r = 0; r < 8; ++r) { a += bflo(wv2[r][i]); c += bfhi(wv2[r][i]); }
            }
            red[(tt >> 2) * 33 + 8 * (tt & 3) + 2 * i] = a; red[(tt >> 2) * 33 + 8 * (tt & 3) + 2 * i + 1] = c;
        }
    }
    if (!conv) {
        __builtin_amdgcn_s_setprio(2);
        {
            if (w1job) {
            lds_sync(smem, sb, 4, lane);
            const float* red = (const float*)(smem + sA);
            const int col = tt & 31, part = tt >> 5;
            float sm = 0.f;
#pragma unroll
            for (int r = 0; r < 8; ++r) sm += red[(8 * part + r) * 33 + col];
            sm = swap32add(sm);
            if (lane < 32) *(float*)(smem + sB + (col * 4 + w) * 4) = sm;
            lds_sync(smem, sb, 8, lane);
#define WV(r) (rnd ? wv2[r][i] : wv1[r][i])
            }
            char* stg = smem + (w == 3 ? sA + 1024 : sB + 512 + w * 8192);
            u32* dtab = (u32*)(smem + sU + w * 256);
            auto flush = [&](u32 dst, bool active, const u32x4 (&o)[8]) {
                dtab[lane] = active ? dst : 0xffffff00u;
#pragma unroll
                for (int i = 0; i < 8; ++i) *(u32x4*)(stg + lane * 128 + ((i ^ (lane & 7)) * 16)) = o[i];
#pragma unroll
                for (int i = 0; i < 8; ++i) {
                    const int sl = 8 * i + (lane >> 3), pc = lane & 7;
                    const u32x4 v = *(const u32x4*)(stg + sl * 128 + ((pc ^ (sl & 7)) * 16));
                    st16<16>(wsr, dtab[sl] + pc * 16, v);
                }
            };
            if (w1job) {
                const int u1 = xcd * 12 + (j - 16), up = u1 >= 48, f = 32 * (up ? u1 - 48 : u1) + 8 * (tt & 3);
                float cm[8];
#pragma unroll
                for (int i = 0; i < 8; ++i) { const f32x4 v = *(const f32x4*)(smem + sB + (8 * (tt & 3) + i) * 16); cm[i] = (v[0] + v[1] + v[2] + v[3]) * (1.f / 640.f); }
                const int sl = f / 96, fl = f % 96, mt = 2 * (fl >> 4) + up;
#pragma unroll
                for (int rnd = 0; rnd < 2; ++rnd) {
                    if (rnd == 0 || w == 0) {
                        const int kg = (tt >> 2) + 64 * rnd;
                        const u32 dst = WS_W1T + ((sl * 10 + (kg >> 3)) * 24 + ((kg >> 2) & 1) * 12 + mt) * 1024 + ((fl & 15) + 16 * (kg & 3)) * 16;
                        u32x4 o[8];
#pragma unroll
                        for (int i = 0; i < 4; ++i) {
                            const float ca = cm[2 * i], cb = cm[2 * i + 1];
                            o[2 * i] = u32x4{cvtpk(bflo(WV(0)) - ca, bflo(WV(1)) - ca), cvtpk(bflo(WV(2)) - ca, bflo(WV(3)) - ca),
                                             cvtpk(bflo(WV(4)) - ca, bflo(WV(5)) - ca), cvtpk(bflo(WV(6)) - ca, bflo(WV(7)) - ca)};
                            o[2 * i + 1] = u32x4{cvtpk(bfhi(WV(0)) - cb, bfhi(WV(1)) - cb), cvtpk(bfhi(WV(2)) - cb, bfhi(WV(3)) - cb),
                                                 cvtpk(bfhi(WV(4)) - cb, bfhi(WV(5)) - cb), cvtpk(bfhi(WV(6)) - cb, bfhi(WV(7)) - cb)};
                        }
                        flush(dst, true, o);
                    }
                }
            } else {
#pragma unroll
                for (int rnd = 0; rnd < 2; ++rnd) {
                    if (rnd == 0 || w < 3 || true) {
                        const bool act = rnd == 0 || tt < 224;
                        const int n = 480 * (xcd * 4 + (j - 28)) + tt + 256 * rnd, dc = n % 80, kg = n / 80;
                        const int ds = dc / 10, dl = 8 * (dc % 10);
                        const u32 dst = WS_W2T + ((ds * 24 + (kg >> 3)) * 10 + ((kg >> 2) & 1) * 5 + (dl >> 4)) * 1024 + ((dl & 15) + 16 * (kg & 3)) * 16;
                        u32x4 o[8];
#pragma unroll
                        for (int i = 0; i < 4; ++i) {
                            o[2 * i] = u32x4{__builtin_amdgcn_perm(WV(1), WV(0), 0x05040100u), __builtin_amdgcn_perm(WV(3), WV(2), 0x05040100u),
                                             __builtin_amdgcn_perm(WV(5), WV(4), 0x05040100u), __builtin_amdgcn_perm(WV(7), WV(6), 0x05040100u)};
                            o[2 * i + 1] = u32x4{__builtin_amdgcn_perm(WV(1), WV(0), 0x07060302u), __builtin_amdgcn_perm(WV(3), WV(2), 0x07060302u),
                                                 __builtin_amdgcn_perm(WV(5), WV(4), 0x07060302u), __builtin_amdgcn_perm(WV(7), WV(6), 0x07060302u)};
                        }
                        flush(dst, act, o);
                    }
                }
            }
        }
        __builtin_amdgcn_s_setprio(0);
        WAITV(0);
        lds_sync(smem, sb, w1job ? 12 : 4, lane);
        if (t == 512) st_flag(ws, WS_FW + (xcd * 16 + (j - 16)) * 128, tag);
        return tag;
    }
    u32x4 g0 = {0, 0, 0, 0}, g1 = {0, 0, 0, 0};
    if (wv < 8) {
        lds_wait(smem, 4, 1);
        if (!early) {
            xg[0] = __builtin_amdgcn_raw_buffer_load_b64(xr0, (2 * pi0 + 0) * (D_ * 2) + xs, 0, 0);
            xg[1] = __builtin_amdgcn_raw_buffer_load_b64(xr0, (2 * pi0 + 1) * (D_ * 2) + xs, 0, 0);
        }
        xg[2] = __builtin_amdgcn_raw_buffer_load_b64(xr0, (2 * pi1 + 0) * (D_ * 2) + xs, 0, 0);
        xg[3] = __builtin_amdgcn_raw_buffer_load_b64(xr0, (2 * pi1 + 1) * (D_ * 2) + xs, 0, 0);
        g0 = __builtin_amdgcn_raw_buffer_load_b128(wsr, WS_GR + (pi0 >> 2) * 128 + (pi0 & 3) * 16, 0, 16);
        g1 = __builtin_amdgcn_raw_buffer_load_b128(wsr, WS_GR + (pi1 >> 2) * 128 + (pi1 & 3) * 16, 0, 16);
    }
    if (w < 3) lds_sync(smem, sb, 4, lane); else lds_wait(smem, sb, 4);
    ka_expand(smem, sA, sB, w, lane);
    if (wv < 8) {
        for (int spin = 0; spin < 100000; ++spin) {
            if (__all(g0[1] == tag && g0[3] == tag && g1[1] == tag && g1[3] == tag)) break;
            __builtin_amdgcn_s_sleep(4);
            asm volatile("" ::: "memory");
            g0 = __builtin_amdgcn_raw_buffer_load_b128(wsr, WS_GR + (pi0 >> 2) * 128 + (pi0 & 3) * 16, 0, 16);
            g1 = __builtin_amdgcn_raw_buffer_load_b128(wsr, WS_GR + (pi1 >> 2) * 128 + (pi1 & 3) * 16, 0, 16);
        }
        const u32 pr[4] = {g0[0], g0[2], g1[0], g1[2]};
        float m[4], rs[4];
#pragma unroll
        for (int r = 0; r < 4; ++r) { m[r] = bflo(pr[r]); rs[r] = __builtin_amdgcn_rcpf(bfhi(pr[r])); }
#pragma unroll
        for (int s = 0; s < 3; ++s) {
            if (s < nch) {
                const int e = e0 + s;
                float xv[4];
#pragma unroll
                for (int r = 0; r < 4; ++r) { const u32 dw = e < 2 ? xg[r][0] : xg[r][1]; xv[r] = (e & 1) ? bfhi(dw) : bflo(dw); }
                const u32 q01 = cvtpk(xv[0] - m[0], xv[1] - m[1]), q23 = cvtpk(xv[2] - m[2], xv[3] - m[3]);
                const u32 u01 = cvtpk(bflo(q01) * rs[0], bfhi(q01) * rs[1]), u23 = cvtpk(bflo(q23) * rs[2], bfhi(q23) * rs[3]);
                char* ub = smem + KA_U + s * KA_USZ;
                *(u32*)(ub + 480 + 4 * pi0) = u01; *(u32*)(ub + 480 + 4 * pi1) = u23;
                *(u32*)(ub + 4608 + 4 * pi0) = q01; *(u32*)(ub + 4608 + 4 * pi1) = q23;
            }
        }
    }
    lds_sync(smem, 3, 4 * nch, lane);
    const int al = lane & 15, g = lane >> 4;
    f32x4 acc[8];
#pragma unroll
    for (int i = 0; i < 8; ++i) acc[i] = f32x4{0.f, 0.f, 0.f, 0.f};
    {
        const LAS char* lds = (const LAS char*)smem;
        const int q = (lane & 15) >> 2, p = lane & 3;
        const LAS char* ub0 = lds + sU + 448 + 32 * al + 16 * g - 128 * w;
        const LAS char* ub1 = ub0 - 64;
        const LAS char* kb0 = lds + sB + q * CV_CS + 56 - 16 * g + 8 * p + 128 * w;
        const LAS char* kb1 = kb0 + 64;
        bf16x8 U0[8], U1[8]; s16x4 A0[8][2], A1[8][2];
        A0[0][0] = trread(kb0 + 8); A0[0][1] = trread(kb0 + 0); U0[0] = *(const LAS bf16x8*)(ub0 + 0); U0[1] = *(const LAS bf16x8*)(ub0 + 512); U0[2] = *(const LAS bf16x8*)(ub0 + 1024); U0[3] = *(const LAS bf16x8*)(ub0 + 1536); U0[4] = *(const LAS bf16x8*)(ub0 + 2048); U0[5] = *(const LAS bf16x8*)(ub0 + 2560); U0[6] = *(const LAS bf16x8*)(ub0 + 3072); U0[7] = *(const LAS bf16x8*)(ub0 + 3584); A0[1][0] = trread(kb0 + 520); A0[1][1] = trread(kb0 + 512); A0[2][0] = trread(kb0 + 1032); A0[2][1] = trread(kb0 + 1024); A0[3][0] = trread(kb0 + 1544); A0[3][1] = trread(kb0 + 1536); A0[4][0] = trread(kb0 + 2056); A0[4][1] = trread(kb0 + 2048); A0[5][0] = trread(kb0 + 2568); A0[5][1] = trread(kb0 + 2560); A0[6][0] = trread(kb0 + 3080); A0[6][1] = trread(kb0 + 3072); A0[7][0] = trread(kb0 + 3592); A0[7][1] = trread(kb0 + 3584);
        SCHEDB();
        acc[0] = MFMA16(cat8(A0[0][0], A0[0][1]), U0[0], acc[0]);
        SCHEDB();
        acc[1] = MFMA16(cat8(A0[0][0], A0[0][1]), U0[1], acc[1]);
        SCHEDB();
        acc[2] = MFMA16(cat8(A0[0][0], A0[0][1]), U0[2], acc[2]);
        SCHEDB();
        acc[3] = MFMA16(cat8(A0[0][0], A0[0][1]), U0[3], acc[3]);
        SCHEDB();
        acc[4] = MFMA16(cat8(A0[0][0], A0[0][1]), U0[4], acc[4]);
        SCHEDB();
        acc[5] = MFMA16(cat8(A0[0][0], A0[0][1]), U0[5], acc[5]);
        SCHEDB();
        acc[6] = MFMA16(cat8(A0[0][0], A0[0][1]), U0[6], acc[6]);
        SCHEDB();
        acc[7] = MFMA16(cat8(A0[0][0], A0[0][1]), U0[7], acc[7]);
        SCHEDB();
        acc[1] = MFMA16(cat8(A0[1][0], A0[1][1]), U0[0], acc[1]);
        A1[0][0] = trread(kb1 + 8);
        SCHEDB();
        acc[2] = MFMA16(cat8(A0[1][0], A0[1][1]), U0[1], acc[2]);
        A1[0][1] = trread(kb1 + 0);
        SCHEDB();
        acc[3] = MFMA16(cat8(A0[1][0], A0[1][1]), U0[2], acc[3]);
        U1[0] = *(const LAS bf16x8*)(ub1 + 0);
        SCHEDB();
        acc[4] = MFMA16(cat8(A0[1][0], A0[1][1]), U0[3], acc[4]);
        U1[1] = *(const LAS bf16x8*)(ub1 + 512);
        SCHEDB();
        acc[5] = MFMA16(cat8(A0[1][0], A0[1][1]), U0[4], acc[5]);
        U1[2] = *(const LAS bf16x8*)(ub1 + 1024);
        SCHEDB();
        acc[6] = MFMA16(cat8(A0[1][0], A0[1][1]), U0[5], acc[6]);
        U1[3] = *(const LAS bf16x8*)(ub1 + 1536);
        SCHEDB();
        acc[7] = MFMA16(cat8(A0[1][0], A0[1][1]), U0[6], acc[7]);
        U1[4] = *(const LAS bf16x8*)(ub1 + 2048);
        SCHEDB();
        acc[2] = MFMA16(cat8(A0[2][0], A0[2][1]), U0[0], acc[2]);
        U1[5] = *(const LAS bf16x8*)(ub1 + 2560);
        SCHEDB();
        acc[3] = MFMA16(cat8(A0[2][0], A0[2][1]), U0[1], acc[3]);
        U1[6] = *(const LAS bf16x8*)(ub1 + 3072);
        SCHEDB();
        acc[4] = MFMA16(cat8(A0[2][0], A0[2][1]), U0[2], acc[4]);
        U1[7] = *(const LAS bf16x8*)(ub1 + 3584);
        SCHEDB();
        acc[5] = MFMA16(cat8(A0[2][0], A0[2][1]), U0[3], acc[5]);
        A1[1][0] = trread(kb1 + 520);
        SCHEDB();
        acc[6] = MFMA16(cat8(A0[2][0], A0[2][1]), U0[4], acc[6]);
        A1[1][1] = trread(kb1 + 512);
        SCHEDB();
        acc[7] = MFMA16(cat8(A0[2][0], A0[2][1]), U0[5], acc[7]);
        A1[2][0] = trread(kb1 + 1032);
        SCHEDB();
        acc[3] = MFMA16(cat8(A0[3][0], A0[3][1]), U0[0], acc[3]);
        A1[2][1] = trread(kb1 + 1024);
        SCHEDB();
        acc[4] = MFMA16(cat8(A0[3][0], A0[3][1]), U0[1], acc[4]);
        A1[3][0] = trread(kb1 + 1544);
        SCHEDB();
        acc[5] = MFMA16(cat8(A0[3][0], A0[3][1]), U0[2], acc[5]);
        A1[3][1] = trread(kb1 + 1536);
        SCHEDB();
        acc[6] = MFMA16(cat8(A0[3][0], A0[3][1]), U0[3], acc[6]);
        A1[4][0] = trread(kb1 + 2056);
        SCHEDB();
        acc[7] = MFMA16(cat8(A0[3][0], A0[3][1]), U0[4], acc[7]);
        A1[4][1] = trread(kb1 + 2048);
        SCHEDB();
        acc[4] = MFMA16(cat8(A0[4][0], A0[4][1]), U0[0], acc[4]);
        A1[5][0] = trread(kb1 + 2568);
        SCHEDB();
        acc[5] = MFMA16(cat8(A0[4][0], A0[4][1]), U0[1], acc[5]);
        A1[5][1] = trread(kb1 + 2560);
        SCHEDB();
        acc[6] = MFMA16(cat8(A0[4][0], A0[4][1]), U0[2], acc[6]);
        A1[6][0] = trread(kb1 + 3080);
        SCHEDB();
        acc[7] = MFMA16(cat8(A0[4][0], A0[4][1]), U0[3], acc[7]);
        A1[6][1] = trread(kb1 + 3072);
        SCHEDB();
        acc[5] = MFMA16(cat8(A0[5][0], A0[5][1]), U0[0], acc[5]);
        A1[7][0] = trread(kb1 + 3592);
        SCHEDB();
        acc[6] = MFMA16(cat8(A0[5][0], A0[5][1]), U0[1], acc[6]);
        A1[7][1] = trread(kb1 + 3584);
        SCHEDB();
        acc[7] = MFMA16(cat8(A0[5][0], A0[5][1]), U0[2], acc[7]);
        SCHEDB();
        acc[6] = MFMA16(cat8(A0[6][0], A0[6][1]), U0[0], acc[6]);
        SCHEDB();
        acc[7] = MFMA16(cat8(A0[6][0], A0[6][1]), U0[1], acc[7]);
        SCHEDB();
        acc[7] = MFMA16(cat8(A0[7][0], A0[7][1]), U0[0], acc[7]);
        SCHEDB();
        acc[0] = MFMA16(cat8(A1[0][0], A1[0][1]), U1[0], acc[0]); SCHEDB();
        acc[1] = MFMA16(cat8(A1[0][0], A1[0][1]), U1[1], acc[1]); SCHEDB();
        acc[2] = MFMA16(cat8(A1[0][0], A1[0][1]), U1[2], acc[2]); SCHEDB();
        acc[3] = MFMA16(cat8(A1[0][0], A1[0][1]), U1[3], acc[3]); SCHEDB();
        acc[4] = MFMA16(cat8(A1[0][0], A1[0][1]), U1[4], acc[4]); SCHEDB();
        acc[5] = MFMA16(cat8(A1[0][0], A1[0][1]), U1[5], acc[5]); SCHEDB();
        acc[6] = MFMA16(cat8(A1[0][0], A1[0][1]), U1[6], acc[6]); SCHEDB();
        acc[7] = MFMA16(cat8(A1[0][0], A1[0][1]), U1[7], acc[7]); SCHEDB();
        acc[1] = MFMA16(cat8(A1[1][0], A1[1][1]), U1[0], acc[1]); SCHEDB();
        acc[2] = MFMA16(cat8(A1[1][0], A1[1][1]), U1[1], acc[2]); SCHEDB();
        acc[3] = MFMA16(cat8(A1[1][0], A1[1][1]), U1[2], acc[3]); SCHEDB();
        acc[4] = MFMA16(cat8(A1[1][0], A1[1][1]), U1[3], acc[4]); SCHEDB();
        acc[5] = MFMA16(cat8(A1[1][0], A1[1][1]), U1[4], acc[5]); SCHEDB();
        acc[6] = MFMA16(cat8(A1[1][0], A1[1][1]), U1[5], acc[6]); SCHEDB();
        acc[7] = MFMA16(cat8(A1[1][0], A1[1][1]), U1[6], acc[7]); SCHEDB();
        acc[2] = MFMA16(cat8(A1[2][0], A1[2][1]), U1[0], acc[2]); SCHEDB();
        acc[3] = MFMA16(cat8(A1[2][0], A1[2][1]), U1[1], acc[3]); SCHEDB();
        acc[4] = MFMA16(cat8(A1[2][0], A1[2][1]), U1[2], acc[4]); SCHEDB();
        acc[5] = MFMA16(cat8(A1[2][0], A1[2][1]), U1[3], acc[5]); SCHEDB();
        acc[6] = MFMA16(cat8(A1[2][0], A1[2][1]), U1[4], acc[6]); SCHEDB();
        acc[7] = MFMA16(cat8(A1[2][0], A1[2][1]), U1[5], acc[7]); SCHEDB();
        acc[3] = MFMA16(cat8(A1[3][0], A1[3][1]), U1[0], acc[3]); SCHEDB();
        acc[4] = MFMA16(cat8(A1[3][0], A1[3][1]), U1[1], acc[4]); SCHEDB();
        acc[5] = MFMA16(cat8(A1[3][0], A1[3][1]), U1[2], acc[5]); SCHEDB();
        acc[6] = MFMA16(cat8(A1[3][0], A1[3][1]), U1[3], acc[6]); SCHEDB();
        acc[7] = MFMA16(cat8(A1[3][0], A1[3][1]), U1[4], acc[7]); SCHEDB();
        acc[4] = MFMA16(cat8(A1[4][0], A1[4][1]), U1[0], acc[4]); SCHEDB();
        acc[5] = MFMA16(cat8(A1[4][0], A1[4][1]), U1[1], acc[5]); SCHEDB();
        acc[6] = MFMA16(cat8(A1[4][0], A1[4][1]), U1[2], acc[6]); SCHEDB();
        acc[7] = MFMA16(cat8(A1[4][0], A1[4][1]), U1[3], acc[7]); SCHEDB();
        acc[5] = MFMA16(cat8(A1[5][0], A1[5][1]), U1[0], acc[5]); SCHEDB();
        acc[6] = MFMA16(cat8(A1[5][0], A1[5][1]), U1[1], acc[6]); SCHEDB();
        acc[7] = MFMA16(cat8(A1[5][0], A1[5][1]), U1[2], acc[7]); SCHEDB();
        acc[6] = MFMA16(cat8(A1[6][0], A1[6][1]), U1[0], acc[6]); SCHEDB();
        acc[7] = MFMA16(cat8(A1[6][0], A1[6][1]), U1[1], acc[7]); SCHEDB();
        acc[7] = MFMA16(cat8(A1[7][0], A1[7][1]), U1[0], acc[7]); SCHEDB();
    }
    lds_sync(smem, sb, 8, lane);
    {
        char* rb = smem + sB + w * 6144 + lane * 16;
        int slot = 0;
#pragma unroll
        for (int tau = 0; tau < 8; ++tau) { if ((tau >> 1) != w) { *(f32x4*)(rb + slot * 1024) = acc[tau]; ++slot; } }
    }
    lds_sync(smem, sb, 12, lane);
    {
#pragma unroll
        for (int i = 0; i < 2; ++i) {
            f32x4 y;
            if (w == 0) y = acc[i]; else if (w == 1) y = acc[2 + i]; else if (w == 2) y = acc[4 + i]; else y = acc[6 + i];
            const int tau = 2 * w + i;
#pragma unroll
            for (int ow = 0; ow < 4; ++ow) {
                if (ow != w) {
                    const int sl = (tau < 2 * ow) ? tau : tau - 2;
                    y += *(const f32x4*)(smem + sB + ow * 6144 + sl * 1024 + lane * 16);
                }
            }
            const int l0 = 256 * tau + 16 * al + 4 * g;
            const u32x2 uv = *(const u32x2*)(smem + sU + 480 + 2 * l0), qv = *(const u32x2*)(smem + sU + 4608 + 2 * l0);
            float hv[4];
#pragma unroll
            for (int r = 0; r < 4; ++r) {
                const u32 uu = uv[r >> 1], qq = qv[r >> 1];
                const float u = (r & 1) ? bfhi(uu) : bflo(uu), q0 = (r & 1) ? bfhi(qq) : bflo(qq);
                const u32 yk = cvtpk(y[r], u * dsk);
                hv[r] = q0 + rbf(bflo(yk) + bfhi(yk));
            }
            st8<16>(wsr, WS_HC + (d * L_ + l0) * 2, u32x2{cvtpk(hv[0], hv[1]), cvtpk(hv[2], hv[3])});
        }
    }
    return tag;
}

constexpr int G1_STAGE = 40960, G1_NS = 4, G1_NST = 10;
constexpr int G1_LDS = G1_NS * G1_STAGE;


DI float phase_gemm1(char* __restrict__ ws, char* smem, u32 tag) {
    const int b = blockIdx.x, t = threadIdx.x, lane = t & 63;
    const int w = __builtin_amdgcn_readfirstlane(t >> 6);
    const int xcd = b & 7, j = b >> 3;
    const int P = (xcd & 1) * 8 + (j & 7), S = (xcd >> 1) * 4 + (j >> 3);
    const u32 lds0 = (u32)(uintptr_t)smem;
    const rsrc_t wsr = mkrsrc(ws, WS_END);
    WAITV(0);
    BAR();
    if (t == 0) st_flag(ws, WS_F2 + b * 128, tag);
    if (w == 8 || w == 9) poll_flags(wsr, WS_FW + (w - 8) * 64 * 128, 64, tag, lane);
    BAR();
    if (w >= 4) {
        const int lw = w - 4;
        const char* wsrc = ws + WS_W1T + S * 10 * 24576 + lw * 1024 + lane * 16;
        const int hk = 16 * ((lw >> 2) & 1) + 4 * ((lw >> 1) & 1) + 8 * ((lane >> 3) & 1) + ((lane >> 1) & 3);
        const char* hsrc = ws + WS_HC + (hk * L_ + 128 * P + 64 * (lw & 1) + 16 * (lane >> 4) + 8 * (lane & 1)) * 2;
        auto issue_h = [&](int st, int slot) {
#pragma unroll
            for (int i = 0; i < 2; ++i) glds16(hsrc + (st * 64 + 32 * i) * L_ * 2, lds0 + slot * G1_STAGE + 24576 + (lw + 8 * i) * 1024);
        };
        auto issue_w = [&](int st, int slot) {
#pragma unroll
            for (int i = 0; i < 3; ++i) glds16(wsrc + st * 24576 + i * 8192, lds0 + slot * G1_STAGE + lw * 1024 + i * 8192);
        };
        issue_w(0, 0); issue_w(1, 1); issue_w(2, 2); issue_w(3, 3);
        BAR();
        if (b == 0 && t == 256) st_flag(ws, WS_EP, tag);
        issue_h(0, 0); issue_h(1, 1); issue_h(2, 2); issue_h(3, 3);
        waitv<6>(); BAR();
        waitv<4>(); BAR(); issue_h(4, 0); issue_w(4, 0);
        waitv<7>(); BAR(); issue_h(5, 1); issue_w(5, 1);
        waitv<10>(); BAR(); issue_h(6, 2); issue_w(6, 2);
        waitv<10>(); BAR(); issue_h(7, 3); issue_w(7, 3);
        waitv<10>(); BAR(); issue_h(8, 0); issue_w(8, 0);
        waitv<10>(); BAR(); issue_h(9, 1); issue_w(9, 1);
        waitv<10>(); BAR();
        waitv<5>(); BAR();
        waitv<0>(); BAR();
        BAR();
        BAR();
        if (lw == 0) {
            const int r = lane & 7, part = lane >> 3;
            float s1 = 0.f, s2 = 0.f;
#pragma unroll
            for (int i = 0; i < 8; ++i) {
                const int e = 8 * part + i;
                const f32x4 v = *(const f32x4*)(smem + ((e >> 4) * 64 + (e & 15) * 4 + (r >> 1)) * 16);
                s1 += (r & 1) ? v[1] : v[0]; s2 += (r & 1) ? v[3] : v[2];
            }
            s1 = dppadd<0x128>(s1); s2 = dppadd<0x128>(s2);
            s1 = swap32add(swap16add(s1)); s2 = swap32add(swap16add(s2));
            const float mean = s1 * (1.f / 640.f);
            float var = s2 * (1.f / 640.f) - mean * mean;
            var = var > 0.f ? var : 0.f;
            const float rs = 1.f / rbf(__builtin_sqrtf(rbf(rbf(var) + 1e-5f)));
            return rs * rs;
        }
        return 0.f;
    }
    poll_flags(wsr, WS_F2 + w * 64 * 128, 64, tag, lane);
    BAR();
    const int wm = w & 1, wn = w >> 1;
    const int g = lane >> 4, q = (lane & 15) >> 2, p = lane & 3;
    f32x4 acc[6][4];
#pragma unroll
    for (int a = 0; a < 6; ++a)
#pragma unroll
        for (int c = 0; c < 4; ++c) acc[a][c] = f32x4{0.f, 0.f, 0.f, 0.f};
    const LAS char* abase = (const LAS char*)smem + wm * 6144 + lane * 16;
    const LAS char* bbase = (const LAS char*)smem + 24576 + (g >> 1) * 4096 + (g & 1) * 128 + q * 32 + 8 * p + wn * 1024;
    const LAS char* sbase = (const LAS char*)smem + 24576 + (2 * w + (lane >> 5)) * 2048 + (S >> 1) * 256 + ((lane >> 2) & 7) * 32 + (S & 1) * 16 + (lane & 3) * 4;
    float s1a = 0.f, s1b = 0.f, s2a = 0.f, s2b = 0.f;
    bf16x8 AL[3], AH[3]; s16x4 B0[4][2], B1[4][2];
    u32 sv;
    BAR();
    AL[0] = *(const LAS bf16x8*)(abase + 0); B0[0][0] = trread(bbase + 0); B0[0][1] = trread(bbase + 2048); B0[1][0] = trread(bbase + 256); B0[1][1] = trread(bbase + 2304); AL[1] = *(const LAS bf16x8*)(abase + 1024); B0[2][0] = trread(bbase + 512); B0[2][1] = trread(bbase + 2560); B0[3][0] = trread(bbase + 768); B0[3][1] = trread(bbase + 2816); AL[2] = *(const LAS bf16x8*)(abase + 2048);
    SCHEDB();
#pragma unroll 1
    for (int st = 0; st < G1_NST; ++st) {
        acc[0+0][0] = MFMA16(AL[0], cat8(B0[0][0], B0[0][1]), acc[0+0][0]);
        AH[0] = *(const LAS bf16x8*)(abase + 3072);
        SCHEDB();
        acc[0+0][1] = MFMA16(AL[0], cat8(B0[1][0], B0[1][1]), acc[0+0][1]);
        AH[1] = *(const LAS bf16x8*)(abase + 4096);
        SCHEDB();
        acc[0+1][0] = MFMA16(AL[1], cat8(B0[0][0], B0[0][1]), acc[0+1][0]);
        B1[0][0] = trread(bbase + 8192);
        SCHEDB();
        acc[0+1][1] = MFMA16(AL[1], cat8(B0[1][0], B0[1][1]), acc[0+1][1]);
        B1[0][1] = trread(bbase + 10240);
        SCHEDB();
        acc[0+0][2] = MFMA16(AL[0], cat8(B0[2][0], B0[2][1]), acc[0+0][2]);
        B1[1][0] = trread(bbase + 8448);
        SCHEDB();
        acc[0+1][2] = MFMA16(AL[1], cat8(B0[2][0], B0[2][1]), acc[0+1][2]);
        B1[1][1] = trread(bbase + 10496);
        SCHEDB();
        acc[0+0][3] = MFMA16(AL[0], cat8(B0[3][0], B0[3][1]), acc[0+0][3]);
        AH[2] = *(const LAS bf16x8*)(abase + 5120);
        SCHEDB();
        acc[0+1][3] = MFMA16(AL[1], cat8(B0[3][0], B0[3][1]), acc[0+1][3]);
        SCHEDB();
        acc[0+2][0] = MFMA16(AL[2], cat8(B0[0][0], B0[0][1]), acc[0+2][0]);
        SCHEDB();
        acc[0+2][1] = MFMA16(AL[2], cat8(B0[1][0], B0[1][1]), acc[0+2][1]);
        SCHEDB();
        acc[0+2][2] = MFMA16(AL[2], cat8(B0[2][0], B0[2][1]), acc[0+2][2]);
        SCHEDB();
        acc[0+2][3] = MFMA16(AL[2], cat8(B0[3][0], B0[3][1]), acc[0+2][3]);
        SCHEDB();
        acc[3+0][0] = MFMA16(AH[0], cat8(B0[0][0], B0[0][1]), acc[3+0][0]);
        AL[0] = *(const LAS bf16x8*)(abase + 12288);
        SCHEDB();
        acc[3+0][1] = MFMA16(AH[0], cat8(B0[1][0], B0[1][1]), acc[3+0][1]);
        AL[1] = *(const LAS bf16x8*)(abase + 13312);
        SCHEDB();
        acc[3+1][0] = MFMA16(AH[1], cat8(B0[0][0], B0[0][1]), acc[3+1][0]);
        sv = *(const LAS u32*)(sbase);
        SCHEDB();
        acc[3+1][1] = MFMA16(AH[1], cat8(B0[1][0], B0[1][1]), acc[3+1][1]);
        B1[2][0] = trread(bbase + 8704);
        SCHEDB();
        acc[3+0][2] = MFMA16(AH[0], cat8(B0[2][0], B0[2][1]), acc[3+0][2]);
        B1[2][1] = trread(bbase + 10752);
        SCHEDB();
        acc[3+1][2] = MFMA16(AH[1], cat8(B0[2][0], B0[2][1]), acc[3+1][2]);
        B1[3][0] = trread(bbase + 8960);
        SCHEDB();
        acc[3+0][3] = MFMA16(AH[0], cat8(B0[3][0], B0[3][1]), acc[3+0][3]);
        B1[3][1] = trread(bbase + 11008);
        SCHEDB();
        acc[3+1][3] = MFMA16(AH[1], cat8(B0[3][0], B0[3][1]), acc[3+1][3]);
        AL[2] = *(const LAS bf16x8*)(abase + 14336);
        SCHEDB();
        acc[3+2][0] = MFMA16(AH[2], cat8(B0[0][0], B0[0][1]), acc[3+2][0]);
        SCHEDB();
        acc[3+2][1] = MFMA16(AH[2], cat8(B0[1][0], B0[1][1]), acc[3+2][1]);
        SCHEDB();
        acc[3+2][2] = MFMA16(AH[2], cat8(B0[2][0], B0[2][1]), acc[3+2][2]);
        SCHEDB();
        acc[3+2][3] = MFMA16(AH[2], cat8(B0[3][0], B0[3][1]), acc[3+2][3]);
        SCHEDB();
        acc[0+0][0] = MFMA16(AL[0], cat8(B1[0][0], B1[0][1]), acc[0+0][0]);
        AH[0] = *(const LAS bf16x8*)(abase + 15360);
        SCHEDB();
        acc[0+0][1] = MFMA16(AL[0], cat8(B1[1][0], B1[1][1]), acc[0+0][1]);
        AH[1] = *(const LAS bf16x8*)(abase + 16384);
        SCHEDB();
        acc[0+1][0] = MFMA16(AL[1], cat8(B1[0][0], B1[0][1]), acc[0+1][0]);
        AH[2] = *(const LAS bf16x8*)(abase + 17408);
        SCHEDB();
        acc[0+1][1] = MFMA16(AL[1], cat8(B1[1][0], B1[1][1]), acc[0+1][1]);
        SCHEDB();
        acc[0+0][2] = MFMA16(AL[0], cat8(B1[2][0], B1[2][1]), acc[0+0][2]);
        SCHEDB();
        acc[0+1][2] = MFMA16(AL[1], cat8(B1[2][0], B1[2][1]), acc[0+1][2]);
        SCHEDB();
        acc[0+0][3] = MFMA16(AL[0], cat8(B1[3][0], B1[3][1]), acc[0+0][3]);
        SCHEDB();
        acc[0+1][3] = MFMA16(AL[1], cat8(B1[3][0], B1[3][1]), acc[0+1][3]);
        SCHEDB();
        acc[0+2][0] = MFMA16(AL[2], cat8(B1[0][0], B1[0][1]), acc[0+2][0]);
        SCHEDB();
        acc[0+2][1] = MFMA16(AL[2], cat8(B1[1][0], B1[1][1]), acc[0+2][1]);
        SCHEDB();
        acc[0+2][2] = MFMA16(AL[2], cat8(B1[2][0], B1[2][1]), acc[0+2][2]);
        SCHEDB();
        acc[0+2][3] = MFMA16(AL[2], cat8(B1[3][0], B1[3][1]), acc[0+2][3]);
        SCHEDB();
        { const float a = bflo(sv), c = bfhi(sv); s1a += a; s1b += c; s2a = __builtin_fmaf(a, a, s2a); s2b = __builtin_fmaf(c, c, s2b); }
        WAITL0();
        BAR();
        { const int dlt = (st & 3) == 3 ? -3 * G1_STAGE : G1_STAGE; abase += dlt; bbase += dlt; sbase += dlt; }
        acc[3+0][0] = MFMA16(AH[0], cat8(B1[0][0], B1[0][1]), acc[3+0][0]);
        AL[0] = *(const LAS bf16x8*)(abase + 0);
        SCHEDB();
        acc[3+0][1] = MFMA16(AH[0], cat8(B1[1][0], B1[1][1]), acc[3+0][1]);
        B0[0][0] = trread(bbase + 0);
        SCHEDB();
        acc[3+1][0] = MFMA16(AH[1], cat8(B1[0][0], B1[0][1]), acc[3+1][0]);
        B0[0][1] = trread(bbase + 2048);
        SCHEDB();
        acc[3+1][1] = MFMA16(AH[1], cat8(B1[1][0], B1[1][1]), acc[3+1][1]);
        B0[1][0] = trread(bbase + 256);
        SCHEDB();
        acc[3+0][2] = MFMA16(AH[0], cat8(B1[2][0], B1[2][1]), acc[3+0][2]);
        B0[1][1] = trread(bbase + 2304);
        SCHEDB();
        acc[3+1][2] = MFMA16(AH[1], cat8(B1[2][0], B1[2][1]), acc[3+1][2]);
        AL[1] = *(const LAS bf16x8*)(abase + 1024);
        SCHEDB();
        acc[3+0][3] = MFMA16(AH[0], cat8(B1[3][0], B1[3][1]), acc[3+0][3]);
        B0[2][0] = trread(bbase + 512);
        SCHEDB();
        acc[3+1][3] = MFMA16(AH[1], cat8(B1[3][0], B1[3][1]), acc[3+1][3]);
        B0[2][1] = trread(bbase + 2560);
        SCHEDB();
        acc[3+2][0] = MFMA16(AH[2], cat8(B1[0][0], B1[0][1]), acc[3+2][0]);
        B0[3][0] = trread(bbase + 768);
        SCHEDB();
        acc[3+2][1] = MFMA16(AH[2], cat8(B1[1][0], B1[1][1]), acc[3+2][1]);
        B0[3][1] = trread(bbase + 2816);
        SCHEDB();
        acc[3+2][2] = MFMA16(AH[2], cat8(B1[2][0], B1[2][1]), acc[3+2][2]);
        AL[2] = *(const LAS bf16x8*)(abase + 2048);
        SCHEDB();
        acc[3+2][3] = MFMA16(AH[2], cat8(B1[3][0], B1[3][1]), acc[3+2][3]);
        SCHEDB();
    }
    *(f32x4*)(smem + (w * 64 + lane) * 16) = f32x4{s1a, s1b, s2a, s2b};
    WAITL0();
    BAR();
    int tz = threadIdx.x, bz = blockIdx.x;
    asm volatile("" : "+v"(tz), "+v"(bz));
    const int lane_e = tz & 63, g_e = lane_e >> 4;
    const int P_e = (bz & 1) * 8 + ((bz >> 3) & 7), S_e = ((bz & 7) >> 1) * 4 + (bz >> 6);
    const rsrc_t rs = wsr;
#pragma unroll
    for (int a = 0; a < 3; ++a) {
        const int f0 = 96 * S_e + 16 * (3 * wm + a);
        const int kb = f0 >> 5, oct = ((f0 & 31) >> 3) + (g_e >> 1);
#pragma unroll
        for (int c = 0; c < 4; c += 2) {
            u32 X[2], Y[2];
#pragma unroll
            for (int e = 0; e < 2; ++e) {
                X[e] = cvtpk(__builtin_fmaxf(acc[2 * a][c][2 * e], 0.f) * acc[2 * a + 1][c][2 * e], __builtin_fmaxf(acc[2 * a][c][2 * e + 1], 0.f) * acc[2 * a + 1][c][2 * e + 1]);
                Y[e] = cvtpk(__builtin_fmaxf(acc[2 * a][c + 1][2 * e], 0.f) * acc[2 * a + 1][c + 1][2 * e], __builtin_fmaxf(acc[2 * a][c + 1][2 * e + 1], 0.f) * acc[2 * a + 1][c + 1][2 * e + 1]);
            }
#pragma unroll
            for (int e = 0; e < 2; ++e) { auto r = __builtin_amdgcn_permlane16_swap(X[e], Y[e], false, false); X[e] = r[0]; Y[e] = r[1]; }
            const int lt = 8 * P_e + 4 * wn + c + (g_e & 1);
            st16<16>(rs, WS_HM + ((lt * 48 + kb) * 64 + (lane_e & 15) + 16 * oct) * 16, u32x4{X[0], X[1], Y[0], Y[1]});
        }
    }
    return 0.f;
}

constexpr int G2_STAGE = 18432, G2_NS = 8, G2_NST = 24;
constexpr int G2_RES = G2_NS * G2_STAGE;
constexpr int G2_LDS = G2_RES + 10240;

DI void phase_gemm2(char* __restrict__ ws, u16* __restrict__ out, char* smem, u32 tag, float rs2pub) {
    const int b = blockIdx.x, t = threadIdx.x, lane = t & 63;
    const int w = __builtin_amdgcn_readfirstlane(t >> 6);
    const int xcd = b & 7, j = b >> 3;
    const int P = (xcd & 3) * 8 + (j & 7), S = (xcd >> 2) * 4 + (j >> 3);
    const u32 lds0 = (u32)(uintptr_t)smem;
    const rsrc_t wsr = mkrsrc(ws, WS_END);
    WAITV(0);
    BAR();
    if (w == 4) {
        const int P1 = (xcd & 1) * 8 + (j & 7), S1 = (xcd >> 1) * 4 + (j >> 3);
        const u32 up1 = __builtin_amdgcn_update_dpp(0, __float_as_uint(rs2pub), 0x111, 0xF, 0xF, true);
        if (lane < 9) st4<16>(wsr, WS_F3 + (P1 * 16 + S1) * 128 + lane * 4, lane == 0 ? tag : up1);
    }
    if (w >= 4) {
        const int lw = w - 4;
        const char* wsrc = ws + WS_W2T + S * 24 * 10240 + lw * 1024 + lane * 16;
        const char* msrc = ws + WS_HM + ((4 * P + (lw >> 1)) * 48 + (lw & 1)) * 1024 + lane * 16;
        auto issue_m = [&](int st, int slot) { glds16(msrc + st * 2048, lds0 + slot * G2_STAGE + 10240 + lw * 1024); };
        auto issue_w = [&](int st, int slot) {
            glds16(wsrc + st * 10240, lds0 + slot * G2_STAGE + lw * 1024);
            if (lw < 2) glds16(wsrc + st * 10240 + 8192, lds0 + slot * G2_STAGE + 8192 + lw * 1024);
        };
        if (lw < 2) {
            issue_w(0, 0);
            issue_w(1, 1);
            issue_w(2, 2);
            issue_w(3, 3);
            issue_w(4, 4);
            issue_w(5, 5);
            issue_w(6, 6);
            issue_w(7, 7);
            BAR();
            issue_m(0, 0);
            issue_m(1, 1);
            issue_m(2, 2);
            issue_m(3, 3);
            issue_m(4, 4);
            issue_m(5, 5);
            issue_m(6, 6);
            issue_m(7, 7);
            waitv<7>(); BAR();
            waitv<6>(); BAR();
            waitv<5>(); BAR();
            issue_m(8, 0); issue_w(8, 0);
            waitv<7>(); BAR();
            issue_m(9, 1); issue_w(9, 1);
            waitv<9>(); BAR();
            issue_m(10, 2); issue_w(10, 2);
            waitv<11>(); BAR();
            issue_m(11, 3); issue_w(11, 3);
            waitv<13>(); BAR();
            issue_m(12, 4); issue_w(12, 4);
            waitv<15>(); BAR();
            issue_m(13, 5); issue_w(13, 5);
            waitv<15>(); BAR();
            issue_m(14, 6); issue_w(14, 6);
            waitv<15>(); BAR();
            issue_m(15, 7); issue_w(15, 7);
            waitv<15>(); BAR();
            issue_m(16, 0); issue_w(16, 0);
            waitv<15>(); BAR();
            issue_m(17, 1); issue_w(17, 1);
            waitv<15>(); BAR();
            issue_m(18, 2); issue_w(18, 2);
            waitv<15>(); BAR();
            issue_m(19, 3); issue_w(19, 3);
            waitv<15>(); BAR();
            issue_m(20, 4); issue_w(20, 4);
            waitv<15>(); BAR();
            issue_m(21, 5); issue_w(21, 5);
            waitv<15>(); BAR();
            issue_m(22, 6); issue_w(22, 6);
            waitv<15>(); BAR();
            issue_m(23, 7); issue_w(23, 7);
            waitv<15>(); BAR();
            waitv<12>(); BAR();
            waitv<9>(); BAR();
            waitv<6>(); BAR();
            waitv<3>(); BAR();
            waitv<0>(); BAR();
        } else {
            issue_w(0, 0);
            issue_w(1, 1);
            issue_w(2, 2);
            issue_w(3, 3);
            issue_w(4, 4);
            issue_w(5, 5);
            issue_w(6, 6);
            issue_w(7, 7);
            BAR();
            issue_m(0, 0);
            issue_m(1, 1);
            issue_m(2, 2);
            issue_m(3, 3);
            issue_m(4, 4);
            issue_m(5, 5);
            issue_m(6, 6);
            issue_m(7, 7);
            waitv<7>(); BAR();
            waitv<6>(); BAR();
            waitv<5>(); BAR();
            issue_m(8, 0); issue_w(8, 0);
            waitv<6>(); BAR();
            issue_m(9, 1); issue_w(9, 1);
            waitv<7>(); BAR();
            issue_m(10, 2); issue_w(10, 2);
            waitv<8>(); BAR();
            issue_m(11, 3); issue_w(11, 3);
            waitv<9>(); BAR();
            issue_m(12, 4); issue_w(12, 4);
            waitv<10>(); BAR();
            issue_m(13, 5); issue_w(13, 5);
            waitv<10>(); BAR();
            issue_m(14, 6); issue_w(14, 6);
            waitv<10>(); BAR();
            issue_m(15, 7); issue_w(15, 7);
            waitv<10>(); BAR();
            issue_m(16, 0); issue_w(16, 0);
            waitv<10>(); BAR();
            issue_m(17, 1); issue_w(17, 1);
            waitv<10>(); BAR();
            issue_m(18, 2); issue_w(18, 2);
            waitv<10>(); BAR();
            issue_m(19, 3); issue_w(19, 3);
            waitv<10>(); BAR();
            issue_m(20, 4); issue_w(20, 4);
            waitv<10>(); BAR();
            issue_m(21, 5); issue_w(21, 5);
            waitv<10>(); BAR();
            issue_m(22, 6); issue_w(22, 6);
            waitv<10>(); BAR();
            issue_m(23, 7); issue_w(23, 7);
            waitv<10>(); BAR();
            waitv<8>(); BAR();
            waitv<6>(); BAR();
            waitv<4>(); BAR();
            waitv<2>(); BAR();
            waitv<0>(); BAR();
        }
        asm volatile(".Lk_block_last:");
        return;
    }
    if (w == 0) poll_flags(wsr, WS_F3 + (P >> 1) * 16 * 128, 16, tag, lane);
    BAR();
#pragma unroll
    for (int i = 0; i < 3; ++i) {
        const int row = 32 * i + (lane >> 1);
        if (row < 80) glds16(ws + WS_HC + ((80 * S + row) * L_ + 64 * P + 16 * w + 8 * (lane & 1)) * 2, lds0 + G2_RES + w * 2560 + i * 1024);
    }
    const int g = lane >> 4, q = (lane & 15) >> 2, p = lane & 3;
    const int l = 64 * P + 16 * w + (lane & 15);
    const float rs2 = __uint_as_float(__builtin_amdgcn_raw_buffer_load_b32(wsr, WS_F3 + (l >> 3) * 128 + 4 + (l & 7) * 4, 0, 16));
    const float m1 = bflo(__builtin_amdgcn_raw_buffer_load_b32(wsr, WS_GR + (l >> 3) * 128 + (l & 7) * 8, 0, 16));
    f32x4 acc[5];
#pragma unroll
    for (int a = 0; a < 5; ++a) acc[a] = f32x4{0.f, 0.f, 0.f, 0.f};
    const LAS char* abase = (const LAS char*)smem + lane * 16;
    const LAS char* bbase = (const LAS char*)smem + 10240 + w * 2048 + lane * 16;
    bf16x8 EA[2][5], EB[2], OA[2][5], OB[2];
    BAR();
    EB[0] = *(const LAS bf16x8*)(bbase); EB[1] = *(const LAS bf16x8*)(bbase + 1024);
#pragma unroll
    for (int a = 0; a < 5; ++a) { EA[0][a] = *(const LAS bf16x8*)(abase + a * 1024); EA[1][a] = *(const LAS bf16x8*)(abase + 5120 + a * 1024); }
    SCHEDB();
#pragma unroll 1
    for (int it = 0; it < 3; ++it) {
        BAR();
        acc[0] = MFMA16(EA[0][0], EB[0], acc[0]);
        OB[0] = *(const LAS bf16x8*)(bbase + 1*G2_STAGE);
        OA[0][0] = *(const LAS bf16x8*)(abase + 1*G2_STAGE + 0);
        SCHEDB();
        acc[1] = MFMA16(EA[0][1], EB[0], acc[1]);
        OA[0][1] = *(const LAS bf16x8*)(abase + 1*G2_STAGE + 1024);
        OA[0][2] = *(const LAS bf16x8*)(abase + 1*G2_STAGE + 2048);
        SCHEDB();
        acc[2] = MFMA16(EA[0][2], EB[0], acc[2]);
        OA[0][3] = *(const LAS bf16x8*)(abase + 1*G2_STAGE + 3072);
        SCHEDB();
        acc[3] = MFMA16(EA[0][3], EB[0], acc[3]);
        OA[0][4] = *(const LAS bf16x8*)(abase + 1*G2_STAGE + 4096);
        SCHEDB();
        acc[4] = MFMA16(EA[0][4], EB[0], acc[4]);
        OB[1] = *(const LAS bf16x8*)(bbase + 1*G2_STAGE + 1024);
        SCHEDB();
        acc[0] = MFMA16(EA[1][0], EB[1], acc[0]);
        OA[1][0] = *(const LAS bf16x8*)(abase + 1*G2_STAGE + 5120);
        SCHEDB();
        acc[1] = MFMA16(EA[1][1], EB[1], acc[1]);
        OA[1][1] = *(const LAS bf16x8*)(abase + 1*G2_STAGE + 6144);
        SCHEDB();
        acc[2] = MFMA16(EA[1][2], EB[1], acc[2]);
        OA[1][2] = *(const LAS bf16x8*)(abase + 1*G2_STAGE + 7168);
        SCHEDB();
        acc[3] = MFMA16(EA[1][3], EB[1], acc[3]);
        OA[1][3] = *(const LAS bf16x8*)(abase + 1*G2_STAGE + 8192);
        SCHEDB();
        acc[4] = MFMA16(EA[1][4], EB[1], acc[4]);
        OA[1][4] = *(const LAS bf16x8*)(abase + 1*G2_STAGE + 9216);
        SCHEDB();
        BAR();
        acc[0] = MFMA16(OA[0][0], OB[0], acc[0]);
        EB[0] = *(const LAS bf16x8*)(bbase + 2*G2_STAGE);
        EA[0][0] = *(const LAS bf16x8*)(abase + 2*G2_STAGE + 0);
        SCHEDB();
        acc[1] = MFMA16(OA[0][1], OB[0], acc[1]);
        EA[0][1] = *(const LAS bf16x8*)(abase + 2*G2_STAGE + 1024);
        EA[0][2] = *(const LAS bf16x8*)(abase + 2*G2_STAGE + 2048);
        SCHEDB();
        acc[2] = MFMA16(OA[0][2], OB[0], acc[2]);
        EA[0][3] = *(const LAS bf16x8*)(abase + 2*G2_STAGE + 3072);
        SCHEDB();
        acc[3] = MFMA16(OA[0][3], OB[0], acc[3]);
        EA[0][4] = *(const LAS bf16x8*)(abase + 2*G2_STAGE + 4096);
        SCHEDB();
        acc[4] = MFMA16(OA[0][4], OB[0], acc[4]);
        EB[1] = *(const LAS bf16x8*)(bbase + 2*G2_STAGE + 1024);
        SCHEDB();
        acc[0] = MFMA16(OA[1][0], OB[1], acc[0]);
        EA[1][0] = *(const LAS bf16x8*)(abase + 2*G2_STAGE + 5120);
        SCHEDB();
        acc[1] = MFMA16(OA[1][1], OB[1], acc[1]);
        EA[1][1] = *(const LAS bf16x8*)(abase + 2*G2_STAGE + 6144);
        SCHEDB();
        acc[2] = MFMA16(OA[1][2], OB[1], acc[2]);
        EA[1][2] = *(const LAS bf16x8*)(abase + 2*G2_STAGE + 7168);
        SCHEDB();
        acc[3] = MFMA16(OA[1][3], OB[1], acc[3]);
        EA[1][3] = *(const LAS bf16x8*)(abase + 2*G2_STAGE + 8192);
        SCHEDB();
        acc[4] = MFMA16(OA[1][4], OB[1], acc[4]);
        EA[1][4] = *(const LAS bf16x8*)(abase + 2*G2_STAGE + 9216);
        SCHEDB();
        BAR();
        acc[0] = MFMA16(EA[0][0], EB[0], acc[0]);
        OB[0] = *(const LAS bf16x8*)(bbase + 3*G2_STAGE);
        OA[0][0] = *(const LAS bf16x8*)(abase + 3*G2_STAGE + 0);
        SCHEDB();
        acc[1] = MFMA16(EA[0][1], EB[0], acc[1]);
        OA[0][1] = *(const LAS bf16x8*)(abase + 3*G2_STAGE + 1024);
        OA[0][2] = *(const LAS bf16x8*)(abase + 3*G2_STAGE + 2048);
        SCHEDB();
        acc[2] = MFMA16(EA[0][2], EB[0], acc[2]);
        OA[0][3] = *(const LAS bf16x8*)(abase + 3*G2_STAGE + 3072);
        SCHEDB();
        acc[3] = MFMA16(EA[0][3], EB[0], acc[3]);
        OA[0][4] = *(const LAS bf16x8*)(abase + 3*G2_STAGE + 4096);
        SCHEDB();
        acc[4] = MFMA16(EA[0][4], EB[0], acc[4]);
        OB[1] = *(const LAS bf16x8*)(bbase + 3*G2_STAGE + 1024);
        SCHEDB();
        acc[0] = MFMA16(EA[1][0], EB[1], acc[0]);
        OA[1][0] = *(const LAS bf16x8*)(abase + 3*G2_STAGE + 5120);
        SCHEDB();
        acc[1] = MFMA16(EA[1][1], EB[1], acc[1]);
        OA[1][1] = *(const LAS bf16x8*)(abase + 3*G2_STAGE + 6144);
        SCHEDB();
        acc[2] = MFMA16(EA[1][2], EB[1], acc[2]);
        OA[1][2] = *(const LAS bf16x8*)(abase + 3*G2_STAGE + 7168);
        SCHEDB();
        acc[3] = MFMA16(EA[1][3], EB[1], acc[3]);
        OA[1][3] = *(const LAS bf16x8*)(abase + 3*G2_STAGE + 8192);
        SCHEDB();
        acc[4] = MFMA16(EA[1][4], EB[1], acc[4]);
        OA[1][4] = *(const LAS bf16x8*)(abase + 3*G2_STAGE + 9216);
        SCHEDB();
        BAR();
        acc[0] = MFMA16(OA[0][0], OB[0], acc[0]);
        EB[0] = *(const LAS bf16x8*)(bbase + 4*G2_STAGE);
        EA[0][0] = *(const LAS bf16x8*)(abase + 4*G2_STAGE + 0);
        SCHEDB();
        acc[1] = MFMA16(OA[0][1], OB[0], acc[1]);
        EA[0][1] = *(const LAS bf16x8*)(abase + 4*G2_STAGE + 1024);
        EA[0][2] = *(const LAS bf16x8*)(abase + 4*G2_STAGE + 2048);
        SCHEDB();
        acc[2] = MFMA16(OA[0][2], OB[0], acc[2]);
        EA[0][3] = *(const LAS bf16x8*)(abase + 4*G2_STAGE + 3072);
        SCHEDB();
        acc[3] = MFMA16(OA[0][3], OB[0], acc[3]);
        EA[0][4] = *(const LAS bf16x8*)(abase + 4*G2_STAGE + 4096);
        SCHEDB();
        acc[4] = MFMA16(OA[0][4], OB[0], acc[4]);
        EB[1] = *(const LAS bf16x8*)(bbase + 4*G2_STAGE + 1024);
        SCHEDB();
        acc[0] = MFMA16(OA[1][0], OB[1], acc[0]);
        EA[1][0] = *(const LAS bf16x8*)(abase + 4*G2_STAGE + 5120);
        SCHEDB();
        acc[1] = MFMA16(OA[1][1], OB[1], acc[1]);
        EA[1][1] = *(const LAS bf16x8*)(abase + 4*G2_STAGE + 6144);
        SCHEDB();
        acc[2] = MFMA16(OA[1][2], OB[1], acc[2]);
        EA[1][2] = *(const LAS bf16x8*)(abase + 4*G2_STAGE + 7168);
        SCHEDB();
        acc[3] = MFMA16(OA[1][3], OB[1], acc[3]);
        EA[1][3] = *(const LAS bf16x8*)(abase + 4*G2_STAGE + 8192);
        SCHEDB();
        acc[4] = MFMA16(OA[1][4], OB[1], acc[4]);
        EA[1][4] = *(const LAS bf16x8*)(abase + 4*G2_STAGE + 9216);
        SCHEDB();
        BAR();
        acc[0] = MFMA16(EA[0][0], EB[0], acc[0]);
        OB[0] = *(const LAS bf16x8*)(bbase + 5*G2_STAGE);
        OA[0][0] = *(const LAS bf16x8*)(abase + 5*G2_STAGE + 0);
        SCHEDB();
        acc[1] = MFMA16(EA[0][1], EB[0], acc[1]);
        OA[0][1] = *(const LAS bf16x8*)(abase + 5*G2_STAGE + 1024);
        OA[0][2] = *(const LAS bf16x8*)(abase + 5*G2_STAGE + 2048);
        SCHEDB();
        acc[2] = MFMA16(EA[0][2], EB[0], acc[2]);
        OA[0][3] = *(const LAS bf16x8*)(abase + 5*G2_STAGE + 3072);
        SCHEDB();
        acc[3] = MFMA16(EA[0][3], EB[0], acc[3]);
        OA[0][4] = *(const LAS bf16x8*)(abase + 5*G2_STAGE + 4096);
        SCHEDB();
        acc[4] = MFMA16(EA[0][4], EB[0], acc[4]);
        OB[1] = *(const LAS bf16x8*)(bbase + 5*G2_STAGE + 1024);
        SCHEDB();
        acc[0] = MFMA16(EA[1][0], EB[1], acc[0]);
        OA[1][0] = *(const LAS bf16x8*)(abase + 5*G2_STAGE + 5120);
        SCHEDB();
        acc[1] = MFMA16(EA[1][1], EB[1], acc[1]);
        OA[1][1] = *(const LAS bf16x8*)(abase + 5*G2_STAGE + 6144);
        SCHEDB();
        acc[2] = MFMA16(EA[1][2], EB[1], acc[2]);
        OA[1][2] = *(const LAS bf16x8*)(abase + 5*G2_STAGE + 7168);
        SCHEDB();
        acc[3] = MFMA16(EA[1][3], EB[1], acc[3]);
        OA[1][3] = *(const LAS bf16x8*)(abase + 5*G2_STAGE + 8192);
        SCHEDB();
        acc[4] = MFMA16(EA[1][4], EB[1], acc[4]);
        OA[1][4] = *(const LAS bf16x8*)(abase + 5*G2_STAGE + 9216);
        SCHEDB();
        BAR();
        acc[0] = MFMA16(OA[0][0], OB[0], acc[0]);
        EB[0] = *(const LAS bf16x8*)(bbase + 6*G2_STAGE);
        EA[0][0] = *(const LAS bf16x8*)(abase + 6*G2_STAGE + 0);
        SCHEDB();
        acc[1] = MFMA16(OA[0][1], OB[0], acc[1]);
        EA[0][1] = *(const LAS bf16x8*)(abase + 6*G2_STAGE + 1024);
        EA[0][2] = *(const LAS bf16x8*)(abase + 6*G2_STAGE + 2048);
        SCHEDB();
        acc[2] = MFMA16(OA[0][2], OB[0], acc[2]);
        EA[0][3] = *(const LAS bf16x8*)(abase + 6*G2_STAGE + 3072);
        SCHEDB();
        acc[3] = MFMA16(OA[0][3], OB[0], acc[3]);
        EA[0][4] = *(const LAS bf16x8*)(abase + 6*G2_STAGE + 4096);
        SCHEDB();
        acc[4] = MFMA16(OA[0][4], OB[0], acc[4]);
        EB[1] = *(const LAS bf16x8*)(bbase + 6*G2_STAGE + 1024);
        SCHEDB();
        acc[0] = MFMA16(OA[1][0], OB[1], acc[0]);
        EA[1][0] = *(const LAS bf16x8*)(abase + 6*G2_STAGE + 5120);
        SCHEDB();
        acc[1] = MFMA16(OA[1][1], OB[1], acc[1]);
        EA[1][1] = *(const LAS bf16x8*)(abase + 6*G2_STAGE + 6144);
        SCHEDB();
        acc[2] = MFMA16(OA[1][2], OB[1], acc[2]);
        EA[1][2] = *(const LAS bf16x8*)(abase + 6*G2_STAGE + 7168);
        SCHEDB();
        acc[3] = MFMA16(OA[1][3], OB[1], acc[3]);
        EA[1][3] = *(const LAS bf16x8*)(abase + 6*G2_STAGE + 8192);
        SCHEDB();
        acc[4] = MFMA16(OA[1][4], OB[1], acc[4]);
        EA[1][4] = *(const LAS bf16x8*)(abase + 6*G2_STAGE + 9216);
        SCHEDB();
        BAR();
        acc[0] = MFMA16(EA[0][0], EB[0], acc[0]);
        OB[0] = *(const LAS bf16x8*)(bbase + 7*G2_STAGE);
        OA[0][0] = *(const LAS bf16x8*)(abase + 7*G2_STAGE + 0);
        SCHEDB();
        acc[1] = MFMA16(EA[0][1], EB[0], acc[1]);
        OA[0][1] = *(const LAS bf16x8*)(abase + 7*G2_STAGE + 1024);
        OA[0][2] = *(const LAS bf16x8*)(abase + 7*G2_STAGE + 2048);
        SCHEDB();
        acc[2] = MFMA16(EA[0][2], EB[0], acc[2]);
        OA[0][3] = *(const LAS bf16x8*)(abase + 7*G2_STAGE + 3072);
        SCHEDB();
        acc[3] = MFMA16(EA[0][3], EB[0], acc[3]);
        OA[0][4] = *(const LAS bf16x8*)(abase + 7*G2_STAGE + 4096);
        SCHEDB();
        acc[4] = MFMA16(EA[0][4], EB[0], acc[4]);
        OB[1] = *(const LAS bf16x8*)(bbase + 7*G2_STAGE + 1024);
        SCHEDB();
        acc[0] = MFMA16(EA[1][0], EB[1], acc[0]);
        OA[1][0] = *(const LAS bf16x8*)(abase + 7*G2_STAGE + 5120);
        SCHEDB();
        acc[1] = MFMA16(EA[1][1], EB[1], acc[1]);
        OA[1][1] = *(const LAS bf16x8*)(abase + 7*G2_STAGE + 6144);
        SCHEDB();
        acc[2] = MFMA16(EA[1][2], EB[1], acc[2]);
        OA[1][2] = *(const LAS bf16x8*)(abase + 7*G2_STAGE + 7168);
        SCHEDB();
        acc[3] = MFMA16(EA[1][3], EB[1], acc[3]);
        OA[1][3] = *(const LAS bf16x8*)(abase + 7*G2_STAGE + 8192);
        SCHEDB();
        acc[4] = MFMA16(EA[1][4], EB[1], acc[4]);
        OA[1][4] = *(const LAS bf16x8*)(abase + 7*G2_STAGE + 9216);
        SCHEDB();
        if (it < 2) BAR();
        acc[0] = MFMA16(OA[0][0], OB[0], acc[0]);
        EB[0] = *(const LAS bf16x8*)(bbase + 0*G2_STAGE);
        EA[0][0] = *(const LAS bf16x8*)(abase + 0*G2_STAGE + 0);
        SCHEDB();
        acc[1] = MFMA16(OA[0][1], OB[0], acc[1]);
        EA[0][1] = *(const LAS bf16x8*)(abase + 0*G2_STAGE + 1024);
        EA[0][2] = *(const LAS bf16x8*)(abase + 0*G2_STAGE + 2048);
        SCHEDB();
        acc[2] = MFMA16(OA[0][2], OB[0], acc[2]);
        EA[0][3] = *(const LAS bf16x8*)(abase + 0*G2_STAGE + 3072);
        SCHEDB();
        acc[3] = MFMA16(OA[0][3], OB[0], acc[3]);
        EA[0][4] = *(const LAS bf16x8*)(abase + 0*G2_STAGE + 4096);
        SCHEDB();
        acc[4] = MFMA16(OA[0][4], OB[0], acc[4]);
        EB[1] = *(const LAS bf16x8*)(bbase + 0*G2_STAGE + 1024);
        SCHEDB();
        acc[0] = MFMA16(OA[1][0], OB[1], acc[0]);
        EA[1][0] = *(const LAS bf16x8*)(abase + 0*G2_STAGE + 5120);
        SCHEDB();
        acc[1] = MFMA16(OA[1][1], OB[1], acc[1]);
        EA[1][1] = *(const LAS bf16x8*)(abase + 0*G2_STAGE + 6144);
        SCHEDB();
        acc[2] = MFMA16(OA[1][2], OB[1], acc[2]);
        EA[1][2] = *(const LAS bf16x8*)(abase + 0*G2_STAGE + 7168);
        SCHEDB();
        acc[3] = MFMA16(OA[1][3], OB[1], acc[3]);
        EA[1][3] = *(const LAS bf16x8*)(abase + 0*G2_STAGE + 8192);
        SCHEDB();
        acc[4] = MFMA16(OA[1][4], OB[1], acc[4]);
        EA[1][4] = *(const LAS bf16x8*)(abase + 0*G2_STAGE + 9216);
        SCHEDB();
    }
    WAITV(0);
    const rsrc_t ro = mkrsrc(out, L_ * D_ * 2);
    char* ost = smem + w * G2_STAGE;
#pragma unroll
    for (int a = 0; a < 5; ++a) {
        const s16x4 hr = trread((const LAS char*)smem + G2_RES + w * 2560 + (16 * a + 4 * g + q) * 32 + 8 * p);
        const u32x2 hh = __builtin_bit_cast(u32x2, hr);
        const u32 f01 = cvtpk(acc[a][0] * rs2, acc[a][1] * rs2), f23 = cvtpk(acc[a][2] * rs2, acc[a][3] * rs2);
        const u32 c01 = hh[0], c23 = hh[1];
        const u32 h01 = cvtpk(bflo(c01) + m1, bfhi(c01) + m1), h23 = cvtpk(bflo(c23) + m1, bfhi(c23) + m1);
        *(u32x2*)(ost + (lane & 15) * 176 + 32 * a + 8 * g) = u32x2{cvtpk(bflo(h01) + bflo(f01), bfhi(h01) + bfhi(f01)), cvtpk(bflo(h23) + bflo(f23), bfhi(h23) + bfhi(f23))};
    }
#pragma unroll
    for (int i = 0; i < 3; ++i) {
        const int n = 64 * i + lane, r = n / 10, c = n - 10 * r;
        if (n < 160) {
            const u32x4 v = *(const u32x4*)(ost + r * 176 + c * 16);
            __builtin_amdgcn_raw_buffer_store_b128(v, ro, ((64 * P + 16 * w + r) * D_ + 80 * S + 8 * c) * 2, 0, 0);
        }
    }
}

constexpr int ALL_LDS = 163840;
static_assert(KA_LDS <= ALL_LDS && G1_LDS <= ALL_LDS && G2_LDS <= ALL_LDS, "LDS");

__global__ __launch_bounds__(768, 3) void k_block(const u16* __restrict__ x, const u16* __restrict__ Are, const u16* __restrict__ Aim, const u16* __restrict__ Bre,
                                                  const u16* __restrict__ Bim, const u16* __restrict__ Cre, const u16* __restrict__ Cim, const u16* __restrict__ logdt,
                                                  const u16* __restrict__ Dskip, const u16* __restrict__ Wg, const u16* __restrict__ Wu, const u16* __restrict__ Wd,
                                                  char* __restrict__ ws, u16* __restrict__ out) {
    __shared__ __attribute__((aligned(1024))) char smem[ALL_LDS];
    if (threadIdx.x >= 704) {
        unsigned long long pc; u32 len;
        asm volatile("s_getpc_b64 %0\n.Lk_block_here:\n\ts_mov_b32 %1, .Lk_block_last-.Lk_block_here" : "=s"(pc), "=s"(len));
        const int lane = threadIdx.x & 63;
        const u32 off = ((blockIdx.x >> 3) * 14 + lane) * 128;
        const char* p = (const char*)(pc + off);
        const u32 dump = (u32)(uintptr_t)smem + ALL_LDS - 256;
        if (lane < 14 && off + 4 <= len) { u32 sv; asm volatile("s_mov_b32 %0, m0\n\ts_mov_b32 m0, %2\n\ts_nop 0\n\tglobal_load_lds_dword %1, off\n\ts_mov_b32 m0, %0" : "=&s"(sv) : "v"(p), "s"(dump) : "memory"); }
    }
    const u32 tag = phase_ssm(x, Are, Aim, Bre, Bim, Cre, Cim, logdt, Dskip, Wg, Wu, Wd, ws, smem);
    const float rs2 = phase_gemm1(ws, smem, tag);
    phase_gemm2(ws, out, smem, tag, rs2);
}

extern "C" void kernel_launch(void* const* d_in, const int* in_sizes, int n_in, void* d_out, int out_size, void* d_ws, size_t ws_size, hipStream_t stream) {
    const u16* x = (const u16*)d_in[0];
    const u16* Are = (const u16*)d_in[1]; const u16* Aim = (const u16*)d_in[2];
    const u16* Bre = (const u16*)d_in[3]; const u16* Bim = (const u16*)d_in[4];
    const u16* Cre = (const u16*)d_in[5]; const u16* Cim = (const u16*)d_in[6];
    const u16* logdt = (const u16*)d_in[7]; const u16* Dskip = (const u16*)d_in[8];
    const u16* Wg = (const u16*)d_in[10]; const u16* Wu = (const u16*)d_in[11]; const u16* Wd = (const u16*)d_in[12];
    k_block<<<256, 768, 0, stream>>>(x, Are, Aim, Bre, Bim, Cre, Cim, logdt, Dskip, Wg, Wu, Wd, (char*)d_ws, (u16*)d_out);
}
```
